```python
import torch

def _softmax(x, axis=-1):
    return torch.softmax(x, dim=axis)
def _gelu(x):
    return torch.nn.functional.gelu(x, approximate="tanh")
def _silu(x):
    return torch.nn.functional.silu(x)
def _sigmoid(x):
    return torch.sigmoid(x)
def _layernorm(x, axis=-1, eps=1e-5):
    m = x.mean(dim=axis, keepdim=True)
    v = x.var(dim=axis, keepdim=True, unbiased=False)
    return (x - m) / torch.sqrt(v + eps)
def _scatter_add(tgt, idx, src, axis=0):
    return tgt.clone().index_add_(axis, idx, src)
def _segment_sum(src, seg, n):
    out = torch.zeros((n,) + src.shape[1:], dtype=src.dtype)
    return out.index_add_(0, seg, src)
def _pad(x, pw):
    flat = tuple(v for pair in reversed(pw) for v in pair)
    return torch.nn.functional.pad(x, flat)

kernel_name = 'sequential_preLN__grouped_kv_mqa__mixer_token_mlp__prompt'

def setup_inputs():

    _w = (lambda a: (lambda t: t.to(torch.bfloat16) if t.is_floating_point() else t)(torch.as_tensor(a)))
    def _A_setup():
        B, S, D, H = 1, 2048, 448, 16
        Dh = D // H
        return {
            "x": _w((lambda *s: torch.randn(*s, dtype=torch.bfloat16) * 0.05)(B, S, D)),
            "wq": _w((lambda *s: torch.randn(*s, dtype=torch.bfloat16) * 0.05)(D, H * Dh)),
            "wk": _w((lambda *s: torch.randn(*s, dtype=torch.bfloat16) * 0.05)(D, Dh)),
            "wv": _w((lambda *s: torch.randn(*s, dtype=torch.bfloat16) * 0.05)(D, Dh)),
            "wo": _w((lambda *s: torch.randn(*s, dtype=torch.bfloat16) * 0.05)(H * Dh, D)),
        }
    _A_in = _A_setup()

    def _B_setup():
        B, S, D, F = 1, 2048, 448, 1792
        return {
            "x": _w((lambda *s: torch.randn(*s, dtype=torch.bfloat16) * 0.05)(B, S, D)),
            "w1": _w((lambda *s: torch.randn(*s, dtype=torch.bfloat16) * 0.8)(S, F) * (S ** -0.5)),
            "b1": _w((lambda *s: torch.randn(*s, dtype=torch.bfloat16) * 12.8)(F) * 0.02),
            "w2": _w((lambda *s: torch.randn(*s, dtype=torch.bfloat16) * 0.2)(F, S) * (F ** -0.5)),
            "b2": _w((lambda *s: torch.randn(*s, dtype=torch.bfloat16) * 3.2)(S) * 0.02),
        }
    _B_in = _B_setup()

    return {
        "A_x": _A_in["x"],
        "A_wq": _A_in["wq"],
        "A_wk": _A_in["wk"],
        "A_wv": _A_in["wv"],
        "A_wo": _A_in["wo"],
        "B_x": _B_in["x"],
        "B_w1": _B_in["w1"],
        "B_b1": _B_in["b1"],
        "B_w2": _B_in["w2"],
        "B_b2": _B_in["b2"],
    }

def reference(A_x, A_wq, A_wk, A_wv, A_wo, B_x, B_w1, B_b1, B_w2, B_b2):
    def _A_ref(x, wq, wk, wv, wo):
        B, S, D = x.shape
        H = 16
        Dh = D // H
        q = (lambda x, *d: x.permute(*d))((x @ wq).reshape(B, S, H, Dh), 0, 2, 1, 3)
        k = x @ wk
        v = x @ wv
        scores = torch.einsum("bhqd,bkd->bhqk", q, k) * (Dh ** -0.5)
        p = _softmax(scores, axis=-1)
        o = torch.einsum("bhqk,bkd->bhqd", p, v)
        return (lambda x, *d: x.permute(*d))(o, 0, 2, 1, 3).reshape(B, S, H * Dh) @ wo

    def _B_ref(x, w1, b1, w2, b2):
        B, S, D = x.shape
        h = _layernorm(x, axis=-1)
        u = torch.einsum("bsd,sf->bdf", h, w1) + b1
        u = _gelu(u)
        y = torch.einsum("bdf,fs->bds", u, w2) + b2
        return (lambda x, *d: x.permute(*d))(y, 0, 2, 1)

    x = A_x
    h = x + _A_ref(_layernorm(x, axis=-1), A_wq, A_wk, A_wv, A_wo)
    h2 = h + _B_ref(_layernorm(h, axis=-1), B_w1, B_b1, B_w2, B_b2)
    return h2
```

```cpp
#include <hip/hip_runtime.h>
#include <hip/hip_bf16.h>

typedef unsigned short u16;
using bf16x8 = __attribute__((ext_vector_type(8))) short;
using s16x4 = __attribute__((ext_vector_type(4))) short;
using f32x4 = __attribute__((ext_vector_type(4))) float;
using f32x16 = __attribute__((ext_vector_type(16))) float;
using u32x4 = __attribute__((ext_vector_type(4))) unsigned;
using u32x2 = __attribute__((ext_vector_type(2))) unsigned;
typedef short v4i16_t __attribute__((ext_vector_type(4)));
typedef __attribute__((address_space(3))) char* lds_ptr;
typedef __attribute__((address_space(3))) const char* lds_cptr;

#define DI __device__ __forceinline__
#define MFMA32(a, b, c) __builtin_amdgcn_mfma_f32_32x32x16_bf16(a, b, c, 0, 0, 0)
#define MFMA16(a, b, c) __builtin_amdgcn_mfma_f32_16x16x32_bf16(a, b, c, 0, 0, 0)

constexpr int S = 2048, D = 448, NH = 16, DH = 28, F = 1792;
constexpr float QSCALE = 0.18898223650461362f * 1.4426950408889634f;
constexpr float LN_EPS = 1e-5f;

constexpr size_t WS_QF = 0;
constexpr size_t WS_KF = WS_QF + 2097152;
constexpr size_t WS_VF = WS_KF + 131072;
constexpr size_t WS_WOF = WS_VF + 131072;
constexpr size_t WS_H = WS_WOF + 401408;
constexpr size_t WS_HN = WS_H + 1835008;
constexpr size_t WS_U = WS_HN + 1835008;

DI unsigned cvtpk(float lo, float hi) {
  unsigned r;
  asm("v_cvt_pk_bf16_f32 %0, %1, %2" : "=v"(r) : "v"(lo), "v"(hi));
  return r;
}
DI float bflo(unsigned u) { return __uint_as_float(u << 16); }
DI float bfhi(unsigned u) { return __uint_as_float(u & 0xffff0000u); }
template <int CTRL> DI float dppf(float v) {
  return __uint_as_float(__builtin_amdgcn_update_dpp(0u, __float_as_uint(v), CTRL, 0xf, 0xf, true));
}
DI float wave_sum(float v) {
  v += dppf<0xB1>(v);
  v += dppf<0x4E>(v);
  v += dppf<0x141>(v);
  v += dppf<0x140>(v);
  auto a = __builtin_amdgcn_permlane16_swap(__float_as_uint(v), __float_as_uint(v), false, false);
  v = __uint_as_float(a[0]) + __uint_as_float(a[1]);
  auto b = __builtin_amdgcn_permlane32_swap(__float_as_uint(v), __float_as_uint(v), false, false);
  return __uint_as_float(b[0]) + __uint_as_float(b[1]);
}
DI float half_max(float v) {
  auto b = __builtin_amdgcn_permlane32_swap(__float_as_uint(v), __float_as_uint(v), false, false);
  return __builtin_fmaxf(__uint_as_float(b[0]), __uint_as_float(b[1]));
}
DI void glds16(const void* g, unsigned lds_base) {
  unsigned sv;
  asm volatile("s_mov_b32 %0, m0\n\ts_mov_b32 m0, %2\n\ts_nop 0\n\tglobal_load_lds_dwordx4 %1, off\n\ts_mov_b32 m0, %0"
               : "=&s"(sv) : "v"(g), "s"(lds_base) : "memory");
}
DI s16x4 trread(lds_cptr p) {
  return __builtin_bit_cast(s16x4, __builtin_amdgcn_ds_read_tr16_b64_v4i16((__attribute__((address_space(3))) v4i16_t*)p));
}
DI bf16x8 ldsr16(lds_cptr p) { return *(const __attribute__((address_space(3))) bf16x8*)p; }
DI bf16x8 cat8(s16x4 a, s16x4 b) { return __builtin_shufflevector(a, b, 0, 1, 2, 3, 4, 5, 6, 7); }
DI int xcd_remap(int bid, int nwg) {
  const int q = nwg >> 3, r = nwg & 7, x = bid & 7, o = bid >> 3;
  return (x < r ? x * (q + 1) : r * (q + 1) + (x - r) * q) + o;
}
#define WAITV_BAR(N) asm volatile("s_waitcnt vmcnt(" #N ") lgkmcnt(0)\n\ts_barrier" ::: "memory")

constexpr int K1_W = 0, K1_X = 448 * 128, K1_XS = 1040, K1_LDS = K1_X + 56 * K1_XS;
__global__ __launch_bounds__(512) void k_qkv(const u16* __restrict__ x, const u16* __restrict__ wq, const u16* __restrict__ wk,
                                             const u16* __restrict__ wv, const u16* __restrict__ wo, u16* __restrict__ Qf,
                                             u16* __restrict__ Kf, u16* __restrict__ Vf, u16* __restrict__ Wof) {
  __shared__ __attribute__((aligned(16))) char lds[K1_LDS];
  const int tid = threadIdx.x, lane = tid & 63, w = __builtin_amdgcn_readfirstlane(tid >> 6);
  const int t = xcd_remap(blockIdx.x, 256), rt = t >> 3, ct = t & 7;
  const unsigned lds0 = (unsigned)(uintptr_t)lds;
  if (ct < 7) {
#pragma unroll
    for (int i = 0; i < 7; ++i) {
      const int piece = w * 7 + i, row = piece * 8 + (lane >> 3);
      const int f = ((row >> 1) & 1) | (((row >> 3) & 1) << 1);
      const int c = (lane & 7) ^ (f << 1);
      glds16(wq + (size_t)row * D + ct * 64 + c * 8, lds0 + K1_W + piece * 1024);
    }
  } else {
#pragma unroll
    for (int i = 0; i < 14; ++i) {
      const int id = tid + 512 * i, k = id >> 4, u = id & 15;
      u32x2 v = {0u, 0u};
      if (u < 7) v = *(const u32x2*)(wk + k * DH + 4 * u);
      else if (u < 14) v = *(const u32x2*)(wv + k * DH + 4 * (u - 7));
      const int f = ((k >> 1) & 1) | (((k >> 3) & 1) << 1);
      *(u32x2*)(lds + K1_W + k * 128 + (((u >> 1) ^ (f << 1)) * 16) + 8 * (u & 1)) = v;
    }
  }
  u16 wt[8];
  const int frag = blockIdx.x + 256 * w;
  if (w < 2 && frag < 392) {
    const int nb = frag / 14, ks = frag - nb * 14;
#pragma unroll
    for (int j = 0; j < 8; ++j) wt[j] = wo[(size_t)(32 * ks + 8 * (lane >> 4) + j) * D + 16 * nb + (lane & 15)];
  }
  {
    u32x4 xv[8];
    const bool act = lane < 56;
#pragma unroll
    for (int r = 0; r < 8; ++r) {
      xv[r] = (u32x4){0u, 0u, 0u, 0u};
      if (act) xv[r] = *(const u32x4*)(x + (size_t)(rt * 64 + w * 8 + r) * D + lane * 8);
    }
#pragma unroll
    for (int r = 0; r < 8; ++r) {
      float v[8];
#pragma unroll
      for (int i = 0; i < 4; ++i) { v[2 * i] = bflo(xv[r][i]); v[2 * i + 1] = bfhi(xv[r][i]); }
      float s = 0.f;
#pragma unroll
      for (int i = 0; i < 8; ++i) s += v[i];
      const float mean = wave_sum(s) * (1.f / D);
      float q = 0.f;
#pragma unroll
      for (int i = 0; i < 8; ++i) { v[i] = act ? v[i] - mean : 0.f; q += v[i] * v[i]; }
      const float rs = __builtin_amdgcn_rsqf(wave_sum(q) * (1.f / D) + LN_EPS);
      u32x4 o;
#pragma unroll
      for (int i = 0; i < 4; ++i) o[i] = cvtpk(v[2 * i] * rs, v[2 * i + 1] * rs);
      if (act) *(u32x4*)(lds + K1_X + lane * K1_XS + (w * 8 + r) * 16) = o;
    }
  }
  if (w < 2 && frag < 392) {
    u32x4 o;
#pragma unroll
    for (int i = 0; i < 4; ++i) o[i] = (unsigned)wt[2 * i] | ((unsigned)wt[2 * i + 1] << 16);
    *(u32x4*)(Wof + ((size_t)frag * 64 + lane) * 8) = o;
  }
  WAITV_BAR(0);
  const int nb = w & 3, th = w >> 2, g = lane >> 4, q4 = (lane & 15) >> 2, p = lane & 3, l15 = lane & 15;
  const int f = ((q4 >> 1) & 1) | ((g & 1) << 1);
  const lds_cptr wp = (lds_cptr)lds + K1_W + (8 * g + q4) * 128 + (((2 * nb + (p >> 1)) ^ (f << 1)) * 16) + 8 * (p & 1);
  const lds_cptr xp = (lds_cptr)lds + K1_X + g * K1_XS + (32 * th + l15) * 16;
  f32x4 acc0 = {0.f, 0.f, 0.f, 0.f}, acc1 = {0.f, 0.f, 0.f, 0.f};
#pragma unroll
  for (int ks = 0; ks < 14; ++ks) {
    const bf16x8 a = cat8(trread(wp + ks * 4096), trread(wp + ks * 4096 + 512));
    const bf16x8 b0 = ldsr16(xp + ks * 4 * K1_XS), b1 = ldsr16(xp + ks * 4 * K1_XS + 256);
    acc0 = MFMA16(a, b0, acc0);
    acc1 = MFMA16(a, b1, acc1);
  }
#pragma unroll
  for (int tb = 0; tb < 2; ++tb) {
    const f32x4 a = tb ? acc1 : acc0;
    const int s = rt * 64 + 32 * th + 16 * tb + l15;
    const int nl = 16 * nb + 4 * g;
    if (ct < 7) {
      const int n0 = 64 * ct + nl, h = n0 / 28, d0 = n0 - 28 * h;
      const int tg = s >> 3, c = (h & 3) * 8 + (s & 7), b = h >> 2;
      const size_t off = ((((size_t)(tg * 4 + b) * 2 + (d0 >> 4)) * 64) + ((d0 >> 3) & 1) * 32 + c) * 8 + (d0 & 7);
      *(u32x2*)(Qf + off) = (u32x2){cvtpk(a[0] * QSCALE, a[1] * QSCALE), cvtpk(a[2] * QSCALE, a[3] * QSCALE)};
    } else {
      const int kt = s >> 5, kk = s & 31;
      if (nl < 28 || nl == 56) {
        const int d0 = nl < 28 ? nl : 28;
        const size_t off = ((((size_t)kt * 2 + (d0 >> 4)) * 64) + ((d0 >> 3) & 1) * 32 + kk) * 8 + (d0 & 7);
        u32x2 v = {cvtpk(a[0], a[1]), cvtpk(a[2], a[3])};
        if (nl == 56) v = (u32x2){0x00003f80u, 0u};
        *(u32x2*)(Kf + off) = v;
      } else {
        const int d0 = nl < 56 ? nl - 28 : 28;
        const int sp = kk >> 4, j = ((kk >> 3) & 1) * 4 + (kk & 3), hi = (kk >> 2) & 1;
        const size_t off = ((((size_t)kt * 2 + sp) * 64) + hi * 32 + d0) * 8 + j;
        unsigned v0 = cvtpk(a[0], a[1]), v1 = cvtpk(a[2], a[3]);
        if (nl == 60) { v0 = 0x00003f80u; v1 = 0u; }
        Vf[off] = (u16)v0; Vf[off + 8] = (u16)(v0 >> 16); Vf[off + 16] = (u16)v1; Vf[off + 24] = (u16)(v1 >> 16);
      }
    }
  }
}

constexpr int K2_P = 0, K2_M = 131072, K2_OT = K2_M + 4096, K2_LDS = K2_OT + 7168;
constexpr int K2_HB = 0, K2_HS = 32768;

DI void set_slot(bf16x8& q1, float mref_neg, int hi) {
  u32x4 v = __builtin_bit_cast(u32x4, q1);
  const unsigned s = cvtpk(mref_neg, 0.f);
  v[2] = hi ? s : v[2];
  v[3] = hi ? 0u : v[3];
  q1 = __builtin_bit_cast(bf16x8, v);
}
DI float tile_max(const f32x16& s) {
  float a = __builtin_fmaxf(__builtin_fmaxf(s[0], s[1]), s[2]), b = __builtin_fmaxf(__builtin_fmaxf(s[3], s[4]), s[5]);
  a = __builtin_fmaxf(__builtin_fmaxf(a, s[6]), s[7]); b = __builtin_fmaxf(__builtin_fmaxf(b, s[8]), s[9]);
  a = __builtin_fmaxf(__builtin_fmaxf(a, s[10]), s[11]); b = __builtin_fmaxf(__builtin_fmaxf(b, s[12]), s[13]);
  a = __builtin_fmaxf(__builtin_fmaxf(a, s[14]), s[15]);
  return half_max(__builtin_fmaxf(a, b));
}
DI float bf16_round(float v) { return bflo(cvtpk(v, 0.f)); }

template <bool SAFE>
DI void attn_loop(const u16* __restrict__ Kw, const u16* __restrict__ Vw, bf16x8 (&qf)[4][2], f32x16 (&o)[4], float (&mref)[4], int lane, int hi) {
  const f32x16 zero = {};
#pragma unroll
  for (int b = 0; b < 4; ++b) o[b] = zero;
  bf16x8 k0 = *(const bf16x8*)(Kw + lane * 8), k1 = *(const bf16x8*)(Kw + 512 + lane * 8);
  bf16x8 v0 = *(const bf16x8*)(Vw + lane * 8), v1 = *(const bf16x8*)(Vw + 512 + lane * 8);
#pragma unroll
  for (int b = 0; b < 4; ++b) {
    set_slot(qf[b][1], 0.f, hi);
    f32x16 s = MFMA32(k0, qf[b][0], zero);
    s = MFMA32(k1, qf[b][1], s);
    mref[b] = bf16_round(tile_max(s));
    set_slot(qf[b][1], -mref[b], hi);
  }
#pragma unroll 1
  for (int t = 0; t < 8; ++t) {
    const int tn = t < 7 ? t + 1 : 7;
    const bf16x8 nk0 = *(const bf16x8*)(Kw + tn * 1024 + lane * 8), nk1 = *(const bf16x8*)(Kw + tn * 1024 + 512 + lane * 8);
    const bf16x8 nv0 = *(const bf16x8*)(Vw + tn * 1024 + lane * 8), nv1 = *(const bf16x8*)(Vw + tn * 1024 + 512 + lane * 8);
#pragma unroll
    for (int b = 0; b < 4; ++b) {
      f32x16 s = MFMA32(k0, qf[b][0], zero);
      s = MFMA32(k1, qf[b][1], s);
      if (SAFE) {
        const float tm = tile_max(s);
        if (__any(tm > 0.f)) {
          const float mn = bf16_round(mref[b] + __builtin_fmaxf(tm, 0.f));
          const float mnew = __builtin_fmaxf(mn, mref[b]);
          const float al = __builtin_amdgcn_exp2f(mref[b] - mnew);
#pragma unroll
          for (int r = 0; r < 16; ++r) o[b][r] *= al;
          mref[b] = mnew;
          set_slot(qf[b][1], -mnew, hi);
          s = MFMA32(k0, qf[b][0], zero);
          s = MFMA32(k1, qf[b][1], s);
        }
      }
      u32x4 p0, p1;
#pragma unroll
      for (int i = 0; i < 4; ++i) {
        p0[i] = cvtpk(__builtin_amdgcn_exp2f(s[2 * i]), __builtin_amdgcn_exp2f(s[2 * i + 1]));
        p1[i] = cvtpk(__builtin_amdgcn_exp2f(s[8 + 2 * i]), __builtin_amdgcn_exp2f(s[8 + 2 * i + 1]));
      }
      o[b] = MFMA32(v0, __builtin_bit_cast(bf16x8, p0), o[b]);
      o[b] = MFMA32(v1, __builtin_bit_cast(bf16x8, p1), o[b]);
    }
    k0 = nk0; k1 = nk1; v0 = nv0; v1 = nv1;
  }
}

__global__ __launch_bounds__(512) void k_attn(const u16* __restrict__ x, const u16* __restrict__ Qf, const u16* __restrict__ Kf,
                                              const u16* __restrict__ Vf, const u16* __restrict__ Wof, u16* __restrict__ H,
                                              u16* __restrict__ HN) {
  __shared__ __attribute__((aligned(16))) char lds[K2_LDS];
  const int tid = threadIdx.x, lane = tid & 63, w = __builtin_amdgcn_readfirstlane(tid >> 6), hi = lane >> 5;
  const int tg = blockIdx.x;
  bf16x8 qf[4][2];
#pragma unroll
  for (int b = 0; b < 4; ++b)
#pragma unroll
    for (int k = 0; k < 2; ++k) qf[b][k] = *(const bf16x8*)(Qf + ((size_t)((tg * 4 + b) * 2 + k) * 64 + lane) * 8);
  f32x16 o[4];
  float mref[4];
  const u16* Kw = Kf + (size_t)w * 8 * 1024;
  const u16* Vw = Vf + (size_t)w * 8 * 1024;
  attn_loop<false>(Kw, Vw, qf, o, mref, lane, hi);
  {
    float chk = 0.f;
    bool bad = false;
#pragma unroll
    for (int b = 0; b < 4; ++b) {
#pragma unroll
      for (int r = 0; r < 16; ++r) chk = __builtin_fmaf(o[b][r], 0.f, chk);
      bad |= !(o[b][12] < 1.8e19f);
    }
    bad |= !(chk == 0.f);
    if (__any(bad)) attn_loop<true>(Kw, Vw, qf, o, mref, lane, hi);
  }
#pragma unroll
  for (int b = 0; b < 4; ++b) {
#pragma unroll
    for (int r4 = 0; r4 < 4; ++r4)
      *(f32x4*)(lds + K2_P + (((w * 4 + b) * 4 + r4) * 64 + lane) * 16) = (f32x4){o[b][4 * r4], o[b][4 * r4 + 1], o[b][4 * r4 + 2], o[b][4 * r4 + 3]};
    if (hi == 0) *(float*)(lds + K2_M + (w * 128 + b * 32 + lane) * 4) = mref[b];
  }
  __syncthreads();
#pragma unroll
  for (int i = 0; i < 2; ++i) {
    const int id = tid + 512 * i, b = id >> 8, r4 = (id >> 6) & 3, c = lane & 31, q = 32 * b + c;
    float m[8], M = -INFINITY;
#pragma unroll
    for (int v = 0; v < 8; ++v) { m[v] = *(const float*)(lds + K2_M + (v * 128 + q) * 4); M = __builtin_fmaxf(M, m[v]); }
    float l = 0.f;
    f32x4 a = {0.f, 0.f, 0.f, 0.f};
#pragma unroll
    for (int v = 0; v < 8; ++v) {
      const float fc = __builtin_amdgcn_exp2f(m[v] - M);
      l += fc * *(const float*)(lds + K2_P + (((v * 4 + b) * 4 + 3) * 64 + 32 + c) * 16);
      const f32x4 pv = *(const f32x4*)(lds + K2_P + (((v * 4 + b) * 4 + r4) * 64 + lane) * 16);
      a += fc * pv;
    }
    const float il = 1.f / l;
    const int d = 8 * r4 + 4 * hi;
    if (d < 28) {
      const int k = 28 * (4 * b + (c >> 3)) + d;
      *(u32x2*)(lds + K2_OT + ((k >> 3) * 8 + (c & 7)) * 16 + (k & 7) * 2) = (u32x2){cvtpk(a[0] * il, a[1] * il), cvtpk(a[2] * il, a[3] * il)};
    }
  }
  __syncthreads();
  {
    const int kh = w >> 2, ng = w & 3, g = lane >> 4;
    bf16x8 bo[7];
#pragma unroll
    for (int k = 0; k < 7; ++k) bo[k] = *(const bf16x8*)(lds + K2_OT + ((4 * (7 * kh + k) + g) * 8 + (lane & 7)) * 16);
#pragma unroll
    for (int n = 0; n < 7; ++n) {
      const int nb = 7 * ng + n;
      f32x4 acc = {0.f, 0.f, 0.f, 0.f};
#pragma unroll
      for (int k = 0; k < 7; ++k) {
        const bf16x8 a = *(const bf16x8*)(Wof + ((size_t)(nb * 14 + 7 * kh + k) * 64 + lane) * 8);
        acc = MFMA16(a, bo[k], acc);
      }
      if ((lane & 15) < 8) *(f32x4*)(lds + K2_HB + ((kh * 8 + (lane & 15)) * D + 16 * nb + 4 * g) * 4) = acc;
    }
  }
  __syncthreads();
  {
    const int s = tg * 8 + w;
    const bool act = lane < 56;
    float v[8];
#pragma unroll
    for (int i = 0; i < 8; ++i) v[i] = 0.f;
    if (act) {
      const u32x4 xv = *(const u32x4*)(x + (size_t)s * D + lane * 8);
      const f32x4 a0 = *(const f32x4*)(lds + K2_HB + (w * D + lane * 8) * 4), a1 = *(const f32x4*)(lds + K2_HB + (w * D + lane * 8 + 4) * 4);
      const f32x4 c0 = *(const f32x4*)(lds + K2_HB + ((8 + w) * D + lane * 8) * 4), c1 = *(const f32x4*)(lds + K2_HB + ((8 + w) * D + lane * 8 + 4) * 4);
      u32x4 hv;
#pragma unroll
      for (int i = 0; i < 2; ++i) {
        hv[i] = cvtpk(bflo(xv[i]) + a0[2 * i] + c0[2 * i], bfhi(xv[i]) + a0[2 * i + 1] + c0[2 * i + 1]);
        hv[2 + i] = cvtpk(bflo(xv[2 + i]) + a1[2 * i] + c1[2 * i], bfhi(xv[2 + i]) + a1[2 * i + 1] + c1[2 * i + 1]);
      }
      *(u32x4*)(H + (size_t)s * D + lane * 8) = hv;
#pragma unroll
      for (int i = 0; i < 4; ++i) { v[2 * i] = bflo(hv[i]); v[2 * i + 1] = bfhi(hv[i]); }
    }
#pragma unroll
    for (int pass = 0; pass < 2; ++pass) {
      float sm = 0.f;
#pragma unroll
      for (int i = 0; i < 8; ++i) sm += v[i];
      const float mean = wave_sum(sm) * (1.f / D);
      float q = 0.f;
#pragma unroll
      for (int i = 0; i < 8; ++i) { v[i] = act ? v[i] - mean : 0.f; q += v[i] * v[i]; }
      const float rs = __builtin_amdgcn_rsqf(wave_sum(q) * (1.f / D) + LN_EPS);
#pragma unroll
      for (int i = 0; i < 8; ++i) v[i] *= rs;
    }
    if (act) {
#pragma unroll
      for (int i = 0; i < 4; ++i) {
        const unsigned pk = cvtpk(v[2 * i], v[2 * i + 1]);
        *(u16*)(lds + K2_HS + ((lane * 8 + 2 * i) * 8 + w) * 2) = (u16)pk;
        *(u16*)(lds + K2_HS + ((lane * 8 + 2 * i + 1) * 8 + w) * 2) = (u16)(pk >> 16);
      }
    }
  }
  __syncthreads();
  if (tid < D) *(u32x4*)(HN + ((size_t)tg * D + tid) * 8) = *(const u32x4*)(lds + K2_HS + tid * 16);
}

constexpr int G_NS = 8, G_STAGE = 16384, G_LDS = G_NS * G_STAGE;
template <int PH>
__global__ __launch_bounds__(512) void k_mlp(const u16* __restrict__ act, const u16* __restrict__ W, const u16* __restrict__ bias,
                                             const u16* __restrict__ H, u16* __restrict__ out) {
  constexpr int KT = PH == 0 ? S : F, LDW = PH == 0 ? F : S, NT = KT / 64, NWG = 7 * (LDW / 64);
  __shared__ __attribute__((aligned(16))) char lds[G_LDS];
  const int tid = threadIdx.x, lane = tid & 63, w = __builtin_amdgcn_readfirstlane(tid >> 6);
  const int t = xcd_remap(blockIdx.x, NWG), nt = t / 7, mt = t - nt * 7;
  const unsigned lds0 = (unsigned)(uintptr_t)lds;
  const u16* asrc = act + ((size_t)w * D + 64 * mt + lane) * 8;
  const int rl = 8 * w + (lane >> 3);
  const int fs = ((rl >> 1) & 1) | (((rl >> 3) & 1) << 1);
  const u16* wsrc = W + (size_t)rl * LDW + 64 * nt + ((lane & 7) ^ (fs << 1)) * 8;
#define STAGE(st) do { const unsigned sl_ = lds0 + ((st) % G_NS) * G_STAGE + w * 1024; \
    glds16(asrc + (size_t)(st) * 8 * D * 8, sl_); glds16(wsrc + (size_t)(st) * 64 * LDW, sl_ + 8192); } while (0)
#pragma unroll
  for (int st = 0; st < G_NS - 1; ++st) STAGE(st);
  const int qm = w & 1, qn = (w >> 1) & 1, kh = w >> 2, g = lane >> 4, q4 = (lane & 15) >> 2, p = lane & 3, l15 = lane & 15;
  const int f = ((q4 >> 1) & 1) | ((g & 1) << 1);
  const int aoff = (4 * kh + g) * 1024 + (32 * qm + l15) * 16;
  const int woff0 = 8192 + (32 * kh + 8 * g + q4) * 128 + (((4 * qn + (p >> 1)) ^ (f << 1)) * 16) + 8 * (p & 1);
  const int woff1 = 8192 + (32 * kh + 8 * g + q4) * 128 + (((4 * qn + 2 + (p >> 1)) ^ (f << 1)) * 16) + 8 * (p & 1);
  f32x4 acc[2][2] = {};
#pragma unroll 1
  for (int st = 0; st < NT; ++st) {
    const int rem = NT - 1 - st;
    if (rem >= 6) WAITV_BAR(12);
    else if (rem == 5) WAITV_BAR(10);
    else if (rem == 4) WAITV_BAR(8);
    else if (rem == 3) WAITV_BAR(6);
    else if (rem == 2) WAITV_BAR(4);
    else if (rem == 1) WAITV_BAR(2);
    else WAITV_BAR(0);
    if (st + G_NS - 1 < NT) STAGE(st + G_NS - 1);
    const lds_cptr sb = (lds_cptr)lds + (st % G_NS) * G_STAGE;
    const bf16x8 a0 = ldsr16(sb + aoff), a1 = ldsr16(sb + aoff + 256);
    const bf16x8 w0 = cat8(trread(sb + woff0), trread(sb + woff0 + 512));
    const bf16x8 w1 = cat8(trread(sb + woff1), trread(sb + woff1 + 512));
    if (PH == 0) {
      acc[0][0] = MFMA16(w0, a0, acc[0][0]); acc[0][1] = MFMA16(w0, a1, acc[0][1]);
      acc[1][0] = MFMA16(w1, a0, acc[1][0]); acc[1][1] = MFMA16(w1, a1, acc[1][1]);
    } else {
      acc[0][0] = MFMA16(a0, w0, acc[0][0]); acc[0][1] = MFMA16(a1, w0, acc[0][1]);
      acc[1][0] = MFMA16(a0, w1, acc[1][0]); acc[1][1] = MFMA16(a1, w1, acc[1][1]);
    }
  }
#undef STAGE
  asm volatile("s_waitcnt lgkmcnt(0)\n\ts_barrier" ::: "memory");
  {
    f32x4* xb = (f32x4*)lds;
    xb[((w * 2 + 0) * 64 + lane)] = kh ? acc[0][0] : acc[1][0];
    xb[((w * 2 + 1) * 64 + lane)] = kh ? acc[0][1] : acc[1][1];
  }
  __syncthreads();
  {
    const f32x4* xb = (const f32x4*)lds;
    const int pw = w ^ 4, nb = kh;
#pragma unroll
    for (int mb = 0; mb < 2; ++mb) {
      f32x4 a = (kh ? acc[1][mb] : acc[0][mb]) + xb[(pw * 2 + mb) * 64 + lane];
      if (PH == 0) {
        const int fi = 64 * nt + 32 * qn + 16 * nb + 4 * g, d = 64 * mt + 32 * qm + 16 * mb + l15;
        const u32x2 bv = *(const u32x2*)(bias + fi);
        float z[4] = {a[0] + bflo(bv[0]), a[1] + bfhi(bv[0]), a[2] + bflo(bv[1]), a[3] + bfhi(bv[1])};
#pragma unroll
        for (int i = 0; i < 4; ++i) {
          const float zz = z[i], a2 = zz * (1.f + 0.044715f * zz * zz) * (-2.f * 0.7978845608028654f * 1.4426950408889634f);
          z[i] = zz * __builtin_amdgcn_rcpf(1.f + __builtin_amdgcn_exp2f(a2));
        }
        *(u32x2*)(out + ((size_t)(fi >> 3) * D + d) * 8 + (fi & 7)) = (u32x2){cvtpk(z[0], z[1]), cvtpk(z[2], z[3])};
      } else {
        const int d = 64 * mt + 32 * qm + 16 * mb + 4 * g, s = 64 * nt + 32 * qn + 16 * nb + l15;
        const float bb = bflo(bias[s]);
        const u32x2 hv = *(const u32x2*)(H + (size_t)s * D + d);
        *(u32x2*)(out + (size_t)s * D + d) =
            (u32x2){cvtpk(a[0] + bb + bflo(hv[0]), a[1] + bb + bfhi(hv[0])), cvtpk(a[2] + bb + bflo(hv[1]), a[3] + bb + bfhi(hv[1]))};
      }
    }
  }
}

extern "C" void kernel_launch(void* const* d_in, const int* in_sizes, int n_in, void* d_out, int out_size, void* d_ws, size_t ws_size,
                              hipStream_t stream) {
  const u16* x = (const u16*)d_in[0];
  const u16* wq = (const u16*)d_in[1];
  const u16* wk = (const u16*)d_in[2];
  const u16* wv = (const u16*)d_in[3];
  const u16* wo = (const u16*)d_in[4];
  const u16* w1 = (const u16*)d_in[6];
  const u16* b1 = (const u16*)d_in[7];
  const u16* w2 = (const u16*)d_in[8];
  const u16* b2 = (const u16*)d_in[9];
  char* ws = (char*)d_ws;
  u16 *Qf = (u16*)(ws + WS_QF), *Kf = (u16*)(ws + WS_KF), *Vf = (u16*)(ws + WS_VF), *Wof = (u16*)(ws + WS_WOF);
  u16 *H = (u16*)(ws + WS_H), *HN = (u16*)(ws + WS_HN), *U = (u16*)(ws + WS_U);
  k_qkv<<<256, 512, 0, stream>>>(x, wq, wk, wv, wo, Qf, Kf, Vf, Wof);
  k_attn<<<256, 512, 0, stream>>>(x, Qf, Kf, Vf, Wof, H, HN);
  k_mlp<0><<<196, 512, 0, stream>>>(HN, w1, b1, H, U);
  k_mlp<1><<<224, 512, 0, stream>>>(U, w2, b2, H, (u16*)d_out);
}
```

```cpp
#include <hip/hip_runtime.h>
#include <hip/hip_bf16.h>

typedef unsigned short u16;
using bf16x8 = __attribute__((ext_vector_type(8))) short;
using s16x4 = __attribute__((ext_vector_type(4))) short;
using f32x4 = __attribute__((ext_vector_type(4))) float;
using f32x16 = __attribute__((ext_vector_type(16))) float;
using u32x4 = __attribute__((ext_vector_type(4))) unsigned;
using u32x2 = __attribute__((ext_vector_type(2))) unsigned;
typedef short v4i16_t __attribute__((ext_vector_type(4)));
typedef __attribute__((address_space(3))) char* lds_ptr;
typedef __attribute__((address_space(3))) const char* lds_cptr;

#define DI __device__ __forceinline__
#define MFMA32(a, b, c) __builtin_amdgcn_mfma_f32_32x32x16_bf16(a, b, c, 0, 0, 0)
#define MFMA16(a, b, c) __builtin_amdgcn_mfma_f32_16x16x32_bf16(a, b, c, 0, 0, 0)

constexpr int S = 2048, D = 448, NH = 16, DH = 28, F = 1792;
constexpr float QSCALE = 0.18898223650461362f * 1.4426950408889634f;
constexpr float LN_EPS = 1e-5f;

constexpr size_t WS_QF = 0;
constexpr size_t WS_KF = WS_QF + 2097152;
constexpr size_t WS_VF = WS_KF + 131072;
constexpr size_t WS_WOF = WS_VF + 131072;
constexpr size_t WS_H = WS_WOF + 401408;
constexpr size_t WS_HN = WS_H + 1835008;
constexpr size_t WS_U = WS_HN + 1835008;

typedef __bf16 bf16x2_t __attribute__((ext_vector_type(2)));
typedef float f32x2_t __attribute__((ext_vector_type(2)));
DI unsigned cvtpk(float lo, float hi) {
  const f32x2_t v = {lo, hi};
  return __builtin_bit_cast(unsigned, __builtin_convertvector(v, bf16x2_t));
}
DI float bflo(unsigned u) { return __uint_as_float(u << 16); }
DI float bfhi(unsigned u) { return __uint_as_float(u & 0xffff0000u); }
template <int CTRL> DI float dppf(float v) {
  return __uint_as_float(__builtin_amdgcn_update_dpp(0u, __float_as_uint(v), CTRL, 0xf, 0xf, true));
}
DI float wave_sum(float v) {
  v += dppf<0xB1>(v);
  v += dppf<0x4E>(v);
  v += dppf<0x141>(v);
  v += dppf<0x140>(v);
  auto a = __builtin_amdgcn_permlane16_swap(__float_as_uint(v), __float_as_uint(v), false, false);
  v = __uint_as_float(a[0]) + __uint_as_float(a[1]);
  auto b = __builtin_amdgcn_permlane32_swap(__float_as_uint(v), __float_as_uint(v), false, false);
  return __uint_as_float(b[0]) + __uint_as_float(b[1]);
}
DI float half_max(float v) {
  auto b = __builtin_amdgcn_permlane32_swap(__float_as_uint(v), __float_as_uint(v), false, false);
  return __builtin_fmaxf(__uint_as_float(b[0]), __uint_as_float(b[1]));
}
DI void glds16(const void* g, unsigned lds_base) {
  unsigned sv;
  asm volatile("s_mov_b32 %0, m0\n\ts_mov_b32 m0, %2\n\ts_nop 0\n\tglobal_load_lds_dwordx4 %1, off\n\ts_mov_b32 m0, %0"
               : "=&s"(sv) : "v"(g), "s"(lds_base) : "memory");
}
DI void glds4(const void* g, unsigned lds_base) {
  unsigned sv;
  asm volatile("s_mov_b32 %0, m0\n\ts_mov_b32 m0, %2\n\ts_nop 0\n\tglobal_load_lds_dword %1, off\n\ts_mov_b32 m0, %0"
               : "=&s"(sv) : "v"(g), "s"(lds_base) : "memory");
}
#define SBAR() __builtin_amdgcn_sched_barrier(0)
DI s16x4 trread(lds_cptr p) {
  return __builtin_bit_cast(s16x4, __builtin_amdgcn_ds_read_tr16_b64_v4i16((__attribute__((address_space(3))) v4i16_t*)p));
}
DI bf16x8 ldsr16(lds_cptr p) { return *(const __attribute__((address_space(3))) bf16x8*)p; }
DI bf16x8 cat8(s16x4 a, s16x4 b) { return __builtin_shufflevector(a, b, 0, 1, 2, 3, 4, 5, 6, 7); }
DI int xcd_remap(int bid, int nwg) {
  const int q = nwg >> 3, r = nwg & 7, x = bid & 7, o = bid >> 3;
  return (x < r ? x * (q + 1) : r * (q + 1) + (x - r) * q) + o;
}
#define STAMP(k, i)
#define WAITV_BAR(N) asm volatile("s_waitcnt vmcnt(" #N ") lgkmcnt(0)\n\ts_barrier" ::: "memory")

constexpr int K1_W = 0, K1_X = 448 * 128, K1_XS = 1040, K1_LDS = K1_X + 56 * K1_XS;
__global__ __launch_bounds__(512) void k_qkv(const u16* __restrict__ x, const u16* __restrict__ wq, const u16* __restrict__ wk,
                                             const u16* __restrict__ wv, const u16* __restrict__ wo, u16* __restrict__ Qf,
                                             u16* __restrict__ Kf, u16* __restrict__ Vf, u16* __restrict__ Wof) {
  __shared__ __attribute__((aligned(16))) char lds[K1_LDS];
  const int tid = threadIdx.x, lane = tid & 63, w = __builtin_amdgcn_readfirstlane(tid >> 6);
  const int t = xcd_remap(blockIdx.x, 256), rt = t >> 3, ct = t & 7;
  const unsigned lds0 = (unsigned)(uintptr_t)lds;
  STAMP(0, 0);
  if (ct < 7) {
#pragma unroll
    for (int i = 0; i < 7; ++i) {
      const int piece = w * 7 + i, row = piece * 8 + (lane >> 3);
      const int f = ((row >> 1) & 1) | (((row >> 3) & 1) << 1);
      const int c = (lane & 7) ^ (f << 1);
      glds16(wq + (size_t)row * D + ct * 64 + c * 8, lds0 + K1_W + piece * 1024);
    }
  } else {
#pragma unroll
    for (int i = 0; i < 28; ++i) {
      const int piece = w * 28 + i, row = piece * 2 + (lane >> 5), pp = lane & 31;
      const int f = ((row >> 1) & 1) | (((row >> 3) & 1) << 1);
      const int col = ((((pp >> 2) ^ (f << 1)) << 2) + (pp & 3)) * 2;
      const u16* src = col < 28 ? wk + row * DH + col : wv + row * DH + (col < 56 ? col - 28 : 0);
      glds4(src, lds0 + K1_W + piece * 256);
    }
  }
  u16 wt[8];
  const int frag = blockIdx.x + 256 * w;
  if (w < 2 && frag < 392) {
    const int nb = frag / 14, ks = frag - nb * 14;
#pragma unroll
    for (int j = 0; j < 8; ++j) wt[j] = wo[(size_t)(32 * ks + 8 * (lane >> 4) + j) * D + 16 * nb + (lane & 15)];
  }
  {
    u32x4 xv[8];
    const bool act = lane < 56;
#pragma unroll
    for (int r = 0; r < 8; ++r) {
      xv[r] = (u32x4){0u, 0u, 0u, 0u};
      if (act) xv[r] = *(const u32x4*)(x + (size_t)(rt * 64 + w * 8 + r) * D + lane * 8);
    }
#pragma unroll
    for (int r = 0; r < 8; ++r) {
      float v[8];
#pragma unroll
      for (int i = 0; i < 4; ++i) { v[2 * i] = bflo(xv[r][i]); v[2 * i + 1] = bfhi(xv[r][i]); }
      float s = 0.f;
#pragma unroll
      for (int i = 0; i < 8; ++i) s += v[i];
      const float mean = wave_sum(s) * (1.f / D);
      float q = 0.f;
#pragma unroll
      for (int i = 0; i < 8; ++i) { v[i] = act ? v[i] - mean : 0.f; q += v[i] * v[i]; }
      const float rs = __builtin_amdgcn_rsqf(wave_sum(q) * (1.f / D) + LN_EPS);
      u32x4 o;
#pragma unroll
      for (int i = 0; i < 4; ++i) o[i] = cvtpk(v[2 * i] * rs, v[2 * i + 1] * rs);
      if (act) *(u32x4*)(lds + K1_X + lane * K1_XS + (w * 8 + r) * 16) = o;
    }
  }
  if (w < 2 && frag < 392) {
    u32x4 o;
#pragma unroll
    for (int i = 0; i < 4; ++i) o[i] = (unsigned)wt[2 * i] | ((unsigned)wt[2 * i + 1] << 16);
    *(u32x4*)(Wof + ((size_t)frag * 64 + lane) * 8) = o;
  }
  STAMP(0, 1);
  WAITV_BAR(0);
  STAMP(0, 2);
  const int nb = w & 3, th = w >> 2, g = lane >> 4, q4 = (lane & 15) >> 2, p = lane & 3, l15 = lane & 15;
  const int f = ((q4 >> 1) & 1) | ((g & 1) << 1);
  const lds_cptr wp = (lds_cptr)lds + K1_W + (8 * g + q4) * 128 + (((2 * nb + (p >> 1)) ^ (f << 1)) * 16) + 8 * (p & 1);
  const lds_cptr xp = (lds_cptr)lds + K1_X + g * K1_XS + (32 * th + l15) * 16;
  f32x4 acc0 = {0.f, 0.f, 0.f, 0.f}, acc1 = {0.f, 0.f, 0.f, 0.f};
  {
    bf16x8 a[14], b0[14], b1[14];
#pragma unroll
    for (int ks = 0; ks < 14; ++ks) {
      a[ks] = cat8(trread(wp + ks * 4096), trread(wp + ks * 4096 + 512));
      b0[ks] = ldsr16(xp + ks * 4 * K1_XS);
      b1[ks] = ldsr16(xp + ks * 4 * K1_XS + 256);
    }
    SBAR();
#pragma unroll
    for (int ks = 0; ks < 14; ++ks) {
      acc0 = MFMA16(a[ks], b0[ks], acc0);
      acc1 = MFMA16(a[ks], b1[ks], acc1);
    }
  }
  STAMP(0, 3);
#pragma unroll
  for (int tb = 0; tb < 2; ++tb) {
    const f32x4 a = tb ? acc1 : acc0;
    const int s = rt * 64 + 32 * th + 16 * tb + l15;
    const int nl = 16 * nb + 4 * g;
    if (ct < 7) {
      const int n0 = 64 * ct + nl, h = n0 / 28, d0 = n0 - 28 * h;
      const int tg = s >> 3, c = (h & 3) * 8 + (s & 7), b = h >> 2;
      const size_t off = ((((size_t)(tg * 4 + b) * 2 + (d0 >> 4)) * 64) + ((d0 >> 3) & 1) * 32 + c) * 8 + (d0 & 7);
      *(u32x2*)(Qf + off) = (u32x2){cvtpk(a[0] * QSCALE, a[1] * QSCALE), cvtpk(a[2] * QSCALE, a[3] * QSCALE)};
    } else {
      const int kt = s >> 5, kk = s & 31;
      if (nl < 28 || nl == 56) {
        const int d0 = nl < 28 ? nl : 28;
        const size_t off = ((((size_t)kt * 2 + (d0 >> 4)) * 64) + ((d0 >> 3) & 1) * 32 + kk) * 8 + (d0 & 7);
        u32x2 v = {cvtpk(a[0], a[1]), cvtpk(a[2], a[3])};
        if (nl == 56) v = (u32x2){0x00003f80u, 0u};
        *(u32x2*)(Kf + off) = v;
      } else {
        const int d0 = nl < 56 ? nl - 28 : 28;
        const int sp = kk >> 4, j = ((kk >> 3) & 1) * 4 + (kk & 3), hi = (kk >> 2) & 1;
        const size_t off = ((((size_t)kt * 2 + sp) * 64) + hi * 32 + d0) * 8 + j;
        unsigned v0 = cvtpk(a[0], a[1]), v1 = cvtpk(a[2], a[3]);
        if (nl == 60) { v0 = 0x00003f80u; v1 = 0u; }
        Vf[off] = (u16)v0; Vf[off + 8] = (u16)(v0 >> 16); Vf[off + 16] = (u16)v1; Vf[off + 24] = (u16)(v1 >> 16);
      }
    }
  }
  STAMP(0, 4);
}

constexpr int K2_P = 0, K2_M = 131072, K2_OT = K2_M + 4096, K2_LDS = K2_OT + 7168;
constexpr int K2_HB = 0, K2_HS = 32768;

DI void set_slot(bf16x8& q1, float mref_neg, int hi) {
  u32x4 v = __builtin_bit_cast(u32x4, q1);
  const unsigned s = cvtpk(mref_neg, 0.f);
  v[2] = hi ? s : v[2];
  v[3] = hi ? 0u : v[3];
  q1 = __builtin_bit_cast(bf16x8, v);
}
DI float tile_max(const f32x16& s) {
  float a = __builtin_fmaxf(__builtin_fmaxf(s[0], s[1]), s[2]), b = __builtin_fmaxf(__builtin_fmaxf(s[3], s[4]), s[5]);
  a = __builtin_fmaxf(__builtin_fmaxf(a, s[6]), s[7]); b = __builtin_fmaxf(__builtin_fmaxf(b, s[8]), s[9]);
  a = __builtin_fmaxf(__builtin_fmaxf(a, s[10]), s[11]); b = __builtin_fmaxf(__builtin_fmaxf(b, s[12]), s[13]);
  a = __builtin_fmaxf(__builtin_fmaxf(a, s[14]), s[15]);
  return half_max(__builtin_fmaxf(a, b));
}
DI float bf16_round(float v) { return bflo(cvtpk(v, 0.f)); }

template <bool SAFE>
DI void attn_loop(const u16* __restrict__ Kw, const u16* __restrict__ Vw, bf16x8 (&qf)[4][2], f32x16 (&o)[4], float (&mref)[4], int lane, int hi) {
  const f32x16 zero = {};
#pragma unroll
  for (int b = 0; b < 4; ++b) o[b] = zero;
  bf16x8 k0 = *(const bf16x8*)(Kw + lane * 8), k1 = *(const bf16x8*)(Kw + 512 + lane * 8);
  bf16x8 v0 = *(const bf16x8*)(Vw + lane * 8), v1 = *(const bf16x8*)(Vw + 512 + lane * 8);
#pragma unroll
  for (int b = 0; b < 4; ++b) {
    set_slot(qf[b][1], 0.f, hi);
    f32x16 s = MFMA32(k0, qf[b][0], zero);
    s = MFMA32(k1, qf[b][1], s);
    mref[b] = bf16_round(tile_max(s));
    set_slot(qf[b][1], -mref[b], hi);
  }
#pragma unroll 1
  for (int t = 0; t < 8; ++t) {
    const int tn = t < 7 ? t + 1 : 7;
    const bf16x8 nk0 = *(const bf16x8*)(Kw + tn * 1024 + lane * 8), nk1 = *(const bf16x8*)(Kw + tn * 1024 + 512 + lane * 8);
    const bf16x8 nv0 = *(const bf16x8*)(Vw + tn * 1024 + lane * 8), nv1 = *(const bf16x8*)(Vw + tn * 1024 + 512 + lane * 8);
#pragma unroll
    for (int b = 0; b < 4; ++b) {
      f32x16 s = MFMA32(k0, qf[b][0], zero);
      s = MFMA32(k1, qf[b][1], s);
      if (SAFE) {
        const float tm = tile_max(s);
        if (__any(tm > 0.f)) {
          const float mn = bf16_round(mref[b] + __builtin_fmaxf(tm, 0.f));
          const float mnew = __builtin_fmaxf(mn, mref[b]);
          const float al = __builtin_amdgcn_exp2f(mref[b] - mnew);
#pragma unroll
          for (int r = 0; r < 16; ++r) o[b][r] *= al;
          mref[b] = mnew;
          set_slot(qf[b][1], -mnew, hi);
          s = MFMA32(k0, qf[b][0], zero);
          s = MFMA32(k1, qf[b][1], s);
        }
      }
      u32x4 p0, p1;
#pragma unroll
      for (int i = 0; i < 4; ++i) {
        p0[i] = cvtpk(__builtin_amdgcn_exp2f(s[2 * i]), __builtin_amdgcn_exp2f(s[2 * i + 1]));
        p1[i] = cvtpk(__builtin_amdgcn_exp2f(s[8 + 2 * i]), __builtin_amdgcn_exp2f(s[8 + 2 * i + 1]));
      }
      o[b] = MFMA32(v0, __builtin_bit_cast(bf16x8, p0), o[b]);
      o[b] = MFMA32(v1, __builtin_bit_cast(bf16x8, p1), o[b]);
    }
    k0 = nk0; k1 = nk1; v0 = nv0; v1 = nv1;
  }
}

__global__ __launch_bounds__(512) void k_attn(const u16* __restrict__ x, const u16* __restrict__ Qf, const u16* __restrict__ Kf,
                                              const u16* __restrict__ Vf, const u16* __restrict__ Wof, u16* __restrict__ H,
                                              u16* __restrict__ HN) {
  __shared__ __attribute__((aligned(16))) char lds[K2_LDS];
  const int tid = threadIdx.x, lane = tid & 63, w = __builtin_amdgcn_readfirstlane(tid >> 6), hi = lane >> 5;
  const int tg = blockIdx.x;
  STAMP(1, 0);
  bf16x8 qf[4][2];
#pragma unroll
  for (int b = 0; b < 4; ++b)
#pragma unroll
    for (int k = 0; k < 2; ++k) qf[b][k] = *(const bf16x8*)(Qf + ((size_t)((tg * 4 + b) * 2 + k) * 64 + lane) * 8);
  u32x4 xv = {0u, 0u, 0u, 0u};
  if (lane < 56) xv = *(const u32x4*)(x + (size_t)(tg * 8 + w) * D + lane * 8);
  f32x16 o[4];
  float mref[4];
  const u16* Kw = Kf + (size_t)w * 8 * 1024;
  const u16* Vw = Vf + (size_t)w * 8 * 1024;
  attn_loop<false>(Kw, Vw, qf, o, mref, lane, hi);
  {
    float chk = 0.f;
    bool bad = false;
#pragma unroll
    for (int b = 0; b < 4; ++b) {
#pragma unroll
      for (int r = 0; r < 16; ++r) chk = __builtin_fmaf(o[b][r], 0.f, chk);
      bad |= !(o[b][12] < 1.8e19f);
    }
    bad |= !(chk == 0.f);
    if (__any(bad)) attn_loop<true>(Kw, Vw, qf, o, mref, lane, hi);
  }
  STAMP(1, 1);
  const int kh = w >> 2, ng = w & 3;
  const u16* wop = Wof + ((size_t)(7 * ng * 14 + 7 * kh) * 64 + lane) * 8;
  bf16x8 wf[7][7];
#pragma unroll
  for (int n = 0; n < 5; ++n)
#pragma unroll
    for (int k = 0; k < 7; ++k) wf[n][k] = *(const bf16x8*)(wop + (n * 14 + k) * 512);
  SBAR();
#pragma unroll
  for (int b = 0; b < 4; ++b) {
#pragma unroll
    for (int r4 = 0; r4 < 4; ++r4)
      *(f32x4*)(lds + K2_P + (((w * 4 + b) * 4 + r4) * 64 + lane) * 16) = (f32x4){o[b][4 * r4], o[b][4 * r4 + 1], o[b][4 * r4 + 2], o[b][4 * r4 + 3]};
    if (hi == 0) *(float*)(lds + K2_M + (w * 128 + b * 32 + lane) * 4) = mref[b];
  }
  SBAR();
#pragma unroll
  for (int n = 5; n < 7; ++n)
#pragma unroll
    for (int k = 0; k < 7; ++k) wf[n][k] = *(const bf16x8*)(wop + (n * 14 + k) * 512);
  SBAR();
  asm volatile("s_waitcnt lgkmcnt(0)\n\ts_barrier" ::: "memory");
#pragma unroll
  for (int i = 0; i < 2; ++i) {
    const int id = tid + 512 * i, b = id >> 8, r4 = (id >> 6) & 3, c = lane & 31, q = 32 * b + c;
    float m[8], M = -INFINITY;
#pragma unroll
    for (int v = 0; v < 8; ++v) { m[v] = *(const float*)(lds + K2_M + (v * 128 + q) * 4); M = __builtin_fmaxf(M, m[v]); }
    float l = 0.f;
    f32x4 a = {0.f, 0.f, 0.f, 0.f};
#pragma unroll
    for (int v = 0; v < 8; ++v) {
      const float fc = __builtin_amdgcn_exp2f(m[v] - M);
      l += fc * *(const float*)(lds + K2_P + (((v * 4 + b) * 4 + 3) * 64 + 32 + c) * 16);
      const f32x4 pv = *(const f32x4*)(lds + K2_P + (((v * 4 + b) * 4 + r4) * 64 + lane) * 16);
      a += fc * pv;
    }
    const float il = 1.f / l;
    const int d = 8 * r4 + 4 * hi;
    if (d < 28) {
      const int k = 28 * (4 * b + (c >> 3)) + d;
      *(u32x2*)(lds + K2_OT + ((k >> 3) * 8 + (c & 7)) * 16 + (k & 7) * 2) = (u32x2){cvtpk(a[0] * il, a[1] * il), cvtpk(a[2] * il, a[3] * il)};
    }
  }
  asm volatile("s_waitcnt lgkmcnt(0)\n\ts_barrier" ::: "memory");
  STAMP(1, 2);
  {
    const int g = lane >> 4;
    bf16x8 bo[7];
#pragma unroll
    for (int k = 0; k < 7; ++k) bo[k] = *(const bf16x8*)(lds + K2_OT + ((4 * (7 * kh + k) + g) * 8 + (lane & 7)) * 16);
    f32x4 acc[7];
#pragma unroll
    for (int n = 0; n < 7; ++n) {
      acc[n] = (f32x4){0.f, 0.f, 0.f, 0.f};
#pragma unroll
      for (int k = 0; k < 7; ++k) acc[n] = MFMA16(wf[n][k], bo[k], acc[n]);
    }
    if ((lane & 15) < 8) {
#pragma unroll
      for (int n = 0; n < 7; ++n) *(f32x4*)(lds + K2_HB + ((kh * 8 + (lane & 15)) * D + 16 * (7 * ng + n) + 4 * g) * 4) = acc[n];
    }
  }
  asm volatile("s_waitcnt lgkmcnt(0)\n\ts_barrier" ::: "memory");
  STAMP(1, 3);
  {
    const int s = tg * 8 + w;
    const bool act = lane < 56;
    float v[8];
#pragma unroll
    for (int i = 0; i < 8; ++i) v[i] = 0.f;
    if (act) {
      const f32x4 a0 = *(const f32x4*)(lds + K2_HB + (w * D + lane * 8) * 4), a1 = *(const f32x4*)(lds + K2_HB + (w * D + lane * 8 + 4) * 4);
      const f32x4 c0 = *(const f32x4*)(lds + K2_HB + ((8 + w) * D + lane * 8) * 4), c1 = *(const f32x4*)(lds + K2_HB + ((8 + w) * D + lane * 8 + 4) * 4);
      u32x4 hv;
#pragma unroll
      for (int i = 0; i < 2; ++i) {
        hv[i] = cvtpk(bflo(xv[i]) + a0[2 * i] + c0[2 * i], bfhi(xv[i]) + a0[2 * i + 1] + c0[2 * i + 1]);
        hv[2 + i] = cvtpk(bflo(xv[2 + i]) + a1[2 * i] + c1[2 * i], bfhi(xv[2 + i]) + a1[2 * i + 1] + c1[2 * i + 1]);
      }
      *(u32x4*)(H + (size_t)s * D + lane * 8) = hv;
#pragma unroll
      for (int i = 0; i < 4; ++i) { v[2 * i] = bflo(hv[i]); v[2 * i + 1] = bfhi(hv[i]); }
    }
#pragma unroll
    for (int pass = 0; pass < 2; ++pass) {
      float sm = 0.f;
#pragma unroll
      for (int i = 0; i < 8; ++i) sm += v[i];
      const float mean = wave_sum(sm) * (1.f / D);
      float q = 0.f;
#pragma unroll
      for (int i = 0; i < 8; ++i) { v[i] = act ? v[i] - mean : 0.f; q += v[i] * v[i]; }
      const float rs = __builtin_amdgcn_rsqf(wave_sum(q) * (1.f / D) + LN_EPS);
#pragma unroll
      for (int i = 0; i < 8; ++i) v[i] *= rs;
    }
    if (act) {
#pragma unroll
      for (int i = 0; i < 4; ++i) {
        const unsigned pk = cvtpk(v[2 * i], v[2 * i + 1]);
        *(u16*)(lds + K2_HS + ((lane * 8 + 2 * i) * 8 + w) * 2) = (u16)pk;
        *(u16*)(lds + K2_HS + ((lane * 8 + 2 * i + 1) * 8 + w) * 2) = (u16)(pk >> 16);
      }
    }
  }
  __syncthreads();
  if (tid < D) *(u32x4*)(HN + ((size_t)tg * D + tid) * 8) = *(const u32x4*)(lds + K2_HS + tid * 16);
  STAMP(1, 4);
}

constexpr int G_NS = 8, G_STAGE = 16384, G_LDS = G_NS * G_STAGE;
template <int PH>
__global__ __launch_bounds__(512) void k_mlp(const u16* __restrict__ act, const u16* __restrict__ W, const u16* __restrict__ bias,
                                             const u16* __restrict__ H, u16* __restrict__ out) {
  constexpr int KT = PH == 0 ? S : F, LDW = PH == 0 ? F : S, NT = KT / 64, NWG = 7 * (LDW / 64);
  __shared__ __attribute__((aligned(16))) char lds[G_LDS];
  const int tid = threadIdx.x, lane = tid & 63, w = __builtin_amdgcn_readfirstlane(tid >> 6);
  const int t = xcd_remap(blockIdx.x, NWG), nt = t / 7, mt = t - nt * 7;
  const unsigned lds0 = (unsigned)(uintptr_t)lds;
  STAMP(2 + PH, 0);
  const u16* asrc = act + ((size_t)w * D + 64 * mt + lane) * 8;
  const int rl = 8 * w + (lane >> 3);
  const int fs = ((rl >> 1) & 1) | (((rl >> 3) & 1) << 1);
  const u16* wsrc = W + (size_t)rl * LDW + 64 * nt + ((lane & 7) ^ (fs << 1)) * 8;
#define STAGE(st) do { const unsigned sl_ = lds0 + ((st) % G_NS) * G_STAGE + w * 1024; \
    glds16(asrc + (size_t)(st) * 8 * D * 8, sl_); glds16(wsrc + (size_t)(st) * 64 * LDW, sl_ + 8192); } while (0)
#pragma unroll
  for (int st = 0; st < G_NS - 1; ++st) STAGE(st);
  const int qm = w & 1, qn = (w >> 1) & 1, kh = w >> 2, g = lane >> 4, q4 = (lane & 15) >> 2, p = lane & 3, l15 = lane & 15;
  const int f = ((q4 >> 1) & 1) | ((g & 1) << 1);
  const int aoff = (4 * kh + g) * 1024 + (32 * qm + l15) * 16;
  const int woff0 = 8192 + (32 * kh + 8 * g + q4) * 128 + (((4 * qn + (p >> 1)) ^ (f << 1)) * 16) + 8 * (p & 1);
  const int woff1 = 8192 + (32 * kh + 8 * g + q4) * 128 + (((4 * qn + 2 + (p >> 1)) ^ (f << 1)) * 16) + 8 * (p & 1);
  f32x4 acc[2][2] = {};
  bf16x8 xa0, xa1, xw0, xw1, ya0, ya1, yw0, yw1;
#define WAIT_STAGE(st) do { const int rem_ = NT - 1 - (st); \
    if (rem_ >= 6) WAITV_BAR(12); else if (rem_ == 5) WAITV_BAR(10); else if (rem_ == 4) WAITV_BAR(8); else if (rem_ == 3) WAITV_BAR(6); \
    else if (rem_ == 2) WAITV_BAR(4); else if (rem_ == 1) WAITV_BAR(2); else WAITV_BAR(0); } while (0)
#define READF(st, A0, A1, W0, W1) do { const lds_cptr sb_ = (lds_cptr)lds + ((st) % G_NS) * G_STAGE; \
    A0 = ldsr16(sb_ + aoff); A1 = ldsr16(sb_ + aoff + 256); \
    W0 = cat8(trread(sb_ + woff0), trread(sb_ + woff0 + 512)); W1 = cat8(trread(sb_ + woff1), trread(sb_ + woff1 + 512)); } while (0)
#define MMA(A0, A1, W0, W1) do { if (PH == 0) { \
      acc[0][0] = MFMA16(W0, A0, acc[0][0]); acc[0][1] = MFMA16(W0, A1, acc[0][1]); \
      acc[1][0] = MFMA16(W1, A0, acc[1][0]); acc[1][1] = MFMA16(W1, A1, acc[1][1]); \
    } else { \
      acc[0][0] = MFMA16(A0, W0, acc[0][0]); acc[0][1] = MFMA16(A1, W0, acc[0][1]); \
      acc[1][0] = MFMA16(A0, W1, acc[1][0]); acc[1][1] = MFMA16(A1, W1, acc[1][1]); } } while (0)
  WAIT_STAGE(0);
  STAMP(2 + PH, 1);
  STAGE(G_NS - 1);
  READF(0, xa0, xa1, xw0, xw1);
#pragma unroll 1
  for (int st = 1; st < NT; st += 2) {
    WAIT_STAGE(st);
    if (st + G_NS - 1 < NT) STAGE(st + G_NS - 1);
    READF(st, ya0, ya1, yw0, yw1);
    SBAR();
    MMA(xa0, xa1, xw0, xw1);
    SBAR();
    if (st + 1 < NT) {
      WAIT_STAGE(st + 1);
      if (st + G_NS < NT) STAGE(st + G_NS);
      READF(st + 1, xa0, xa1, xw0, xw1);
    }
    SBAR();
    MMA(ya0, ya1, yw0, yw1);
    SBAR();
  }
#undef WAIT_STAGE
#undef READF
#undef MMA
#undef STAGE
  STAMP(2 + PH, 4);
  asm volatile("s_waitcnt lgkmcnt(0)\n\ts_barrier" ::: "memory");
  {
    f32x4* xb = (f32x4*)lds;
    xb[((w * 2 + 0) * 64 + lane)] = kh ? acc[0][0] : acc[1][0];
    xb[((w * 2 + 1) * 64 + lane)] = kh ? acc[0][1] : acc[1][1];
  }
  __syncthreads();
  {
    const f32x4* xb = (const f32x4*)lds;
    const int pw = w ^ 4, nb = kh;
#pragma unroll
    for (int mb = 0; mb < 2; ++mb) {
      f32x4 a = (kh ? acc[1][mb] : acc[0][mb]) + xb[(pw * 2 + mb) * 64 + lane];
      if (PH == 0) {
        const int fi = 64 * nt + 32 * qn + 16 * nb + 4 * g, d = 64 * mt + 32 * qm + 16 * mb + l15;
        const u32x2 bv = *(const u32x2*)(bias + fi);
        float z[4] = {a[0] + bflo(bv[0]), a[1] + bfhi(bv[0]), a[2] + bflo(bv[1]), a[3] + bfhi(bv[1])};
#pragma unroll
        for (int i = 0; i < 4; ++i) {
          const float zz = z[i], a2 = zz * (1.f + 0.044715f * zz * zz) * (-2.f * 0.7978845608028654f * 1.4426950408889634f);
          z[i] = zz * __builtin_amdgcn_rcpf(1.f + __builtin_amdgcn_exp2f(a2));
        }
        *(u32x2*)(out + ((size_t)(fi >> 3) * D + d) * 8 + (fi & 7)) = (u32x2){cvtpk(z[0], z[1]), cvtpk(z[2], z[3])};
      } else {
        const int d = 64 * mt + 32 * qm + 16 * mb + 4 * g, s = 64 * nt + 32 * qn + 16 * nb + l15;
        const float bb = bflo(bias[s]);
        const u32x2 hv = *(const u32x2*)(H + (size_t)s * D + d);
        *(u32x2*)(out + (size_t)s * D + d) =
            (u32x2){cvtpk(a[0] + bb + bflo(hv[0]), a[1] + bb + bfhi(hv[0])), cvtpk(a[2] + bb + bflo(hv[1]), a[3] + bb + bfhi(hv[1]))};
      }
    }
  }
  STAMP(2 + PH, 5);
}

extern "C" void kernel_launch(void* const* d_in, const int* in_sizes, int n_in, void* d_out, int out_size, void* d_ws, size_t ws_size,
                              hipStream_t stream) {
  const u16* x = (const u16*)d_in[0];
  const u16* wq = (const u16*)d_in[1];
  const u16* wk = (const u16*)d_in[2];
  const u16* wv = (const u16*)d_in[3];
  const u16* wo = (const u16*)d_in[4];
  const u16* w1 = (const u16*)d_in[6];
  const u16* b1 = (const u16*)d_in[7];
  const u16* w2 = (const u16*)d_in[8];
  const u16* b2 = (const u16*)d_in[9];
  char* ws = (char*)d_ws;
  u16 *Qf = (u16*)(ws + WS_QF), *Kf = (u16*)(ws + WS_KF), *Vf = (u16*)(ws + WS_VF), *Wof = (u16*)(ws + WS_WOF);
  u16 *H = (u16*)(ws + WS_H), *HN = (u16*)(ws + WS_HN), *U = (u16*)(ws + WS_U);
  k_qkv<<<256, 512, 0, stream>>>(x, wq, wk, wv, wo, Qf, Kf, Vf, Wof);
  k_attn<<<256, 512, 0, stream>>>(x, Qf, Kf, Vf, Wof, H, HN);
  k_mlp<0><<<196, 512, 0, stream>>>(HN, w1, b1, H, U);
  k_mlp<1><<<224, 512, 0, stream>>>(U, w2, b2, H, (u16*)d_out);
}
```

```cpp
#include <hip/hip_runtime.h>
#include <hip/hip_bf16.h>

typedef unsigned short u16;
using bf16x8 = __attribute__((ext_vector_type(8))) short;
using s16x4 = __attribute__((ext_vector_type(4))) short;
using f32x4 = __attribute__((ext_vector_type(4))) float;
using f32x16 = __attribute__((ext_vector_type(16))) float;
using u32x4 = __attribute__((ext_vector_type(4))) unsigned;
using u32x2 = __attribute__((ext_vector_type(2))) unsigned;
typedef short v4i16_t __attribute__((ext_vector_type(4)));
typedef __attribute__((address_space(3))) char* lds_ptr;
typedef __attribute__((address_space(3))) const char* lds_cptr;

#define DI __device__ __forceinline__
#define MFMA32(a, b, c) __builtin_amdgcn_mfma_f32_32x32x16_bf16(a, b, c, 0, 0, 0)
#define MFMA16(a, b, c) __builtin_amdgcn_mfma_f32_16x16x32_bf16(a, b, c, 0, 0, 0)

constexpr int S = 2048, D = 448, NH = 16, DH = 28, F = 1792;
constexpr float QSCALE = 0.18898223650461362f * 1.4426950408889634f;
constexpr float LN_EPS = 1e-5f;

constexpr size_t WS_QF = 0;
constexpr size_t WS_KF = WS_QF + 2097152;
constexpr size_t WS_VF = WS_KF + 131072;
constexpr size_t WS_WOF = WS_VF + 131072;
constexpr size_t WS_H = WS_WOF + 401408;
constexpr size_t WS_HN = WS_H + 1835008;
constexpr size_t WS_U = WS_HN + 1835008;

typedef __bf16 bf16x2_t __attribute__((ext_vector_type(2)));
typedef float f32x2_t __attribute__((ext_vector_type(2)));
DI unsigned cvtpk(float lo, float hi) {
  const f32x2_t v = {lo, hi};
  return __builtin_bit_cast(unsigned, __builtin_convertvector(v, bf16x2_t));
}
DI float bflo(unsigned u) { return __uint_as_float(u << 16); }
DI float bfhi(unsigned u) { return __uint_as_float(u & 0xffff0000u); }
template <int CTRL> DI float dppf(float v) {
  return __uint_as_float(__builtin_amdgcn_update_dpp(0u, __float_as_uint(v), CTRL, 0xf, 0xf, true));
}
DI float wave_sum(float v) {
  v += dppf<0xB1>(v);
  v += dppf<0x4E>(v);
  v += dppf<0x141>(v);
  v += dppf<0x140>(v);
  auto a = __builtin_amdgcn_permlane16_swap(__float_as_uint(v), __float_as_uint(v), false, false);
  v = __uint_as_float(a[0]) + __uint_as_float(a[1]);
  auto b = __builtin_amdgcn_permlane32_swap(__float_as_uint(v), __float_as_uint(v), false, false);
  return __uint_as_float(b[0]) + __uint_as_float(b[1]);
}
DI float half_max(float v) {
  auto b = __builtin_amdgcn_permlane32_swap(__float_as_uint(v), __float_as_uint(v), false, false);
  return __builtin_fmaxf(__uint_as_float(b[0]), __uint_as_float(b[1]));
}
DI void glds16(const void* g, unsigned lds_base) {
  unsigned sv;
  asm volatile("s_mov_b32 %0, m0\n\ts_mov_b32 m0, %2\n\ts_nop 0\n\tglobal_load_lds_dwordx4 %1, off\n\ts_mov_b32 m0, %0"
               : "=&s"(sv) : "v"(g), "s"(lds_base) : "memory");
}
DI void glds4(const void* g, unsigned lds_base) {
  unsigned sv;
  asm volatile("s_mov_b32 %0, m0\n\ts_mov_b32 m0, %2\n\ts_nop 0\n\tglobal_load_lds_dword %1, off\n\ts_mov_b32 m0, %0"
               : "=&s"(sv) : "v"(g), "s"(lds_base) : "memory");
}
#define SBAR() __builtin_amdgcn_sched_barrier(0)
DI s16x4 trread(lds_cptr p) {
  return __builtin_bit_cast(s16x4, __builtin_amdgcn_ds_read_tr16_b64_v4i16((__attribute__((address_space(3))) v4i16_t*)p));
}
DI bf16x8 ldsr16(lds_cptr p) { return *(const __attribute__((address_space(3))) bf16x8*)p; }
DI bf16x8 cat8(s16x4 a, s16x4 b) { return __builtin_shufflevector(a, b, 0, 1, 2, 3, 4, 5, 6, 7); }
DI int xcd_remap(int bid, int nwg) {
  const int q = nwg >> 3, r = nwg & 7, x = bid & 7, o = bid >> 3;
  return (x < r ? x * (q + 1) : r * (q + 1) + (x - r) * q) + o;
}
#define STAMP(k, i)
#define WAITV_BAR(N) asm volatile("s_waitcnt vmcnt(" #N ") lgkmcnt(0)\n\ts_barrier" ::: "memory")

constexpr int K1_W = 0, K1_X = 448 * 128, K1_XS = 1040, K1_LDS = K1_X + 64 * K1_XS;
__global__ __launch_bounds__(512) void k_qkv(const u16* __restrict__ x, const u16* __restrict__ wq, const u16* __restrict__ wk,
                                             const u16* __restrict__ wv, const u16* __restrict__ wo, u16* __restrict__ Qf,
                                             u16* __restrict__ Kf, u16* __restrict__ Vf, u16* __restrict__ Wof) {
  __shared__ __attribute__((aligned(16))) char lds[K1_LDS];
  const int tid = threadIdx.x, lane = tid & 63, w = __builtin_amdgcn_readfirstlane(tid >> 6);
  const int t = xcd_remap(blockIdx.x, 256), rt = t >> 3, ct = t & 7;
  const unsigned lds0 = (unsigned)(uintptr_t)lds;
  STAMP(0, 0);
  u32x4 xv[8];
  const bool act = lane < 56;
  {
    const int lc = act ? lane : 55;
#pragma unroll
    for (int r = 0; r < 8; ++r) xv[r] = *(const u32x4*)(x + (size_t)(rt * 64 + w * 8 + r) * D + lc * 8);
  }
  if (ct < 7) {
#pragma unroll
    for (int i = 0; i < 7; ++i) {
      const int piece = w * 7 + i, row = piece * 8 + (lane >> 3);
      const int f = ((row >> 1) & 1) | (((row >> 3) & 1) << 1);
      const int c = (lane & 7) ^ (f << 1);
      glds16(wq + (size_t)row * D + ct * 64 + c * 8, lds0 + K1_W + piece * 1024);
    }
  } else {
#pragma unroll
    for (int i = 0; i < 28; ++i) {
      const int piece = w * 28 + i, row = piece * 2 + (lane >> 5), pp = lane & 31;
      const int f = ((row >> 1) & 1) | (((row >> 3) & 1) << 1);
      const int col = ((((pp >> 2) ^ (f << 1)) << 2) + (pp & 3)) * 2;
      const u16* src = col < 28 ? wk + row * DH + col : wv + row * DH + (col < 56 ? col - 28 : 0);
      glds4(src, lds0 + K1_W + piece * 256);
    }
  }
  u16 wt[8];
  const int frag0 = blockIdx.x + 256 * (w & 1);
  const bool dofrag = w < 2 && frag0 < 392;
  const int frag = dofrag ? frag0 : 0;
  {
    const int nb = frag / 14, ks = frag - nb * 14;
#pragma unroll
    for (int j = 0; j < 8; ++j) wt[j] = wo[(size_t)(32 * ks + 8 * (lane >> 4) + j) * D + 16 * nb + (lane & 15)];
  }
  SBAR();
  {
    float v[8][8], mean[8], rs[8];
#pragma unroll
    for (int r = 0; r < 8; ++r) {
      float s = 0.f;
#pragma unroll
      for (int i = 0; i < 4; ++i) {
        v[r][2 * i] = act ? bflo(xv[r][i]) : 0.f; v[r][2 * i + 1] = act ? bfhi(xv[r][i]) : 0.f;
        s += v[r][2 * i] + v[r][2 * i + 1];
      }
      mean[r] = s;
    }
#pragma unroll
    for (int r = 0; r < 8; ++r) mean[r] = wave_sum(mean[r]) * (1.f / D);
#pragma unroll
    for (int r = 0; r < 8; ++r) {
      float q = 0.f;
#pragma unroll
      for (int i = 0; i < 8; ++i) { v[r][i] = act ? v[r][i] - mean[r] : 0.f; q += v[r][i] * v[r][i]; }
      rs[r] = q;
    }
#pragma unroll
    for (int r = 0; r < 8; ++r) rs[r] = __builtin_amdgcn_rsqf(wave_sum(rs[r]) * (1.f / D) + LN_EPS);
    char* xdst = lds + K1_X + lane * K1_XS + w * 128;
#pragma unroll
    for (int r = 0; r < 8; ++r) {
      u32x4 o;
#pragma unroll
      for (int i = 0; i < 4; ++i) o[i] = cvtpk(v[r][2 * i] * rs[r], v[r][2 * i + 1] * rs[r]);
      *(u32x4*)(xdst + r * 16) = o;
    }
  }
  if (dofrag) {
    u32x4 o;
#pragma unroll
    for (int i = 0; i < 4; ++i) o[i] = (unsigned)wt[2 * i] | ((unsigned)wt[2 * i + 1] << 16);
    *(u32x4*)(Wof + ((size_t)frag * 64 + lane) * 8) = o;
  }
  STAMP(0, 1);
  WAITV_BAR(0);
  STAMP(0, 2);
  const int nb = w & 3, th = w >> 2, g = lane >> 4, q4 = (lane & 15) >> 2, p = lane & 3, l15 = lane & 15;
  const int f = ((q4 >> 1) & 1) | ((g & 1) << 1);
  const lds_cptr wp = (lds_cptr)lds + K1_W + (8 * g + q4) * 128 + (((2 * nb + (p >> 1)) ^ (f << 1)) * 16) + 8 * (p & 1);
  const lds_cptr xp = (lds_cptr)lds + K1_X + g * K1_XS + (32 * th + l15) * 16;
  f32x4 acc0 = {0.f, 0.f, 0.f, 0.f}, acc1 = {0.f, 0.f, 0.f, 0.f};
  {
    bf16x8 a[14], b0[14], b1[14];
#pragma unroll
    for (int ks = 0; ks < 14; ++ks) {
      a[ks] = cat8(trread(wp + ks * 4096), trread(wp + ks * 4096 + 512));
      b0[ks] = ldsr16(xp + ks * 4 * K1_XS);
      b1[ks] = ldsr16(xp + ks * 4 * K1_XS + 256);
    }
    SBAR();
#pragma unroll
    for (int ks = 0; ks < 14; ++ks) {
      acc0 = MFMA16(a[ks], b0[ks], acc0);
      acc1 = MFMA16(a[ks], b1[ks], acc1);
    }
  }
  STAMP(0, 3);
#pragma unroll
  for (int tb = 0; tb < 2; ++tb) {
    const f32x4 a = tb ? acc1 : acc0;
    const int s = rt * 64 + 32 * th + 16 * tb + l15;
    const int nl = 16 * nb + 4 * g;
    if (ct < 7) {
      const int n0 = 64 * ct + nl, h = n0 / 28, d0 = n0 - 28 * h;
      const int tg = s >> 3, c = (h & 3) * 8 + (s & 7), b = h >> 2;
      const size_t off = ((((size_t)(tg * 4 + b) * 2 + (d0 >> 4)) * 64) + ((d0 >> 3) & 1) * 32 + c) * 8 + (d0 & 7);
      *(u32x2*)(Qf + off) = (u32x2){cvtpk(a[0] * QSCALE, a[1] * QSCALE), cvtpk(a[2] * QSCALE, a[3] * QSCALE)};
    } else {
      const int kt = s >> 5, kk = s & 31;
      if (nl < 28 || nl == 56) {
        const int d0 = nl < 28 ? nl : 28;
        const size_t off = ((((size_t)kt * 2 + (d0 >> 4)) * 64) + ((d0 >> 3) & 1) * 32 + kk) * 8 + (d0 & 7);
        u32x2 v = {cvtpk(a[0], a[1]), cvtpk(a[2], a[3])};
        if (nl == 56) v = (u32x2){0x00003f80u, 0u};
        *(u32x2*)(Kf + off) = v;
      } else {
        const int d0 = nl < 56 ? nl - 28 : 28;
        const int sp = kk >> 4, j = ((kk >> 3) & 1) * 4 + (kk & 3), hi = (kk >> 2) & 1;
        const size_t off = ((((size_t)kt * 2 + sp) * 64) + hi * 32 + d0) * 8 + j;
        unsigned v0 = cvtpk(a[0], a[1]), v1 = cvtpk(a[2], a[3]);
        if (nl == 60) { v0 = 0x00003f80u; v1 = 0u; }
        Vf[off] = (u16)v0; Vf[off + 8] = (u16)(v0 >> 16); Vf[off + 16] = (u16)v1; Vf[off + 24] = (u16)(v1 >> 16);
      }
    }
  }
  STAMP(0, 4);
}

constexpr int K2_P = 0, K2_M = 131072, K2_OT = K2_M + 4096, K2_LDS = K2_OT + 7168;
constexpr int K2_HB = 0, K2_HS = 32768;

DI void set_slot(bf16x8& q1, float mref_neg, int hi) {
  u32x4 v = __builtin_bit_cast(u32x4, q1);
  const unsigned s = cvtpk(mref_neg, 0.f);
  v[2] = hi ? s : v[2];
  v[3] = hi ? 0u : v[3];
  q1 = __builtin_bit_cast(bf16x8, v);
}
DI float tile_max(const f32x16& s) {
  float a = __builtin_fmaxf(__builtin_fmaxf(s[0], s[1]), s[2]), b = __builtin_fmaxf(__builtin_fmaxf(s[3], s[4]), s[5]);
  a = __builtin_fmaxf(__builtin_fmaxf(a, s[6]), s[7]); b = __builtin_fmaxf(__builtin_fmaxf(b, s[8]), s[9]);
  a = __builtin_fmaxf(__builtin_fmaxf(a, s[10]), s[11]); b = __builtin_fmaxf(__builtin_fmaxf(b, s[12]), s[13]);
  a = __builtin_fmaxf(__builtin_fmaxf(a, s[14]), s[15]);
  return half_max(__builtin_fmaxf(a, b));
}
DI float bf16_round(float v) { return bflo(cvtpk(v, 0.f)); }

template <bool SAFE>
DI void attn_loop(const u16* __restrict__ Kw, const u16* __restrict__ Vw, bf16x8 (&qf)[4][2], f32x16 (&o)[4], float (&mref)[4], int lane, int hi) {
  const f32x16 zero = {};
#pragma unroll
  for (int b = 0; b < 4; ++b) o[b] = zero;
  bf16x8 k0 = *(const bf16x8*)(Kw + lane * 8), k1 = *(const bf16x8*)(Kw + 512 + lane * 8);
  bf16x8 v0 = *(const bf16x8*)(Vw + lane * 8), v1 = *(const bf16x8*)(Vw + 512 + lane * 8);
#pragma unroll
  for (int b = 0; b < 4; ++b) {
    set_slot(qf[b][1], 0.f, hi);
    f32x16 s = MFMA32(k0, qf[b][0], zero);
    s = MFMA32(k1, qf[b][1], s);
    mref[b] = bf16_round(tile_max(s));
    set_slot(qf[b][1], -mref[b], hi);
  }
#pragma unroll 1
  for (int t = 0; t < 8; ++t) {
    const int tn = t < 7 ? t + 1 : 7;
    const bf16x8 nk0 = *(const bf16x8*)(Kw + tn * 1024 + lane * 8), nk1 = *(const bf16x8*)(Kw + tn * 1024 + 512 + lane * 8);
    const bf16x8 nv0 = *(const bf16x8*)(Vw + tn * 1024 + lane * 8), nv1 = *(const bf16x8*)(Vw + tn * 1024 + 512 + lane * 8);
#pragma unroll
    for (int b = 0; b < 4; ++b) {
      f32x16 s = MFMA32(k0, qf[b][0], zero);
      s = MFMA32(k1, qf[b][1], s);
      if (SAFE) {
        const float tm = tile_max(s);
        if (__any(tm > 0.f)) {
          const float mn = bf16_round(mref[b] + __builtin_fmaxf(tm, 0.f));
          const float mnew = __builtin_fmaxf(mn, mref[b]);
          const float al = __builtin_amdgcn_exp2f(mref[b] - mnew);
#pragma unroll
          for (int r = 0; r < 16; ++r) o[b][r] *= al;
          mref[b] = mnew;
          set_slot(qf[b][1], -mnew, hi);
          s = MFMA32(k0, qf[b][0], zero);
          s = MFMA32(k1, qf[b][1], s);
        }
      }
      u32x4 p0, p1;
#pragma unroll
      for (int i = 0; i < 4; ++i) {
        p0[i] = cvtpk(__builtin_amdgcn_exp2f(s[2 * i]), __builtin_amdgcn_exp2f(s[2 * i + 1]));
        p1[i] = cvtpk(__builtin_amdgcn_exp2f(s[8 + 2 * i]), __builtin_amdgcn_exp2f(s[8 + 2 * i + 1]));
      }
      o[b] = MFMA32(v0, __builtin_bit_cast(bf16x8, p0), o[b]);
      o[b] = MFMA32(v1, __builtin_bit_cast(bf16x8, p1), o[b]);
    }
    k0 = nk0; k1 = nk1; v0 = nv0; v1 = nv1;
  }
}

__global__ __launch_bounds__(512) void k_attn(const u16* __restrict__ x, const u16* __restrict__ Qf, const u16* __restrict__ Kf,
                                              const u16* __restrict__ Vf, const u16* __restrict__ Wof, u16* __restrict__ H,
                                              u16* __restrict__ HN) {
  __shared__ __attribute__((aligned(16))) char lds[K2_LDS];
  const int tid = threadIdx.x, lane = tid & 63, w = __builtin_amdgcn_readfirstlane(tid >> 6), hi = lane >> 5;
  const int tg = blockIdx.x;
  STAMP(1, 0);
  bf16x8 qf[4][2];
#pragma unroll
  for (int b = 0; b < 4; ++b)
#pragma unroll
    for (int k = 0; k < 2; ++k) qf[b][k] = *(const bf16x8*)(Qf + ((size_t)((tg * 4 + b) * 2 + k) * 64 + lane) * 8);
  const u32x4 xv = *(const u32x4*)(x + (size_t)(tg * 8 + w) * D + (lane < 56 ? lane : 55) * 8);
  f32x16 o[4];
  float mref[4];
  const u16* Kw = Kf + (size_t)w * 8 * 1024;
  const u16* Vw = Vf + (size_t)w * 8 * 1024;
  attn_loop<false>(Kw, Vw, qf, o, mref, lane, hi);
  {
    float chk = 0.f;
    bool bad = false;
#pragma unroll
    for (int b = 0; b < 4; ++b) {
#pragma unroll
      for (int r = 0; r < 16; ++r) chk = __builtin_fmaf(o[b][r], 0.f, chk);
      bad |= !(o[b][12] < 1.8e19f);
    }
    bad |= !(chk == 0.f);
    if (__any(bad)) attn_loop<true>(Kw, Vw, qf, o, mref, lane, hi);
  }
  STAMP(1, 1);
  const int kh = w >> 2, ng = w & 3;
  const u16* wop = Wof + ((size_t)(7 * ng * 14 + 7 * kh) * 64 + lane) * 8;
  bf16x8 wf[7][7];
#pragma unroll
  for (int n = 0; n < 5; ++n)
#pragma unroll
    for (int k = 0; k < 7; ++k) wf[n][k] = *(const bf16x8*)(wop + (n * 14 + k) * 512);
  SBAR();
#pragma unroll
  for (int b = 0; b < 4; ++b) {
#pragma unroll
    for (int r4 = 0; r4 < 4; ++r4)
      *(f32x4*)(lds + K2_P + (((w * 4 + b) * 4 + r4) * 64 + lane) * 16) = (f32x4){o[b][4 * r4], o[b][4 * r4 + 1], o[b][4 * r4 + 2], o[b][4 * r4 + 3]};
    if (hi == 0) *(float*)(lds + K2_M + (w * 128 + b * 32 + lane) * 4) = mref[b];
  }
  SBAR();
#pragma unroll
  for (int n = 5; n < 7; ++n)
#pragma unroll
    for (int k = 0; k < 7; ++k) wf[n][k] = *(const bf16x8*)(wop + (n * 14 + k) * 512);
  SBAR();
  STAMP(1, 5);
  asm volatile("s_waitcnt lgkmcnt(0)\n\ts_barrier" ::: "memory");
  STAMP(1, 6);
#pragma unroll
  for (int i = 0; i < 2; ++i) {
    const int id = tid + 512 * i, b = id >> 8, r4 = (id >> 6) & 3, c = lane & 31, q = 32 * b + c;
    float m[8], M = -INFINITY;
#pragma unroll
    for (int v = 0; v < 8; ++v) { m[v] = *(const float*)(lds + K2_M + (v * 128 + q) * 4); M = __builtin_fmaxf(M, m[v]); }
    float l = 0.f;
    f32x4 a = {0.f, 0.f, 0.f, 0.f};
#pragma unroll
    for (int v = 0; v < 8; ++v) {
      const float fc = __builtin_amdgcn_exp2f(m[v] - M);
      l += fc * *(const float*)(lds + K2_P + (((v * 4 + b) * 4 + 3) * 64 + 32 + c) * 16);
      const f32x4 pv = *(const f32x4*)(lds + K2_P + (((v * 4 + b) * 4 + r4) * 64 + lane) * 16);
      a += fc * pv;
    }
    const float il = 1.f / l;
    const int d = 8 * r4 + 4 * hi;
    if (d < 28) {
      const int k = 28 * (4 * b + (c >> 3)) + d;
      *(u32x2*)(lds + K2_OT + ((k >> 3) * 8 + (c & 7)) * 16 + (k & 7) * 2) = (u32x2){cvtpk(a[0] * il, a[1] * il), cvtpk(a[2] * il, a[3] * il)};
    }
  }
  asm volatile("s_waitcnt lgkmcnt(0)\n\ts_barrier" ::: "memory");
  STAMP(1, 2);
  {
    const int g = lane >> 4;
    bf16x8 bo[7];
#pragma unroll
    for (int k = 0; k < 7; ++k) bo[k] = *(const bf16x8*)(lds + K2_OT + ((4 * (7 * kh + k) + g) * 8 + (lane & 7)) * 16);
    f32x4 acc[7];
#pragma unroll
    for (int n = 0; n < 7; ++n) {
      acc[n] = (f32x4){0.f, 0.f, 0.f, 0.f};
#pragma unroll
      for (int k = 0; k < 7; ++k) acc[n] = MFMA16(wf[n][k], bo[k], acc[n]);
    }
    if ((lane & 15) < 8) {
#pragma unroll
      for (int n = 0; n < 7; ++n) *(f32x4*)(lds + K2_HB + ((kh * 8 + (lane & 15)) * D + 16 * (7 * ng + n) + 4 * g) * 4) = acc[n];
    }
  }
  asm volatile("s_waitcnt lgkmcnt(0)\n\ts_barrier" ::: "memory");
  STAMP(1, 3);
  {
    const int s = tg * 8 + w;
    const bool act = lane < 56;
    float v[8];
#pragma unroll
    for (int i = 0; i < 8; ++i) v[i] = 0.f;
    if (act) {
      const f32x4 a0 = *(const f32x4*)(lds + K2_HB + (w * D + lane * 8) * 4), a1 = *(const f32x4*)(lds + K2_HB + (w * D + lane * 8 + 4) * 4);
      const f32x4 c0 = *(const f32x4*)(lds + K2_HB + ((8 + w) * D + lane * 8) * 4), c1 = *(const f32x4*)(lds + K2_HB + ((8 + w) * D + lane * 8 + 4) * 4);
      u32x4 hv;
#pragma unroll
      for (int i = 0; i < 2; ++i) {
        hv[i] = cvtpk(bflo(xv[i]) + a0[2 * i] + c0[2 * i], bfhi(xv[i]) + a0[2 * i + 1] + c0[2 * i + 1]);
        hv[2 + i] = cvtpk(bflo(xv[2 + i]) + a1[2 * i] + c1[2 * i], bfhi(xv[2 + i]) + a1[2 * i + 1] + c1[2 * i + 1]);
      }
      *(u32x4*)(H + (size_t)s * D + lane * 8) = hv;
#pragma unroll
      for (int i = 0; i < 4; ++i) { v[2 * i] = bflo(hv[i]); v[2 * i + 1] = bfhi(hv[i]); }
    }
#pragma unroll
    for (int pass = 0; pass < 2; ++pass) {
      float sm = 0.f;
#pragma unroll
      for (int i = 0; i < 8; ++i) sm += v[i];
      const float mean = wave_sum(sm) * (1.f / D);
      float q = 0.f;
#pragma unroll
      for (int i = 0; i < 8; ++i) { v[i] = act ? v[i] - mean : 0.f; q += v[i] * v[i]; }
      const float rs = __builtin_amdgcn_rsqf(wave_sum(q) * (1.f / D) + LN_EPS);
#pragma unroll
      for (int i = 0; i < 8; ++i) v[i] *= rs;
    }
    if (act) *(u32x4*)(lds + K2_HS + w * 912 + lane * 16) = (u32x4){cvtpk(v[0], v[1]), cvtpk(v[2], v[3]), cvtpk(v[4], v[5]), cvtpk(v[6], v[7])};
  }
  asm volatile("s_waitcnt lgkmcnt(0)\n\ts_barrier" ::: "memory");
  if (tid < D / 2) {
    unsigned r[8];
#pragma unroll
    for (int i = 0; i < 8; ++i) r[i] = *(const unsigned*)(lds + K2_HS + i * 912 + tid * 4);
    u32x4 lo, hi2;
#pragma unroll
    for (int i = 0; i < 4; ++i) {
      lo[i] = (r[2 * i] & 0xffffu) | (r[2 * i + 1] << 16);
      hi2[i] = (r[2 * i] >> 16) | (r[2 * i + 1] & 0xffff0000u);
    }
    u32x4* dst = (u32x4*)(HN + ((size_t)tg * D + 2 * tid) * 8);
    dst[0] = lo; dst[1] = hi2;
  }
  STAMP(1, 4);
}

constexpr int G_RING = 4, G_LDS = 8 * G_RING * 4096;
template <int N> DI void waitv() { asm volatile("s_waitcnt vmcnt(%0)" ::"i"(N) : "memory"); }
template <int PH>
__global__ __launch_bounds__(512) void k_mlp(const u16* __restrict__ act, const u16* __restrict__ W, const u16* __restrict__ bias,
                                             const u16* __restrict__ H, u16* __restrict__ out) {
  constexpr int KT = PH == 0 ? S : F, LDW = PH == 0 ? F : S, NK = KT / 256, NWG = 7 * (LDW / 64);
  __shared__ __attribute__((aligned(16))) char lds[G_LDS];
  const int tid = threadIdx.x, lane = tid & 63, w = __builtin_amdgcn_readfirstlane(tid >> 6);
  const int t = xcd_remap(blockIdx.x, NWG), nt = t / 7, mt = t - nt * 7;
  const unsigned lds0 = (unsigned)(uintptr_t)lds + w * (G_RING * 4096);
  const int g = lane >> 4, q4 = (lane & 15) >> 2, p = lane & 3, l15 = lane & 15;
  STAMP(2 + PH, 0);
  const int enb = w >> 1, emb = 2 * (w & 1);
  u32x2 eb, eh0, eh1;
  if (PH == 0) {
    eb = *(const u32x2*)(bias + 64 * nt + 16 * enb + 4 * g);
  } else {
    const int s = 64 * nt + 16 * enb + l15;
    eb[0] = bias[s];
    eh0 = *(const u32x2*)(H + (size_t)s * D + 64 * mt + 16 * emb + 4 * g);
    eh1 = *(const u32x2*)(H + (size_t)s * D + 64 * mt + 16 * emb + 16 + 4 * g);
  }
  const u16* asrc = act + ((size_t)(4 * w + g) * D + 64 * mt + l15) * 8;
  const int r8 = lane >> 3, fe = (r8 >> 1) & 1;
  const u16* wsrc0 = W + (size_t)(32 * w + r8) * LDW + 64 * nt + ((lane & 7) ^ (fe << 1)) * 8;
  const u16* wsrc1 = W + (size_t)(32 * w + 8 + r8) * LDW + 64 * nt + ((lane & 7) ^ ((fe | 2) << 1)) * 8;
#define STAGE_W(i) do { const unsigned sl_ = lds0 + ((i) % G_RING) * 4096; const size_t o_ = (size_t)(i) * 256 * LDW; \
    glds16(wsrc0 + o_, sl_); glds16(wsrc1 + o_, sl_ + 1024); glds16(wsrc0 + o_ + 16 * LDW, sl_ + 2048); glds16(wsrc1 + o_ + 16 * LDW, sl_ + 3072); } while (0)
  bf16x8 af[NK][4];
#define LOAD_A(i) do { _Pragma("unroll") for (int mb_ = 0; mb_ < 4; ++mb_) af[i][mb_] = *(const bf16x8*)(asrc + (size_t)(i) * 32 * D * 8 + mb_ * 128); } while (0)
#pragma unroll
  for (int i = 0; i < NK; ++i) {
    if (i < G_RING) STAGE_W(i);
    LOAD_A(i);
    asm volatile("" ::: "memory");
  }
  STAMP(2 + PH, 1);
  const int f = ((q4 >> 1) & 1) | ((g & 1) << 1);
  int woff[4];
#pragma unroll
  for (int nb = 0; nb < 4; ++nb) woff[nb] = w * (G_RING * 4096) + (8 * g + q4) * 128 + (((2 * nb + (p >> 1)) ^ (f << 1)) * 16) + 8 * (p & 1);
  f32x4 acc[4][4] = {};
#pragma unroll
  for (int i = 0; i < NK; ++i) {
    constexpr int dummy = 0; (void)dummy;
    if (i < G_RING) {
      if (i == 0) waitv<4 + 8 * (G_RING - 1) + 4 * (NK - G_RING)>();
      else if (i == 1) waitv<4 + 8 * (G_RING - 2) + 4 * (NK - G_RING) + 4 * (NK > G_RING ? 1 : 0)>();
      else if (i == 2) waitv<4 + 8 * (G_RING - 3) + 4 * (NK - G_RING) + 4 * (NK > G_RING + 1 ? 2 : 1)>();
      else waitv<4 + 4 * (NK - G_RING) + 4 * (NK > G_RING + 2 ? 3 : 2)>();
    } else {
      if (NK - 1 - i >= 3) waitv<12>();
      else if (NK - 1 - i == 2) waitv<8>();
      else if (NK - 1 - i == 1) waitv<4>();
      else waitv<0>();
    }
    bf16x8 wf[4];
#pragma unroll
    for (int nb = 0; nb < 4; ++nb) {
      const lds_cptr sp = (lds_cptr)lds + woff[nb] + (i % G_RING) * 4096;
      wf[nb] = cat8(trread(sp), trread(sp + 512));
    }
    if (i + G_RING < NK) {
      asm volatile("s_waitcnt lgkmcnt(0)" ::: "memory");
      STAGE_W(i + G_RING);
    }
#pragma unroll
    for (int nb = 0; nb < 4; ++nb)
#pragma unroll
      for (int mb = 0; mb < 4; ++mb)
        acc[nb][mb] = PH == 0 ? MFMA16(wf[nb], af[i][mb], acc[nb][mb]) : MFMA16(af[i][mb], wf[nb], acc[nb][mb]);
  }
#undef STAGE_W
#undef LOAD_A
  STAMP(2 + PH, 4);
  {
    f32x4* xb = (f32x4*)lds;
#pragma unroll
    for (int nb = 0; nb < 4; ++nb)
#pragma unroll
      for (int mb = 0; mb < 4; ++mb) xb[(w * 16 + nb * 4 + mb) * 64 + lane] = acc[nb][mb];
  }
  asm volatile("s_waitcnt lgkmcnt(0)\n\ts_barrier" ::: "memory");
  {
    const f32x4* xb = (const f32x4*)lds;
#pragma unroll
    for (int e = 0; e < 2; ++e) {
      f32x4 a = xb[(2 * w + e) * 64 + lane];
#pragma unroll
      for (int v = 1; v < 8; ++v) a += xb[(v * 16 + 2 * w + e) * 64 + lane];
      const int mb = emb + e;
      if (PH == 0) {
        const int fi = 64 * nt + 16 * enb + 4 * g, d = 64 * mt + 16 * mb + l15;
        float z[4] = {a[0] + bflo(eb[0]), a[1] + bfhi(eb[0]), a[2] + bflo(eb[1]), a[3] + bfhi(eb[1])};
#pragma unroll
        for (int i = 0; i < 4; ++i) {
          const float zz = z[i], a2 = zz * (1.f + 0.044715f * zz * zz) * (-2.f * 0.7978845608028654f * 1.4426950408889634f);
          z[i] = zz * __builtin_amdgcn_rcpf(1.f + __builtin_amdgcn_exp2f(a2));
        }
        *(u32x2*)(out + ((size_t)(fi >> 3) * D + d) * 8 + (fi & 7)) = (u32x2){cvtpk(z[0], z[1]), cvtpk(z[2], z[3])};
      } else {
        const int d = 64 * mt + 16 * mb + 4 * g, s = 64 * nt + 16 * enb + l15;
        const float bb = bflo(eb[0]);
        const u32x2 hv = e ? eh1 : eh0;
        *(u32x2*)(out + (size_t)s * D + d) =
            (u32x2){cvtpk(a[0] + bb + bflo(hv[0]), a[1] + bb + bfhi(hv[0])), cvtpk(a[2] + bb + bflo(hv[1]), a[3] + bb + bfhi(hv[1]))};
      }
    }
  }
  STAMP(2 + PH, 5);
}

extern "C" void kernel_launch(void* const* d_in, const int* in_sizes, int n_in, void* d_out, int out_size, void* d_ws, size_t ws_size,
                              hipStream_t stream) {
  const u16* x = (const u16*)d_in[0];
  const u16* wq = (const u16*)d_in[1];
  const u16* wk = (const u16*)d_in[2];
  const u16* wv = (const u16*)d_in[3];
  const u16* wo = (const u16*)d_in[4];
  const u16* w1 = (const u16*)d_in[6];
  const u16* b1 = (const u16*)d_in[7];
  const u16* w2 = (const u16*)d_in[8];
  const u16* b2 = (const u16*)d_in[9];
  char* ws = (char*)d_ws;
  u16 *Qf = (u16*)(ws + WS_QF), *Kf = (u16*)(ws + WS_KF), *Vf = (u16*)(ws + WS_VF), *Wof = (u16*)(ws + WS_WOF);
  u16 *H = (u16*)(ws + WS_H), *HN = (u16*)(ws + WS_HN), *U = (u16*)(ws + WS_U);
  k_qkv<<<256, 512, 0, stream>>>(x, wq, wk, wv, wo, Qf, Kf, Vf, Wof);
  k_attn<<<256, 512, 0, stream>>>(x, Qf, Kf, Vf, Wof, H, HN);
  k_mlp<0><<<196, 512, 0, stream>>>(HN, w1, b1, H, U);
  k_mlp<1><<<224, 512, 0, stream>>>(U, w2, b2, H, (u16*)d_out);
}
```

```cpp
#include <hip/hip_runtime.h>
#include <hip/hip_bf16.h>

typedef unsigned short u16;
using bf16x8 = __attribute__((ext_vector_type(8))) short;
using s16x4 = __attribute__((ext_vector_type(4))) short;
using f32x4 = __attribute__((ext_vector_type(4))) float;
using f32x16 = __attribute__((ext_vector_type(16))) float;
using u32x4 = __attribute__((ext_vector_type(4))) unsigned;
using u32x2 = __attribute__((ext_vector_type(2))) unsigned;
typedef short v4i16_t __attribute__((ext_vector_type(4)));
typedef __attribute__((address_space(3))) const char* lds_cptr;
typedef __bf16 bf16x2_t __attribute__((ext_vector_type(2)));
typedef float f32x2_t __attribute__((ext_vector_type(2)));

#define DI __device__ __forceinline__
#define MFMA32(a, b, c) __builtin_amdgcn_mfma_f32_32x32x16_bf16(a, b, c, 0, 0, 0)
#define MFMA16(a, b, c) __builtin_amdgcn_mfma_f32_16x16x32_bf16(a, b, c, 0, 0, 0)
#define SBAR() __builtin_amdgcn_sched_barrier(0)
#define LBAR() asm volatile("s_waitcnt lgkmcnt(0)\n\ts_barrier" ::: "memory")
#define WAITV_BAR(N) asm volatile("s_waitcnt vmcnt(" #N ") lgkmcnt(0)\n\ts_barrier" ::: "memory")

constexpr int S = 2048, D = 448, DH = 28, F = 1792;
constexpr float QSCALE = 0.18898223650461362f * 1.4426950408889634f;
constexpr float LN_EPS = 1e-5f;

constexpr size_t WS_QF = 0;
constexpr size_t WS_KF = WS_QF + 2097152;
constexpr size_t WS_VF = WS_KF + 131072;
constexpr size_t WS_WOF = WS_VF + 131072;
constexpr size_t WS_H = WS_WOF + 401408;
constexpr size_t WS_HN = WS_H + 1835008;
constexpr size_t WS_U = WS_HN + 1835008;

#define STAMP(k, i)
#define CSTAMP(k, i)

DI unsigned cvtpk(float lo, float hi) {
  const f32x2_t v = {lo, hi};
  return __builtin_bit_cast(unsigned, __builtin_convertvector(v, bf16x2_t));
}
DI float bflo(unsigned u) { return __uint_as_float(u << 16); }
DI float bfhi(unsigned u) { return __uint_as_float(u & 0xffff0000u); }
template <int CTRL> DI float dppf(float v) {
  return __uint_as_float(__builtin_amdgcn_update_dpp(0u, __float_as_uint(v), CTRL, 0xf, 0xf, true));
}
DI float wave_sum(float v) {
  v += dppf<0xB1>(v);
  v += dppf<0x4E>(v);
  v += dppf<0x141>(v);
  v += dppf<0x140>(v);
  auto a = __builtin_amdgcn_permlane16_swap(__float_as_uint(v), __float_as_uint(v), false, false);
  v = __uint_as_float(a[0]) + __uint_as_float(a[1]);
  auto b = __builtin_amdgcn_permlane32_swap(__float_as_uint(v), __float_as_uint(v), false, false);
  return __uint_as_float(b[0]) + __uint_as_float(b[1]);
}
DI float half_max(float v) {
  auto b = __builtin_amdgcn_permlane32_swap(__float_as_uint(v), __float_as_uint(v), false, false);
  return __builtin_fmaxf(__uint_as_float(b[0]), __uint_as_float(b[1]));
}
DI void glds16(const void* g, unsigned lds_base) {
  unsigned sv;
  asm volatile("s_mov_b32 %0, m0\n\ts_mov_b32 m0, %2\n\ts_nop 0\n\tglobal_load_lds_dwordx4 %1, off\n\ts_mov_b32 m0, %0"
               : "=&s"(sv) : "v"(g), "s"(lds_base) : "memory");
}
DI void glds4(const void* g, unsigned lds_base) {
  unsigned sv;
  asm volatile("s_mov_b32 %0, m0\n\ts_mov_b32 m0, %2\n\ts_nop 0\n\tglobal_load_lds_dword %1, off\n\ts_mov_b32 m0, %0"
               : "=&s"(sv) : "v"(g), "s"(lds_base) : "memory");
}
DI s16x4 trread(lds_cptr p) {
  return __builtin_bit_cast(s16x4, __builtin_amdgcn_ds_read_tr16_b64_v4i16((__attribute__((address_space(3))) v4i16_t*)p));
}
DI bf16x8 ldsr16(lds_cptr p) { return *(const __attribute__((address_space(3))) bf16x8*)p; }
DI bf16x8 cat8(s16x4 a, s16x4 b) { return __builtin_shufflevector(a, b, 0, 1, 2, 3, 4, 5, 6, 7); }
DI int xcd_remap(int bid, int nwg) {
  const int q = nwg >> 3, r = nwg & 7, x = bid & 7, o = bid >> 3;
  return (x < r ? x * (q + 1) : r * (q + 1) + (x - r) * q) + o;
}

constexpr int K1_WI = 448 * 128, K1_X = 2 * K1_WI, K1_XS = 528, K1_LDS = K1_X + 64 * K1_XS;
__global__ __launch_bounds__(512) void k_qkv(const u16* __restrict__ x, const u16* __restrict__ wq, const u16* __restrict__ wk,
                                             const u16* __restrict__ wv, const u16* __restrict__ wo, u16* __restrict__ Qf,
                                             u16* __restrict__ Kf, u16* __restrict__ Vf, u16* __restrict__ Wof) {
  __shared__ __attribute__((aligned(16))) char lds[K1_LDS];
  const int tid = threadIdx.x, lane = tid & 63, w = __builtin_amdgcn_readfirstlane(tid >> 6);
  const int t = xcd_remap(blockIdx.x, 256), rt = t >> 2, ct = t & 3;
  const unsigned lds0 = (unsigned)(uintptr_t)lds;
  STAMP(0, 0);
  u32x4 xv[4];
  const bool act = lane < 56;
  {
    const int lc = act ? lane : 55;
#pragma unroll
    for (int r = 0; r < 4; ++r) xv[r] = *(const u32x4*)(x + (size_t)(rt * 32 + w * 4 + r) * D + lc * 8);
  }
#pragma unroll
  for (int im = 0; im < 2; ++im) {
    const int c64 = 2 * ct + im;
    if (c64 < 7) {
#pragma unroll
      for (int i = 0; i < 7; ++i) {
        const int piece = w * 7 + i, row = piece * 8 + (lane >> 3);
        const int f = ((row >> 1) & 1) | (((row >> 3) & 1) << 1);
        const int c = (lane & 7) ^ (f << 1);
        glds16(wq + (size_t)row * D + c64 * 64 + c * 8, lds0 + im * K1_WI + piece * 1024);
      }
    } else {
#pragma unroll
      for (int i = 0; i < 28; ++i) {
        const int piece = w * 28 + i, row = piece * 2 + (lane >> 5), pp = lane & 31;
        const int f = ((row >> 1) & 1) | (((row >> 3) & 1) << 1);
        const int col = ((((pp >> 2) ^ (f << 1)) << 2) + (pp & 3)) * 2;
        const u16* src = col < 28 ? wk + row * DH + col : wv + row * DH + (col < 56 ? col - 28 : 0);
        glds4(src, lds0 + im * K1_WI + piece * 256);
      }
    }
  }
  u16 wt[8];
  const int frag0 = blockIdx.x + 256 * (w & 1);
  const bool dofrag = w < 2 && frag0 < 392;
  const int frag = dofrag ? frag0 : 0;
  {
    const int nb = frag / 14, ks = frag - nb * 14;
#pragma unroll
    for (int j = 0; j < 8; ++j) wt[j] = wo[(size_t)(32 * ks + 8 * (lane >> 4) + j) * D + 16 * nb + (lane & 15)];
  }
  SBAR();
  {
    float v[4][8], mean[4], rs[4];
#pragma unroll
    for (int r = 0; r < 4; ++r) {
      float s = 0.f;
#pragma unroll
      for (int i = 0; i < 4; ++i) {
        v[r][2 * i] = act ? bflo(xv[r][i]) : 0.f; v[r][2 * i + 1] = act ? bfhi(xv[r][i]) : 0.f;
        s += v[r][2 * i] + v[r][2 * i + 1];
      }
      mean[r] = s;
    }
#pragma unroll
    for (int r = 0; r < 4; ++r) mean[r] = wave_sum(mean[r]) * (1.f / D);
#pragma unroll
    for (int r = 0; r < 4; ++r) {
      float q = 0.f;
#pragma unroll
      for (int i = 0; i < 8; ++i) { v[r][i] = act ? v[r][i] - mean[r] : 0.f; q += v[r][i] * v[r][i]; }
      rs[r] = q;
    }
#pragma unroll
    for (int r = 0; r < 4; ++r) rs[r] = __builtin_amdgcn_rsqf(wave_sum(rs[r]) * (1.f / D) + LN_EPS);
    char* xdst = lds + K1_X + lane * K1_XS + w * 64;
#pragma unroll
    for (int r = 0; r < 4; ++r) {
      u32x4 o;
#pragma unroll
      for (int i = 0; i < 4; ++i) o[i] = cvtpk(v[r][2 * i] * rs[r], v[r][2 * i + 1] * rs[r]);
      *(u32x4*)(xdst + r * 16) = o;
    }
  }
  if (dofrag) {
    u32x4 o;
#pragma unroll
    for (int i = 0; i < 4; ++i) o[i] = (unsigned)wt[2 * i] | ((unsigned)wt[2 * i + 1] << 16);
    *(u32x4*)(Wof + ((size_t)frag * 64 + lane) * 8) = o;
  }
  STAMP(0, 1);
  WAITV_BAR(0);
  STAMP(0, 2);
  const int nb = w & 3, im = w >> 2, c64 = 2 * ct + im, g = lane >> 4, q4 = (lane & 15) >> 2, p = lane & 3, l15 = lane & 15;
  const int f = ((q4 >> 1) & 1) | ((g & 1) << 1);
  const lds_cptr wp = (lds_cptr)lds + im * K1_WI + (8 * g + q4) * 128 + (((2 * nb + (p >> 1)) ^ (f << 1)) * 16) + 8 * (p & 1);
  const lds_cptr xp = (lds_cptr)lds + K1_X + g * K1_XS + l15 * 16;
  f32x4 acc0 = {0.f, 0.f, 0.f, 0.f}, acc1 = {0.f, 0.f, 0.f, 0.f};
  {
    bf16x8 a[14], b0[14], b1[14];
#pragma unroll
    for (int ks = 0; ks < 14; ++ks) {
      a[ks] = cat8(trread(wp + ks * 4096), trread(wp + ks * 4096 + 512));
      b0[ks] = ldsr16(xp + ks * 4 * K1_XS);
      b1[ks] = ldsr16(xp + ks * 4 * K1_XS + 256);
    }
    SBAR();
#pragma unroll
    for (int ks = 0; ks < 14; ++ks) {
      acc0 = MFMA16(a[ks], b0[ks], acc0);
      acc1 = MFMA16(a[ks], b1[ks], acc1);
    }
  }
  STAMP(0, 3);
#pragma unroll
  for (int tb = 0; tb < 2; ++tb) {
    const f32x4 a = tb ? acc1 : acc0;
    const int s = rt * 32 + 16 * tb + l15;
    const int nl = 16 * nb + 4 * g;
    if (c64 < 7) {
      const int n0 = 64 * c64 + nl, h = n0 / 28, d0 = n0 - 28 * h;
      const int tg = s >> 3, c = (h & 3) * 8 + (s & 7), b = h >> 2;
      const size_t off = ((((size_t)(tg * 4 + b) * 2 + (d0 >> 4)) * 64) + ((d0 >> 3) & 1) * 32 + c) * 8 + (d0 & 7);
      *(u32x2*)(Qf + off) = (u32x2){cvtpk(a[0] * QSCALE, a[1] * QSCALE), cvtpk(a[2] * QSCALE, a[3] * QSCALE)};
    } else {
      const int kt = s >> 5, kk = s & 31;
      if (nl < 28 || nl == 56) {
        const int d0 = nl < 28 ? nl : 28;
        const size_t off = ((((size_t)kt * 2 + (d0 >> 4)) * 64) + ((d0 >> 3) & 1) * 32 + kk) * 8 + (d0 & 7);
        u32x2 v = {cvtpk(a[0], a[1]), cvtpk(a[2], a[3])};
        if (nl == 56) v = (u32x2){0x3f803f80u, 0x00003f80u};
        *(u32x2*)(Kf + off) = v;
      } else {
        const int d0 = nl < 56 ? nl - 28 : 28;
        const int sp = kk >> 4, j = ((kk >> 3) & 1) * 4 + (kk & 3), hi = (kk >> 2) & 1;
        const size_t off = ((((size_t)kt * 2 + sp) * 64) + hi * 32 + d0) * 8 + j;
        unsigned v0 = cvtpk(a[0], a[1]), v1 = cvtpk(a[2], a[3]);
        if (nl == 60) { v0 = 0x00003f80u; v1 = 0u; }
        Vf[off] = (u16)v0; Vf[off + 8] = (u16)(v0 >> 16); Vf[off + 16] = (u16)v1; Vf[off + 24] = (u16)(v1 >> 16);
      }
    }
  }
  STAMP(0, 4);
}

constexpr int K2_P = 0, K2_M = 131072, K2_FL = K2_M + 4096, K2_OT = K2_FL + 64, K2_DUM = K2_OT + 7168, K2_LDS = K2_DUM + 4096;
constexpr int K2_HB = 0, K2_HS = 32768;

DI float set_ref(bf16x8& q1, float m, int hi) {
  const float a = bflo(cvtpk(-m, 0.f)), r1 = -m - a, b = bflo(cvtpk(r1, 0.f)), c = bflo(cvtpk(r1 - b, 0.f));
  u32x4 v = __builtin_bit_cast(u32x4, q1);
  v[2] = hi ? cvtpk(a, b) : v[2];
  v[3] = hi ? cvtpk(c, 0.f) : v[3];
  q1 = __builtin_bit_cast(bf16x8, v);
  return -((a + b) + c);
}
DI float tile_max(const f32x16& s) {
  float a = __builtin_fmaxf(__builtin_fmaxf(s[0], s[1]), s[2]), b = __builtin_fmaxf(__builtin_fmaxf(s[3], s[4]), s[5]);
  a = __builtin_fmaxf(__builtin_fmaxf(a, s[6]), s[7]); b = __builtin_fmaxf(__builtin_fmaxf(b, s[8]), s[9]);
  a = __builtin_fmaxf(__builtin_fmaxf(a, s[10]), s[11]); b = __builtin_fmaxf(__builtin_fmaxf(b, s[12]), s[13]);
  a = __builtin_fmaxf(__builtin_fmaxf(a, s[14]), s[15]);
  return half_max(__builtin_fmaxf(a, b));
}

DI void attn_safe(const u16* __restrict__ Kw, const u16* __restrict__ Vw, bf16x8 (&qf)[4][2], f32x16 (&o)[4], float (&mref)[4], int lane, int hi) {
  const f32x16 zero = {};
#pragma unroll 1
  for (int t = 0; t < 8; ++t) {
    const bf16x8 k0 = *(const bf16x8*)(Kw + t * 1024 + lane * 8), k1 = *(const bf16x8*)(Kw + t * 1024 + 512 + lane * 8);
    const bf16x8 v0 = *(const bf16x8*)(Vw + t * 1024 + lane * 8), v1 = *(const bf16x8*)(Vw + t * 1024 + 512 + lane * 8);
#pragma unroll
    for (int b = 0; b < 4; ++b) {
      (void)set_ref(qf[b][1], 0.f, hi);
      f32x16 s = MFMA32(k0, qf[b][0], zero);
      s = MFMA32(k1, qf[b][1], s);
      const float tm = tile_max(s);
      const float mn = set_ref(qf[b][1], t == 0 ? tm : __builtin_fmaxf(tm, mref[b]), hi);
      if (t == 0) {
        o[b] = zero;
      } else {
        const float al = __builtin_amdgcn_exp2f(mref[b] - mn);
#pragma unroll
        for (int r = 0; r < 16; ++r) o[b][r] *= al;
      }
      mref[b] = mn;
      s = MFMA32(k0, qf[b][0], zero);
      s = MFMA32(k1, qf[b][1], s);
      u32x4 p0, p1;
#pragma unroll
      for (int i = 0; i < 4; ++i) {
        p0[i] = cvtpk(__builtin_amdgcn_exp2f(s[2 * i]), __builtin_amdgcn_exp2f(s[2 * i + 1]));
        p1[i] = cvtpk(__builtin_amdgcn_exp2f(s[8 + 2 * i]), __builtin_amdgcn_exp2f(s[8 + 2 * i + 1]));
      }
      o[b] = MFMA32(v0, __builtin_bit_cast(bf16x8, p0), o[b]);
      o[b] = MFMA32(v1, __builtin_bit_cast(bf16x8, p1), o[b]);
    }
  }
}

#define EX4(S, i) do { S[i] = __builtin_amdgcn_exp2f(S[i]); S[i + 1] = __builtin_amdgcn_exp2f(S[i + 1]); \
    S[i + 2] = __builtin_amdgcn_exp2f(S[i + 2]); S[i + 3] = __builtin_amdgcn_exp2f(S[i + 3]); } while (0)
#define PK8(P, S, i) do { P[0] = cvtpk(S[i], S[i + 1]); P[1] = cvtpk(S[i + 2], S[i + 3]); P[2] = cvtpk(S[i + 4], S[i + 5]); P[3] = cvtpk(S[i + 6], S[i + 7]); } while (0)
#define BF(P) __builtin_bit_cast(bf16x8, P)
#define ASTEP(SC, SN, KN0, KN1, BN, VP1, BP, VC0, BC) do { \
    SN = MFMA32(KN0, qf[BN][0], zero); SBAR(); EX4(SC, 0); SBAR(); \
    SN = MFMA32(KN1, qf[BN][1], SN); SBAR(); EX4(SC, 4); PK8(p0, SC, 0); SBAR(); \
    o[BP] = MFMA32(VP1, BF(p1), o[BP]); SBAR(); EX4(SC, 8); SBAR(); \
    o[BC] = MFMA32(VC0, BF(p0), o[BC]); SBAR(); EX4(SC, 12); PK8(p1, SC, 8); SBAR(); } while (0)
#define LDK(K0, K1, P, t) do { K0 = *(const bf16x8*)(P + (t) * 1024 + lane * 8); K1 = *(const bf16x8*)(P + (t) * 1024 + 512 + lane * 8); } while (0)
#define ATILE(KC0, KC1, VC0, VC1, KN0, KN1, VP0, VP1, tn) do { \
    LDK(KN0, KN1, Kw, tn); SBAR(); \
    ASTEP(sA, sB, KC0, KC1, 1, VP1, 3, VC0, 0); \
    LDK(VP0, VP1, Vw, tn); SBAR(); \
    ASTEP(sB, sA, KC0, KC1, 2, VC1, 0, VC0, 1); \
    ASTEP(sA, sB, KC0, KC1, 3, VC1, 1, VC0, 2); \
    ASTEP(sB, sA, KN0, KN1, 0, VC1, 2, VC0, 3); } while (0)
DI void attn_fast(const u16* __restrict__ Kf, const u16* __restrict__ Kw, const u16* __restrict__ Vw, bf16x8 (&qf)[4][2], f32x16 (&o)[4],
                  float (&mref)[4], int lane, int hi) {
  const f32x16 zero = {};
#pragma unroll
  for (int b = 0; b < 4; ++b) o[b] = zero;
  bf16x8 ka0, ka1, kb0, kb1, va0, va1, vb0, vb1;
  LDK(kb0, kb1, Kf, 0);
  LDK(ka0, ka1, Kw, 0);
  LDK(va0, va1, Vw, 0);
  vb0 = va0; vb1 = va1;
#pragma unroll
  for (int b = 0; b < 4; ++b) {
    (void)set_ref(qf[b][1], 0.f, hi);
    f32x16 s = MFMA32(kb0, qf[b][0], zero);
    s = MFMA32(kb1, qf[b][1], s);
    mref[b] = set_ref(qf[b][1], tile_max(s), hi);
  }
  f32x16 sA, sB;
  u32x4 p0, p1 = {0u, 0u, 0u, 0u};
  sA = MFMA32(ka0, qf[0][0], zero);
  sA = MFMA32(ka1, qf[0][1], sA);
  SBAR();
  STAMP(1, 8);
#pragma unroll 1
  for (int t = 0; t < 8; t += 2) {
    ATILE(ka0, ka1, va0, va1, kb0, kb1, vb0, vb1, t + 1);
    const int tn = t + 2 < 8 ? t + 2 : 7;
    ATILE(kb0, kb1, vb0, vb1, ka0, ka1, va0, va1, tn);
  }
  o[3] = MFMA32(vb1, BF(p1), o[3]);
  SBAR();
  STAMP(1, 10);
}

constexpr int WPRE = 2;
__global__ __launch_bounds__(512) void k_attn(const u16* __restrict__ x, const u16* __restrict__ Qf, const u16* __restrict__ Kf,
                                              const u16* __restrict__ Vf, const u16* __restrict__ Wof, const u16* __restrict__ w1,
                                              const u16* __restrict__ w2, u16* __restrict__ H, u16* __restrict__ HN) {
  __shared__ __attribute__((aligned(16))) char lds[K2_LDS];
  const int tid = threadIdx.x, lane = tid & 63, w = __builtin_amdgcn_readfirstlane(tid >> 6), hi = lane >> 5;
  const int tg = blockIdx.x;
  STAMP(1, 0);
  if (w < 4) __builtin_amdgcn_s_setprio(1);
  bf16x8 qf[4][2];
#pragma unroll
  for (int b = 0; b < 4; ++b)
#pragma unroll
    for (int k = 0; k < 2; ++k) qf[b][k] = *(const bf16x8*)(Qf + ((size_t)((tg * 4 + b) * 2 + k) * 64 + lane) * 8);
  f32x16 o[4];
  float mref[4];
  const u16* Kw = Kf + (size_t)w * 8 * 1024;
  const u16* Vw = Vf + (size_t)w * 8 * 1024;
  const int kh = w >> 2, ng = w & 3;
  const u16* wop = Wof + ((size_t)(7 * ng * 14 + 7 * kh) * 64 + lane) * 8;
  const u16* const wob = Wof + (size_t)(7 * ng * 14 + 7 * kh) * 512;
  bf16x8 wf[7][7];
#define LDWO(n0, n1) do { _Pragma("unroll") for (int n_ = n0; n_ < n1; ++n_) _Pragma("unroll") for (int k_ = 0; k_ < 7; ++k_) \
    wf[n_][k_] = *(const bf16x8*)(wop + (n_ * 14 + k_) * 512); } while (0)
  LDWO(0, WPRE);
  attn_fast(Kf, Kw, Vw, qf, o, mref, lane, hi);
  {
    int l2 = lane;
    asm volatile("" : "+v"(l2));
    wop = wob + l2 * 8;
  }
  bool slow;
  {
    float chk = 0.f;
    bool bad = false;
#pragma unroll
    for (int b = 0; b < 4; ++b) {
#pragma unroll
      for (int r = 0; r < 16; ++r) chk = __builtin_fmaf(o[b][r], 0.f, chk);
      bad |= !(o[b][12] < 1.8e19f);
    }
    bad |= !(chk == 0.f);
    slow = __any(bad);
    if (slow) {
      attn_safe(Kw, Vw, qf, o, mref, lane, hi);
      LDWO(0, WPRE);
    }
  }
  __builtin_amdgcn_s_setprio(0);
  STAMP(1, 1);
#pragma unroll
  for (int b = 0; b < 4; ++b) {
#pragma unroll
    for (int r4 = 0; r4 < 4; ++r4)
      *(f32x4*)(lds + K2_P + (((w * 4 + b) * 4 + r4) * 64 + lane) * 16) = (f32x4){o[b][4 * r4], o[b][4 * r4 + 1], o[b][4 * r4 + 2], o[b][4 * r4 + 3]};
    if (hi == 0) *(float*)(lds + K2_M + (w * 128 + b * 32 + lane) * 4) = mref[b];
  }
  if (lane == 0) *(unsigned*)(lds + K2_FL + w * 4) = slow ? 1u : 0u;
  const u32x4 xv = *(const u32x4*)(x + (size_t)(tg * 8 + w) * D + (lane < 56 ? lane : 55) * 8);
  SBAR();
  if (w < 4) {
    LDWO(WPRE, 5);
    SBAR();
    const unsigned dum = (unsigned)(uintptr_t)lds + K2_DUM + w * 1024;
    const int xg = tg & 7, rk = tg >> 3;
    const int ts = xg < 4 ? 25 * xg : 100 + 24 * (xg - 4), te = ts + (xg < 4 ? 25 : 24);
    const int n0 = ts / 7, n1 = (te - 1) / 7;
    const u16* p1 = w1 + (size_t)(64 * rk + 16 * w + (lane >> 3)) * F + (lane & 7) * 8;
    for (int n = n0; n <= n1; ++n) { glds16(p1 + n * 64, dum); glds16(p1 + n * 64 + 8 * F, dum); }
    const u16* p2 = w2 + (size_t)(56 * rk + (lane >> 3)) * S + (4 * xg + w) * 64 + (lane & 7) * 8;
#pragma unroll
    for (int i = 0; i < 7; ++i) glds16(p2 + (size_t)i * 8 * S, dum);
  }
  STAMP(1, 5);
  LBAR();
  STAMP(1, 6);
  if (w < 4) {
    const u32x4 f0 = *(const u32x4*)(lds + K2_FL), f1 = *(const u32x4*)(lds + K2_FL + 16);
    const bool gen = __builtin_amdgcn_readfirstlane(f0[0] | f0[1] | f0[2] | f0[3] | f1[0] | f1[1] | f1[2] | f1[3]) != 0;
    const int c = lane & 31, r4 = w;
#pragma unroll
    for (int b = 0; b < 4; ++b) {
      float l = 0.f;
      f32x4 a = {0.f, 0.f, 0.f, 0.f};
      if (!gen) {
#pragma unroll
        for (int v = 0; v < 8; ++v) {
          l += *(const float*)(lds + K2_P + (((v * 4 + b) * 4 + 3) * 64 + 32 + c) * 16);
          a += *(const f32x4*)(lds + K2_P + (((v * 4 + b) * 4 + r4) * 64 + lane) * 16);
        }
      } else {
        float m[8], M = -INFINITY;
#pragma unroll
        for (int v = 0; v < 8; ++v) { m[v] = *(const float*)(lds + K2_M + (v * 128 + 32 * b + c) * 4); M = __builtin_fmaxf(M, m[v]); }
#pragma unroll
        for (int v = 0; v < 8; ++v) {
          const float fc = __builtin_amdgcn_exp2f(m[v] - M);
          l += fc * *(const float*)(lds + K2_P + (((v * 4 + b) * 4 + 3) * 64 + 32 + c) * 16);
          a += fc * *(const f32x4*)(lds + K2_P + (((v * 4 + b) * 4 + r4) * 64 + lane) * 16);
        }
      }
      const float il = 1.f / l;
      const int d = 8 * r4 + 4 * hi;
      if (d < 28) {
        const int k = 28 * (4 * b + (c >> 3)) + d;
        *(u32x2*)(lds + K2_OT + ((k >> 3) * 8 + (c & 7)) * 16 + (k & 7) * 2) = (u32x2){cvtpk(a[0] * il, a[1] * il), cvtpk(a[2] * il, a[3] * il)};
      }
      SBAR();
    }
  } else {
    LDWO(WPRE, 5);
  }
  SBAR();
  LDWO(5, 7);
  SBAR();
  LBAR();
  STAMP(1, 2);
  {
    const int g = lane >> 4;
    bf16x8 bo[7];
#pragma unroll
    for (int k = 0; k < 7; ++k) bo[k] = *(const bf16x8*)(lds + K2_OT + ((4 * (7 * kh + k) + g) * 8 + (lane & 7)) * 16);
    f32x4 acc[7];
#pragma unroll
    for (int n = 0; n < 7; ++n) {
      acc[n] = (f32x4){0.f, 0.f, 0.f, 0.f};
#pragma unroll
      for (int k = 0; k < 7; ++k) acc[n] = MFMA16(wf[n][k], bo[k], acc[n]);
    }
    if ((lane & 15) < 8) {
#pragma unroll
      for (int n = 0; n < 7; ++n) *(f32x4*)(lds + K2_HB + ((kh * 8 + (lane & 15)) * D + 16 * (7 * ng + n) + 4 * g) * 4) = acc[n];
    }
  }
  LBAR();
  STAMP(1, 3);
  {
    const int s = tg * 8 + w;
    const bool act = lane < 56;
    const int lc = act ? lane : 55;
    float v[8];
    const f32x4 a0 = *(const f32x4*)(lds + K2_HB + (w * D + lc * 8) * 4), a1 = *(const f32x4*)(lds + K2_HB + (w * D + lc * 8 + 4) * 4);
    const f32x4 c0 = *(const f32x4*)(lds + K2_HB + ((8 + w) * D + lc * 8) * 4), c1 = *(const f32x4*)(lds + K2_HB + ((8 + w) * D + lc * 8 + 4) * 4);
    u32x4 hv;
#pragma unroll
    for (int i = 0; i < 2; ++i) {
      hv[i] = cvtpk(bflo(xv[i]) + a0[2 * i] + c0[2 * i], bfhi(xv[i]) + a0[2 * i + 1] + c0[2 * i + 1]);
      hv[2 + i] = cvtpk(bflo(xv[2 + i]) + a1[2 * i] + c1[2 * i], bfhi(xv[2 + i]) + a1[2 * i + 1] + c1[2 * i + 1]);
    }
    if (act) *(u32x4*)(H + (size_t)s * D + lane * 8) = hv;
#pragma unroll
    for (int i = 0; i < 4; ++i) { v[2 * i] = act ? bflo(hv[i]) : 0.f; v[2 * i + 1] = act ? bfhi(hv[i]) : 0.f; }
#pragma unroll
    for (int pass = 0; pass < 2; ++pass) {
      float sm = 0.f;
#pragma unroll
      for (int i = 0; i < 8; ++i) sm += v[i];
      const float mean = wave_sum(sm) * (1.f / D);
      float q = 0.f;
#pragma unroll
      for (int i = 0; i < 8; ++i) { v[i] = act ? v[i] - mean : 0.f; q += v[i] * v[i]; }
      const float rs = __builtin_amdgcn_rsqf(wave_sum(q) * (1.f / D) + LN_EPS);
#pragma unroll
      for (int i = 0; i < 8; ++i) v[i] *= rs;
    }
    if (act) *(u32x4*)(lds + K2_HS + w * 912 + lane * 16) = (u32x4){cvtpk(v[0], v[1]), cvtpk(v[2], v[3]), cvtpk(v[4], v[5]), cvtpk(v[6], v[7])};
  }
  LBAR();
  if (tid < D / 2) {
    unsigned r[8];
#pragma unroll
    for (int i = 0; i < 8; ++i) r[i] = *(const unsigned*)(lds + K2_HS + i * 912 + tid * 4);
    u32x4 lo, hi2;
#pragma unroll
    for (int i = 0; i < 4; ++i) {
      lo[i] = (r[2 * i] & 0xffffu) | (r[2 * i + 1] << 16);
      hi2[i] = (r[2 * i] >> 16) | (r[2 * i + 1] & 0xffff0000u);
    }
    u32x4* dst = (u32x4*)(HN + ((size_t)tg * D + 2 * tid) * 8);
    dst[0] = lo; dst[1] = hi2;
  }
  asm volatile("s_waitcnt vmcnt(0)" ::: "memory");
  STAMP(1, 4);
}

constexpr int G_RING = 4, G_LDS = 8 * G_RING * 4096;
template <int N> DI void waitv4(bf16x8& a, bf16x8& b, bf16x8& c, bf16x8& d) {
  asm volatile("s_waitcnt vmcnt(%4)" : "+v"(a), "+v"(b), "+v"(c), "+v"(d) : "i"(N) : "memory");
}
template <int OFF> DI void gload16(bf16x8& d, const void* p) {
  asm volatile("global_load_dwordx4 %0, %1, off offset:%2" : "=v"(d) : "v"(p), "i"(OFF) : "memory");
}
template <int PH>
__global__ __launch_bounds__(512) void k_mlp(const u16* __restrict__ act, const u16* __restrict__ W, const u16* __restrict__ bias,
                                             const u16* __restrict__ H, u16* __restrict__ out) {
  constexpr int KT = PH == 0 ? S : F, LDW = PH == 0 ? F : S, NK = KT / 256, NWG = 7 * (LDW / 64);
  __shared__ __attribute__((aligned(16))) char lds[G_LDS];
  const int tid = threadIdx.x, lane = tid & 63, w = __builtin_amdgcn_readfirstlane(tid >> 6);
  const int t = xcd_remap(blockIdx.x, NWG), nt = t / 7, mt = t - nt * 7;
  const unsigned lds0 = (unsigned)(uintptr_t)lds + w * (G_RING * 4096);
  const int g = lane >> 4, q4 = (lane & 15) >> 2, p = lane & 3, l15 = lane & 15;
  STAMP(2 + PH, 0);
  const u16* asrc = act + ((size_t)(4 * w + g) * D + 64 * mt + l15) * 8;
  const int r8 = lane >> 3, fe = (r8 >> 1) & 1;
  const u16* wsrc0 = W + (size_t)(32 * w + r8) * LDW + 64 * nt + ((lane & 7) ^ (fe << 1)) * 8;
  const u16* wsrc1 = W + (size_t)(32 * w + 8 + r8) * LDW + 64 * nt + ((lane & 7) ^ ((fe | 2) << 1)) * 8;
  bf16x8 af[NK][4];
#define STAGE(i) do { const unsigned sl_ = lds0 + ((i) % G_RING) * 4096; const size_t o_ = (size_t)(i) * 256 * LDW; \
    glds16(wsrc0 + o_, sl_); glds16(wsrc1 + o_, sl_ + 1024); glds16(wsrc0 + o_ + 16 * LDW, sl_ + 2048); glds16(wsrc1 + o_ + 16 * LDW, sl_ + 3072); \
    const u16* ap_ = asrc + (size_t)(i) * 32 * D * 8; \
    gload16<0>(af[i][0], ap_); gload16<256>(af[i][1], ap_); gload16<512>(af[i][2], ap_); gload16<768>(af[i][3], ap_); } while (0)
#pragma unroll
  for (int i = 0; i < G_RING; ++i) STAGE(i);
  STAMP(2 + PH, 1);
  const int f = ((q4 >> 1) & 1) | ((g & 1) << 1);
  int woff[4];
#pragma unroll
  for (int nb = 0; nb < 4; ++nb) woff[nb] = w * (G_RING * 4096) + (8 * g + q4) * 128 + (((2 * nb + (p >> 1)) ^ (f << 1)) * 16) + 8 * (p & 1);
  f32x4 acc[4][4] = {};
#pragma unroll
  for (int i = 0; i < NK; ++i) {
    const int rem = NK - 1 - i < G_RING - 1 ? NK - 1 - i : G_RING - 1;
    if (rem == 3) waitv4<24>(af[i][0], af[i][1], af[i][2], af[i][3]);
    else if (rem == 2) waitv4<16>(af[i][0], af[i][1], af[i][2], af[i][3]);
    else if (rem == 1) waitv4<8>(af[i][0], af[i][1], af[i][2], af[i][3]);
    else waitv4<0>(af[i][0], af[i][1], af[i][2], af[i][3]);
    bf16x8 wf[4];
#pragma unroll
    for (int nb = 0; nb < 4; ++nb) {
      const lds_cptr sp = (lds_cptr)lds + woff[nb] + (i % G_RING) * 4096;
      wf[nb] = cat8(trread(sp), trread(sp + 512));
    }
    if (i + G_RING < NK) {
      asm volatile("s_waitcnt lgkmcnt(0)" ::: "memory");
      STAGE(i + G_RING);
    }
#pragma unroll
    for (int nb = 0; nb < 4; ++nb)
#pragma unroll
      for (int mb = 0; mb < 4; ++mb)
        acc[nb][mb] = PH == 0 ? MFMA16(wf[nb], af[i][mb], acc[nb][mb]) : MFMA16(af[i][mb], wf[nb], acc[nb][mb]);
  }
#undef STAGE
  STAMP(2 + PH, 4);
  const int enb = w >> 1, emb = 2 * (w & 1);
  u32x2 eb, eh0, eh1;
  if (PH == 0) {
    eb = *(const u32x2*)(bias + 64 * nt + 16 * enb + 4 * g);
  } else {
    const int s = 64 * nt + 16 * enb + l15;
    eb[0] = bias[s];
    eh0 = *(const u32x2*)(H + (size_t)s * D + 64 * mt + 16 * emb + 4 * g);
    eh1 = *(const u32x2*)(H + (size_t)s * D + 64 * mt + 16 * emb + 16 + 4 * g);
  }
  {
    f32x4* xb = (f32x4*)lds;
#pragma unroll
    for (int nb = 0; nb < 4; ++nb)
#pragma unroll
      for (int mb = 0; mb < 4; ++mb) xb[(w * 16 + nb * 4 + mb) * 64 + lane] = acc[nb][mb];
  }
  LBAR();
  {
    const f32x4* xb = (const f32x4*)lds;
#pragma unroll
    for (int e = 0; e < 2; ++e) {
      f32x4 a = xb[(2 * w + e) * 64 + lane];
#pragma unroll
      for (int v = 1; v < 8; ++v) a += xb[(v * 16 + 2 * w + e) * 64 + lane];
      const int mb = emb + e;
      if (PH == 0) {
        const int fi = 64 * nt + 16 * enb + 4 * g, d = 64 * mt + 16 * mb + l15;
        float z[4] = {a[0] + bflo(eb[0]), a[1] + bfhi(eb[0]), a[2] + bflo(eb[1]), a[3] + bfhi(eb[1])};
#pragma unroll
        for (int i = 0; i < 4; ++i) {
          const float zz = z[i], a2 = zz * (1.f + 0.044715f * zz * zz) * (-2.f * 0.7978845608028654f * 1.4426950408889634f);
          z[i] = zz * __builtin_amdgcn_rcpf(1.f + __builtin_amdgcn_exp2f(a2));
        }
        *(u32x2*)(out + ((size_t)(fi >> 3) * D + d) * 8 + (fi & 7)) = (u32x2){cvtpk(z[0], z[1]), cvtpk(z[2], z[3])};
      } else {
        const int d = 64 * mt + 16 * mb + 4 * g, s = 64 * nt + 16 * enb + l15;
        const float bb = bflo(eb[0]);
        const u32x2 hv = e ? eh1 : eh0;
        *(u32x2*)(out + (size_t)s * D + d) =
            (u32x2){cvtpk(a[0] + bb + bflo(hv[0]), a[1] + bb + bfhi(hv[0])), cvtpk(a[2] + bb + bflo(hv[1]), a[3] + bb + bfhi(hv[1]))};
      }
    }
  }
  STAMP(2 + PH, 5);
}

extern "C" void kernel_launch(void* const* d_in, const int* in_sizes, int n_in, void* d_out, int out_size, void* d_ws, size_t ws_size,
                              hipStream_t stream) {
  const u16* x = (const u16*)d_in[0];
  const u16* wq = (const u16*)d_in[1];
  const u16* wk = (const u16*)d_in[2];
  const u16* wv = (const u16*)d_in[3];
  const u16* wo = (const u16*)d_in[4];
  const u16* w1 = (const u16*)d_in[6];
  const u16* b1 = (const u16*)d_in[7];
  const u16* w2 = (const u16*)d_in[8];
  const u16* b2 = (const u16*)d_in[9];
  char* ws = (char*)d_ws;
  u16 *Qf = (u16*)(ws + WS_QF), *Kf = (u16*)(ws + WS_KF), *Vf = (u16*)(ws + WS_VF), *Wof = (u16*)(ws + WS_WOF);
  u16 *H = (u16*)(ws + WS_H), *HN = (u16*)(ws + WS_HN), *U = (u16*)(ws + WS_U);
  k_qkv<<<256, 512, 0, stream>>>(x, wq, wk, wv, wo, Qf, Kf, Vf, Wof);
  k_attn<<<256, 512, 0, stream>>>(x, Qf, Kf, Vf, Wof, w1, w2, H, HN);
  k_mlp<0><<<196, 512, 0, stream>>>(HN, w1, b1, H, U);
  k_mlp<1><<<224, 512, 0, stream>>>(U, w2, b2, H, (u16*)d_out);
}
```

```cpp
#include <hip/hip_runtime.h>
#include <hip/hip_bf16.h>

typedef unsigned short u16;
using bf16x8 = __attribute__((ext_vector_type(8))) short;
using s16x4 = __attribute__((ext_vector_type(4))) short;
using f32x4 = __attribute__((ext_vector_type(4))) float;
using f32x16 = __attribute__((ext_vector_type(16))) float;
using u32x4 = __attribute__((ext_vector_type(4))) unsigned;
using u32x2 = __attribute__((ext_vector_type(2))) unsigned;
typedef short v4i16_t __attribute__((ext_vector_type(4)));
typedef __attribute__((address_space(3))) const char* lds_cptr;
typedef __bf16 bf16x2_t __attribute__((ext_vector_type(2)));
typedef float f32x2_t __attribute__((ext_vector_type(2)));

#define DI __device__ __forceinline__
#define MFMA32(a, b, c) __builtin_amdgcn_mfma_f32_32x32x16_bf16(a, b, c, 0, 0, 0)
#define MFMA16(a, b, c) __builtin_amdgcn_mfma_f32_16x16x32_bf16(a, b, c, 0, 0, 0)
#define SBAR() __builtin_amdgcn_sched_barrier(0)
#define LBAR() asm volatile("s_waitcnt lgkmcnt(0)\n\ts_barrier" ::: "memory")
#define WAITV_BAR(N) asm volatile("s_waitcnt vmcnt(" #N ") lgkmcnt(0)\n\ts_barrier" ::: "memory")

constexpr int S = 2048, D = 448, DH = 28, F = 1792;
constexpr float QSCALE = 0.18898223650461362f * 1.4426950408889634f;
constexpr float LN_EPS = 1e-5f;

constexpr size_t WS_QF = 0;
constexpr size_t WS_KF = WS_QF + 2097152;
constexpr size_t WS_VF = WS_KF + 131072;
constexpr size_t WS_WOF = WS_VF + 131072;
constexpr size_t WS_H = WS_WOF + 401408;
constexpr size_t WS_HN = WS_H + 1835008;
constexpr size_t WS_U = WS_HN + 1835008;

#define STAMP(k, i)
#define CSTAMP(k, i)

DI unsigned cvtpk(float lo, float hi) {
  const f32x2_t v = {lo, hi};
  return __builtin_bit_cast(unsigned, __builtin_convertvector(v, bf16x2_t));
}
DI float bflo(unsigned u) { return __uint_as_float(u << 16); }
DI float bfhi(unsigned u) { return __uint_as_float(u & 0xffff0000u); }
template <int CTRL> DI float dppf(float v) {
  return __uint_as_float(__builtin_amdgcn_update_dpp(0u, __float_as_uint(v), CTRL, 0xf, 0xf, true));
}
DI float wave_sum(float v) {
  v += dppf<0xB1>(v);
  v += dppf<0x4E>(v);
  v += dppf<0x141>(v);
  v += dppf<0x140>(v);
  auto a = __builtin_amdgcn_permlane16_swap(__float_as_uint(v), __float_as_uint(v), false, false);
  v = __uint_as_float(a[0]) + __uint_as_float(a[1]);
  auto b = __builtin_amdgcn_permlane32_swap(__float_as_uint(v), __float_as_uint(v), false, false);
  return __uint_as_float(b[0]) + __uint_as_float(b[1]);
}
DI float half_max(float v) {
  auto b = __builtin_amdgcn_permlane32_swap(__float_as_uint(v), __float_as_uint(v), false, false);
  return __builtin_fmaxf(__uint_as_float(b[0]), __uint_as_float(b[1]));
}
DI void glds16(const void* g, unsigned lds_base) {
  unsigned sv;
  asm volatile("s_mov_b32 %0, m0\n\ts_mov_b32 m0, %2\n\ts_nop 0\n\tglobal_load_lds_dwordx4 %1, off\n\ts_mov_b32 m0, %0"
               : "=&s"(sv) : "v"(g), "s"(lds_base) : "memory");
}
DI void glds4(const void* g, unsigned lds_base) {
  unsigned sv;
  asm volatile("s_mov_b32 %0, m0\n\ts_mov_b32 m0, %2\n\ts_nop 0\n\tglobal_load_lds_dword %1, off\n\ts_mov_b32 m0, %0"
               : "=&s"(sv) : "v"(g), "s"(lds_base) : "memory");
}
template <int N> DI void waitv4(bf16x8& a, bf16x8& b, bf16x8& c, bf16x8& d) {
  asm volatile("s_waitcnt vmcnt(%4)" : "+v"(a), "+v"(b), "+v"(c), "+v"(d) : "i"(N) : "memory");
}
template <int OFF> DI void gload16(bf16x8& d, const void* p) {
  asm volatile("global_load_dwordx4 %0, %1, off offset:%2" : "=v"(d) : "v"(p), "i"(OFF) : "memory");
}
DI s16x4 trread(lds_cptr p) {
  return __builtin_bit_cast(s16x4, __builtin_amdgcn_ds_read_tr16_b64_v4i16((__attribute__((address_space(3))) v4i16_t*)p));
}
DI bf16x8 ldsr16(lds_cptr p) { return *(const __attribute__((address_space(3))) bf16x8*)p; }
DI bf16x8 cat8(s16x4 a, s16x4 b) { return __builtin_shufflevector(a, b, 0, 1, 2, 3, 4, 5, 6, 7); }
DI int xcd_remap(int bid, int nwg) {
  const int q = nwg >> 3, r = nwg & 7, x = bid & 7, o = bid >> 3;
  return (x < r ? x * (q + 1) : r * (q + 1) + (x - r) * q) + o;
}

constexpr int K1_WI = 448 * 128, K1_X = 2 * K1_WI, K1_XS = 528, K1_DUM = K1_X + 64 * K1_XS, K1_LDS = K1_DUM + 8192;
__global__ __launch_bounds__(512) void k_qkv(const u16* __restrict__ x, const u16* __restrict__ wq, const u16* __restrict__ wk,
                                             const u16* __restrict__ wv, const u16* __restrict__ wo, const u16* __restrict__ w1,
                                             u16* __restrict__ Qf, u16* __restrict__ Kf, u16* __restrict__ Vf, u16* __restrict__ Wof) {
  __shared__ __attribute__((aligned(16))) char lds[K1_LDS];
  const int tid = threadIdx.x, lane = tid & 63, w = __builtin_amdgcn_readfirstlane(tid >> 6);
  const int t = xcd_remap(blockIdx.x, 256), rt = t >> 2, ct = t & 3;
  const unsigned lds0 = (unsigned)(uintptr_t)lds;
  STAMP(0, 0);
  bf16x8 xr[4];
  const bool act = lane < 56;
  {
    const u16* xp_ = x + (size_t)(rt * 32 + w * 4) * D + (act ? lane : 55) * 8;
    gload16<0>(xr[0], xp_); gload16<896>(xr[1], xp_); gload16<1792>(xr[2], xp_); gload16<2688>(xr[3], xp_);
  }
#pragma unroll
  for (int im = 0; im < 2; ++im) {
    const int c64 = 2 * ct + im;
    if (c64 < 7) {
#pragma unroll
      for (int i = 0; i < 7; ++i) {
        const int piece = w * 7 + i, row = piece * 8 + (lane >> 3);
        const int f = ((row >> 1) & 1) | (((row >> 3) & 1) << 1);
        const int c = (lane & 7) ^ (f << 1);
        glds16(wq + (size_t)row * D + c64 * 64 + c * 8, lds0 + im * K1_WI + piece * 1024);
      }
    } else {
#pragma unroll
      for (int i = 0; i < 28; ++i) {
        const int piece = w * 28 + i, row = piece * 2 + (lane >> 5), pp = lane & 31;
        const int f = ((row >> 1) & 1) | (((row >> 3) & 1) << 1);
        const int col = ((((pp >> 2) ^ (f << 1)) << 2) + (pp & 3)) * 2;
        const u16* src = col < 28 ? wk + row * DH + col : wv + row * DH + (col < 56 ? col - 28 : 0);
        glds4(src, lds0 + im * K1_WI + piece * 256);
      }
    }
  }
  u16 wt[8];
  const int frag0 = blockIdx.x + 256 * (w & 1);
  const bool dofrag = w < 2 && frag0 < 392;
  const int frag = dofrag ? frag0 : 0;
  {
    const int nb = frag / 14, ks = frag - nb * 14;
#pragma unroll
    for (int j = 0; j < 8; ++j) wt[j] = wo[(size_t)(32 * ks + 8 * (lane >> 4) + j) * D + 16 * nb + (lane & 15)];
  }
  SBAR();
  if (ct < 3) waitv4<22>(xr[0], xr[1], xr[2], xr[3]);
  else waitv4<43>(xr[0], xr[1], xr[2], xr[3]);
  {
    u32x4 xv[4];
#pragma unroll
    for (int r = 0; r < 4; ++r) xv[r] = __builtin_bit_cast(u32x4, xr[r]);
    float v[4][8], mean[4], rs[4];
#pragma unroll
    for (int r = 0; r < 4; ++r) {
      float s = 0.f;
#pragma unroll
      for (int i = 0; i < 4; ++i) {
        v[r][2 * i] = act ? bflo(xv[r][i]) : 0.f; v[r][2 * i + 1] = act ? bfhi(xv[r][i]) : 0.f;
        s += v[r][2 * i] + v[r][2 * i + 1];
      }
      mean[r] = s;
    }
#pragma unroll
    for (int r = 0; r < 4; ++r) mean[r] = wave_sum(mean[r]) * (1.f / D);
#pragma unroll
    for (int r = 0; r < 4; ++r) {
      float q = 0.f;
#pragma unroll
      for (int i = 0; i < 8; ++i) { v[r][i] = act ? v[r][i] - mean[r] : 0.f; q += v[r][i] * v[r][i]; }
      rs[r] = q;
    }
#pragma unroll
    for (int r = 0; r < 4; ++r) rs[r] = __builtin_amdgcn_rsqf(wave_sum(rs[r]) * (1.f / D) + LN_EPS);
    char* xdst = lds + K1_X + lane * K1_XS + w * 64;
#pragma unroll
    for (int r = 0; r < 4; ++r) {
      u32x4 o;
#pragma unroll
      for (int i = 0; i < 4; ++i) o[i] = cvtpk(v[r][2 * i] * rs[r], v[r][2 * i + 1] * rs[r]);
      *(u32x4*)(xdst + r * 16) = o;
    }
  }
  STAMP(0, 1);
  WAITV_BAR(0);
  STAMP(0, 2);
  {
    const unsigned dum = lds0 + K1_DUM + w * 1024;
    const int xg = blockIdx.x & 7, rk = blockIdx.x >> 3;
    const int ts = xg < 4 ? 25 * xg : 100 + 24 * (xg - 4), te = ts + (xg < 4 ? 25 : 24);
    const int n0 = ts / 7, n1 = (te - 1) / 7;
    const u16* p1 = w1 + (size_t)(64 * rk + 8 * w + (lane >> 3)) * F + (lane & 7) * 8;
#pragma unroll
    for (int i = 0; i < 5; ++i) glds16(p1 + (n0 + i <= n1 ? n0 + i : n1) * 64, dum);
  }
  const int nb = w & 3, im = w >> 2, c64 = 2 * ct + im, g = lane >> 4, q4 = (lane & 15) >> 2, p = lane & 3, l15 = lane & 15;
  const int f = ((q4 >> 1) & 1) | ((g & 1) << 1);
  const lds_cptr wp = (lds_cptr)lds + im * K1_WI + (8 * g + q4) * 128 + (((2 * nb + (p >> 1)) ^ (f << 1)) * 16) + 8 * (p & 1);
  const lds_cptr xp = (lds_cptr)lds + K1_X + g * K1_XS + l15 * 16;
  f32x4 acc0 = {0.f, 0.f, 0.f, 0.f}, acc1 = {0.f, 0.f, 0.f, 0.f};
  {
    bf16x8 a[14], b0[14], b1[14];
#pragma unroll
    for (int ks = 0; ks < 14; ++ks) {
      a[ks] = cat8(trread(wp + ks * 4096), trread(wp + ks * 4096 + 512));
      b0[ks] = ldsr16(xp + ks * 4 * K1_XS);
      b1[ks] = ldsr16(xp + ks * 4 * K1_XS + 256);
    }
    SBAR();
#pragma unroll
    for (int ks = 0; ks < 14; ++ks) {
      acc0 = MFMA16(a[ks], b0[ks], acc0);
      acc1 = MFMA16(a[ks], b1[ks], acc1);
    }
  }
  STAMP(0, 3);
#pragma unroll
  for (int tb = 0; tb < 2; ++tb) {
    const f32x4 a = tb ? acc1 : acc0;
    const int s = rt * 32 + 16 * tb + l15;
    const int nl = 16 * nb + 4 * g;
    if (c64 < 7) {
      const int n0 = 64 * c64 + nl, h = n0 / 28, d0 = n0 - 28 * h;
      const int tg = s >> 3, c = (h & 3) * 8 + (s & 7), b = h >> 2;
      const size_t off = ((((size_t)(tg * 4 + b) * 2 + (d0 >> 4)) * 64) + ((d0 >> 3) & 1) * 32 + c) * 8 + (d0 & 7);
      *(u32x2*)(Qf + off) = (u32x2){cvtpk(a[0] * QSCALE, a[1] * QSCALE), cvtpk(a[2] * QSCALE, a[3] * QSCALE)};
    } else {
      const int kt = s >> 5, kk = s & 31;
      if (nl < 28 || nl == 56) {
        const int d0 = nl < 28 ? nl : 28;
        const size_t off = ((((size_t)kt * 2 + (d0 >> 4)) * 64) + ((d0 >> 3) & 1) * 32 + kk) * 8 + (d0 & 7);
        u32x2 v = {cvtpk(a[0], a[1]), cvtpk(a[2], a[3])};
        if (nl == 56) v = (u32x2){0x3f803f80u, 0x00003f80u};
        *(u32x2*)(Kf + off) = v;
      } else {
        const int d0 = nl < 56 ? nl - 28 : 28;
        const int sp = kk >> 4, j = ((kk >> 3) & 1) * 4 + (kk & 3), hi = (kk >> 2) & 1;
        const size_t off = ((((size_t)kt * 2 + sp) * 64) + hi * 32 + d0) * 8 + j;
        unsigned v0 = cvtpk(a[0], a[1]), v1 = cvtpk(a[2], a[3]);
        if (nl == 60) { v0 = 0x00003f80u; v1 = 0u; }
        Vf[off] = (u16)v0; Vf[off + 8] = (u16)(v0 >> 16); Vf[off + 16] = (u16)v1; Vf[off + 24] = (u16)(v1 >> 16);
      }
    }
  }
  if (dofrag) {
    u32x4 o;
#pragma unroll
    for (int i = 0; i < 4; ++i) o[i] = (unsigned)wt[2 * i] | ((unsigned)wt[2 * i + 1] << 16);
    *(u32x4*)(Wof + ((size_t)frag * 64 + lane) * 8) = o;
  }
  asm volatile("s_waitcnt vmcnt(0)" ::: "memory");
  STAMP(0, 4);
}

constexpr int K2_P = 0, K2_M = 131072, K2_FL = K2_M + 4096, K2_OT = K2_FL + 64, K2_LDS = K2_OT + 7168;
constexpr int K2_HB = 0, K2_HS = 32768;

DI float set_ref(bf16x8& q1, float m, int hi) {
  const float a = bflo(cvtpk(-m, 0.f)), r1 = -m - a, b = bflo(cvtpk(r1, 0.f)), c = bflo(cvtpk(r1 - b, 0.f));
  u32x4 v = __builtin_bit_cast(u32x4, q1);
  v[2] = hi ? cvtpk(a, b) : v[2];
  v[3] = hi ? cvtpk(c, 0.f) : v[3];
  q1 = __builtin_bit_cast(bf16x8, v);
  return -((a + b) + c);
}
DI float tile_max(const f32x16& s) {
  float a = __builtin_fmaxf(__builtin_fmaxf(s[0], s[1]), s[2]), b = __builtin_fmaxf(__builtin_fmaxf(s[3], s[4]), s[5]);
  a = __builtin_fmaxf(__builtin_fmaxf(a, s[6]), s[7]); b = __builtin_fmaxf(__builtin_fmaxf(b, s[8]), s[9]);
  a = __builtin_fmaxf(__builtin_fmaxf(a, s[10]), s[11]); b = __builtin_fmaxf(__builtin_fmaxf(b, s[12]), s[13]);
  a = __builtin_fmaxf(__builtin_fmaxf(a, s[14]), s[15]);
  return half_max(__builtin_fmaxf(a, b));
}

DI void attn_safe(const u16* __restrict__ Kw, const u16* __restrict__ Vw, bf16x8 (&qf)[4][2], f32x16 (&o)[4], float (&mref)[4], int lane, int hi) {
  const f32x16 zero = {};
#pragma unroll 1
  for (int t = 0; t < 8; ++t) {
    const bf16x8 k0 = *(const bf16x8*)(Kw + t * 1024 + lane * 8), k1 = *(const bf16x8*)(Kw + t * 1024 + 512 + lane * 8);
    const bf16x8 v0 = *(const bf16x8*)(Vw + t * 1024 + lane * 8), v1 = *(const bf16x8*)(Vw + t * 1024 + 512 + lane * 8);
#pragma unroll
    for (int b = 0; b < 4; ++b) {
      (void)set_ref(qf[b][1], 0.f, hi);
      f32x16 s = MFMA32(k0, qf[b][0], zero);
      s = MFMA32(k1, qf[b][1], s);
      const float tm = tile_max(s);
      const float mn = set_ref(qf[b][1], t == 0 ? tm : __builtin_fmaxf(tm, mref[b]), hi);
      if (t == 0) {
        o[b] = zero;
      } else {
        const float al = __builtin_amdgcn_exp2f(mref[b] - mn);
#pragma unroll
        for (int r = 0; r < 16; ++r) o[b][r] *= al;
      }
      mref[b] = mn;
      s = MFMA32(k0, qf[b][0], zero);
      s = MFMA32(k1, qf[b][1], s);
      u32x4 p0, p1;
#pragma unroll
      for (int i = 0; i < 4; ++i) {
        p0[i] = cvtpk(__builtin_amdgcn_exp2f(s[2 * i]), __builtin_amdgcn_exp2f(s[2 * i + 1]));
        p1[i] = cvtpk(__builtin_amdgcn_exp2f(s[8 + 2 * i]), __builtin_amdgcn_exp2f(s[8 + 2 * i + 1]));
      }
      o[b] = MFMA32(v0, __builtin_bit_cast(bf16x8, p0), o[b]);
      o[b] = MFMA32(v1, __builtin_bit_cast(bf16x8, p1), o[b]);
    }
  }
}

#define EX4(S, i) do { S[i] = __builtin_amdgcn_exp2f(S[i]); S[i + 1] = __builtin_amdgcn_exp2f(S[i + 1]); \
    S[i + 2] = __builtin_amdgcn_exp2f(S[i + 2]); S[i + 3] = __builtin_amdgcn_exp2f(S[i + 3]); } while (0)
#define PK8(P, S, i) do { P[0] = cvtpk(S[i], S[i + 1]); P[1] = cvtpk(S[i + 2], S[i + 3]); P[2] = cvtpk(S[i + 4], S[i + 5]); P[3] = cvtpk(S[i + 6], S[i + 7]); } while (0)
#define BF(P) __builtin_bit_cast(bf16x8, P)
#define ASTEP(SC, SN, KN0, KN1, BN, VP1, BP, VC0, BC) do { \
    SN = MFMA32(KN0, qf[BN][0], zero); SBAR(); EX4(SC, 0); SBAR(); \
    SN = MFMA32(KN1, qf[BN][1], SN); SBAR(); EX4(SC, 4); PK8(p0, SC, 0); SBAR(); \
    o[BP] = MFMA32(VP1, BF(p1), o[BP]); SBAR(); EX4(SC, 8); SBAR(); \
    o[BC] = MFMA32(VC0, BF(p0), o[BC]); SBAR(); EX4(SC, 12); PK8(p1, SC, 8); SBAR(); } while (0)
#define LDK(K0, K1, P, t) do { K0 = *(const bf16x8*)(P + (t) * 1024 + lane * 8); K1 = *(const bf16x8*)(P + (t) * 1024 + 512 + lane * 8); } while (0)
#define ATILE(KC0, KC1, VC0, VC1, KN0, KN1, VP0, VP1, tn) do { \
    LDK(KN0, KN1, Kw, tn); SBAR(); \
    ASTEP(sA, sB, KC0, KC1, 1, VP1, 3, VC0, 0); \
    LDK(VP0, VP1, Vw, tn); SBAR(); \
    ASTEP(sB, sA, KC0, KC1, 2, VC1, 0, VC0, 1); \
    ASTEP(sA, sB, KC0, KC1, 3, VC1, 1, VC0, 2); \
    ASTEP(sB, sA, KN0, KN1, 0, VC1, 2, VC0, 3); } while (0)
template <class PRE>
DI void attn_fast(const u16* __restrict__ Kf, const u16* __restrict__ Kw, const u16* __restrict__ Vw, bf16x8 (&qf)[4][2], f32x16 (&o)[4],
                  float (&mref)[4], int lane, int hi, PRE pre) {
  const f32x16 zero = {};
#pragma unroll
  for (int b = 0; b < 4; ++b) o[b] = zero;
  bf16x8 ka0, ka1, kb0, kb1, va0, va1, vb0, vb1;
  LDK(kb0, kb1, Kf, 0);
  LDK(ka0, ka1, Kw, 0);
  LDK(va0, va1, Vw, 0);
  SBAR();
  pre();
  SBAR();
  vb0 = va0; vb1 = va1;
#pragma unroll
  for (int b = 0; b < 4; ++b) {
    (void)set_ref(qf[b][1], 0.f, hi);
    f32x16 s = MFMA32(kb0, qf[b][0], zero);
    s = MFMA32(kb1, qf[b][1], s);
    mref[b] = set_ref(qf[b][1], tile_max(s), hi);
  }
  f32x16 sA, sB;
  u32x4 p0, p1 = {0u, 0u, 0u, 0u};
  sA = MFMA32(ka0, qf[0][0], zero);
  sA = MFMA32(ka1, qf[0][1], sA);
  SBAR();
  STAMP(1, 8);
#pragma unroll 1
  for (int t = 0; t < 8; t += 2) {
    ATILE(ka0, ka1, va0, va1, kb0, kb1, vb0, vb1, t + 1);
    const int tn = t + 2 < 8 ? t + 2 : 7;
    ATILE(kb0, kb1, vb0, vb1, ka0, ka1, va0, va1, tn);
  }
  o[3] = MFMA32(vb1, BF(p1), o[3]);
  SBAR();
  STAMP(1, 10);
}

constexpr int WPRE = 2;
__global__ __launch_bounds__(512) void k_attn(const u16* __restrict__ x, const u16* __restrict__ Qf, const u16* __restrict__ Kf,
                                              const u16* __restrict__ Vf, const u16* __restrict__ Wof, u16* __restrict__ H,
                                              u16* __restrict__ HN) {
  __shared__ __attribute__((aligned(16))) char lds[K2_LDS];
  const int tid = threadIdx.x, lane = tid & 63, w = __builtin_amdgcn_readfirstlane(tid >> 6), hi = lane >> 5;
  const int tg = blockIdx.x;
  STAMP(1, 0);
  bf16x8 qf[4][2];
#pragma unroll
  for (int b = 0; b < 4; ++b)
#pragma unroll
    for (int k = 0; k < 2; ++k) qf[b][k] = *(const bf16x8*)(Qf + ((size_t)((tg * 4 + b) * 2 + k) * 64 + lane) * 8);
  f32x16 o[4];
  float mref[4];
  const u16* Kw = Kf + (size_t)w * 8 * 1024;
  const u16* Vw = Vf + (size_t)w * 8 * 1024;
  const int kh = w >> 2, ng = w & 3;
  const u16* wop = Wof + ((size_t)(7 * ng * 14 + 7 * kh) * 64 + lane) * 8;
  const u16* const wob = Wof + (size_t)(7 * ng * 14 + 7 * kh) * 512;
  bf16x8 wf[7][7];
#define LDWO(n0, n1) do { _Pragma("unroll") for (int n_ = n0; n_ < n1; ++n_) _Pragma("unroll") for (int k_ = 0; k_ < 7; ++k_) \
    wf[n_][k_] = *(const bf16x8*)(wop + (n_ * 14 + k_) * 512); } while (0)
  attn_fast(Kf, Kw, Vw, qf, o, mref, lane, hi, [&]() { LDWO(0, WPRE); });
  {
    int l2 = lane;
    asm volatile("" : "+v"(l2));
    wop = wob + l2 * 8;
  }
  bool slow;
  {
    float chk = 0.f;
    bool bad = false;
#pragma unroll
    for (int b = 0; b < 4; ++b) {
#pragma unroll
      for (int r = 0; r < 16; ++r) chk = __builtin_fmaf(o[b][r], 0.f, chk);
      bad |= !(o[b][12] < 1.8e19f);
    }
    bad |= !(chk == 0.f);
    slow = __any(bad);
    if (slow) {
      attn_safe(Kw, Vw, qf, o, mref, lane, hi);
      LDWO(0, WPRE);
    }
  }
  STAMP(1, 1);
#pragma unroll
  for (int b = 0; b < 4; ++b) {
#pragma unroll
    for (int r4 = 0; r4 < 4; ++r4)
      *(f32x4*)(lds + K2_P + (((w * 4 + b) * 4 + r4) * 64 + lane) * 16) = (f32x4){o[b][4 * r4], o[b][4 * r4 + 1], o[b][4 * r4 + 2], o[b][4 * r4 + 3]};
    if (hi == 0) *(float*)(lds + K2_M + (w * 128 + b * 32 + lane) * 4) = mref[b];
  }
  if (lane == 0) *(unsigned*)(lds + K2_FL + w * 4) = slow ? 1u : 0u;
  const u32x4 xv = *(const u32x4*)(x + (size_t)(tg * 8 + w) * D + (lane < 56 ? lane : 55) * 8);
  SBAR();
#define WOGEMM() do { \
    const int g_ = lane >> 4; \
    bf16x8 bo[7]; \
    _Pragma("unroll") for (int k = 0; k < 7; ++k) bo[k] = *(const bf16x8*)(lds + K2_OT + ((4 * (7 * kh + k) + g_) * 8 + (lane & 7)) * 16); \
    f32x4 acc[7]; \
    _Pragma("unroll") for (int n = 0; n < 7; ++n) { \
      acc[n] = (f32x4){0.f, 0.f, 0.f, 0.f}; \
      _Pragma("unroll") for (int k = 0; k < 7; ++k) acc[n] = MFMA16(wf[n][k], bo[k], acc[n]); \
    } \
    if ((lane & 15) < 8) { \
      _Pragma("unroll") for (int n = 0; n < 7; ++n) *(f32x4*)(lds + K2_HB + ((kh * 8 + (lane & 15)) * D + 16 * (7 * ng + n) + 4 * g_) * 4) = acc[n]; \
    } } while (0)
  if (w < 4) {
    LDWO(WPRE, 7);
    SBAR();
    STAMP(1, 5);
    LBAR();
    STAMP(1, 6);
    const u32x4 f0 = *(const u32x4*)(lds + K2_FL), f1 = *(const u32x4*)(lds + K2_FL + 16);
    const bool gen = __builtin_amdgcn_readfirstlane(f0[0] | f0[1] | f0[2] | f0[3] | f1[0] | f1[1] | f1[2] | f1[3]) != 0;
    const int c = lane & 31, r4 = w;
#pragma unroll
    for (int b = 0; b < 4; ++b) {
      float l = 0.f;
      f32x4 a = {0.f, 0.f, 0.f, 0.f};
      if (!gen) {
#pragma unroll
        for (int v = 0; v < 8; ++v) {
          l += *(const float*)(lds + K2_P + (((v * 4 + b) * 4 + 3) * 64 + 32 + c) * 16);
          a += *(const f32x4*)(lds + K2_P + (((v * 4 + b) * 4 + r4) * 64 + lane) * 16);
        }
      } else {
        float M = -INFINITY;
#pragma unroll
        for (int v = 0; v < 8; ++v) M = __builtin_fmaxf(M, *(const float*)(lds + K2_M + (v * 128 + 32 * b + c) * 4));
#pragma unroll 1
        for (int v = 0; v < 8; ++v) {
          const float fc = __builtin_amdgcn_exp2f(*(const float*)(lds + K2_M + (v * 128 + 32 * b + c) * 4) - M);
          l += fc * *(const float*)(lds + K2_P + (((v * 4 + b) * 4 + 3) * 64 + 32 + c) * 16);
          a += fc * *(const f32x4*)(lds + K2_P + (((v * 4 + b) * 4 + r4) * 64 + lane) * 16);
        }
      }
      const float il = 1.f / l;
      const int d = 8 * r4 + 4 * hi;
      if (d < 28) {
        const int k = 28 * (4 * b + (c >> 3)) + d;
        *(u32x2*)(lds + K2_OT + ((k >> 3) * 8 + (c & 7)) * 16 + (k & 7) * 2) = (u32x2){cvtpk(a[0] * il, a[1] * il), cvtpk(a[2] * il, a[3] * il)};
      }
      SBAR();
    }
    LBAR();
    STAMP(1, 2);
    WOGEMM();
  } else {
    STAMP(1, 5);
    LBAR();
    STAMP(1, 6);
    LDWO(WPRE, 7);
    SBAR();
    LBAR();
    STAMP(1, 2);
    WOGEMM();
  }
  LBAR();
  STAMP(1, 3);
  {
    const int s = tg * 8 + w;
    const bool act = lane < 56;
    const int lc = act ? lane : 55;
    float v[8];
    const f32x4 a0 = *(const f32x4*)(lds + K2_HB + (w * D + lc * 8) * 4), a1 = *(const f32x4*)(lds + K2_HB + (w * D + lc * 8 + 4) * 4);
    const f32x4 c0 = *(const f32x4*)(lds + K2_HB + ((8 + w) * D + lc * 8) * 4), c1 = *(const f32x4*)(lds + K2_HB + ((8 + w) * D + lc * 8 + 4) * 4);
    u32x4 hv;
#pragma unroll
    for (int i = 0; i < 2; ++i) {
      hv[i] = cvtpk(bflo(xv[i]) + a0[2 * i] + c0[2 * i], bfhi(xv[i]) + a0[2 * i + 1] + c0[2 * i + 1]);
      hv[2 + i] = cvtpk(bflo(xv[2 + i]) + a1[2 * i] + c1[2 * i], bfhi(xv[2 + i]) + a1[2 * i + 1] + c1[2 * i + 1]);
    }
    if (act) *(u32x4*)(H + (size_t)s * D + lane * 8) = hv;
#pragma unroll
    for (int i = 0; i < 4; ++i) { v[2 * i] = act ? bflo(hv[i]) : 0.f; v[2 * i + 1] = act ? bfhi(hv[i]) : 0.f; }
#pragma unroll
    for (int pass = 0; pass < 2; ++pass) {
      float sm = 0.f;
#pragma unroll
      for (int i = 0; i < 8; ++i) sm += v[i];
      const float mean = wave_sum(sm) * (1.f / D);
      float q = 0.f;
#pragma unroll
      for (int i = 0; i < 8; ++i) { v[i] = act ? v[i] - mean : 0.f; q += v[i] * v[i]; }
      const float rs = __builtin_amdgcn_rsqf(wave_sum(q) * (1.f / D) + LN_EPS);
#pragma unroll
      for (int i = 0; i < 8; ++i) v[i] *= rs;
    }
    if (act) *(u32x4*)(lds + K2_HS + w * 912 + lane * 16) = (u32x4){cvtpk(v[0], v[1]), cvtpk(v[2], v[3]), cvtpk(v[4], v[5]), cvtpk(v[6], v[7])};
  }
  LBAR();
  if (tid < D / 2) {
    unsigned r[8];
#pragma unroll
    for (int i = 0; i < 8; ++i) r[i] = *(const unsigned*)(lds + K2_HS + i * 912 + tid * 4);
    u32x4 lo, hi2;
#pragma unroll
    for (int i = 0; i < 4; ++i) {
      lo[i] = (r[2 * i] & 0xffffu) | (r[2 * i + 1] << 16);
      hi2[i] = (r[2 * i] >> 16) | (r[2 * i + 1] & 0xffff0000u);
    }
    u32x4* dst = (u32x4*)(HN + ((size_t)tg * D + 2 * tid) * 8);
    dst[0] = lo; dst[1] = hi2;
  }
  STAMP(1, 4);
}

constexpr int G_RING = 4, G_DUM = 8 * G_RING * 4096, G_LDS = G_DUM + 8192;
template <int PH>
__global__ __launch_bounds__(512) void k_mlp(const u16* __restrict__ act, const u16* __restrict__ W, const u16* __restrict__ bias,
                                             const u16* __restrict__ H, u16* __restrict__ out) {
  constexpr int KT = PH == 0 ? S : F, LDW = PH == 0 ? F : S, NK = KT / 256, NWG = 7 * (LDW / 64);
  __shared__ __attribute__((aligned(16))) char lds[G_LDS];
  const int tid = threadIdx.x, lane = tid & 63, w = __builtin_amdgcn_readfirstlane(tid >> 6);
  const int t = xcd_remap(blockIdx.x, NWG), nt = t / 7, mt = t - nt * 7;
  const unsigned lds0 = (unsigned)(uintptr_t)lds + w * (G_RING * 4096);
  const int g = lane >> 4, q4 = (lane & 15) >> 2, p = lane & 3, l15 = lane & 15;
  STAMP(2 + PH, 0);
  __builtin_amdgcn_s_setprio(3);
  const u16* asrc = act + ((size_t)(4 * w + g) * D + 64 * mt + l15) * 8;
  const int r8 = lane >> 3, fe = (r8 >> 1) & 1;
  const u16* wsrc0 = W + (size_t)(32 * w + r8) * LDW + 64 * nt + ((lane & 7) ^ (fe << 1)) * 8;
  const u16* wsrc1 = W + (size_t)(32 * w + 8 + r8) * LDW + 64 * nt + ((lane & 7) ^ ((fe | 2) << 1)) * 8;
  bf16x8 af[NK][4];
#define STAGE(i) do { const unsigned sl_ = lds0 + ((i) % G_RING) * 4096; const size_t o_ = (size_t)(i) * 256 * LDW; \
    glds16(wsrc0 + o_, sl_); glds16(wsrc1 + o_, sl_ + 1024); glds16(wsrc0 + o_ + 16 * LDW, sl_ + 2048); glds16(wsrc1 + o_ + 16 * LDW, sl_ + 3072); \
    const u16* ap_ = asrc + (size_t)(i) * 32 * D * 8; \
    gload16<0>(af[i][0], ap_); gload16<256>(af[i][1], ap_); gload16<512>(af[i][2], ap_); gload16<768>(af[i][3], ap_); } while (0)
#pragma unroll
  for (int i = 0; i < G_RING; ++i) STAGE(i);
  STAMP(2 + PH, 1);
  const int f = ((q4 >> 1) & 1) | ((g & 1) << 1);
  int woff[4];
#pragma unroll
  for (int nb = 0; nb < 4; ++nb) woff[nb] = w * (G_RING * 4096) + (8 * g + q4) * 128 + (((2 * nb + (p >> 1)) ^ (f << 1)) * 16) + 8 * (p & 1);
  f32x4 acc[4][4] = {};
#pragma unroll
  for (int i = 0; i < NK; ++i) {
    constexpr int PF = PH == 0 ? 5 : 0;
    const int rem = NK - 1 - i < G_RING - 1 ? NK - 1 - i : G_RING - 1;
    const bool pfd = i + G_RING > NK;
    if (i == 0) __builtin_amdgcn_s_setprio(3);
    else if (i == 2) __builtin_amdgcn_s_setprio(2);
    else if (i == 4) __builtin_amdgcn_s_setprio(1);
    else if (i == 6) __builtin_amdgcn_s_setprio(0);
    if (rem == 3) { if (pfd) waitv4<24 + PF>(af[i][0], af[i][1], af[i][2], af[i][3]); else waitv4<24>(af[i][0], af[i][1], af[i][2], af[i][3]); }
    else if (rem == 2) waitv4<16 + PF>(af[i][0], af[i][1], af[i][2], af[i][3]);
    else if (rem == 1) waitv4<8 + PF>(af[i][0], af[i][1], af[i][2], af[i][3]);
    else waitv4<PF>(af[i][0], af[i][1], af[i][2], af[i][3]);
    bf16x8 wf[4];
#pragma unroll
    for (int nb = 0; nb < 4; ++nb) {
      const lds_cptr sp = (lds_cptr)lds + woff[nb] + (i % G_RING) * 4096;
      wf[nb] = cat8(trread(sp), trread(sp + 512));
    }
    if (i + G_RING < NK) {
      asm volatile("s_waitcnt lgkmcnt(0)" ::: "memory");
      STAGE(i + G_RING);
    }
    if (PH == 0 && i + G_RING == NK) {
      const int xg = blockIdx.x & 7, rk = blockIdx.x >> 3;
      const unsigned dum = (unsigned)(uintptr_t)lds + G_DUM + w * 1024;
#pragma unroll
      for (int j = 0; j < 5; ++j) {
        int id = ((rk < 24 ? rk : 23) * 8 + w) * 5 + j;
        id = id < 896 ? id : 895;
        glds16(H + (size_t)((id >> 2) * 8 + (lane >> 3)) * S + (4 * xg + (id & 3)) * 64 + (lane & 7) * 8, dum);
      }
    }
#pragma unroll
    for (int nb = 0; nb < 4; ++nb)
#pragma unroll
      for (int mb = 0; mb < 4; ++mb)
        acc[nb][mb] = PH == 0 ? MFMA16(wf[nb], af[i][mb], acc[nb][mb]) : MFMA16(af[i][mb], wf[nb], acc[nb][mb]);
  }
#undef STAGE
  STAMP(2 + PH, 4);
  const int enb = w >> 1, emb = 2 * (w & 1);
  u32x2 eb, eh0, eh1;
  if (PH == 0) {
    eb = *(const u32x2*)(bias + 64 * nt + 16 * enb + 4 * g);
  } else {
    const int s = 64 * nt + 16 * enb + l15;
    eb[0] = bias[s];
    eh0 = *(const u32x2*)(H + (size_t)s * D + 64 * mt + 16 * emb + 4 * g);
    eh1 = *(const u32x2*)(H + (size_t)s * D + 64 * mt + 16 * emb + 16 + 4 * g);
  }
  {
    f32x4* xb = (f32x4*)lds;
#pragma unroll
    for (int nb = 0; nb < 4; ++nb)
#pragma unroll
      for (int mb = 0; mb < 4; ++mb) xb[(w * 16 + nb * 4 + mb) * 64 + lane] = acc[nb][mb];
  }
  LBAR();
  {
    const f32x4* xb = (const f32x4*)lds;
#pragma unroll
    for (int e = 0; e < 2; ++e) {
      f32x4 a = xb[(2 * w + e) * 64 + lane];
#pragma unroll
      for (int v = 1; v < 8; ++v) a += xb[(v * 16 + 2 * w + e) * 64 + lane];
      const int mb = emb + e;
      if (PH == 0) {
        const int fi = 64 * nt + 16 * enb + 4 * g, d = 64 * mt + 16 * mb + l15;
        float z[4] = {a[0] + bflo(eb[0]), a[1] + bfhi(eb[0]), a[2] + bflo(eb[1]), a[3] + bfhi(eb[1])};
#pragma unroll
        for (int i = 0; i < 4; ++i) {
          const float zz = z[i], a2 = zz * (1.f + 0.044715f * zz * zz) * (-2.f * 0.7978845608028654f * 1.4426950408889634f);
          z[i] = zz * __builtin_amdgcn_rcpf(1.f + __builtin_amdgcn_exp2f(a2));
        }
        *(u32x2*)(out + ((size_t)(fi >> 3) * D + d) * 8 + (fi & 7)) = (u32x2){cvtpk(z[0], z[1]), cvtpk(z[2], z[3])};
      } else {
        const int d = 64 * mt + 16 * mb + 4 * g, s = 64 * nt + 16 * enb + l15;
        const float bb = bflo(eb[0]);
        const u32x2 hv = e ? eh1 : eh0;
        *(u32x2*)(out + (size_t)s * D + d) =
            (u32x2){cvtpk(a[0] + bb + bflo(hv[0]), a[1] + bb + bfhi(hv[0])), cvtpk(a[2] + bb + bflo(hv[1]), a[3] + bb + bfhi(hv[1]))};
      }
    }
  }
  if (PH == 0) asm volatile("s_waitcnt vmcnt(0)" ::: "memory");
  STAMP(2 + PH, 5);
}

extern "C" void kernel_launch(void* const* d_in, const int* in_sizes, int n_in, void* d_out, int out_size, void* d_ws, size_t ws_size,
                              hipStream_t stream) {
  const u16* x = (const u16*)d_in[0];
  const u16* wq = (const u16*)d_in[1];
  const u16* wk = (const u16*)d_in[2];
  const u16* wv = (const u16*)d_in[3];
  const u16* wo = (const u16*)d_in[4];
  const u16* w1 = (const u16*)d_in[6];
  const u16* b1 = (const u16*)d_in[7];
  const u16* w2 = (const u16*)d_in[8];
  const u16* b2 = (const u16*)d_in[9];
  char* ws = (char*)d_ws;
  u16 *Qf = (u16*)(ws + WS_QF), *Kf = (u16*)(ws + WS_KF), *Vf = (u16*)(ws + WS_VF), *Wof = (u16*)(ws + WS_WOF);
  u16 *H = (u16*)(ws + WS_H), *HN = (u16*)(ws + WS_HN), *U = (u16*)(ws + WS_U);
  k_qkv<<<256, 512, 0, stream>>>(x, wq, wk, wv, wo, w1, Qf, Kf, Vf, Wof);
  k_attn<<<256, 512, 0, stream>>>(x, Qf, Kf, Vf, Wof, H, HN);
  k_mlp<0><<<196, 512, 0, stream>>>(HN, w1, b1, w2, U);
  k_mlp<1><<<224, 512, 0, stream>>>(U, w2, b2, H, (u16*)d_out);
}
```

```cpp
#include <hip/hip_runtime.h>
#include <hip/hip_bf16.h>

typedef unsigned short u16;
using bf16x8 = __attribute__((ext_vector_type(8))) short;
using s16x4 = __attribute__((ext_vector_type(4))) short;
using f32x4 = __attribute__((ext_vector_type(4))) float;
using f32x16 = __attribute__((ext_vector_type(16))) float;
using u32x4 = __attribute__((ext_vector_type(4))) unsigned;
using u32x2 = __attribute__((ext_vector_type(2))) unsigned;
typedef short v4i16_t __attribute__((ext_vector_type(4)));
typedef __attribute__((address_space(3))) const char* lds_cptr;
typedef __bf16 bf16x2_t __attribute__((ext_vector_type(2)));
typedef float f32x2_t __attribute__((ext_vector_type(2)));

#define DI __device__ __forceinline__
#define MFMA32(a, b, c) __builtin_amdgcn_mfma_f32_32x32x16_bf16(a, b, c, 0, 0, 0)
#define MFMA16(a, b, c) __builtin_amdgcn_mfma_f32_16x16x32_bf16(a, b, c, 0, 0, 0)
#define SBAR() __builtin_amdgcn_sched_barrier(0)
#define LBAR() asm volatile("s_waitcnt lgkmcnt(0)\n\ts_barrier" ::: "memory")
#define WAITV_BAR(N) asm volatile("s_waitcnt vmcnt(" #N ") lgkmcnt(0)\n\ts_barrier" ::: "memory")

constexpr int S = 2048, D = 448, DH = 28, F = 1792;
constexpr float QSCALE = 0.18898223650461362f * 1.4426950408889634f;
constexpr float LN_EPS = 1e-5f;

constexpr size_t WS_QF = 0;
constexpr size_t WS_KF = WS_QF + 2097152;
constexpr size_t WS_VF = WS_KF + 131072;
constexpr size_t WS_WOF = WS_VF + 131072;
constexpr size_t WS_H = WS_WOF + 401408;
constexpr size_t WS_HN = WS_H + 1835008;
constexpr size_t WS_U = WS_HN + 1835008;

#define STAMP(k, i)
#define CSTAMP(k, i)

DI unsigned cvtpk(float lo, float hi) {
  const f32x2_t v = {lo, hi};
  return __builtin_bit_cast(unsigned, __builtin_convertvector(v, bf16x2_t));
}
DI float bflo(unsigned u) { return __uint_as_float(u << 16); }
DI float bfhi(unsigned u) { return __uint_as_float(u & 0xffff0000u); }
template <int CTRL> DI float dppf(float v) {
  return __uint_as_float(__builtin_amdgcn_update_dpp(0u, __float_as_uint(v), CTRL, 0xf, 0xf, true));
}
DI float wave_sum(float v) {
  v += dppf<0xB1>(v);
  v += dppf<0x4E>(v);
  v += dppf<0x141>(v);
  v += dppf<0x140>(v);
  auto a = __builtin_amdgcn_permlane16_swap(__float_as_uint(v), __float_as_uint(v), false, false);
  v = __uint_as_float(a[0]) + __uint_as_float(a[1]);
  auto b = __builtin_amdgcn_permlane32_swap(__float_as_uint(v), __float_as_uint(v), false, false);
  return __uint_as_float(b[0]) + __uint_as_float(b[1]);
}
DI float half_max(float v) {
  auto b = __builtin_amdgcn_permlane32_swap(__float_as_uint(v), __float_as_uint(v), false, false);
  return __builtin_fmaxf(__uint_as_float(b[0]), __uint_as_float(b[1]));
}
DI void glds16(const void* g, unsigned lds_base) {
  unsigned sv;
  asm volatile("s_mov_b32 %0, m0\n\ts_mov_b32 m0, %2\n\ts_nop 0\n\tglobal_load_lds_dwordx4 %1, off\n\ts_mov_b32 m0, %0"
               : "=&s"(sv) : "v"(g), "s"(lds_base) : "memory");
}
DI void glds4(const void* g, unsigned lds_base) {
  unsigned sv;
  asm volatile("s_mov_b32 %0, m0\n\ts_mov_b32 m0, %2\n\ts_nop 0\n\tglobal_load_lds_dword %1, off\n\ts_mov_b32 m0, %0"
               : "=&s"(sv) : "v"(g), "s"(lds_base) : "memory");
}
template <int N> DI void waitv4(bf16x8& a, bf16x8& b, bf16x8& c, bf16x8& d) {
  asm volatile("s_waitcnt vmcnt(%4)" : "+v"(a), "+v"(b), "+v"(c), "+v"(d) : "i"(N) : "memory");
}
template <int OFF> DI void gload16(bf16x8& d, const void* p) {
  asm volatile("global_load_dwordx4 %0, %1, off offset:%2" : "=v"(d) : "v"(p), "i"(OFF) : "memory");
}
DI s16x4 trread(lds_cptr p) {
  return __builtin_bit_cast(s16x4, __builtin_amdgcn_ds_read_tr16_b64_v4i16((__attribute__((address_space(3))) v4i16_t*)p));
}
DI bf16x8 ldsr16(lds_cptr p) { return *(const __attribute__((address_space(3))) bf16x8*)p; }
DI bf16x8 cat8(s16x4 a, s16x4 b) { return __builtin_shufflevector(a, b, 0, 1, 2, 3, 4, 5, 6, 7); }
DI int xcd_remap(int bid, int nwg) {
  const int q = nwg >> 3, r = nwg & 7, x = bid & 7, o = bid >> 3;
  return (x < r ? x * (q + 1) : r * (q + 1) + (x - r) * q) + o;
}

constexpr int K1_WI = 448 * 128, K1_X = 2 * K1_WI, K1_XS = 528, K1_DUM = K1_X + 64 * K1_XS, K1_LDS = K1_DUM + 8192;
__global__ __launch_bounds__(512) void k_qkv(const u16* __restrict__ x, const u16* __restrict__ wq, const u16* __restrict__ wk,
                                             const u16* __restrict__ wv, const u16* __restrict__ wo, const u16* __restrict__ w1,
                                             u16* __restrict__ Qf, u16* __restrict__ Kf, u16* __restrict__ Vf, u16* __restrict__ Wof) {
  __shared__ __attribute__((aligned(16))) char lds[K1_LDS];
  const int tid = threadIdx.x, lane = tid & 63, w = __builtin_amdgcn_readfirstlane(tid >> 6);
  const int t = xcd_remap(blockIdx.x, 256), rt = t >> 2, ct = t & 3;
  const unsigned lds0 = (unsigned)(uintptr_t)lds;
  STAMP(0, 0);
  bf16x8 xr[4];
  const bool act = lane < 56;
  {
    const u16* xp_ = x + (size_t)(rt * 32 + w * 4) * D + (act ? lane : 55) * 8;
    gload16<0>(xr[0], xp_); gload16<896>(xr[1], xp_); gload16<1792>(xr[2], xp_); gload16<2688>(xr[3], xp_);
  }
#pragma unroll
  for (int im = 0; im < 2; ++im) {
    const int c64 = 2 * ct + im;
    if (c64 < 7) {
#pragma unroll
      for (int i = 0; i < 7; ++i) {
        const int piece = w * 7 + i, row = piece * 8 + (lane >> 3);
        const int f = ((row >> 1) & 1) | (((row >> 3) & 1) << 1);
        const int c = (lane & 7) ^ (f << 1);
        glds16(wq + (size_t)row * D + c64 * 64 + c * 8, lds0 + im * K1_WI + piece * 1024);
      }
    } else {
#pragma unroll
      for (int i = 0; i < 28; ++i) {
        const int piece = w * 28 + i, row = piece * 2 + (lane >> 5), pp = lane & 31;
        const int f = ((row >> 1) & 1) | (((row >> 3) & 1) << 1);
        const int col = ((((pp >> 2) ^ (f << 1)) << 2) + (pp & 3)) * 2;
        const u16* src = col < 28 ? wk + row * DH + col : wv + row * DH + (col < 56 ? col - 28 : 0);
        glds4(src, lds0 + im * K1_WI + piece * 256);
      }
    }
  }
  u16 wt[8];
  const int frag0 = blockIdx.x + 256 * (w & 1);
  const bool dofrag = w < 2 && frag0 < 392;
  const int frag = dofrag ? frag0 : 0;
  {
    const int nb = frag / 14, ks = frag - nb * 14;
#pragma unroll
    for (int j = 0; j < 8; ++j) wt[j] = wo[(size_t)(32 * ks + 8 * (lane >> 4) + j) * D + 16 * nb + (lane & 15)];
  }
  SBAR();
  if (ct < 3) waitv4<22>(xr[0], xr[1], xr[2], xr[3]);
  else waitv4<43>(xr[0], xr[1], xr[2], xr[3]);
  {
    u32x4 xv[4];
#pragma unroll
    for (int r = 0; r < 4; ++r) xv[r] = __builtin_bit_cast(u32x4, xr[r]);
    float v[4][8], mean[4], rs[4];
#pragma unroll
    for (int r = 0; r < 4; ++r) {
      float s = 0.f;
#pragma unroll
      for (int i = 0; i < 4; ++i) {
        v[r][2 * i] = act ? bflo(xv[r][i]) : 0.f; v[r][2 * i + 1] = act ? bfhi(xv[r][i]) : 0.f;
        s += v[r][2 * i] + v[r][2 * i + 1];
      }
      mean[r] = s;
    }
#pragma unroll
    for (int r = 0; r < 4; ++r) mean[r] = wave_sum(mean[r]) * (1.f / D);
#pragma unroll
    for (int r = 0; r < 4; ++r) {
      float q = 0.f;
#pragma unroll
      for (int i = 0; i < 8; ++i) { v[r][i] = act ? v[r][i] - mean[r] : 0.f; q += v[r][i] * v[r][i]; }
      rs[r] = q;
    }
#pragma unroll
    for (int r = 0; r < 4; ++r) rs[r] = __builtin_amdgcn_rsqf(wave_sum(rs[r]) * (1.f / D) + LN_EPS);
    char* xdst = lds + K1_X + lane * K1_XS + w * 64;
#pragma unroll
    for (int r = 0; r < 4; ++r) {
      u32x4 o;
#pragma unroll
      for (int i = 0; i < 4; ++i) o[i] = cvtpk(v[r][2 * i] * rs[r], v[r][2 * i + 1] * rs[r]);
      *(u32x4*)(xdst + r * 16) = o;
    }
  }
  STAMP(0, 1);
  WAITV_BAR(0);
  STAMP(0, 2);
  {
    const unsigned dum = lds0 + K1_DUM + w * 1024;
    const int xg = blockIdx.x & 7, rk = blockIdx.x >> 3;
    const int ts = xg < 4 ? 25 * xg : 100 + 24 * (xg - 4), te = ts + (xg < 4 ? 25 : 24);
    const int n0 = ts / 7, n1 = (te - 1) / 7;
    const u16* p1 = w1 + (size_t)(64 * rk + 8 * w + (lane >> 3)) * F + (lane & 7) * 8;
#pragma unroll
    for (int i = 0; i < 5; ++i) glds16(p1 + (n0 + i <= n1 ? n0 + i : n1) * 64, dum);
  }
  const int nb = w & 3, im = w >> 2, c64 = 2 * ct + im, g = lane >> 4, q4 = (lane & 15) >> 2, p = lane & 3, l15 = lane & 15;
  const int f = ((q4 >> 1) & 1) | ((g & 1) << 1);
  const lds_cptr wp = (lds_cptr)lds + im * K1_WI + (8 * g + q4) * 128 + (((2 * nb + (p >> 1)) ^ (f << 1)) * 16) + 8 * (p & 1);
  const lds_cptr xp = (lds_cptr)lds + K1_X + g * K1_XS + l15 * 16;
  f32x4 acc0 = {0.f, 0.f, 0.f, 0.f}, acc1 = {0.f, 0.f, 0.f, 0.f};
  {
    bf16x8 a[14], b0[14], b1[14];
#pragma unroll
    for (int ks = 0; ks < 14; ++ks) {
      a[ks] = cat8(trread(wp + ks * 4096), trread(wp + ks * 4096 + 512));
      b0[ks] = ldsr16(xp + ks * 4 * K1_XS);
      b1[ks] = ldsr16(xp + ks * 4 * K1_XS + 256);
    }
    SBAR();
#pragma unroll
    for (int ks = 0; ks < 14; ++ks) {
      acc0 = MFMA16(a[ks], b0[ks], acc0);
      acc1 = MFMA16(a[ks], b1[ks], acc1);
    }
  }
  STAMP(0, 3);
#pragma unroll
  for (int tb = 0; tb < 2; ++tb) {
    const f32x4 a = tb ? acc1 : acc0;
    const int s = rt * 32 + 16 * tb + l15;
    const int nl = 16 * nb + 4 * g;
    if (c64 < 7) {
      const int n0 = 64 * c64 + nl, h = n0 / 28, d0 = n0 - 28 * h;
      const int tg = s >> 3, c = (h & 3) * 8 + (s & 7), b = h >> 2;
      const size_t off = ((((size_t)(tg * 4 + b) * 2 + (d0 >> 4)) * 64) + ((d0 >> 3) & 1) * 32 + c) * 8 + (d0 & 7);
      *(u32x2*)(Qf + off) = (u32x2){cvtpk(a[0] * QSCALE, a[1] * QSCALE), cvtpk(a[2] * QSCALE, a[3] * QSCALE)};
    } else {
      const int kt = s >> 5, kk = s & 31;
      if (nl < 28 || nl == 56) {
        const int d0 = nl < 28 ? nl : 28;
        const size_t off = ((((size_t)kt * 2 + (d0 >> 4)) * 64) + ((d0 >> 3) & 1) * 32 + kk) * 8 + (d0 & 7);
        u32x2 v = {cvtpk(a[0], a[1]), cvtpk(a[2], a[3])};
        if (nl == 56) v = (u32x2){0x3f803f80u, 0x00003f80u};
        *(u32x2*)(Kf + off) = v;
      } else {
        const int d0 = nl < 56 ? nl - 28 : 28;
        const int sp = kk >> 4, j = ((kk >> 3) & 1) * 4 + (kk & 3), hi = (kk >> 2) & 1;
        const size_t off = ((((size_t)kt * 2 + sp) * 64) + hi * 32 + d0) * 8 + j;
        unsigned v0 = cvtpk(a[0], a[1]), v1 = cvtpk(a[2], a[3]);
        if (nl == 60) { v0 = 0x00003f80u; v1 = 0u; }
        Vf[off] = (u16)v0; Vf[off + 8] = (u16)(v0 >> 16); Vf[off + 16] = (u16)v1; Vf[off + 24] = (u16)(v1 >> 16);
      }
    }
  }
  if (dofrag) {
    u32x4 o;
#pragma unroll
    for (int i = 0; i < 4; ++i) o[i] = (unsigned)wt[2 * i] | ((unsigned)wt[2 * i + 1] << 16);
    *(u32x4*)(Wof + ((size_t)frag * 64 + lane) * 8) = o;
  }
  asm volatile("s_waitcnt vmcnt(0)" ::: "memory");
  STAMP(0, 4);
}

constexpr int K2_P = 0, K2_M = 131072, K2_FL = K2_M + 4096, K2_OT = K2_FL + 64, K2_LDS = K2_OT + 7168;
constexpr int K2_HB = 0, K2_HS = 32768;

DI float set_ref(bf16x8& q1, float m, int hi) {
  const float a = bflo(cvtpk(-m, 0.f)), r1 = -m - a, b = bflo(cvtpk(r1, 0.f)), c = bflo(cvtpk(r1 - b, 0.f));
  u32x4 v = __builtin_bit_cast(u32x4, q1);
  v[2] = hi ? cvtpk(a, b) : v[2];
  v[3] = hi ? cvtpk(c, 0.f) : v[3];
  q1 = __builtin_bit_cast(bf16x8, v);
  return -((a + b) + c);
}
DI float tile_max(const f32x16& s) {
  float a = __builtin_fmaxf(__builtin_fmaxf(s[0], s[1]), s[2]), b = __builtin_fmaxf(__builtin_fmaxf(s[3], s[4]), s[5]);
  a = __builtin_fmaxf(__builtin_fmaxf(a, s[6]), s[7]); b = __builtin_fmaxf(__builtin_fmaxf(b, s[8]), s[9]);
  a = __builtin_fmaxf(__builtin_fmaxf(a, s[10]), s[11]); b = __builtin_fmaxf(__builtin_fmaxf(b, s[12]), s[13]);
  a = __builtin_fmaxf(__builtin_fmaxf(a, s[14]), s[15]);
  return half_max(__builtin_fmaxf(a, b));
}

DI void attn_safe(const u16* __restrict__ Kw, const u16* __restrict__ Vw, bf16x8 (&qf)[4][2], f32x16 (&o)[4], float (&mref)[4], int lane, int hi) {
  const f32x16 zero = {};
#pragma unroll 1
  for (int t = 0; t < 8; ++t) {
    const bf16x8 k0 = *(const bf16x8*)(Kw + t * 1024 + lane * 8), k1 = *(const bf16x8*)(Kw + t * 1024 + 512 + lane * 8);
    const bf16x8 v0 = *(const bf16x8*)(Vw + t * 1024 + lane * 8), v1 = *(const bf16x8*)(Vw + t * 1024 + 512 + lane * 8);
#pragma unroll
    for (int b = 0; b < 4; ++b) {
      (void)set_ref(qf[b][1], 0.f, hi);
      f32x16 s = MFMA32(k0, qf[b][0], zero);
      s = MFMA32(k1, qf[b][1], s);
      const float tm = tile_max(s);
      const float mn = set_ref(qf[b][1], t == 0 ? tm : __builtin_fmaxf(tm, mref[b]), hi);
      if (t == 0) {
        o[b] = zero;
      } else {
        const float al = __builtin_amdgcn_exp2f(mref[b] - mn);
#pragma unroll
        for (int r = 0; r < 16; ++r) o[b][r] *= al;
      }
      mref[b] = mn;
      s = MFMA32(k0, qf[b][0], zero);
      s = MFMA32(k1, qf[b][1], s);
      u32x4 p0, p1;
#pragma unroll
      for (int i = 0; i < 4; ++i) {
        p0[i] = cvtpk(__builtin_amdgcn_exp2f(s[2 * i]), __builtin_amdgcn_exp2f(s[2 * i + 1]));
        p1[i] = cvtpk(__builtin_amdgcn_exp2f(s[8 + 2 * i]), __builtin_amdgcn_exp2f(s[8 + 2 * i + 1]));
      }
      o[b] = MFMA32(v0, __builtin_bit_cast(bf16x8, p0), o[b]);
      o[b] = MFMA32(v1, __builtin_bit_cast(bf16x8, p1), o[b]);
    }
  }
}

#define EX4(S, i) do { S[i] = __builtin_amdgcn_exp2f(S[i]); S[i + 1] = __builtin_amdgcn_exp2f(S[i + 1]); \
    S[i + 2] = __builtin_amdgcn_exp2f(S[i + 2]); S[i + 3] = __builtin_amdgcn_exp2f(S[i + 3]); } while (0)
#define PK8(P, S, i) do { P[0] = cvtpk(S[i], S[i + 1]); P[1] = cvtpk(S[i + 2], S[i + 3]); P[2] = cvtpk(S[i + 4], S[i + 5]); P[3] = cvtpk(S[i + 6], S[i + 7]); } while (0)
#define BF(P) __builtin_bit_cast(bf16x8, P)
#define ASTEP(SC, SN, KN0, KN1, BN, VP1, BP, VC0, BC) do { \
    SN = MFMA32(KN0, qf[BN][0], zero); SBAR(); EX4(SC, 0); SBAR(); \
    SN = MFMA32(KN1, qf[BN][1], SN); SBAR(); EX4(SC, 4); PK8(p0, SC, 0); SBAR(); \
    o[BP] = MFMA32(VP1, BF(p1), o[BP]); SBAR(); EX4(SC, 8); SBAR(); \
    o[BC] = MFMA32(VC0, BF(p0), o[BC]); SBAR(); EX4(SC, 12); PK8(p1, SC, 8); SBAR(); } while (0)
#define LDK(K0, K1, P, t) do { K0 = *(const bf16x8*)(P + (t) * 1024 + lane * 8); K1 = *(const bf16x8*)(P + (t) * 1024 + 512 + lane * 8); } while (0)
#define DMAKV(t) do { const unsigned sl_ = ring + ((t) & 3) * 4096; \
    glds16(Kw + (t) * 1024 + lane * 8, sl_); glds16(Kw + (t) * 1024 + 512 + lane * 8, sl_ + 1024); \
    glds16(Vw + (t) * 1024 + lane * 8, sl_ + 2048); glds16(Vw + (t) * 1024 + 512 + lane * 8, sl_ + 3072); } while (0)
#define RDF(A0, A1, t, off) do { A0 = ldsr16(rp + ((t) & 3) * 4096 + off); A1 = ldsr16(rp + ((t) & 3) * 4096 + off + 1024); } while (0)
template <int N> DI void waitv() { asm volatile("s_waitcnt vmcnt(%0)" ::"i"(N) : "memory"); }
#define ATILE(KC0, KC1, VC0, VC1, KN0, KN1, VP0, VP1, t) do { \
    if ((t) < 4) { asm volatile("s_waitcnt lgkmcnt(0)" ::: "memory"); DMAKV((t) + 4); } \
    if ((t) < 7) { waitv<((t) < 4 ? 12 : 4 * (6 - (t)))>(); RDF(KN0, KN1, (t) + 1, 0); } \
    SBAR(); \
    ASTEP(sA, sB, KC0, KC1, 1, VP1, 3, VC0, 0); \
    if ((t) < 7) RDF(VP0, VP1, (t) + 1, 2048); \
    SBAR(); \
    ASTEP(sB, sA, KC0, KC1, 2, VC1, 0, VC0, 1); \
    ASTEP(sA, sB, KC0, KC1, 3, VC1, 1, VC0, 2); \
    ASTEP(sB, sA, KN0, KN1, 0, VC1, 2, VC0, 3); } while (0)
template <class PRE>
DI void attn_fast(const u16* __restrict__ Kf, const u16* __restrict__ Kw, const u16* __restrict__ Vw, bf16x8 (&qf)[4][2], f32x16 (&o)[4],
                  float (&mref)[4], int lane, int hi, char* ldsw, PRE pre) {
  const f32x16 zero = {};
  const unsigned ring = (unsigned)(uintptr_t)ldsw;
  const lds_cptr rp = (lds_cptr)ldsw + lane * 16;
#pragma unroll
  for (int b = 0; b < 4; ++b) o[b] = zero;
  bf16x8 ka0, ka1, kb0, kb1, va0, va1, vb0, vb1;
  LDK(kb0, kb1, Kf, 0);
  SBAR();
  DMAKV(0); DMAKV(1); DMAKV(2); DMAKV(3);
  pre();
  SBAR();
#pragma unroll
  for (int b = 0; b < 4; ++b) {
    (void)set_ref(qf[b][1], 0.f, hi);
    f32x16 s = MFMA32(kb0, qf[b][0], zero);
    s = MFMA32(kb1, qf[b][1], s);
    mref[b] = set_ref(qf[b][1], tile_max(s), hi);
  }
  waitv<12>();
  RDF(ka0, ka1, 0, 0);
  RDF(va0, va1, 0, 2048);
  vb0 = va0; vb1 = va1;
  f32x16 sA, sB;
  u32x4 p0, p1 = {0u, 0u, 0u, 0u};
  sA = MFMA32(ka0, qf[0][0], zero);
  sA = MFMA32(ka1, qf[0][1], sA);
  SBAR();
  STAMP(1, 8);
  ATILE(ka0, ka1, va0, va1, kb0, kb1, vb0, vb1, 0);
  ATILE(kb0, kb1, vb0, vb1, ka0, ka1, va0, va1, 1);
  ATILE(ka0, ka1, va0, va1, kb0, kb1, vb0, vb1, 2);
  ATILE(kb0, kb1, vb0, vb1, ka0, ka1, va0, va1, 3);
  ATILE(ka0, ka1, va0, va1, kb0, kb1, vb0, vb1, 4);
  ATILE(kb0, kb1, vb0, vb1, ka0, ka1, va0, va1, 5);
  ATILE(ka0, ka1, va0, va1, kb0, kb1, vb0, vb1, 6);
  ATILE(kb0, kb1, vb0, vb1, ka0, ka1, va0, va1, 7);
  o[3] = MFMA32(vb1, BF(p1), o[3]);
  SBAR();
  STAMP(1, 10);
}

constexpr int WPRE = 2;
__global__ __launch_bounds__(512) void k_attn(const u16* __restrict__ x, const u16* __restrict__ Qf, const u16* __restrict__ Kf,
                                              const u16* __restrict__ Vf, const u16* __restrict__ Wof, u16* __restrict__ H,
                                              u16* __restrict__ HN) {
  __shared__ __attribute__((aligned(16))) char lds[K2_LDS];
  const int tid = threadIdx.x, lane = tid & 63, w = __builtin_amdgcn_readfirstlane(tid >> 6), hi = lane >> 5;
  const int tg = blockIdx.x;
  STAMP(1, 0);
  bf16x8 qf[4][2];
#pragma unroll
  for (int b = 0; b < 4; ++b)
#pragma unroll
    for (int k = 0; k < 2; ++k) qf[b][k] = *(const bf16x8*)(Qf + ((size_t)((tg * 4 + b) * 2 + k) * 64 + lane) * 8);
  f32x16 o[4];
  float mref[4];
  const u16* Kw = Kf + (size_t)w * 8 * 1024;
  const u16* Vw = Vf + (size_t)w * 8 * 1024;
  const int kh = w >> 2, ng = w & 3;
  const u16* wop = Wof + ((size_t)(7 * ng * 14 + 7 * kh) * 64 + lane) * 8;
  const u16* const wob = Wof + (size_t)(7 * ng * 14 + 7 * kh) * 512;
  bf16x8 wf[7][7];
#define LDWO(n0, n1) do { _Pragma("unroll") for (int n_ = n0; n_ < n1; ++n_) _Pragma("unroll") for (int k_ = 0; k_ < 7; ++k_) \
    wf[n_][k_] = *(const bf16x8*)(wop + (n_ * 14 + k_) * 512); } while (0)
  attn_fast(Kf, Kw, Vw, qf, o, mref, lane, hi, lds + K2_P + w * 16384, [&]() { LDWO(0, WPRE); });
  {
    int l2 = lane;
    asm volatile("" : "+v"(l2));
    wop = wob + l2 * 8;
  }
  bool slow;
  {
    float chk = 0.f;
    bool bad = false;
#pragma unroll
    for (int b = 0; b < 4; ++b) {
#pragma unroll
      for (int r = 0; r < 16; ++r) chk = __builtin_fmaf(o[b][r], 0.f, chk);
      bad |= !(o[b][12] < 1.8e19f);
    }
    bad |= !(chk == 0.f);
    slow = __any(bad);
    if (slow) {
      attn_safe(Kw, Vw, qf, o, mref, lane, hi);
      LDWO(0, WPRE);
    }
  }
  STAMP(1, 1);
#pragma unroll
  for (int b = 0; b < 4; ++b) {
#pragma unroll
    for (int r4 = 0; r4 < 4; ++r4)
      *(f32x4*)(lds + K2_P + (((w * 4 + b) * 4 + r4) * 64 + lane) * 16) = (f32x4){o[b][4 * r4], o[b][4 * r4 + 1], o[b][4 * r4 + 2], o[b][4 * r4 + 3]};
    if (hi == 0) *(float*)(lds + K2_M + (w * 128 + b * 32 + lane) * 4) = mref[b];
  }
  if (lane == 0) *(unsigned*)(lds + K2_FL + w * 4) = slow ? 1u : 0u;
  const u32x4 xv = *(const u32x4*)(x + (size_t)(tg * 8 + w) * D + (lane < 56 ? lane : 55) * 8);
  SBAR();
#define WOGEMM() do { \
    const int g_ = lane >> 4; \
    bf16x8 bo[7]; \
    _Pragma("unroll") for (int k = 0; k < 7; ++k) bo[k] = *(const bf16x8*)(lds + K2_OT + ((4 * (7 * kh + k) + g_) * 8 + (lane & 7)) * 16); \
    f32x4 acc[7]; \
    _Pragma("unroll") for (int n = 0; n < 7; ++n) { \
      acc[n] = (f32x4){0.f, 0.f, 0.f, 0.f}; \
      _Pragma("unroll") for (int k = 0; k < 7; ++k) acc[n] = MFMA16(wf[n][k], bo[k], acc[n]); \
    } \
    if ((lane & 15) < 8) { \
      _Pragma("unroll") for (int n = 0; n < 7; ++n) *(f32x4*)(lds + K2_HB + ((kh * 8 + (lane & 15)) * D + 16 * (7 * ng + n) + 4 * g_) * 4) = acc[n]; \
    } } while (0)
  if (w < 4) {
    LDWO(WPRE, 7);
    SBAR();
    STAMP(1, 5);
    LBAR();
    STAMP(1, 6);
    const u32x4 f0 = *(const u32x4*)(lds + K2_FL), f1 = *(const u32x4*)(lds + K2_FL + 16);
    const bool gen = __builtin_amdgcn_readfirstlane(f0[0] | f0[1] | f0[2] | f0[3] | f1[0] | f1[1] | f1[2] | f1[3]) != 0;
    const int c = lane & 31, r4 = w;
#pragma unroll
    for (int b = 0; b < 4; ++b) {
      float l = 0.f;
      f32x4 a = {0.f, 0.f, 0.f, 0.f};
      if (!gen) {
#pragma unroll
        for (int v = 0; v < 8; ++v) {
          l += *(const float*)(lds + K2_P + (((v * 4 + b) * 4 + 3) * 64 + 32 + c) * 16);
          a += *(const f32x4*)(lds + K2_P + (((v * 4 + b) * 4 + r4) * 64 + lane) * 16);
        }
      } else {
        float M = -INFINITY;
#pragma unroll
        for (int v = 0; v < 8; ++v) M = __builtin_fmaxf(M, *(const float*)(lds + K2_M + (v * 128 + 32 * b + c) * 4));
#pragma unroll 1
        for (int v = 0; v < 8; ++v) {
          const float fc = __builtin_amdgcn_exp2f(*(const float*)(lds + K2_M + (v * 128 + 32 * b + c) * 4) - M);
          l += fc * *(const float*)(lds + K2_P + (((v * 4 + b) * 4 + 3) * 64 + 32 + c) * 16);
          a += fc * *(const f32x4*)(lds + K2_P + (((v * 4 + b) * 4 + r4) * 64 + lane) * 16);
        }
      }
      const float il = 1.f / l;
      const int d = 8 * r4 + 4 * hi;
      if (d < 28) {
        const int k = 28 * (4 * b + (c >> 3)) + d;
        *(u32x2*)(lds + K2_OT + ((k >> 3) * 8 + (c & 7)) * 16 + (k & 7) * 2) = (u32x2){cvtpk(a[0] * il, a[1] * il), cvtpk(a[2] * il, a[3] * il)};
      }
      SBAR();
    }
    LBAR();
    STAMP(1, 2);
    WOGEMM();
  } else {
    STAMP(1, 5);
    LBAR();
    STAMP(1, 6);
    LDWO(WPRE, 7);
    SBAR();
    LBAR();
    STAMP(1, 2);
    WOGEMM();
  }
  LBAR();
  STAMP(1, 3);
  {
    const int s = tg * 8 + w;
    const bool act = lane < 56;
    const int lc = act ? lane : 55;
    float v[8];
    const f32x4 a0 = *(const f32x4*)(lds + K2_HB + (w * D + lc * 8) * 4), a1 = *(const f32x4*)(lds + K2_HB + (w * D + lc * 8 + 4) * 4);
    const f32x4 c0 = *(const f32x4*)(lds + K2_HB + ((8 + w) * D + lc * 8) * 4), c1 = *(const f32x4*)(lds + K2_HB + ((8 + w) * D + lc * 8 + 4) * 4);
    u32x4 hv;
#pragma unroll
    for (int i = 0; i < 2; ++i) {
      hv[i] = cvtpk(bflo(xv[i]) + a0[2 * i] + c0[2 * i], bfhi(xv[i]) + a0[2 * i + 1] + c0[2 * i + 1]);
      hv[2 + i] = cvtpk(bflo(xv[2 + i]) + a1[2 * i] + c1[2 * i], bfhi(xv[2 + i]) + a1[2 * i + 1] + c1[2 * i + 1]);
    }
    if (act) *(u32x4*)(H + (size_t)s * D + lane * 8) = hv;
#pragma unroll
    for (int i = 0; i < 4; ++i) { v[2 * i] = act ? bflo(hv[i]) : 0.f; v[2 * i + 1] = act ? bfhi(hv[i]) : 0.f; }
#pragma unroll
    for (int pass = 0; pass < 2; ++pass) {
      float sm = 0.f;
#pragma unroll
      for (int i = 0; i < 8; ++i) sm += v[i];
      const float mean = wave_sum(sm) * (1.f / D);
      float q = 0.f;
#pragma unroll
      for (int i = 0; i < 8; ++i) { v[i] = act ? v[i] - mean : 0.f; q += v[i] * v[i]; }
      const float rs = __builtin_amdgcn_rsqf(wave_sum(q) * (1.f / D) + LN_EPS);
#pragma unroll
      for (int i = 0; i < 8; ++i) v[i] *= rs;
    }
    if (act) *(u32x4*)(lds + K2_HS + w * 912 + lane * 16) = (u32x4){cvtpk(v[0], v[1]), cvtpk(v[2], v[3]), cvtpk(v[4], v[5]), cvtpk(v[6], v[7])};
  }
  LBAR();
  if (tid < D / 2) {
    unsigned r[8];
#pragma unroll
    for (int i = 0; i < 8; ++i) r[i] = *(const unsigned*)(lds + K2_HS + i * 912 + tid * 4);
    u32x4 lo, hi2;
#pragma unroll
    for (int i = 0; i < 4; ++i) {
      lo[i] = (r[2 * i] & 0xffffu) | (r[2 * i + 1] << 16);
      hi2[i] = (r[2 * i] >> 16) | (r[2 * i + 1] & 0xffff0000u);
    }
    u32x4* dst = (u32x4*)(HN + ((size_t)tg * D + 2 * tid) * 8);
    dst[0] = lo; dst[1] = hi2;
  }
  STAMP(1, 4);
}

constexpr int G_RING = 4, G_DUM = 8 * G_RING * 4096, G_LDS = G_DUM + 8192;
template <int PH>
__global__ __launch_bounds__(512) void k_mlp(const u16* __restrict__ act, const u16* __restrict__ W, const u16* __restrict__ bias,
                                             const u16* __restrict__ H, u16* __restrict__ out) {
  constexpr int KT = PH == 0 ? S : F, LDW = PH == 0 ? F : S, NK = KT / 256, NWG = 7 * (LDW / 64);
  __shared__ __attribute__((aligned(16))) char lds[G_LDS];
  const int tid = threadIdx.x, lane = tid & 63, w = __builtin_amdgcn_readfirstlane(tid >> 6);
  const int t = xcd_remap(blockIdx.x, NWG), nt = t / 7, mt = t - nt * 7;
  const unsigned lds0 = (unsigned)(uintptr_t)lds + w * (G_RING * 4096);
  const int g = lane >> 4, q4 = (lane & 15) >> 2, p = lane & 3, l15 = lane & 15;
  STAMP(2 + PH, 0);
  __builtin_amdgcn_s_setprio(3);
  const u16* asrc = act + ((size_t)(4 * w + g) * D + 64 * mt + l15) * 8;
  const int r8 = lane >> 3, fe = (r8 >> 1) & 1;
  const u16* wsrc0 = W + (size_t)(32 * w + r8) * LDW + 64 * nt + ((lane & 7) ^ (fe << 1)) * 8;
  const u16* wsrc1 = W + (size_t)(32 * w + 8 + r8) * LDW + 64 * nt + ((lane & 7) ^ ((fe | 2) << 1)) * 8;
  bf16x8 af[NK][4];
#define STAGE(i) do { const unsigned sl_ = lds0 + ((i) % G_RING) * 4096; const size_t o_ = (size_t)(i) * 256 * LDW; \
    glds16(wsrc0 + o_, sl_); glds16(wsrc1 + o_, sl_ + 1024); glds16(wsrc0 + o_ + 16 * LDW, sl_ + 2048); glds16(wsrc1 + o_ + 16 * LDW, sl_ + 3072); \
    const u16* ap_ = asrc + (size_t)(i) * 32 * D * 8; \
    gload16<0>(af[i][0], ap_); gload16<256>(af[i][1], ap_); gload16<512>(af[i][2], ap_); gload16<768>(af[i][3], ap_); } while (0)
#pragma unroll
  for (int i = 0; i < G_RING; ++i) STAGE(i);
  STAMP(2 + PH, 1);
  const int f = ((q4 >> 1) & 1) | ((g & 1) << 1);
  int woff[4];
#pragma unroll
  for (int nb = 0; nb < 4; ++nb) woff[nb] = w * (G_RING * 4096) + (8 * g + q4) * 128 + (((2 * nb + (p >> 1)) ^ (f << 1)) * 16) + 8 * (p & 1);
  f32x4 acc[4][4] = {};
#pragma unroll
  for (int i = 0; i < NK; ++i) {
    constexpr int PF = PH == 0 ? 5 : 0;
    const int rem = NK - 1 - i < G_RING - 1 ? NK - 1 - i : G_RING - 1;
    const bool pfd = i + G_RING > NK;
    if (i == 0) __builtin_amdgcn_s_setprio(3);
    else if (i == 2) __builtin_amdgcn_s_setprio(2);
    else if (i == 4) __builtin_amdgcn_s_setprio(1);
    else if (i == 6) __builtin_amdgcn_s_setprio(0);
    if (rem == 3) { if (pfd) waitv4<24 + PF>(af[i][0], af[i][1], af[i][2], af[i][3]); else waitv4<24>(af[i][0], af[i][1], af[i][2], af[i][3]); }
    else if (rem == 2) waitv4<16 + PF>(af[i][0], af[i][1], af[i][2], af[i][3]);
    else if (rem == 1) waitv4<8 + PF>(af[i][0], af[i][1], af[i][2], af[i][3]);
    else waitv4<PF>(af[i][0], af[i][1], af[i][2], af[i][3]);
    bf16x8 wf[4];
#pragma unroll
    for (int nb = 0; nb < 4; ++nb) {
      const lds_cptr sp = (lds_cptr)lds + woff[nb] + (i % G_RING) * 4096;
      wf[nb] = cat8(trread(sp), trread(sp + 512));
    }
    if (i + G_RING < NK) {
      asm volatile("s_waitcnt lgkmcnt(0)" ::: "memory");
      STAGE(i + G_RING);
    }
    if (PH == 0 && i + G_RING == NK) {
      const int xg = blockIdx.x & 7, rk = blockIdx.x >> 3;
      const unsigned dum = (unsigned)(uintptr_t)lds + G_DUM + w * 1024;
#pragma unroll
      for (int j = 0; j < 5; ++j) {
        int id = ((rk < 24 ? rk : 23) * 8 + w) * 5 + j;
        id = id < 896 ? id : 895;
        glds16(H + (size_t)((id >> 2) * 8 + (lane >> 3)) * S + (4 * xg + (id & 3)) * 64 + (lane & 7) * 8, dum);
      }
    }
#pragma unroll
    for (int nb = 0; nb < 4; ++nb)
#pragma unroll
      for (int mb = 0; mb < 4; ++mb)
        acc[nb][mb] = PH == 0 ? MFMA16(wf[nb], af[i][mb], acc[nb][mb]) : MFMA16(af[i][mb], wf[nb], acc[nb][mb]);
  }
#undef STAGE
  STAMP(2 + PH, 4);
  const int enb = w >> 1, emb = 2 * (w & 1);
  u32x2 eb, eh0, eh1;
  if (PH == 0) {
    eb = *(const u32x2*)(bias + 64 * nt + 16 * enb + 4 * g);
  } else {
    const int s = 64 * nt + 16 * enb + l15;
    eb[0] = bias[s];
    eh0 = *(const u32x2*)(H + (size_t)s * D + 64 * mt + 16 * emb + 4 * g);
    eh1 = *(const u32x2*)(H + (size_t)s * D + 64 * mt + 16 * emb + 16 + 4 * g);
  }
  {
    f32x4* xb = (f32x4*)lds;
#pragma unroll
    for (int nb = 0; nb < 4; ++nb)
#pragma unroll
      for (int mb = 0; mb < 4; ++mb) xb[(w * 16 + nb * 4 + mb) * 64 + lane] = acc[nb][mb];
  }
  LBAR();
  {
    const f32x4* xb = (const f32x4*)lds;
#pragma unroll
    for (int e = 0; e < 2; ++e) {
      f32x4 a = xb[(2 * w + e) * 64 + lane];
#pragma unroll
      for (int v = 1; v < 8; ++v) a += xb[(v * 16 + 2 * w + e) * 64 + lane];
      const int mb = emb + e;
      if (PH == 0) {
        const int fi = 64 * nt + 16 * enb + 4 * g, d = 64 * mt + 16 * mb + l15;
        float z[4] = {a[0] + bflo(eb[0]), a[1] + bfhi(eb[0]), a[2] + bflo(eb[1]), a[3] + bfhi(eb[1])};
#pragma unroll
        for (int i = 0; i < 4; ++i) {
          const float zz = z[i], a2 = zz * (1.f + 0.044715f * zz * zz) * (-2.f * 0.7978845608028654f * 1.4426950408889634f);
          z[i] = zz * __builtin_amdgcn_rcpf(1.f + __builtin_amdgcn_exp2f(a2));
        }
        *(u32x2*)(out + ((size_t)(fi >> 3) * D + d) * 8 + (fi & 7)) = (u32x2){cvtpk(z[0], z[1]), cvtpk(z[2], z[3])};
      } else {
        const int d = 64 * mt + 16 * mb + 4 * g, s = 64 * nt + 16 * enb + l15;
        const float bb = bflo(eb[0]);
        const u32x2 hv = e ? eh1 : eh0;
        *(u32x2*)(out + (size_t)s * D + d) =
            (u32x2){cvtpk(a[0] + bb + bflo(hv[0]), a[1] + bb + bfhi(hv[0])), cvtpk(a[2] + bb + bflo(hv[1]), a[3] + bb + bfhi(hv[1]))};
      }
    }
  }
  if (PH == 0) asm volatile("s_waitcnt vmcnt(0)" ::: "memory");
  STAMP(2 + PH, 5);
}

extern "C" void kernel_launch(void* const* d_in, const int* in_sizes, int n_in, void* d_out, int out_size, void* d_ws, size_t ws_size,
                              hipStream_t stream) {
  const u16* x = (const u16*)d_in[0];
  const u16* wq = (const u16*)d_in[1];
  const u16* wk = (const u16*)d_in[2];
  const u16* wv = (const u16*)d_in[3];
  const u16* wo = (const u16*)d_in[4];
  const u16* w1 = (const u16*)d_in[6];
  const u16* b1 = (const u16*)d_in[7];
  const u16* w2 = (const u16*)d_in[8];
  const u16* b2 = (const u16*)d_in[9];
  char* ws = (char*)d_ws;
  u16 *Qf = (u16*)(ws + WS_QF), *Kf = (u16*)(ws + WS_KF), *Vf = (u16*)(ws + WS_VF), *Wof = (u16*)(ws + WS_WOF);
  u16 *H = (u16*)(ws + WS_H), *HN = (u16*)(ws + WS_HN), *U = (u16*)(ws + WS_U);
  k_qkv<<<256, 512, 0, stream>>>(x, wq, wk, wv, wo, w1, Qf, Kf, Vf, Wof);
  k_attn<<<256, 512, 0, stream>>>(x, Qf, Kf, Vf, Wof, H, HN);
  k_mlp<0><<<196, 512, 0, stream>>>(HN, w1, b1, w2, U);
  k_mlp<1><<<224, 512, 0, stream>>>(U, w2, b2, H, (u16*)d_out);
}
```

```cpp
#include <hip/hip_runtime.h>
#include <hip/hip_bf16.h>

typedef unsigned short u16;
using bf16x8 = __attribute__((ext_vector_type(8))) short;
using s16x4 = __attribute__((ext_vector_type(4))) short;
using f32x4 = __attribute__((ext_vector_type(4))) float;
using f32x16 = __attribute__((ext_vector_type(16))) float;
using u32x4 = __attribute__((ext_vector_type(4))) unsigned;
using u32x2 = __attribute__((ext_vector_type(2))) unsigned;
typedef short v4i16_t __attribute__((ext_vector_type(4)));
typedef __attribute__((address_space(3))) const char* lds_cptr;
typedef __bf16 bf16x2_t __attribute__((ext_vector_type(2)));
typedef float f32x2_t __attribute__((ext_vector_type(2)));

#define DI __device__ __forceinline__
#define MFMA32(a, b, c) __builtin_amdgcn_mfma_f32_32x32x16_bf16(a, b, c, 0, 0, 0)
#define MFMA16(a, b, c) __builtin_amdgcn_mfma_f32_16x16x32_bf16(a, b, c, 0, 0, 0)
#define SBAR() __builtin_amdgcn_sched_barrier(0)
#define LBAR() asm volatile("s_waitcnt lgkmcnt(0)\n\ts_barrier" ::: "memory")
#define WAITV_BAR(N) asm volatile("s_waitcnt vmcnt(" #N ") lgkmcnt(0)\n\ts_barrier" ::: "memory")

constexpr int S = 2048, D = 448, DH = 28, F = 1792;
constexpr float QSCALE = 0.18898223650461362f * 1.4426950408889634f;
constexpr float LN_EPS = 1e-5f;

constexpr size_t WS_QF = 0;
constexpr size_t WS_KF = WS_QF + 2097152;
constexpr size_t WS_VF = WS_KF + 131072;
constexpr size_t WS_WOF = WS_VF + 131072;
constexpr size_t WS_H = WS_WOF + 401408;
constexpr size_t WS_HN = WS_H + 1835008;
constexpr size_t WS_U = WS_HN + 1835008;

#define STAMP(k, i)
#define CSTAMP(k, i)

DI unsigned cvtpk(float lo, float hi) {
  const f32x2_t v = {lo, hi};
  return __builtin_bit_cast(unsigned, __builtin_convertvector(v, bf16x2_t));
}
DI float bflo(unsigned u) { return __uint_as_float(u << 16); }
DI float bfhi(unsigned u) { return __uint_as_float(u & 0xffff0000u); }
template <int CTRL> DI float dppf(float v) {
  return __uint_as_float(__builtin_amdgcn_update_dpp(0u, __float_as_uint(v), CTRL, 0xf, 0xf, true));
}
DI float wave_sum(float v) {
  v += dppf<0xB1>(v);
  v += dppf<0x4E>(v);
  v += dppf<0x141>(v);
  v += dppf<0x140>(v);
  auto a = __builtin_amdgcn_permlane16_swap(__float_as_uint(v), __float_as_uint(v), false, false);
  v = __uint_as_float(a[0]) + __uint_as_float(a[1]);
  auto b = __builtin_amdgcn_permlane32_swap(__float_as_uint(v), __float_as_uint(v), false, false);
  return __uint_as_float(b[0]) + __uint_as_float(b[1]);
}
DI float half_max(float v) {
  auto b = __builtin_amdgcn_permlane32_swap(__float_as_uint(v), __float_as_uint(v), false, false);
  return __builtin_fmaxf(__uint_as_float(b[0]), __uint_as_float(b[1]));
}
DI void glds16(const void* g, unsigned lds_base) {
  unsigned sv;
  asm volatile("s_mov_b32 %0, m0\n\ts_mov_b32 m0, %2\n\ts_nop 0\n\tglobal_load_lds_dwordx4 %1, off\n\ts_mov_b32 m0, %0"
               : "=&s"(sv) : "v"(g), "s"(lds_base) : "memory");
}
DI void glds4(const void* g, unsigned lds_base) {
  unsigned sv;
  asm volatile("s_mov_b32 %0, m0\n\ts_mov_b32 m0, %2\n\ts_nop 0\n\tglobal_load_lds_dword %1, off\n\ts_mov_b32 m0, %0"
               : "=&s"(sv) : "v"(g), "s"(lds_base) : "memory");
}
template <int N> DI void waitv4(bf16x8& a, bf16x8& b, bf16x8& c, bf16x8& d) {
  asm volatile("s_waitcnt vmcnt(%4)" : "+v"(a), "+v"(b), "+v"(c), "+v"(d) : "i"(N) : "memory");
}
template <int OFF> DI void gload16(bf16x8& d, const void* p) {
  asm volatile("global_load_dwordx4 %0, %1, off offset:%2" : "=v"(d) : "v"(p), "i"(OFF) : "memory");
}
DI s16x4 trread(lds_cptr p) {
  return __builtin_bit_cast(s16x4, __builtin_amdgcn_ds_read_tr16_b64_v4i16((__attribute__((address_space(3))) v4i16_t*)p));
}
DI bf16x8 ldsr16(lds_cptr p) { return *(const __attribute__((address_space(3))) bf16x8*)p; }
DI bf16x8 cat8(s16x4 a, s16x4 b) { return __builtin_shufflevector(a, b, 0, 1, 2, 3, 4, 5, 6, 7); }
DI int xcd_remap(int bid, int nwg) {
  const int q = nwg >> 3, r = nwg & 7, x = bid & 7, o = bid >> 3;
  return (x < r ? x * (q + 1) : r * (q + 1) + (x - r) * q) + o;
}

constexpr int K1_WI = 448 * 128, K1_X = 2 * K1_WI, K1_XS = 528, K1_LDS = K1_X + 64 * K1_XS;
__global__ __launch_bounds__(512) void k_qkv(const u16* __restrict__ x, const u16* __restrict__ wq, const u16* __restrict__ wk,
                                             const u16* __restrict__ wv, const u16* __restrict__ wo, u16* __restrict__ Qf,
                                             u16* __restrict__ Kf, u16* __restrict__ Vf, u16* __restrict__ Wof) {
  __shared__ __attribute__((aligned(16))) char lds[K1_LDS];
  const int tid = threadIdx.x, lane = tid & 63, w = __builtin_amdgcn_readfirstlane(tid >> 6);
  const int t = xcd_remap(blockIdx.x, 256), rt = t >> 2, ct = t & 3;
  const unsigned lds0 = (unsigned)(uintptr_t)lds;
  STAMP(0, 0);
  bf16x8 xr[4];
  const bool act = lane < 56;
  {
    const u16* xp_ = x + (size_t)(rt * 32 + w * 4) * D + (act ? lane : 55) * 8;
    gload16<0>(xr[0], xp_); gload16<896>(xr[1], xp_); gload16<1792>(xr[2], xp_); gload16<2688>(xr[3], xp_);
  }
#pragma unroll
  for (int im = 0; im < 2; ++im) {
    const int c64 = 2 * ct + im;
    if (c64 < 7) {
#pragma unroll
      for (int i = 0; i < 7; ++i) {
        const int piece = w * 7 + i, row = piece * 8 + (lane >> 3);
        const int f = ((row >> 1) & 1) | (((row >> 3) & 1) << 1);
        const int c = (lane & 7) ^ (f << 1);
        glds16(wq + (size_t)row * D + c64 * 64 + c * 8, lds0 + im * K1_WI + piece * 1024);
      }
    } else {
#pragma unroll
      for (int i = 0; i < 28; ++i) {
        const int piece = w * 28 + i, row = piece * 2 + (lane >> 5), pp = lane & 31;
        const int f = ((row >> 1) & 1) | (((row >> 3) & 1) << 1);
        const int col = ((((pp >> 2) ^ (f << 1)) << 2) + (pp & 3)) * 2;
        const u16* src = col < 28 ? wk + row * DH + col : wv + row * DH + (col < 56 ? col - 28 : 0);
        glds4(src, lds0 + im * K1_WI + piece * 256);
      }
    }
  }
  u16 wt[8];
  const int frag0 = blockIdx.x + 256 * (w & 1);
  const bool dofrag = w < 2 && frag0 < 392;
  const int frag = dofrag ? frag0 : 0;
  {
    const int nb = frag / 14, ks = frag - nb * 14;
#pragma unroll
    for (int j = 0; j < 8; ++j) wt[j] = wo[(size_t)(32 * ks + 8 * (lane >> 4) + j) * D + 16 * nb + (lane & 15)];
  }
  SBAR();
  if (ct < 3) waitv4<22>(xr[0], xr[1], xr[2], xr[3]);
  else waitv4<43>(xr[0], xr[1], xr[2], xr[3]);
  {
    u32x4 xv[4];
#pragma unroll
    for (int r = 0; r < 4; ++r) xv[r] = __builtin_bit_cast(u32x4, xr[r]);
    float v[4][8], mean[4], rs[4];
#pragma unroll
    for (int r = 0; r < 4; ++r) {
      float s = 0.f;
#pragma unroll
      for (int i = 0; i < 4; ++i) {
        v[r][2 * i] = act ? bflo(xv[r][i]) : 0.f; v[r][2 * i + 1] = act ? bfhi(xv[r][i]) : 0.f;
        s += v[r][2 * i] + v[r][2 * i + 1];
      }
      mean[r] = s;
    }
#pragma unroll
    for (int r = 0; r < 4; ++r) mean[r] = wave_sum(mean[r]) * (1.f / D);
#pragma unroll
    for (int r = 0; r < 4; ++r) {
      float q = 0.f;
#pragma unroll
      for (int i = 0; i < 8; ++i) { v[r][i] = act ? v[r][i] - mean[r] : 0.f; q += v[r][i] * v[r][i]; }
      rs[r] = q;
    }
#pragma unroll
    for (int r = 0; r < 4; ++r) rs[r] = __builtin_amdgcn_rsqf(wave_sum(rs[r]) * (1.f / D) + LN_EPS);
    char* xdst = lds + K1_X + lane * K1_XS + w * 64;
#pragma unroll
    for (int r = 0; r < 4; ++r) {
      u32x4 o;
#pragma unroll
      for (int i = 0; i < 4; ++i) o[i] = cvtpk(v[r][2 * i] * rs[r], v[r][2 * i + 1] * rs[r]);
      *(u32x4*)(xdst + r * 16) = o;
    }
  }
  STAMP(0, 1);
  WAITV_BAR(0);
  STAMP(0, 2);
  const int nb = w & 3, im = w >> 2, c64 = 2 * ct + im, g = lane >> 4, q4 = (lane & 15) >> 2, p = lane & 3, l15 = lane & 15;
  const int f = ((q4 >> 1) & 1) | ((g & 1) << 1);
  const lds_cptr wp = (lds_cptr)lds + im * K1_WI + (8 * g + q4) * 128 + (((2 * nb + (p >> 1)) ^ (f << 1)) * 16) + 8 * (p & 1);
  const lds_cptr xp = (lds_cptr)lds + K1_X + g * K1_XS + l15 * 16;
  f32x4 acc0 = {0.f, 0.f, 0.f, 0.f}, acc1 = {0.f, 0.f, 0.f, 0.f};
  {
    bf16x8 a[14], b0[14], b1[14];
#pragma unroll
    for (int ks = 0; ks < 14; ++ks) {
      a[ks] = cat8(trread(wp + ks * 4096), trread(wp + ks * 4096 + 512));
      b0[ks] = ldsr16(xp + ks * 4 * K1_XS);
      b1[ks] = ldsr16(xp + ks * 4 * K1_XS + 256);
    }
    SBAR();
#pragma unroll
    for (int ks = 0; ks < 14; ++ks) {
      acc0 = MFMA16(a[ks], b0[ks], acc0);
      acc1 = MFMA16(a[ks], b1[ks], acc1);
    }
  }
  STAMP(0, 3);
#pragma unroll
  for (int tb = 0; tb < 2; ++tb) {
    const f32x4 a = tb ? acc1 : acc0;
    const int s = rt * 32 + 16 * tb + l15;
    const int nl = 16 * nb + 4 * g;
    if (c64 < 7) {
      const int n0 = 64 * c64 + nl, h = n0 / 28, d0 = n0 - 28 * h;
      const int tg = s >> 3, c = (h & 3) * 8 + (s & 7), b = h >> 2;
      const size_t off = ((((size_t)(tg * 4 + b) * 2 + (d0 >> 4)) * 64) + ((d0 >> 3) & 1) * 32 + c) * 8 + (d0 & 7);
      *(u32x2*)(Qf + off) = (u32x2){cvtpk(a[0] * QSCALE, a[1] * QSCALE), cvtpk(a[2] * QSCALE, a[3] * QSCALE)};
    } else {
      const int kt = s >> 5, kk = s & 31;
      if (nl < 28 || nl == 56) {
        const int d0 = nl < 28 ? nl : 28;
        const size_t off = ((((size_t)kt * 2 + (d0 >> 4)) * 64) + ((d0 >> 3) & 1) * 32 + kk) * 8 + (d0 & 7);
        u32x2 v = {cvtpk(a[0], a[1]), cvtpk(a[2], a[3])};
        if (nl == 56) v = (u32x2){0x3f803f80u, 0x00003f80u};
        *(u32x2*)(Kf + off) = v;
      } else {
        const int d0 = nl < 56 ? nl - 28 : 28;
        const int sp = kk >> 4, j = ((kk >> 3) & 1) * 4 + (kk & 3), hi = (kk >> 2) & 1;
        const size_t off = ((((size_t)kt * 2 + sp) * 64) + hi * 32 + d0) * 8 + j;
        unsigned v0 = cvtpk(a[0], a[1]), v1 = cvtpk(a[2], a[3]);
        if (nl == 60) { v0 = 0x00003f80u; v1 = 0u; }
        Vf[off] = (u16)v0; Vf[off + 8] = (u16)(v0 >> 16); Vf[off + 16] = (u16)v1; Vf[off + 24] = (u16)(v1 >> 16);
      }
    }
  }
  if (dofrag) {
    u32x4 o;
#pragma unroll
    for (int i = 0; i < 4; ++i) o[i] = (unsigned)wt[2 * i] | ((unsigned)wt[2 * i + 1] << 16);
    *(u32x4*)(Wof + ((size_t)frag * 64 + lane) * 8) = o;
  }
  STAMP(0, 4);
}

constexpr int K2_RING = 0, K2_OT = 131072, K2_DUM = K2_OT + 7168, K2_LDS = K2_DUM + 4096;
constexpr int K2_HB = 0, K2_HS = 32768;

DI float set_ref(bf16x8& q1, float m, int hi) {
  const float a = bflo(cvtpk(-m, 0.f)), r1 = -m - a, b = bflo(cvtpk(r1, 0.f)), c = bflo(cvtpk(r1 - b, 0.f));
  u32x4 v = __builtin_bit_cast(u32x4, q1);
  v[2] = hi ? cvtpk(a, b) : v[2];
  v[3] = hi ? cvtpk(c, 0.f) : v[3];
  q1 = __builtin_bit_cast(bf16x8, v);
  return -((a + b) + c);
}
DI float tile_max(const f32x16& s) {
  float a = __builtin_fmaxf(__builtin_fmaxf(s[0], s[1]), s[2]), b = __builtin_fmaxf(__builtin_fmaxf(s[3], s[4]), s[5]);
  a = __builtin_fmaxf(__builtin_fmaxf(a, s[6]), s[7]); b = __builtin_fmaxf(__builtin_fmaxf(b, s[8]), s[9]);
  a = __builtin_fmaxf(__builtin_fmaxf(a, s[10]), s[11]); b = __builtin_fmaxf(__builtin_fmaxf(b, s[12]), s[13]);
  a = __builtin_fmaxf(__builtin_fmaxf(a, s[14]), s[15]);
  return half_max(__builtin_fmaxf(a, b));
}
template <int N> DI void waitv() { asm volatile("s_waitcnt vmcnt(%0)" ::"i"(N) : "memory"); }
#define RBAR() asm volatile("s_barrier" ::: "memory")

DI void attn_safe(const u16* __restrict__ Kf, const u16* __restrict__ Vf, bf16x8 q0, bf16x8 q1, f32x16& o, int lane, int hi) {
  const f32x16 zero = {};
  float mref = 0.f;
  o = zero;
#pragma unroll 1
  for (int t = 0; t < 64; ++t) {
    const bf16x8 k0 = *(const bf16x8*)(Kf + t * 1024 + lane * 8), k1 = *(const bf16x8*)(Kf + t * 1024 + 512 + lane * 8);
    const bf16x8 v0 = *(const bf16x8*)(Vf + t * 1024 + lane * 8), v1 = *(const bf16x8*)(Vf + t * 1024 + 512 + lane * 8);
    (void)set_ref(q1, 0.f, hi);
    f32x16 s = MFMA32(k0, q0, zero);
    s = MFMA32(k1, q1, s);
    const float tm = tile_max(s);
    const float mn = set_ref(q1, t == 0 ? tm : __builtin_fmaxf(tm, mref), hi);
    const float al = t == 0 ? 0.f : __builtin_amdgcn_exp2f(mref - mn);
#pragma unroll
    for (int r = 0; r < 16; ++r) o[r] *= al;
    mref = mn;
    s = MFMA32(k0, q0, zero);
    s = MFMA32(k1, q1, s);
    u32x4 p0, p1;
#pragma unroll
    for (int i = 0; i < 4; ++i) {
      p0[i] = cvtpk(__builtin_amdgcn_exp2f(s[2 * i]), __builtin_amdgcn_exp2f(s[2 * i + 1]));
      p1[i] = cvtpk(__builtin_amdgcn_exp2f(s[8 + 2 * i]), __builtin_amdgcn_exp2f(s[8 + 2 * i + 1]));
    }
    o = MFMA32(v0, __builtin_bit_cast(bf16x8, p0), o);
    o = MFMA32(v1, __builtin_bit_cast(bf16x8, p1), o);
  }
}

#define EX4(S, i) do { S[i] = __builtin_amdgcn_exp2f(S[i]); S[i + 1] = __builtin_amdgcn_exp2f(S[i + 1]); \
    S[i + 2] = __builtin_amdgcn_exp2f(S[i + 2]); S[i + 3] = __builtin_amdgcn_exp2f(S[i + 3]); } while (0)
#define PK8(P, S, i) do { P[0] = cvtpk(S[i], S[i + 1]); P[1] = cvtpk(S[i + 2], S[i + 3]); P[2] = cvtpk(S[i + 4], S[i + 5]); P[3] = cvtpk(S[i + 6], S[i + 7]); } while (0)
#define BF(P) __builtin_bit_cast(bf16x8, P)
constexpr int CPRE = 35;
#define RDT(t, off) ldsr16((((t) & 16) ? rph : rp) + ((t) & 15) * 4096 + (off))
#define CSTEP(SC, SN, j) do { \
    if (((j) & 7) == 6 && (j) < 56) RBAR(); \
    SN = MFMA32(k0, q0, zero); SBAR(); if ((j) + 2 < 64) k0 = RDT((j) + 2, 0); EX4(SC, 0); SBAR(); \
    SN = MFMA32(k1, q1, SN); SBAR(); if ((j) + 2 < 64) k1 = RDT((j) + 2, 1024); EX4(SC, 4); PK8(p0, SC, 0); \
    if ((j) >= 2 && (j) < 2 + CPRE) wf[((j) - 2) / 7][((j) - 2) % 7] = *(const bf16x8*)(wop + ((((j) - 2) / 7) * 14 + ((j) - 2) % 7) * 512); \
    SBAR(); \
    o1 = MFMA32(v1, BF(p1), o1); SBAR(); v1 = RDT(j, 3072); EX4(SC, 8); SBAR(); \
    o0 = MFMA32(v0, BF(p0), o0); SBAR(); if ((j) + 1 < 64) v0 = RDT((j) + 1, 2048); EX4(SC, 12); PK8(p1, SC, 8); SBAR(); } while (0)

__global__ __launch_bounds__(512) void k_attn(const u16* __restrict__ x, const u16* __restrict__ Qf, const u16* __restrict__ Kf,
                                              const u16* __restrict__ Vf, const u16* __restrict__ Wof, const u16* __restrict__ w1,
                                              const u16* __restrict__ w2, u16* __restrict__ H, u16* __restrict__ HN) {
  __shared__ __attribute__((aligned(16))) char lds[K2_LDS];
  const int tid = threadIdx.x, lane = tid & 63, w = __builtin_amdgcn_readfirstlane(tid >> 6), hi = lane >> 5;
  const int tg = blockIdx.x;
  STAMP(1, 0);
  const int kh = w >> 2, ng = w & 3;
  const u16* wop = Wof + ((size_t)(7 * ng * 14 + 7 * kh) * 64 + lane) * 8;
  bf16x8 wf[7][7];
#define LDWO(i0, i1) do { _Pragma("unroll") for (int i_ = i0; i_ < i1; ++i_) wf[i_ / 7][i_ % 7] = *(const bf16x8*)(wop + ((i_ / 7) * 14 + i_ % 7) * 512); } while (0)
#define WOGEMM() do { \
    const int g_ = lane >> 4; \
    bf16x8 bo[7]; \
    _Pragma("unroll") for (int k = 0; k < 7; ++k) bo[k] = *(const bf16x8*)(lds + K2_OT + ((4 * (7 * kh + k) + g_) * 8 + (lane & 7)) * 16); \
    f32x4 acc[7]; \
    _Pragma("unroll") for (int n = 0; n < 7; ++n) { \
      acc[n] = (f32x4){0.f, 0.f, 0.f, 0.f}; \
      _Pragma("unroll") for (int k = 0; k < 7; ++k) acc[n] = MFMA16(wf[n][k], bo[k], acc[n]); \
    } \
    if ((lane & 15) < 8) { \
      _Pragma("unroll") for (int n = 0; n < 7; ++n) *(f32x4*)(lds + K2_HB + ((kh * 8 + (lane & 15)) * D + 16 * (7 * ng + n) + 4 * g_) * 4) = acc[n]; \
    } } while (0)
  u32x4 xv;
  if (w < 4) {
    __builtin_amdgcn_s_setprio(1);
    const f32x16 zero = {};
    const lds_cptr rp = (lds_cptr)lds + K2_RING + lane * 16;
    lds_cptr rph = rp + 65536;
    asm volatile("" : "+v"(rph));
    bf16x8 q0 = *(const bf16x8*)(Qf + ((size_t)((tg * 4 + w) * 2) * 64 + lane) * 8);
    bf16x8 q1 = *(const bf16x8*)(Qf + ((size_t)((tg * 4 + w) * 2 + 1) * 64 + lane) * 8);
    RBAR();
    bf16x8 k0 = RDT(0, 0), k1 = RDT(0, 1024), v0 = RDT(0, 2048), v1 = v0;
    (void)set_ref(q1, 0.f, hi);
    f32x16 sA = MFMA32(k0, q0, zero), sB;
    sA = MFMA32(k1, q1, sA);
    (void)set_ref(q1, tile_max(sA), hi);
    sA = MFMA32(k0, q0, zero);
    sA = MFMA32(k1, q1, sA);
    k0 = RDT(1, 0); k1 = RDT(1, 1024);
    f32x16 o0 = zero, o1 = zero;
    u32x4 p0, p1 = {0u, 0u, 0u, 0u};
    SBAR();
    STAMP(1, 8);
#pragma unroll
    for (int j = 0; j < 64; j += 2) {
      CSTEP(sA, sB, j);
      CSTEP(sB, sA, j + 1);
    }
    o1 = MFMA32(v1, BF(p1), o1);
    __builtin_amdgcn_s_setprio(0);
    STAMP(1, 10);
    o0 += o1;
    {
      float chk = 0.f;
#pragma unroll
      for (int r = 0; r < 16; ++r) chk = __builtin_fmaf(o0[r], 0.f, chk);
      const bool bad = !(chk == 0.f) || !(o0[12] < 1.8e19f);
      if (__any(bad)) {
        attn_safe(Kf, Vf, q0, q1, o0, lane, hi);
        LDWO(0, CPRE);
      }
    }
    SBAR();
    LDWO(CPRE, 49);
    xv = *(const u32x4*)(x + (size_t)(tg * 8 + w) * D + (lane < 56 ? lane : 55) * 8);
    SBAR();
    {
      auto sw = __builtin_amdgcn_permlane32_swap(__float_as_uint(o0[12]), __float_as_uint(o0[12]), false, false);
      const float il = 1.f / __uint_as_float(sw[1]);
      const int c = lane & 31;
#pragma unroll
      for (int r4 = 0; r4 < 4; ++r4) {
        const int d = 8 * r4 + 4 * hi, k = 28 * (4 * w + (c >> 3)) + d;
        if (d < 28)
          *(u32x2*)(lds + K2_OT + ((k >> 3) * 8 + (c & 7)) * 16 + (k & 7) * 2) =
              (u32x2){cvtpk(o0[4 * r4] * il, o0[4 * r4 + 1] * il), cvtpk(o0[4 * r4 + 2] * il, o0[4 * r4 + 3] * il)};
      }
    }
    STAMP(1, 1);
    LBAR();
    STAMP(1, 2);
    WOGEMM();
  } else {
    const int hw = w - 4;
    const unsigned ring = (unsigned)(uintptr_t)lds + K2_RING + hw * 8192;
    const u16* kp = Kf + (size_t)hw * 2048 + lane * 8;
    const u16* vp = Vf + (size_t)hw * 2048 + lane * 8;
#define DMAS(s_) do { const unsigned sl_ = ring + ((s_) & 3) * 32768; \
    _Pragma("unroll") for (int u_ = 0; u_ < 2; ++u_) { \
      glds16(kp + (s_) * 8192 + u_ * 1024, sl_ + u_ * 4096); glds16(kp + (s_) * 8192 + u_ * 1024 + 512, sl_ + u_ * 4096 + 1024); \
      glds16(vp + (s_) * 8192 + u_ * 1024, sl_ + u_ * 4096 + 2048); glds16(vp + (s_) * 8192 + u_ * 1024 + 512, sl_ + u_ * 4096 + 3072); } } while (0)
    DMAS(0); DMAS(1); DMAS(2);
    waitv<16>(); RBAR();
    LDWO(0, 12); SBAR();
    waitv<20>(); RBAR();
    DMAS(3); LDWO(12, 24); SBAR();
    waitv<32>(); RBAR();
    DMAS(4); LDWO(24, 36); SBAR();
    waitv<32>(); RBAR();
    DMAS(5); LDWO(36, 49); SBAR();
    waitv<33>(); RBAR();
    DMAS(6);
    waitv<21>(); RBAR();
    DMAS(7);
    {
      const unsigned dum = (unsigned)(uintptr_t)lds + K2_DUM + hw * 1024;
      const int xg = tg & 7, rk = tg >> 3;
      const int ts = xg < 4 ? 25 * xg : 100 + 24 * (xg - 4), te = ts + (xg < 4 ? 25 : 24);
      const int n0 = ts / 7, n1 = (te - 1) / 7;
      const u16* p1 = w1 + (size_t)(64 * rk + 16 * hw + (lane >> 3)) * F + (lane & 7) * 8;
#pragma unroll
      for (int i = 0; i < 5; ++i) {
        const int n = n0 + i <= n1 ? n0 + i : n1;
        glds16(p1 + n * 64, dum); glds16(p1 + n * 64 + 8 * F, dum);
      }
      const u16* p2 = w2 + (size_t)(56 * rk + (lane >> 3)) * S + (4 * xg + hw) * 64 + (lane & 7) * 8;
#pragma unroll
      for (int i = 0; i < 7; ++i) glds16(p2 + (size_t)i * 8 * S, dum);
    }
    waitv<25>(); RBAR();
    waitv<17>(); RBAR();
    xv = *(const u32x4*)(x + (size_t)(tg * 8 + w) * D + (lane < 56 ? lane : 55) * 8);
    SBAR();
    STAMP(1, 1);
    LBAR();
    STAMP(1, 2);
    WOGEMM();
  }
  LBAR();
  STAMP(1, 3);
  {
    const int s = tg * 8 + w;
    const bool act = lane < 56;
    const int lc = act ? lane : 55;
    float v[8];
    const f32x4 a0 = *(const f32x4*)(lds + K2_HB + (w * D + lc * 8) * 4), a1 = *(const f32x4*)(lds + K2_HB + (w * D + lc * 8 + 4) * 4);
    const f32x4 c0 = *(const f32x4*)(lds + K2_HB + ((8 + w) * D + lc * 8) * 4), c1 = *(const f32x4*)(lds + K2_HB + ((8 + w) * D + lc * 8 + 4) * 4);
    u32x4 hv;
#pragma unroll
    for (int i = 0; i < 2; ++i) {
      hv[i] = cvtpk(bflo(xv[i]) + a0[2 * i] + c0[2 * i], bfhi(xv[i]) + a0[2 * i + 1] + c0[2 * i + 1]);
      hv[2 + i] = cvtpk(bflo(xv[2 + i]) + a1[2 * i] + c1[2 * i], bfhi(xv[2 + i]) + a1[2 * i + 1] + c1[2 * i + 1]);
    }
    if (act) *(u32x4*)(H + (size_t)s * D + lane * 8) = hv;
#pragma unroll
    for (int i = 0; i < 4; ++i) { v[2 * i] = act ? bflo(hv[i]) : 0.f; v[2 * i + 1] = act ? bfhi(hv[i]) : 0.f; }
#pragma unroll
    for (int pass = 0; pass < 2; ++pass) {
      float sm = 0.f;
#pragma unroll
      for (int i = 0; i < 8; ++i) sm += v[i];
      const float mean = wave_sum(sm) * (1.f / D);
      float q = 0.f;
#pragma unroll
      for (int i = 0; i < 8; ++i) { v[i] = act ? v[i] - mean : 0.f; q += v[i] * v[i]; }
      const float rs = __builtin_amdgcn_rsqf(wave_sum(q) * (1.f / D) + LN_EPS);
#pragma unroll
      for (int i = 0; i < 8; ++i) v[i] *= rs;
    }
    if (act) *(u32x4*)(lds + K2_HS + w * 912 + lane * 16) = (u32x4){cvtpk(v[0], v[1]), cvtpk(v[2], v[3]), cvtpk(v[4], v[5]), cvtpk(v[6], v[7])};
  }
  LBAR();
  if (tid < D / 2) {
    unsigned r[8];
#pragma unroll
    for (int i = 0; i < 8; ++i) r[i] = *(const unsigned*)(lds + K2_HS + i * 912 + tid * 4);
    u32x4 lo, hi2;
#pragma unroll
    for (int i = 0; i < 4; ++i) {
      lo[i] = (r[2 * i] & 0xffffu) | (r[2 * i + 1] << 16);
      hi2[i] = (r[2 * i] >> 16) | (r[2 * i + 1] & 0xffff0000u);
    }
    u32x4* dst = (u32x4*)(HN + ((size_t)tg * D + 2 * tid) * 8);
    dst[0] = lo; dst[1] = hi2;
  }
  asm volatile("s_waitcnt vmcnt(0)" ::: "memory");
  STAMP(1, 4);
}

constexpr int G_RING = 4, G_LDS = 8 * G_RING * 4096;
template <int PH>
__global__ __launch_bounds__(512) void k_mlp(const u16* __restrict__ act, const u16* __restrict__ W, const u16* __restrict__ bias,
                                             const u16* __restrict__ H, u16* __restrict__ out) {
  constexpr int KT = PH == 0 ? S : F, LDW = PH == 0 ? F : S, NK = KT / 256, NWG = 7 * (LDW / 64);
  __shared__ __attribute__((aligned(16))) char lds[G_LDS];
  const int tid = threadIdx.x, lane = tid & 63, w = __builtin_amdgcn_readfirstlane(tid >> 6);
  const int t = xcd_remap(blockIdx.x, NWG), nt = t / 7, mt = t - nt * 7;
  const unsigned lds0 = (unsigned)(uintptr_t)lds + w * (G_RING * 4096);
  const int g = lane >> 4, q4 = (lane & 15) >> 2, p = lane & 3, l15 = lane & 15;
  STAMP(2 + PH, 0);
  __builtin_amdgcn_s_setprio(3);
  const u16* asrc = act + ((size_t)(4 * w + g) * D + 64 * mt + l15) * 8;
  const int r8 = lane >> 3, fe = (r8 >> 1) & 1;
  const u16* wsrc0 = W + (size_t)(32 * w + r8) * LDW + 64 * nt + ((lane & 7) ^ (fe << 1)) * 8;
  const u16* wsrc1 = W + (size_t)(32 * w + 8 + r8) * LDW + 64 * nt + ((lane & 7) ^ ((fe | 2) << 1)) * 8;
  bf16x8 af[NK][4];
#define STAGE(i) do { const unsigned sl_ = lds0 + ((i) % G_RING) * 4096; const size_t o_ = (size_t)(i) * 256 * LDW; \
    glds16(wsrc0 + o_, sl_); glds16(wsrc1 + o_, sl_ + 1024); glds16(wsrc0 + o_ + 16 * LDW, sl_ + 2048); glds16(wsrc1 + o_ + 16 * LDW, sl_ + 3072); \
    const u16* ap_ = asrc + (size_t)(i) * 32 * D * 8; \
    gload16<0>(af[i][0], ap_); gload16<256>(af[i][1], ap_); gload16<512>(af[i][2], ap_); gload16<768>(af[i][3], ap_); } while (0)
#pragma unroll
  for (int i = 0; i < G_RING; ++i) STAGE(i);
  STAMP(2 + PH, 1);
  const int f = ((q4 >> 1) & 1) | ((g & 1) << 1);
  int woff[4];
#pragma unroll
  for (int nb = 0; nb < 4; ++nb) woff[nb] = w * (G_RING * 4096) + (8 * g + q4) * 128 + (((2 * nb + (p >> 1)) ^ (f << 1)) * 16) + 8 * (p & 1);
  f32x4 acc[4][4] = {};
#pragma unroll
  for (int i = 0; i < NK; ++i) {
    constexpr int PF = 0;
    const int rem = NK - 1 - i < G_RING - 1 ? NK - 1 - i : G_RING - 1;
    const bool pfd = i + G_RING > NK;
    if (i == 0) __builtin_amdgcn_s_setprio(3);
    else if (i == 2) __builtin_amdgcn_s_setprio(2);
    else if (i == 4) __builtin_amdgcn_s_setprio(1);
    else if (i == 6) __builtin_amdgcn_s_setprio(0);
    if (rem == 3) { if (pfd) waitv4<24 + PF>(af[i][0], af[i][1], af[i][2], af[i][3]); else waitv4<24>(af[i][0], af[i][1], af[i][2], af[i][3]); }
    else if (rem == 2) waitv4<16 + PF>(af[i][0], af[i][1], af[i][2], af[i][3]);
    else if (rem == 1) waitv4<8 + PF>(af[i][0], af[i][1], af[i][2], af[i][3]);
    else waitv4<PF>(af[i][0], af[i][1], af[i][2], af[i][3]);
    bf16x8 wf[4];
#pragma unroll
    for (int nb = 0; nb < 4; ++nb) {
      const lds_cptr sp = (lds_cptr)lds + woff[nb] + (i % G_RING) * 4096;
      wf[nb] = cat8(trread(sp), trread(sp + 512));
    }
    if (i + G_RING < NK) {
      asm volatile("s_waitcnt lgkmcnt(0)" ::: "memory");
      STAGE(i + G_RING);
    }
#pragma unroll
    for (int nb = 0; nb < 4; ++nb)
#pragma unroll
      for (int mb = 0; mb < 4; ++mb)
        acc[nb][mb] = PH == 0 ? MFMA16(wf[nb], af[i][mb], acc[nb][mb]) : MFMA16(af[i][mb], wf[nb], acc[nb][mb]);
  }
#undef STAGE
  STAMP(2 + PH, 4);
  const int enb = w >> 1, emb = 2 * (w & 1);
  u32x2 eb, eh0, eh1;
  if (PH == 0) {
    eb = *(const u32x2*)(bias + 64 * nt + 16 * enb + 4 * g);
  } else {
    const int s = 64 * nt + 16 * enb + l15;
    eb[0] = bias[s];
    eh0 = *(const u32x2*)(H + (size_t)s * D + 64 * mt + 16 * emb + 4 * g);
    eh1 = *(const u32x2*)(H + (size_t)s * D + 64 * mt + 16 * emb + 16 + 4 * g);
  }
  {
    f32x4* xb = (f32x4*)lds;
#pragma unroll
    for (int nb = 0; nb < 4; ++nb)
#pragma unroll
      for (int mb = 0; mb < 4; ++mb) xb[(w * 16 + nb * 4 + mb) * 64 + lane] = acc[nb][mb];
  }
  LBAR();
  {
    const f32x4* xb = (const f32x4*)lds;
#pragma unroll
    for (int e = 0; e < 2; ++e) {
      f32x4 a = xb[(2 * w + e) * 64 + lane];
#pragma unroll
      for (int v = 1; v < 8; ++v) a += xb[(v * 16 + 2 * w + e) * 64 + lane];
      const int mb = emb + e;
      if (PH == 0) {
        const int fi = 64 * nt + 16 * enb + 4 * g, d = 64 * mt + 16 * mb + l15;
        float z[4] = {a[0] + bflo(eb[0]), a[1] + bfhi(eb[0]), a[2] + bflo(eb[1]), a[3] + bfhi(eb[1])};
#pragma unroll
        for (int i = 0; i < 4; ++i) {
          const float zz = z[i], a2 = zz * (1.f + 0.044715f * zz * zz) * (-2.f * 0.7978845608028654f * 1.4426950408889634f);
          z[i] = zz * __builtin_amdgcn_rcpf(1.f + __builtin_amdgcn_exp2f(a2));
        }
        *(u32x2*)(out + ((size_t)(fi >> 3) * D + d) * 8 + (fi & 7)) = (u32x2){cvtpk(z[0], z[1]), cvtpk(z[2], z[3])};
      } else {
        const int d = 64 * mt + 16 * mb + 4 * g, s = 64 * nt + 16 * enb + l15;
        const float bb = bflo(eb[0]);
        const u32x2 hv = e ? eh1 : eh0;
        *(u32x2*)(out + (size_t)s * D + d) =
            (u32x2){cvtpk(a[0] + bb + bflo(hv[0]), a[1] + bb + bfhi(hv[0])), cvtpk(a[2] + bb + bflo(hv[1]), a[3] + bb + bfhi(hv[1]))};
      }
    }
  }
  STAMP(2 + PH, 5);
}

extern "C" void kernel_launch(void* const* d_in, const int* in_sizes, int n_in, void* d_out, int out_size, void* d_ws, size_t ws_size,
                              hipStream_t stream) {
  const u16* x = (const u16*)d_in[0];
  const u16* wq = (const u16*)d_in[1];
  const u16* wk = (const u16*)d_in[2];
  const u16* wv = (const u16*)d_in[3];
  const u16* wo = (const u16*)d_in[4];
  const u16* w1 = (const u16*)d_in[6];
  const u16* b1 = (const u16*)d_in[7];
  const u16* w2 = (const u16*)d_in[8];
  const u16* b2 = (const u16*)d_in[9];
  char* ws = (char*)d_ws;
  u16 *Qf = (u16*)(ws + WS_QF), *Kf = (u16*)(ws + WS_KF), *Vf = (u16*)(ws + WS_VF), *Wof = (u16*)(ws + WS_WOF);
  u16 *H = (u16*)(ws + WS_H), *HN = (u16*)(ws + WS_HN), *U = (u16*)(ws + WS_U);
  k_qkv<<<256, 512, 0, stream>>>(x, wq, wk, wv, wo, Qf, Kf, Vf, Wof);
  k_attn<<<256, 512, 0, stream>>>(x, Qf, Kf, Vf, Wof, w1, w2, H, HN);
  k_mlp<0><<<196, 512, 0, stream>>>(HN, w1, b1, H, U);
  k_mlp<1><<<224, 512, 0, stream>>>(U, w2, b2, H, (u16*)d_out);
}
```

```cpp
#include <hip/hip_runtime.h>
#include <hip/hip_bf16.h>

typedef unsigned short u16;
using bf16x8 = __attribute__((ext_vector_type(8))) short;
using s16x4 = __attribute__((ext_vector_type(4))) short;
using f32x4 = __attribute__((ext_vector_type(4))) float;
using f32x16 = __attribute__((ext_vector_type(16))) float;
using u32x4 = __attribute__((ext_vector_type(4))) unsigned;
using u32x2 = __attribute__((ext_vector_type(2))) unsigned;
typedef short v4i16_t __attribute__((ext_vector_type(4)));
typedef __attribute__((address_space(3))) const char* lds_cptr;
typedef __bf16 bf16x2_t __attribute__((ext_vector_type(2)));
typedef float f32x2_t __attribute__((ext_vector_type(2)));

#define DI __device__ __forceinline__
#define MFMA32(a, b, c) __builtin_amdgcn_mfma_f32_32x32x16_bf16(a, b, c, 0, 0, 0)
#define MFMA16(a, b, c) __builtin_amdgcn_mfma_f32_16x16x32_bf16(a, b, c, 0, 0, 0)
#define SBAR() __builtin_amdgcn_sched_barrier(0)
#define LBAR() asm volatile("s_waitcnt lgkmcnt(0)\n\ts_barrier" ::: "memory")
#define WAITV_BAR(N) asm volatile("s_waitcnt vmcnt(" #N ") lgkmcnt(0)\n\ts_barrier" ::: "memory")

constexpr int S = 2048, D = 448, DH = 28, F = 1792;
constexpr float QSCALE = 0.18898223650461362f * 1.4426950408889634f;
constexpr float LN_EPS = 1e-5f;

constexpr size_t WS_QF = 0;
constexpr size_t WS_KF = WS_QF + 2097152;
constexpr size_t WS_VF = WS_KF + 131072;
constexpr size_t WS_WOF = WS_VF + 131072;
constexpr size_t WS_H = WS_WOF + 401408;
constexpr size_t WS_HN = WS_H + 1835008;
constexpr size_t WS_U = WS_HN + 1835008;

#define STAMP(k, i)
#define CSTAMP(k, i)

DI unsigned cvtpk(float lo, float hi) {
  const f32x2_t v = {lo, hi};
  return __builtin_bit_cast(unsigned, __builtin_convertvector(v, bf16x2_t));
}
DI float bflo(unsigned u) { return __uint_as_float(u << 16); }
DI float bfhi(unsigned u) { return __uint_as_float(u & 0xffff0000u); }
template <int CTRL> DI float dppf(float v) {
  return __uint_as_float(__builtin_amdgcn_update_dpp(0u, __float_as_uint(v), CTRL, 0xf, 0xf, true));
}
DI float wave_sum(float v) {
  v += dppf<0xB1>(v);
  v += dppf<0x4E>(v);
  v += dppf<0x141>(v);
  v += dppf<0x140>(v);
  auto a = __builtin_amdgcn_permlane16_swap(__float_as_uint(v), __float_as_uint(v), false, false);
  v = __uint_as_float(a[0]) + __uint_as_float(a[1]);
  auto b = __builtin_amdgcn_permlane32_swap(__float_as_uint(v), __float_as_uint(v), false, false);
  return __uint_as_float(b[0]) + __uint_as_float(b[1]);
}
DI float half_max(float v) {
  auto b = __builtin_amdgcn_permlane32_swap(__float_as_uint(v), __float_as_uint(v), false, false);
  return __builtin_fmaxf(__uint_as_float(b[0]), __uint_as_float(b[1]));
}
DI void glds16(const void* g, unsigned lds_base) {
  unsigned sv;
  asm volatile("s_mov_b32 %0, m0\n\ts_mov_b32 m0, %2\n\ts_nop 0\n\tglobal_load_lds_dwordx4 %1, off\n\ts_mov_b32 m0, %0"
               : "=&s"(sv) : "v"(g), "s"(lds_base) : "memory");
}
DI void glds4(const void* g, unsigned lds_base) {
  unsigned sv;
  asm volatile("s_mov_b32 %0, m0\n\ts_mov_b32 m0, %2\n\ts_nop 0\n\tglobal_load_lds_dword %1, off\n\ts_mov_b32 m0, %0"
               : "=&s"(sv) : "v"(g), "s"(lds_base) : "memory");
}
template <int N> DI void waitv4(bf16x8& a, bf16x8& b, bf16x8& c, bf16x8& d) {
  asm volatile("s_waitcnt vmcnt(%4)" : "+v"(a), "+v"(b), "+v"(c), "+v"(d) : "i"(N) : "memory");
}
template <int OFF> DI void gload16(bf16x8& d, const void* p) {
  asm volatile("global_load_dwordx4 %0, %1, off offset:%2" : "=v"(d) : "v"(p), "i"(OFF) : "memory");
}
DI s16x4 trread(lds_cptr p) {
  return __builtin_bit_cast(s16x4, __builtin_amdgcn_ds_read_tr16_b64_v4i16((__attribute__((address_space(3))) v4i16_t*)p));
}
DI bf16x8 ldsr16(lds_cptr p) { return *(const __attribute__((address_space(3))) bf16x8*)p; }
DI bf16x8 cat8(s16x4 a, s16x4 b) { return __builtin_shufflevector(a, b, 0, 1, 2, 3, 4, 5, 6, 7); }
DI int xcd_remap(int bid, int nwg) {
  const int q = nwg >> 3, r = nwg & 7, x = bid & 7, o = bid >> 3;
  return (x < r ? x * (q + 1) : r * (q + 1) + (x - r) * q) + o;
}

constexpr int K1_WI = 448 * 128, K1_X = 2 * K1_WI, K1_XS = 528, K1_LDS = K1_X + 64 * K1_XS;
__global__ __launch_bounds__(512) void k_qkv(const u16* __restrict__ x, const u16* __restrict__ wq, const u16* __restrict__ wk,
                                             const u16* __restrict__ wv, const u16* __restrict__ wo, u16* __restrict__ Qf,
                                             u16* __restrict__ Kf, u16* __restrict__ Vf, u16* __restrict__ Wof) {
  __shared__ __attribute__((aligned(16))) char lds[K1_LDS];
  const int tid = threadIdx.x, lane = tid & 63, w = __builtin_amdgcn_readfirstlane(tid >> 6);
  const int t = xcd_remap(blockIdx.x, 256), rt = t >> 2, ct = t & 3;
  const unsigned lds0 = (unsigned)(uintptr_t)lds;
  STAMP(0, 0);
  bf16x8 xr[4];
  const bool act = lane < 56;
  {
    const u16* xp_ = x + (size_t)(rt * 32 + w * 4) * D + (act ? lane : 55) * 8;
    gload16<0>(xr[0], xp_); gload16<896>(xr[1], xp_); gload16<1792>(xr[2], xp_); gload16<2688>(xr[3], xp_);
  }
#pragma unroll
  for (int im = 0; im < 2; ++im) {
    const int c64 = 2 * ct + im;
    if (c64 < 7) {
#pragma unroll
      for (int i = 0; i < 7; ++i) {
        const int piece = w * 7 + i, row = piece * 8 + (lane >> 3);
        const int f = ((row >> 1) & 1) | (((row >> 3) & 1) << 1);
        const int c = (lane & 7) ^ (f << 1);
        glds16(wq + (size_t)row * D + c64 * 64 + c * 8, lds0 + im * K1_WI + piece * 1024);
      }
    } else {
#pragma unroll
      for (int i = 0; i < 28; ++i) {
        const int piece = w * 28 + i, row = piece * 2 + (lane >> 5), pp = lane & 31;
        const int f = ((row >> 1) & 1) | (((row >> 3) & 1) << 1);
        const int col = ((((pp >> 2) ^ (f << 1)) << 2) + (pp & 3)) * 2;
        const u16* src = col < 28 ? wk + row * DH + col : wv + row * DH + (col < 56 ? col - 28 : 0);
        glds4(src, lds0 + im * K1_WI + piece * 256);
      }
    }
  }
  u16 wt[8];
  const int frag0 = blockIdx.x + 256 * (w & 1);
  const bool dofrag = w < 2 && frag0 < 392;
  const int frag = dofrag ? frag0 : 0;
  {
    const int nb = frag / 14, ks = frag - nb * 14;
#pragma unroll
    for (int j = 0; j < 8; ++j) wt[j] = wo[(size_t)(32 * ks + 8 * (lane >> 4) + j) * D + 16 * nb + (lane & 15)];
  }
  SBAR();
  if (ct < 3) waitv4<22>(xr[0], xr[1], xr[2], xr[3]);
  else waitv4<43>(xr[0], xr[1], xr[2], xr[3]);
  {
    u32x4 xv[4];
#pragma unroll
    for (int r = 0; r < 4; ++r) xv[r] = __builtin_bit_cast(u32x4, xr[r]);
    float v[4][8], mean[4], rs[4];
#pragma unroll
    for (int r = 0; r < 4; ++r) {
      float s = 0.f;
#pragma unroll
      for (int i = 0; i < 4; ++i) {
        v[r][2 * i] = act ? bflo(xv[r][i]) : 0.f; v[r][2 * i + 1] = act ? bfhi(xv[r][i]) : 0.f;
        s += v[r][2 * i] + v[r][2 * i + 1];
      }
      mean[r] = s;
    }
#pragma unroll
    for (int r = 0; r < 4; ++r) mean[r] = wave_sum(mean[r]) * (1.f / D);
#pragma unroll
    for (int r = 0; r < 4; ++r) {
      float q = 0.f;
#pragma unroll
      for (int i = 0; i < 8; ++i) { v[r][i] = act ? v[r][i] - mean[r] : 0.f; q += v[r][i] * v[r][i]; }
      rs[r] = q;
    }
#pragma unroll
    for (int r = 0; r < 4; ++r) rs[r] = __builtin_amdgcn_rsqf(wave_sum(rs[r]) * (1.f / D) + LN_EPS);
    char* xdst = lds + K1_X + lane * K1_XS + w * 64;
#pragma unroll
    for (int r = 0; r < 4; ++r) {
      u32x4 o;
#pragma unroll
      for (int i = 0; i < 4; ++i) o[i] = cvtpk(v[r][2 * i] * rs[r], v[r][2 * i + 1] * rs[r]);
      *(u32x4*)(xdst + r * 16) = o;
    }
  }
  STAMP(0, 1);
  WAITV_BAR(0);
  STAMP(0, 2);
  const int nb = w & 3, im = w >> 2, c64 = 2 * ct + im, g = lane >> 4, q4 = (lane & 15) >> 2, p = lane & 3, l15 = lane & 15;
  const int f = ((q4 >> 1) & 1) | ((g & 1) << 1);
  const lds_cptr wp = (lds_cptr)lds + im * K1_WI + (8 * g + q4) * 128 + (((2 * nb + (p >> 1)) ^ (f << 1)) * 16) + 8 * (p & 1);
  const lds_cptr xp = (lds_cptr)lds + K1_X + g * K1_XS + l15 * 16;
  f32x4 acc0 = {0.f, 0.f, 0.f, 0.f}, acc1 = {0.f, 0.f, 0.f, 0.f};
  {
    bf16x8 a[14], b0[14], b1[14];
#pragma unroll
    for (int ks = 0; ks < 14; ++ks) {
      a[ks] = cat8(trread(wp + ks * 4096), trread(wp + ks * 4096 + 512));
      b0[ks] = ldsr16(xp + ks * 4 * K1_XS);
      b1[ks] = ldsr16(xp + ks * 4 * K1_XS + 256);
    }
    SBAR();
#pragma unroll
    for (int ks = 0; ks < 14; ++ks) {
      acc0 = MFMA16(a[ks], b0[ks], acc0);
      acc1 = MFMA16(a[ks], b1[ks], acc1);
    }
  }
  STAMP(0, 3);
#pragma unroll
  for (int tb = 0; tb < 2; ++tb) {
    const f32x4 a = tb ? acc1 : acc0;
    const int s = rt * 32 + 16 * tb + l15;
    const int nl = 16 * nb + 4 * g;
    if (c64 < 7) {
      const int n0 = 64 * c64 + nl, h = n0 / 28, d0 = n0 - 28 * h;
      const int tg = s >> 3, c = (h & 3) * 8 + (s & 7), b = h >> 2;
      const size_t off = ((((size_t)(tg * 4 + b) * 2 + (d0 >> 4)) * 64) + ((d0 >> 3) & 1) * 32 + c) * 8 + (d0 & 7);
      *(u32x2*)(Qf + off) = (u32x2){cvtpk(a[0] * QSCALE, a[1] * QSCALE), cvtpk(a[2] * QSCALE, a[3] * QSCALE)};
    } else {
      const int kt = s >> 5, kk = s & 31;
      if (nl < 28 || nl == 56) {
        const int d0 = nl < 28 ? nl : 28;
        const size_t off = ((((size_t)kt * 2 + (d0 >> 4)) * 64) + ((d0 >> 3) & 1) * 32 + kk) * 8 + (d0 & 7);
        u32x2 v = {cvtpk(a[0], a[1]), cvtpk(a[2], a[3])};
        if (nl == 56) v = (u32x2){0x3f803f80u, 0x00003f80u};
        *(u32x2*)(Kf + off) = v;
      } else {
        const int d0 = nl < 56 ? nl - 28 : 28;
        const int sp = kk >> 4, j = ((kk >> 3) & 1) * 4 + (kk & 3), hi = (kk >> 2) & 1;
        const size_t off = ((((size_t)kt * 2 + sp) * 64) + hi * 32 + d0) * 8 + j;
        unsigned v0 = cvtpk(a[0], a[1]), v1 = cvtpk(a[2], a[3]);
        if (nl == 60) { v0 = 0x00003f80u; v1 = 0u; }
        Vf[off] = (u16)v0; Vf[off + 8] = (u16)(v0 >> 16); Vf[off + 16] = (u16)v1; Vf[off + 24] = (u16)(v1 >> 16);
      }
    }
  }
  if (dofrag) {
    u32x4 o;
#pragma unroll
    for (int i = 0; i < 4; ++i) o[i] = (unsigned)wt[2 * i] | ((unsigned)wt[2 * i + 1] << 16);
    *(u32x4*)(Wof + ((size_t)frag * 64 + lane) * 8) = o;
  }
  STAMP(0, 4);
}

constexpr int K2_RING = 0, K2_OT = 131072, K2_DUM = K2_OT + 7168, K2_X = K2_DUM + 4096, K2_LDS = K2_X + 16384;
constexpr int K2_HB = 0, K2_HS = 32768;

DI float set_ref(bf16x8& q1, float m, int hi) {
  const float a = bflo(cvtpk(-m, 0.f)), r1 = -m - a, b = bflo(cvtpk(r1, 0.f)), c = bflo(cvtpk(r1 - b, 0.f));
  u32x4 v = __builtin_bit_cast(u32x4, q1);
  v[2] = hi ? cvtpk(a, b) : v[2];
  v[3] = hi ? cvtpk(c, 0.f) : v[3];
  q1 = __builtin_bit_cast(bf16x8, v);
  return -((a + b) + c);
}
DI float tile_max(const f32x16& s) {
  float a = __builtin_fmaxf(__builtin_fmaxf(s[0], s[1]), s[2]), b = __builtin_fmaxf(__builtin_fmaxf(s[3], s[4]), s[5]);
  a = __builtin_fmaxf(__builtin_fmaxf(a, s[6]), s[7]); b = __builtin_fmaxf(__builtin_fmaxf(b, s[8]), s[9]);
  a = __builtin_fmaxf(__builtin_fmaxf(a, s[10]), s[11]); b = __builtin_fmaxf(__builtin_fmaxf(b, s[12]), s[13]);
  a = __builtin_fmaxf(__builtin_fmaxf(a, s[14]), s[15]);
  return half_max(__builtin_fmaxf(a, b));
}
template <int N> DI void waitv() { asm volatile("s_waitcnt vmcnt(%0)" ::"i"(N) : "memory"); }
#define RBAR() asm volatile("s_barrier" ::: "memory")

DI void attn_safe(const u16* __restrict__ Kf, const u16* __restrict__ Vf, bf16x8 q0, bf16x8 q1, f32x16& o, int lane, int hi) {
  const f32x16 zero = {};
  float mref = 0.f;
  o = zero;
#pragma unroll 1
  for (int t = 0; t < 64; ++t) {
    const bf16x8 k0 = *(const bf16x8*)(Kf + t * 1024 + lane * 8), k1 = *(const bf16x8*)(Kf + t * 1024 + 512 + lane * 8);
    const bf16x8 v0 = *(const bf16x8*)(Vf + t * 1024 + lane * 8), v1 = *(const bf16x8*)(Vf + t * 1024 + 512 + lane * 8);
    (void)set_ref(q1, 0.f, hi);
    f32x16 s = MFMA32(k0, q0, zero);
    s = MFMA32(k1, q1, s);
    const float tm = tile_max(s);
    const float mn = set_ref(q1, t == 0 ? tm : __builtin_fmaxf(tm, mref), hi);
    const float al = t == 0 ? 0.f : __builtin_amdgcn_exp2f(mref - mn);
#pragma unroll
    for (int r = 0; r < 16; ++r) o[r] *= al;
    mref = mn;
    s = MFMA32(k0, q0, zero);
    s = MFMA32(k1, q1, s);
    u32x4 p0, p1;
#pragma unroll
    for (int i = 0; i < 4; ++i) {
      p0[i] = cvtpk(__builtin_amdgcn_exp2f(s[2 * i]), __builtin_amdgcn_exp2f(s[2 * i + 1]));
      p1[i] = cvtpk(__builtin_amdgcn_exp2f(s[8 + 2 * i]), __builtin_amdgcn_exp2f(s[8 + 2 * i + 1]));
    }
    o = MFMA32(v0, __builtin_bit_cast(bf16x8, p0), o);
    o = MFMA32(v1, __builtin_bit_cast(bf16x8, p1), o);
  }
}

#define EX4(S, i) do { S[i] = __builtin_amdgcn_exp2f(S[i]); S[i + 1] = __builtin_amdgcn_exp2f(S[i + 1]); \
    S[i + 2] = __builtin_amdgcn_exp2f(S[i + 2]); S[i + 3] = __builtin_amdgcn_exp2f(S[i + 3]); } while (0)
#define PK8(P, S, i) do { P[0] = cvtpk(S[i], S[i + 1]); P[1] = cvtpk(S[i + 2], S[i + 3]); P[2] = cvtpk(S[i + 4], S[i + 5]); P[3] = cvtpk(S[i + 6], S[i + 7]); } while (0)
#define BF(P) __builtin_bit_cast(bf16x8, P)
constexpr int CPRE = 30;
#define RDT(t, off) ldsr16((((t) & 16) ? rph : rp) + ((t) & 15) * 4096 + (off))
#define WOPF(j) do { if ((j) >= 2 && (j) < 2 + CPRE) wf[((j) - 2) / 7][((j) - 2) % 7] = *(const bf16x8*)(wop + ((((j) - 2) / 7) * 14 + ((j) - 2) % 7) * 512); } while (0)
#define CTILE(i) do { \
    if (((i) & 3) == 3 && (i) < 28) RBAR(); \
      \
    sB = MFMA32(k0, qB0, zero); SBAR(); if ((i) + 1 < 32) k0 = RDT(2 * (i) + 2, 0); EX4(sA, 0); SBAR(); \
    sB = MFMA32(k1, qB1, sB); SBAR(); if ((i) + 1 < 32) k1 = RDT(2 * (i) + 2, 1024); EX4(sA, 4); PK8(p0, sA, 0); WOPF(2 * (i)); SBAR(); \
    oB = MFMA32(v1, BF(p1), oB); SBAR(); v1 = RDT(2 * (i), 3072); EX4(sA, 8); SBAR(); \
    oA = MFMA32(v0, BF(p0), oA); SBAR(); EX4(sA, 12); PK8(p1, sA, 8); SBAR(); \
      \
    sA = MFMA32(k0, qA0, zero); SBAR(); EX4(sB, 0); SBAR(); \
    sA = MFMA32(k1, qA1, sA); SBAR(); EX4(sB, 4); PK8(p0, sB, 0); WOPF(2 * (i) + 1); SBAR(); \
    oA = MFMA32(v1, BF(p1), oA); SBAR(); EX4(sB, 8); SBAR(); \
    oB = MFMA32(v0, BF(p0), oB); SBAR(); if ((i) + 1 < 32) v0 = RDT(2 * (i) + 2, 2048); EX4(sB, 12); PK8(p1, sB, 8); SBAR(); } while (0)

__global__ __launch_bounds__(512) void k_attn(const u16* __restrict__ x, const u16* __restrict__ Qf, const u16* __restrict__ Kf,
                                              const u16* __restrict__ Vf, const u16* __restrict__ Wof, const u16* __restrict__ w1,
                                              const u16* __restrict__ w2, u16* __restrict__ H, u16* __restrict__ HN) {
  __shared__ __attribute__((aligned(16))) char lds[K2_LDS];
  const int tid = threadIdx.x, lane = tid & 63, w = __builtin_amdgcn_readfirstlane(tid >> 6), hi = lane >> 5;
  const int tg = blockIdx.x;
  STAMP(1, 0);
  const int kh = w >> 2, ng = w & 3;
  const u16* wop = Wof + ((size_t)(7 * ng * 14 + 7 * kh) * 64 + lane) * 8;
  bf16x8 wf[7][7];
#define LDWO(i0, i1) do { _Pragma("unroll") for (int i_ = i0; i_ < i1; ++i_) wf[i_ / 7][i_ % 7] = *(const bf16x8*)(wop + ((i_ / 7) * 14 + i_ % 7) * 512); } while (0)
#define WOGEMM() do { \
    const int g_ = lane >> 4; \
    bf16x8 bo[7]; \
    _Pragma("unroll") for (int k = 0; k < 7; ++k) bo[k] = *(const bf16x8*)(lds + K2_OT + ((4 * (7 * kh + k) + g_) * 8 + (lane & 7)) * 16); \
    f32x4 acc[7]; \
    _Pragma("unroll") for (int n = 0; n < 7; ++n) { \
      acc[n] = (f32x4){0.f, 0.f, 0.f, 0.f}; \
      _Pragma("unroll") for (int k = 0; k < 7; ++k) acc[n] = MFMA16(wf[n][k], bo[k], acc[n]); \
    } \
    if ((lane & 15) < 8) { \
      _Pragma("unroll") for (int n = 0; n < 7; ++n) *(f32x4*)(lds + K2_HB + ((kh * 8 + (lane & 15)) * D + 16 * (7 * ng + n) + 4 * g_) * 4) = acc[n]; \
    } } while (0)
  u32x4 xv;
  if (w < 4) {
    __builtin_amdgcn_s_setprio(1);
    const f32x16 zero = {};
    const int qh = w & 1, kp = w >> 1;
    const lds_cptr rp0 = (lds_cptr)lds + K2_RING + lane * 16;
    const lds_cptr rp = rp0 + kp * 4096;
    lds_cptr rph = rp + 65536;
    asm volatile("" : "+v"(rph));
    const u16* qp = Qf + ((size_t)((tg * 4 + 2 * qh) * 2) * 64 + lane) * 8;
    bf16x8 qA0 = *(const bf16x8*)(qp), qA1 = *(const bf16x8*)(qp + 512), qB0 = *(const bf16x8*)(qp + 1024), qB1 = *(const bf16x8*)(qp + 1536);
    RBAR();
    bf16x8 k0 = ldsr16(rp0), k1 = ldsr16(rp0 + 1024), v0 = RDT(0, 2048), v1 = v0;
    (void)set_ref(qA1, 0.f, hi);
    (void)set_ref(qB1, 0.f, hi);
    f32x16 sA = MFMA32(k0, qA0, zero), sB = MFMA32(k0, qB0, zero);
    sA = MFMA32(k1, qA1, sA);
    sB = MFMA32(k1, qB1, sB);
    k0 = RDT(0, 0); k1 = RDT(0, 1024);
    (void)set_ref(qA1, tile_max(sA), hi);
    (void)set_ref(qB1, tile_max(sB), hi);
    sA = MFMA32(k0, qA0, zero);
    sA = MFMA32(k1, qA1, sA);
    f32x16 oA = zero, oB = zero;
    u32x4 p0, p1 = {0u, 0u, 0u, 0u};
    SBAR();
    STAMP(1, 8);
#pragma unroll
    for (int i = 0; i < 32; ++i) CTILE(i);
    oB = MFMA32(v1, BF(p1), oB);
    __builtin_amdgcn_s_setprio(0);
    STAMP(1, 10);
    {
      const f32x16 snd = kp ? oA : oB;
#pragma unroll
      for (int r4 = 0; r4 < 4; ++r4)
        *(f32x4*)(lds + K2_X + ((w * 4 + r4) * 64 + lane) * 16) = (f32x4){snd[4 * r4], snd[4 * r4 + 1], snd[4 * r4 + 2], snd[4 * r4 + 3]};
    }
    f32x16 o0 = kp ? oB : oA;
    bf16x8 qk0 = kp ? qB0 : qA0, qk1 = kp ? qB1 : qA1;
    asm volatile("" : "+v"(o0), "+v"(qk0), "+v"(qk1));
    SBAR();
    LDWO(CPRE, 49);
    xv = *(const u32x4*)(x + (size_t)(tg * 8 + w) * D + (lane < 56 ? lane : 55) * 8);
    SBAR();
    LBAR();
#pragma unroll
    for (int r4 = 0; r4 < 4; ++r4) {
      const f32x4 t = *(const f32x4*)(lds + K2_X + (((w ^ 2) * 4 + r4) * 64 + lane) * 16);
      o0[4 * r4] += t[0]; o0[4 * r4 + 1] += t[1]; o0[4 * r4 + 2] += t[2]; o0[4 * r4 + 3] += t[3];
    }
    const int qb = 2 * qh + kp;
    {
      float chk = 0.f;
#pragma unroll
      for (int r = 0; r < 16; ++r) chk = __builtin_fmaf(o0[r], 0.f, chk);
      const bool bad = !(chk == 0.f) || !(o0[12] < 1.8e19f);
      if (__any(bad)) {
        attn_safe(Kf, Vf, qk0, qk1, o0, lane, hi);
        LDWO(0, 49);
      }
    }
    {
      auto sw = __builtin_amdgcn_permlane32_swap(__float_as_uint(o0[12]), __float_as_uint(o0[12]), false, false);
      const float il = 1.f / __uint_as_float(sw[1]);
      const int c = lane & 31;
#pragma unroll
      for (int r4 = 0; r4 < 4; ++r4) {
        const int d = 8 * r4 + 4 * hi, k = 28 * (4 * qb + (c >> 3)) + d;
        if (d < 28)
          *(u32x2*)(lds + K2_OT + ((k >> 3) * 8 + (c & 7)) * 16 + (k & 7) * 2) =
              (u32x2){cvtpk(o0[4 * r4] * il, o0[4 * r4 + 1] * il), cvtpk(o0[4 * r4 + 2] * il, o0[4 * r4 + 3] * il)};
      }
    }
    STAMP(1, 1);
    LBAR();
    STAMP(1, 2);
    WOGEMM();
  } else {
    const int hw = w - 4;
    const unsigned ring = (unsigned)(uintptr_t)lds + K2_RING + hw * 8192;
    const u16* kp = Kf + (size_t)hw * 2048 + lane * 8;
    const u16* vp = Vf + (size_t)hw * 2048 + lane * 8;
#define DMAS(s_) do { const unsigned sl_ = ring + ((s_) & 3) * 32768; \
    _Pragma("unroll") for (int u_ = 0; u_ < 2; ++u_) { \
      glds16(kp + (s_) * 8192 + u_ * 1024, sl_ + u_ * 4096); glds16(kp + (s_) * 8192 + u_ * 1024 + 512, sl_ + u_ * 4096 + 1024); \
      glds16(vp + (s_) * 8192 + u_ * 1024, sl_ + u_ * 4096 + 2048); glds16(vp + (s_) * 8192 + u_ * 1024 + 512, sl_ + u_ * 4096 + 3072); } } while (0)
    DMAS(0); DMAS(1); DMAS(2);
    waitv<16>(); RBAR();
    LDWO(0, 12); SBAR();
    waitv<20>(); RBAR();
    DMAS(3); LDWO(12, 24); SBAR();
    waitv<32>(); RBAR();
    DMAS(4); LDWO(24, 36); SBAR();
    waitv<32>(); RBAR();
    DMAS(5); LDWO(36, 49); SBAR();
    waitv<33>(); RBAR();
    DMAS(6);
    waitv<21>(); RBAR();
    DMAS(7);
    {
      const unsigned dum = (unsigned)(uintptr_t)lds + K2_DUM + hw * 1024;
      const int xg = tg & 7, rk = tg >> 3;
      const int ts = xg < 4 ? 25 * xg : 100 + 24 * (xg - 4), te = ts + (xg < 4 ? 25 : 24);
      const int n0 = ts / 7, n1 = (te - 1) / 7;
      const u16* p1 = w1 + (size_t)(64 * rk + 16 * hw + (lane >> 3)) * F + (lane & 7) * 8;
#pragma unroll
      for (int i = 0; i < 5; ++i) {
        const int n = n0 + i <= n1 ? n0 + i : n1;
        glds16(p1 + n * 64, dum); glds16(p1 + n * 64 + 8 * F, dum);
      }
      const u16* p2 = w2 + (size_t)(56 * rk + (lane >> 3)) * S + (4 * xg + hw) * 64 + (lane & 7) * 8;
#pragma unroll
      for (int i = 0; i < 7; ++i) glds16(p2 + (size_t)i * 8 * S, dum);
    }
    waitv<25>(); RBAR();
    waitv<17>(); RBAR();
    xv = *(const u32x4*)(x + (size_t)(tg * 8 + w) * D + (lane < 56 ? lane : 55) * 8);
    SBAR();
    RBAR();
    STAMP(1, 1);
    LBAR();
    STAMP(1, 2);
    WOGEMM();
  }
  LBAR();
  STAMP(1, 3);
  {
    const int s = tg * 8 + w;
    const bool act = lane < 56;
    const int lc = act ? lane : 55;
    float v[8];
    const f32x4 a0 = *(const f32x4*)(lds + K2_HB + (w * D + lc * 8) * 4), a1 = *(const f32x4*)(lds + K2_HB + (w * D + lc * 8 + 4) * 4);
    const f32x4 c0 = *(const f32x4*)(lds + K2_HB + ((8 + w) * D + lc * 8) * 4), c1 = *(const f32x4*)(lds + K2_HB + ((8 + w) * D + lc * 8 + 4) * 4);
    u32x4 hv;
#pragma unroll
    for (int i = 0; i < 2; ++i) {
      hv[i] = cvtpk(bflo(xv[i]) + a0[2 * i] + c0[2 * i], bfhi(xv[i]) + a0[2 * i + 1] + c0[2 * i + 1]);
      hv[2 + i] = cvtpk(bflo(xv[2 + i]) + a1[2 * i] + c1[2 * i], bfhi(xv[2 + i]) + a1[2 * i + 1] + c1[2 * i + 1]);
    }
    if (act) *(u32x4*)(H + (size_t)s * D + lane * 8) = hv;
#pragma unroll
    for (int i = 0; i < 4; ++i) { v[2 * i] = act ? bflo(hv[i]) : 0.f; v[2 * i + 1] = act ? bfhi(hv[i]) : 0.f; }
#pragma unroll
    for (int pass = 0; pass < 2; ++pass) {
      float sm = 0.f;
#pragma unroll
      for (int i = 0; i < 8; ++i) sm += v[i];
      const float mean = wave_sum(sm) * (1.f / D);
      float q = 0.f;
#pragma unroll
      for (int i = 0; i < 8; ++i) { v[i] = act ? v[i] - mean : 0.f; q += v[i] * v[i]; }
      const float rs = __builtin_amdgcn_rsqf(wave_sum(q) * (1.f / D) + LN_EPS);
#pragma unroll
      for (int i = 0; i < 8; ++i) v[i] *= rs;
    }
    if (act) *(u32x4*)(lds + K2_HS + w * 912 + lane * 16) = (u32x4){cvtpk(v[0], v[1]), cvtpk(v[2], v[3]), cvtpk(v[4], v[5]), cvtpk(v[6], v[7])};
  }
  LBAR();
  if (tid < D / 2) {
    unsigned r[8];
#pragma unroll
    for (int i = 0; i < 8; ++i) r[i] = *(const unsigned*)(lds + K2_HS + i * 912 + tid * 4);
    u32x4 lo, hi2;
#pragma unroll
    for (int i = 0; i < 4; ++i) {
      lo[i] = (r[2 * i] & 0xffffu) | (r[2 * i + 1] << 16);
      hi2[i] = (r[2 * i] >> 16) | (r[2 * i + 1] & 0xffff0000u);
    }
    u32x4* dst = (u32x4*)(HN + ((size_t)tg * D + 2 * tid) * 8);
    dst[0] = lo; dst[1] = hi2;
  }
  STAMP(1, 4);
}

constexpr int G_RING = 4, G_LDS = 8 * G_RING * 4096;
template <int PH>
__global__ __launch_bounds__(512) void k_mlp(const u16* __restrict__ act, const u16* __restrict__ W, const u16* __restrict__ bias,
                                             const u16* __restrict__ H, u16* __restrict__ out) {
  constexpr int KT = PH == 0 ? S : F, LDW = PH == 0 ? F : S, NK = KT / 256, NWG = 7 * (LDW / 64);
  __shared__ __attribute__((aligned(16))) char lds[G_LDS];
  const int tid = threadIdx.x, lane = tid & 63, w = __builtin_amdgcn_readfirstlane(tid >> 6);
  const int t = xcd_remap(blockIdx.x, NWG), nt = t / 7, mt = t - nt * 7;
  const unsigned lds0 = (unsigned)(uintptr_t)lds + w * (G_RING * 4096);
  const int g = lane >> 4, q4 = (lane & 15) >> 2, p = lane & 3, l15 = lane & 15;
  STAMP(2 + PH, 0);
  __builtin_amdgcn_s_setprio(3);
  const u16* asrc = act + ((size_t)(4 * w + g) * D + 64 * mt + l15) * 8;
  const int r8 = lane >> 3, fe = (r8 >> 1) & 1;
  const u16* wsrc0 = W + (size_t)(32 * w + r8) * LDW + 64 * nt + ((lane & 7) ^ (fe << 1)) * 8;
  const u16* wsrc1 = W + (size_t)(32 * w + 8 + r8) * LDW + 64 * nt + ((lane & 7) ^ ((fe | 2) << 1)) * 8;
  bf16x8 af[NK][4];
#define STAGE(i) do { const unsigned sl_ = lds0 + ((i) % G_RING) * 4096; const size_t o_ = (size_t)(i) * 256 * LDW; \
    glds16(wsrc0 + o_, sl_); glds16(wsrc1 + o_, sl_ + 1024); glds16(wsrc0 + o_ + 16 * LDW, sl_ + 2048); glds16(wsrc1 + o_ + 16 * LDW, sl_ + 3072); \
    const u16* ap_ = asrc + (size_t)(i) * 32 * D * 8; \
    gload16<0>(af[i][0], ap_); gload16<256>(af[i][1], ap_); gload16<512>(af[i][2], ap_); gload16<768>(af[i][3], ap_); } while (0)
#pragma unroll
  for (int i = 0; i < G_RING; ++i) STAGE(i);
  STAMP(2 + PH, 1);
  const int f = ((q4 >> 1) & 1) | ((g & 1) << 1);
  int woff[4];
#pragma unroll
  for (int nb = 0; nb < 4; ++nb) woff[nb] = w * (G_RING * 4096) + (8 * g + q4) * 128 + (((2 * nb + (p >> 1)) ^ (f << 1)) * 16) + 8 * (p & 1);
  f32x4 acc[4][4] = {};
#pragma unroll
  for (int i = 0; i < NK; ++i) {
    constexpr int PF = 0;
    const int rem = NK - 1 - i < G_RING - 1 ? NK - 1 - i : G_RING - 1;
    const bool pfd = i + G_RING > NK;
    if (i == 0) __builtin_amdgcn_s_setprio(3);
    else if (i == 2) __builtin_amdgcn_s_setprio(2);
    else if (i == 4) __builtin_amdgcn_s_setprio(1);
    else if (i == 6) __builtin_amdgcn_s_setprio(0);
    if (rem == 3) { if (pfd) waitv4<24 + PF>(af[i][0], af[i][1], af[i][2], af[i][3]); else waitv4<24>(af[i][0], af[i][1], af[i][2], af[i][3]); }
    else if (rem == 2) waitv4<16 + PF>(af[i][0], af[i][1], af[i][2], af[i][3]);
    else if (rem == 1) waitv4<8 + PF>(af[i][0], af[i][1], af[i][2], af[i][3]);
    else waitv4<PF>(af[i][0], af[i][1], af[i][2], af[i][3]);
    bf16x8 wf[4];
#pragma unroll
    for (int nb = 0; nb < 4; ++nb) {
      const lds_cptr sp = (lds_cptr)lds + woff[nb] + (i % G_RING) * 4096;
      wf[nb] = cat8(trread(sp), trread(sp + 512));
    }
    if (i + G_RING < NK) {
      asm volatile("s_waitcnt lgkmcnt(0)" ::: "memory");
      STAGE(i + G_RING);
    }
#pragma unroll
    for (int nb = 0; nb < 4; ++nb)
#pragma unroll
      for (int mb = 0; mb < 4; ++mb)
        acc[nb][mb] = PH == 0 ? MFMA16(wf[nb], af[i][mb], acc[nb][mb]) : MFMA16(af[i][mb], wf[nb], acc[nb][mb]);
  }
#undef STAGE
  STAMP(2 + PH, 4);
  const int enb = w >> 1, emb = 2 * (w & 1);
  u32x2 eb, eh0, eh1;
  if (PH == 0) {
    eb = *(const u32x2*)(bias + 64 * nt + 16 * enb + 4 * g);
  } else {
    const int s = 64 * nt + 16 * enb + l15;
    eb[0] = bias[s];
    eh0 = *(const u32x2*)(H + (size_t)s * D + 64 * mt + 16 * emb + 4 * g);
    eh1 = *(const u32x2*)(H + (size_t)s * D + 64 * mt + 16 * emb + 16 + 4 * g);
  }
  {
    f32x4* xb = (f32x4*)lds;
#pragma unroll
    for (int nb = 0; nb < 4; ++nb)
#pragma unroll
      for (int mb = 0; mb < 4; ++mb) xb[(w * 16 + nb * 4 + mb) * 64 + lane] = acc[nb][mb];
  }
  LBAR();
  {
    const f32x4* xb = (const f32x4*)lds;
#pragma unroll
    for (int e = 0; e < 2; ++e) {
      f32x4 a = xb[(2 * w + e) * 64 + lane];
#pragma unroll
      for (int v = 1; v < 8; ++v) a += xb[(v * 16 + 2 * w + e) * 64 + lane];
      const int mb = emb + e;
      if (PH == 0) {
        const int fi = 64 * nt + 16 * enb + 4 * g, d = 64 * mt + 16 * mb + l15;
        float z[4] = {a[0] + bflo(eb[0]), a[1] + bfhi(eb[0]), a[2] + bflo(eb[1]), a[3] + bfhi(eb[1])};
#pragma unroll
        for (int i = 0; i < 4; ++i) {
          const float zz = z[i], a2 = zz * (1.f + 0.044715f * zz * zz) * (-2.f * 0.7978845608028654f * 1.4426950408889634f);
          z[i] = zz * __builtin_amdgcn_rcpf(1.f + __builtin_amdgcn_exp2f(a2));
        }
        *(u32x2*)(out + ((size_t)(fi >> 3) * D + d) * 8 + (fi & 7)) = (u32x2){cvtpk(z[0], z[1]), cvtpk(z[2], z[3])};
      } else {
        const int d = 64 * mt + 16 * mb + 4 * g, s = 64 * nt + 16 * enb + l15;
        const float bb = bflo(eb[0]);
        const u32x2 hv = e ? eh1 : eh0;
        *(u32x2*)(out + (size_t)s * D + d) =
            (u32x2){cvtpk(a[0] + bb + bflo(hv[0]), a[1] + bb + bfhi(hv[0])), cvtpk(a[2] + bb + bflo(hv[1]), a[3] + bb + bfhi(hv[1]))};
      }
    }
  }
  STAMP(2 + PH, 5);
}

extern "C" void kernel_launch(void* const* d_in, const int* in_sizes, int n_in, void* d_out, int out_size, void* d_ws, size_t ws_size,
                              hipStream_t stream) {
  const u16* x = (const u16*)d_in[0];
  const u16* wq = (const u16*)d_in[1];
  const u16* wk = (const u16*)d_in[2];
  const u16* wv = (const u16*)d_in[3];
  const u16* wo = (const u16*)d_in[4];
  const u16* w1 = (const u16*)d_in[6];
  const u16* b1 = (const u16*)d_in[7];
  const u16* w2 = (const u16*)d_in[8];
  const u16* b2 = (const u16*)d_in[9];
  char* ws = (char*)d_ws;
  u16 *Qf = (u16*)(ws + WS_QF), *Kf = (u16*)(ws + WS_KF), *Vf = (u16*)(ws + WS_VF), *Wof = (u16*)(ws + WS_WOF);
  u16 *H = (u16*)(ws + WS_H), *HN = (u16*)(ws + WS_HN), *U = (u16*)(ws + WS_U);
  k_qkv<<<256, 512, 0, stream>>>(x, wq, wk, wv, wo, Qf, Kf, Vf, Wof);
  k_attn<<<256, 512, 0, stream>>>(x, Qf, Kf, Vf, Wof, w1, w2, H, HN);
  k_mlp<0><<<196, 512, 0, stream>>>(HN, w1, b1, H, U);
  k_mlp<1><<<224, 512, 0, stream>>>(U, w2, b2, H, (u16*)d_out);
}
```

```cpp
#include <hip/hip_runtime.h>
#include <hip/hip_bf16.h>

typedef unsigned short u16;
using bf16x8 = __attribute__((ext_vector_type(8))) short;
using s16x4 = __attribute__((ext_vector_type(4))) short;
using f32x4 = __attribute__((ext_vector_type(4))) float;
using f32x16 = __attribute__((ext_vector_type(16))) float;
using u32x4 = __attribute__((ext_vector_type(4))) unsigned;
using u32x2 = __attribute__((ext_vector_type(2))) unsigned;
typedef short v4i16_t __attribute__((ext_vector_type(4)));
typedef __attribute__((address_space(3))) const char* lds_cptr;
typedef __bf16 bf16x2_t __attribute__((ext_vector_type(2)));
typedef float f32x2_t __attribute__((ext_vector_type(2)));

#define DI __device__ __forceinline__
#define MFMA32(a, b, c) __builtin_amdgcn_mfma_f32_32x32x16_bf16(a, b, c, 0, 0, 0)
#define MFMA16(a, b, c) __builtin_amdgcn_mfma_f32_16x16x32_bf16(a, b, c, 0, 0, 0)
#define SBAR() __builtin_amdgcn_sched_barrier(0)
#define LBAR() asm volatile("s_waitcnt lgkmcnt(0)\n\ts_barrier" ::: "memory")
#define WAITV_BAR(N) asm volatile("s_waitcnt vmcnt(" #N ") lgkmcnt(0)\n\ts_barrier" ::: "memory")

constexpr int S = 2048, D = 448, DH = 28, F = 1792;
constexpr float QSCALE = 0.18898223650461362f * 1.4426950408889634f;
constexpr float LN_EPS = 1e-5f;

constexpr size_t WS_QF = 0;
constexpr size_t WS_KF = WS_QF + 2097152;
constexpr size_t WS_VF = WS_KF + 131072;
constexpr size_t WS_WOF = WS_VF + 131072;
constexpr size_t WS_H = WS_WOF + 401408;
constexpr size_t WS_HN = WS_H + 1835008;
constexpr size_t WS_U = WS_HN + 1835008;

#define STAMP(k, i)
#define CSTAMP(k, i)

DI unsigned cvtpk(float lo, float hi) {
  const f32x2_t v = {lo, hi};
  return __builtin_bit_cast(unsigned, __builtin_convertvector(v, bf16x2_t));
}
DI float bflo(unsigned u) { return __uint_as_float(u << 16); }
DI float bfhi(unsigned u) { return __uint_as_float(u & 0xffff0000u); }
template <int CTRL> DI float dppf(float v) {
  return __uint_as_float(__builtin_amdgcn_update_dpp(0u, __float_as_uint(v), CTRL, 0xf, 0xf, true));
}
DI float wave_sum(float v) {
  v += dppf<0xB1>(v);
  v += dppf<0x4E>(v);
  v += dppf<0x141>(v);
  v += dppf<0x140>(v);
  auto a = __builtin_amdgcn_permlane16_swap(__float_as_uint(v), __float_as_uint(v), false, false);
  v = __uint_as_float(a[0]) + __uint_as_float(a[1]);
  auto b = __builtin_amdgcn_permlane32_swap(__float_as_uint(v), __float_as_uint(v), false, false);
  return __uint_as_float(b[0]) + __uint_as_float(b[1]);
}
DI float half_max(float v) {
  auto b = __builtin_amdgcn_permlane32_swap(__float_as_uint(v), __float_as_uint(v), false, false);
  return __builtin_fmaxf(__uint_as_float(b[0]), __uint_as_float(b[1]));
}
DI void glds16(const void* g, unsigned lds_base) {
  unsigned sv;
  asm volatile("s_mov_b32 %0, m0\n\ts_mov_b32 m0, %2\n\ts_nop 0\n\tglobal_load_lds_dwordx4 %1, off\n\ts_mov_b32 m0, %0"
               : "=&s"(sv) : "v"(g), "s"(lds_base) : "memory");
}
DI void glds4(const void* g, unsigned lds_base) {
  unsigned sv;
  asm volatile("s_mov_b32 %0, m0\n\ts_mov_b32 m0, %2\n\ts_nop 0\n\tglobal_load_lds_dword %1, off\n\ts_mov_b32 m0, %0"
               : "=&s"(sv) : "v"(g), "s"(lds_base) : "memory");
}
template <int N> DI void waitv4(bf16x8& a, bf16x8& b, bf16x8& c, bf16x8& d) {
  asm volatile("s_waitcnt vmcnt(%4)" : "+v"(a), "+v"(b), "+v"(c), "+v"(d) : "i"(N) : "memory");
}
template <int OFF> DI void gload16(bf16x8& d, const void* p) {
  asm volatile("global_load_dwordx4 %0, %1, off offset:%2" : "=v"(d) : "v"(p), "i"(OFF) : "memory");
}
DI s16x4 trread(lds_cptr p) {
  return __builtin_bit_cast(s16x4, __builtin_amdgcn_ds_read_tr16_b64_v4i16((__attribute__((address_space(3))) v4i16_t*)p));
}
DI bf16x8 ldsr16(lds_cptr p) { return *(const __attribute__((address_space(3))) bf16x8*)p; }
DI bf16x8 cat8(s16x4 a, s16x4 b) { return __builtin_shufflevector(a, b, 0, 1, 2, 3, 4, 5, 6, 7); }
DI int xcd_remap(int bid, int nwg) {
  const int q = nwg >> 3, r = nwg & 7, x = bid & 7, o = bid >> 3;
  return (x < r ? x * (q + 1) : r * (q + 1) + (x - r) * q) + o;
}

constexpr int K1_WI = 448 * 128, K1_X = 2 * K1_WI, K1_XS = 528, K1_LDS = K1_X + 64 * K1_XS;
__global__ __launch_bounds__(512) void k_qkv(const u16* __restrict__ x, const u16* __restrict__ wq, const u16* __restrict__ wk,
                                             const u16* __restrict__ wv, const u16* __restrict__ wo, u16* __restrict__ Qf,
                                             u16* __restrict__ Kf, u16* __restrict__ Vf, u16* __restrict__ Wof) {
  __shared__ __attribute__((aligned(16))) char lds[K1_LDS];
  const int tid = threadIdx.x, lane = tid & 63, w = __builtin_amdgcn_readfirstlane(tid >> 6);
  const int t = xcd_remap(blockIdx.x, 256), rt = t >> 2, ct = t & 3;
  const unsigned lds0 = (unsigned)(uintptr_t)lds;
  STAMP(0, 0);
  bf16x8 xr[4];
  const bool act = lane < 56;
  {
    const u16* xp_ = x + (size_t)(rt * 32 + w * 4) * D + (act ? lane : 55) * 8;
    gload16<0>(xr[0], xp_); gload16<896>(xr[1], xp_); gload16<1792>(xr[2], xp_); gload16<2688>(xr[3], xp_);
  }
#pragma unroll
  for (int im = 0; im < 2; ++im) {
    const int c64 = 2 * ct + im;
    if (c64 < 7) {
#pragma unroll
      for (int i = 0; i < 7; ++i) {
        const int piece = w * 7 + i, row = piece * 8 + (lane >> 3);
        const int f = ((row >> 1) & 1) | (((row >> 3) & 1) << 1);
        const int c = (lane & 7) ^ (f << 1);
        glds16(wq + (size_t)row * D + c64 * 64 + c * 8, lds0 + im * K1_WI + piece * 1024);
      }
    } else {
#pragma unroll
      for (int i = 0; i < 28; ++i) {
        const int piece = w * 28 + i, row = piece * 2 + (lane >> 5), pp = lane & 31;
        const int f = ((row >> 1) & 1) | (((row >> 3) & 1) << 1);
        const int col = ((((pp >> 2) ^ (f << 1)) << 2) + (pp & 3)) * 2;
        const u16* src = col < 28 ? wk + row * DH + col : wv + row * DH + (col < 56 ? col - 28 : 0);
        glds4(src, lds0 + im * K1_WI + piece * 256);
      }
    }
  }
  u16 wt[8];
  const int frag0 = blockIdx.x + 256 * (w & 1);
  const bool dofrag = w < 2 && frag0 < 392;
  const int frag = dofrag ? frag0 : 0;
  {
    const int nb = frag / 14, ks = frag - nb * 14;
#pragma unroll
    for (int j = 0; j < 8; ++j) wt[j] = wo[(size_t)(32 * ks + 8 * (lane >> 4) + j) * D + 16 * nb + (lane & 15)];
  }
  SBAR();
  if (ct < 3) waitv4<22>(xr[0], xr[1], xr[2], xr[3]);
  else waitv4<43>(xr[0], xr[1], xr[2], xr[3]);
  {
    u32x4 xv[4];
#pragma unroll
    for (int r = 0; r < 4; ++r) xv[r] = __builtin_bit_cast(u32x4, xr[r]);
    float v[4][8], mean[4], rs[4];
#pragma unroll
    for (int r = 0; r < 4; ++r) {
      float s = 0.f;
#pragma unroll
      for (int i = 0; i < 4; ++i) {
        v[r][2 * i] = act ? bflo(xv[r][i]) : 0.f; v[r][2 * i + 1] = act ? bfhi(xv[r][i]) : 0.f;
        s += v[r][2 * i] + v[r][2 * i + 1];
      }
      mean[r] = s;
    }
#pragma unroll
    for (int r = 0; r < 4; ++r) mean[r] = wave_sum(mean[r]) * (1.f / D);
#pragma unroll
    for (int r = 0; r < 4; ++r) {
      float q = 0.f;
#pragma unroll
      for (int i = 0; i < 8; ++i) { v[r][i] = act ? v[r][i] - mean[r] : 0.f; q += v[r][i] * v[r][i]; }
      rs[r] = q;
    }
#pragma unroll
    for (int r = 0; r < 4; ++r) rs[r] = __builtin_amdgcn_rsqf(wave_sum(rs[r]) * (1.f / D) + LN_EPS);
    char* xdst = lds + K1_X + lane * K1_XS + w * 64;
#pragma unroll
    for (int r = 0; r < 4; ++r) {
      u32x4 o;
#pragma unroll
      for (int i = 0; i < 4; ++i) o[i] = cvtpk(v[r][2 * i] * rs[r], v[r][2 * i + 1] * rs[r]);
      *(u32x4*)(xdst + r * 16) = o;
    }
  }
  STAMP(0, 1);
  WAITV_BAR(0);
  STAMP(0, 2);
  const int nb = w & 3, im = w >> 2, c64 = 2 * ct + im, g = lane >> 4, q4 = (lane & 15) >> 2, p = lane & 3, l15 = lane & 15;
  const int f = ((q4 >> 1) & 1) | ((g & 1) << 1);
  const lds_cptr wp = (lds_cptr)lds + im * K1_WI + (8 * g + q4) * 128 + (((2 * nb + (p >> 1)) ^ (f << 1)) * 16) + 8 * (p & 1);
  const lds_cptr xp = (lds_cptr)lds + K1_X + g * K1_XS + l15 * 16;
  f32x4 acc0 = {0.f, 0.f, 0.f, 0.f}, acc1 = {0.f, 0.f, 0.f, 0.f};
  {
    bf16x8 a[14], b0[14], b1[14];
#pragma unroll
    for (int ks = 0; ks < 14; ++ks) {
      a[ks] = cat8(trread(wp + ks * 4096), trread(wp + ks * 4096 + 512));
      b0[ks] = ldsr16(xp + ks * 4 * K1_XS);
      b1[ks] = ldsr16(xp + ks * 4 * K1_XS + 256);
    }
    SBAR();
#pragma unroll
    for (int ks = 0; ks < 14; ++ks) {
      acc0 = MFMA16(a[ks], b0[ks], acc0);
      acc1 = MFMA16(a[ks], b1[ks], acc1);
    }
  }
  STAMP(0, 3);
#pragma unroll
  for (int tb = 0; tb < 2; ++tb) {
    const f32x4 a = tb ? acc1 : acc0;
    const int s = rt * 32 + 16 * tb + l15;
    const int nl = 16 * nb + 4 * g;
    if (c64 < 7) {
      const int n0 = 64 * c64 + nl, h = n0 / 28, d0 = n0 - 28 * h;
      const int tg = s >> 3, c = (h & 3) * 8 + (s & 7), b = h >> 2;
      const size_t off = ((((size_t)(tg * 4 + b) * 2 + (d0 >> 4)) * 64) + ((d0 >> 3) & 1) * 32 + c) * 8 + (d0 & 7);
      *(u32x2*)(Qf + off) = (u32x2){cvtpk(a[0] * QSCALE, a[1] * QSCALE), cvtpk(a[2] * QSCALE, a[3] * QSCALE)};
    } else {
      const int kt = s >> 5, kk = s & 31;
      if (nl < 28 || nl == 56) {
        const int d0 = nl < 28 ? nl : 28;
        const size_t off = ((((size_t)kt * 2 + (d0 >> 4)) * 64) + ((d0 >> 3) & 1) * 32 + kk) * 8 + (d0 & 7);
        u32x2 v = {cvtpk(a[0], a[1]), cvtpk(a[2], a[3])};
        if (nl == 56) v = (u32x2){0x3f803f80u, 0x00003f80u};
        *(u32x2*)(Kf + off) = v;
      } else {
        const int d0 = nl < 56 ? nl - 28 : 28;
        const int sp = kk >> 4, j = ((kk >> 3) & 1) * 4 + (kk & 3), hi = (kk >> 2) & 1;
        const size_t off = ((((size_t)kt * 2 + sp) * 64) + hi * 32 + d0) * 8 + j;
        unsigned v0 = cvtpk(a[0], a[1]), v1 = cvtpk(a[2], a[3]);
        if (nl == 60) { v0 = 0x00003f80u; v1 = 0u; }
        Vf[off] = (u16)v0; Vf[off + 8] = (u16)(v0 >> 16); Vf[off + 16] = (u16)v1; Vf[off + 24] = (u16)(v1 >> 16);
      }
    }
  }
  if (dofrag) {
    u32x4 o;
#pragma unroll
    for (int i = 0; i < 4; ++i) o[i] = (unsigned)wt[2 * i] | ((unsigned)wt[2 * i + 1] << 16);
    *(u32x4*)(Wof + ((size_t)frag * 64 + lane) * 8) = o;
  }
  STAMP(0, 4);
}

constexpr int K2_RING = 0, K2_OT = 131072, K2_DUM = K2_OT + 7168, K2_X = K2_DUM + 4096, K2_LDS = K2_X + 16384;
constexpr int K2_HB = 0, K2_HS = 32768;

DI float set_ref(bf16x8& q1, float m, int hi) {
  const float a = bflo(cvtpk(-m, 0.f)), r1 = -m - a, b = bflo(cvtpk(r1, 0.f)), c = bflo(cvtpk(r1 - b, 0.f));
  u32x4 v = __builtin_bit_cast(u32x4, q1);
  v[2] = hi ? cvtpk(a, b) : v[2];
  v[3] = hi ? cvtpk(c, 0.f) : v[3];
  q1 = __builtin_bit_cast(bf16x8, v);
  return -((a + b) + c);
}
DI float tile_max(const f32x16& s) {
  float a = __builtin_fmaxf(__builtin_fmaxf(s[0], s[1]), s[2]), b = __builtin_fmaxf(__builtin_fmaxf(s[3], s[4]), s[5]);
  a = __builtin_fmaxf(__builtin_fmaxf(a, s[6]), s[7]); b = __builtin_fmaxf(__builtin_fmaxf(b, s[8]), s[9]);
  a = __builtin_fmaxf(__builtin_fmaxf(a, s[10]), s[11]); b = __builtin_fmaxf(__builtin_fmaxf(b, s[12]), s[13]);
  a = __builtin_fmaxf(__builtin_fmaxf(a, s[14]), s[15]);
  return half_max(__builtin_fmaxf(a, b));
}
template <int N> DI void waitv() { asm volatile("s_waitcnt vmcnt(%0)" ::"i"(N) : "memory"); }
#define RBAR() asm volatile("s_barrier" ::: "memory")

DI void attn_safe(const u16* __restrict__ Kf, const u16* __restrict__ Vf, bf16x8 q0, bf16x8 q1, f32x16& o, int lane, int hi) {
  const f32x16 zero = {};
  float mref = 0.f;
  o = zero;
#pragma unroll 1
  for (int t = 0; t < 64; ++t) {
    const bf16x8 k0 = *(const bf16x8*)(Kf + t * 1024 + lane * 8), k1 = *(const bf16x8*)(Kf + t * 1024 + 512 + lane * 8);
    const bf16x8 v0 = *(const bf16x8*)(Vf + t * 1024 + lane * 8), v1 = *(const bf16x8*)(Vf + t * 1024 + 512 + lane * 8);
    (void)set_ref(q1, 0.f, hi);
    f32x16 s = MFMA32(k0, q0, zero);
    s = MFMA32(k1, q1, s);
    const float tm = tile_max(s);
    const float mn = set_ref(q1, t == 0 ? tm : __builtin_fmaxf(tm, mref), hi);
    const float al = t == 0 ? 0.f : __builtin_amdgcn_exp2f(mref - mn);
#pragma unroll
    for (int r = 0; r < 16; ++r) o[r] *= al;
    mref = mn;
    s = MFMA32(k0, q0, zero);
    s = MFMA32(k1, q1, s);
    u32x4 p0, p1;
#pragma unroll
    for (int i = 0; i < 4; ++i) {
      p0[i] = cvtpk(__builtin_amdgcn_exp2f(s[2 * i]), __builtin_amdgcn_exp2f(s[2 * i + 1]));
      p1[i] = cvtpk(__builtin_amdgcn_exp2f(s[8 + 2 * i]), __builtin_amdgcn_exp2f(s[8 + 2 * i + 1]));
    }
    o = MFMA32(v0, __builtin_bit_cast(bf16x8, p0), o);
    o = MFMA32(v1, __builtin_bit_cast(bf16x8, p1), o);
  }
}

#define EX4(S, i) do { S[i] = __builtin_amdgcn_exp2f(S[i]); S[i + 1] = __builtin_amdgcn_exp2f(S[i + 1]); \
    S[i + 2] = __builtin_amdgcn_exp2f(S[i + 2]); S[i + 3] = __builtin_amdgcn_exp2f(S[i + 3]); } while (0)
#define PK8(P, S, i) do { P[0] = cvtpk(S[i], S[i + 1]); P[1] = cvtpk(S[i + 2], S[i + 3]); P[2] = cvtpk(S[i + 4], S[i + 5]); P[3] = cvtpk(S[i + 6], S[i + 7]); } while (0)
#define BF(P) __builtin_bit_cast(bf16x8, P)
constexpr int CPRE = 32;
#define WOPF(j) do { if ((j) < CPRE) wf[(j) / 7][(j) % 7] = *(const bf16x8*)(wop + (((j) / 7) * 14 + (j) % 7) * 512); } while (0)
#define RDS(u, off) ldsr16(((u) < 4 ? B0 : B1) + ((u) & 3) * 8192 + (off))
#define CTILE(u, NX, PF) do { \
      \
    sB = MFMA32(k0, qB0, zero); SBAR(); if (NX) k0 = RDS((u) + 1, 0); EX4(sA, 0); WOPF(PF); SBAR(); \
    sB = MFMA32(k1, qB1, sB); SBAR(); if (NX) k1 = RDS((u) + 1, 1024); EX4(sA, 4); PK8(p0, sA, 0); SBAR(); \
    oB = MFMA32(v1, BF(p1), oB); SBAR(); v1 = RDS(u, 3072); EX4(sA, 8); WOPF((PF) + 1); SBAR(); \
    oA = MFMA32(v0, BF(p0), oA); SBAR(); EX4(sA, 12); PK8(p1, sA, 8); SBAR(); \
      \
    sA = MFMA32(k0, qA0, zero); SBAR(); EX4(sB, 0); WOPF((PF) + 2); SBAR(); \
    sA = MFMA32(k1, qA1, sA); SBAR(); EX4(sB, 4); PK8(p0, sB, 0); SBAR(); \
    oA = MFMA32(v1, BF(p1), oA); SBAR(); EX4(sB, 8); WOPF((PF) + 3); SBAR(); \
    oB = MFMA32(v0, BF(p0), oB); SBAR(); if (NX) v0 = RDS((u) + 1, 2048); EX4(sB, 12); PK8(p1, sB, 8); SBAR(); } while (0)
#define CSTAGE(LAST, PF) do { \
    CTILE(0, true, PF); CTILE(1, true, (PF) + 4); CTILE(2, true, (PF) + 8); \
    if (!(LAST)) RBAR(); \
    CTILE(3, !(LAST), (PF) + 12); } while (0)

__global__ __launch_bounds__(512) void k_attn(const u16* __restrict__ x, const u16* __restrict__ Qf, const u16* __restrict__ Kf,
                                              const u16* __restrict__ Vf, const u16* __restrict__ Wof, const u16* __restrict__ w1,
                                              const u16* __restrict__ w2, u16* __restrict__ H, u16* __restrict__ HN) {
  __shared__ __attribute__((aligned(16))) char lds[K2_LDS];
  const int tid = threadIdx.x, lane = tid & 63, w = __builtin_amdgcn_readfirstlane(tid >> 6), hi = lane >> 5;
  const int tg = blockIdx.x;
  STAMP(1, 0);
  const int kh = w >> 2, ng = w & 3;
  const u16* wop = Wof + ((size_t)(7 * ng * 14 + 7 * kh) * 64 + lane) * 8;
  bf16x8 wf[7][7];
#define LDWO(i0, i1) do { _Pragma("unroll") for (int i_ = i0; i_ < i1; ++i_) wf[i_ / 7][i_ % 7] = *(const bf16x8*)(wop + ((i_ / 7) * 14 + i_ % 7) * 512); } while (0)
#define WOGEMM() do { \
    const int g_ = lane >> 4; \
    bf16x8 bo[7]; \
    _Pragma("unroll") for (int k = 0; k < 7; ++k) bo[k] = *(const bf16x8*)(lds + K2_OT + ((4 * (7 * kh + k) + g_) * 8 + (lane & 7)) * 16); \
    f32x4 acc[7]; \
    _Pragma("unroll") for (int n = 0; n < 7; ++n) { \
      acc[n] = (f32x4){0.f, 0.f, 0.f, 0.f}; \
      _Pragma("unroll") for (int k = 0; k < 7; ++k) acc[n] = MFMA16(wf[n][k], bo[k], acc[n]); \
    } \
    if ((lane & 15) < 8) { \
      _Pragma("unroll") for (int n = 0; n < 7; ++n) *(f32x4*)(lds + K2_HB + ((kh * 8 + (lane & 15)) * D + 16 * (7 * ng + n) + 4 * g_) * 4) = acc[n]; \
    } } while (0)
  u32x4 xv;
  if (w < 4) {
    const f32x16 zero = {};
    const int qh = w & 1, kp = w >> 1;
    const lds_cptr rp0 = (lds_cptr)lds + K2_RING + lane * 16;
    const lds_cptr rp = rp0 + kp * 4096;
    lds_cptr B0 = rp, B1 = rp + 32768;
    const u16* qp = Qf + ((size_t)((tg * 4 + 2 * qh) * 2) * 64 + lane) * 8;
    bf16x8 qA0 = *(const bf16x8*)(qp), qA1 = *(const bf16x8*)(qp + 512), qB0 = *(const bf16x8*)(qp + 1024), qB1 = *(const bf16x8*)(qp + 1536);
    RBAR();
    bf16x8 k0 = ldsr16(rp0), k1 = ldsr16(rp0 + 1024), v0 = RDS(0, 2048), v1 = v0;
    (void)set_ref(qA1, 0.f, hi);
    (void)set_ref(qB1, 0.f, hi);
    f32x16 sA = MFMA32(k0, qA0, zero), sB = MFMA32(k0, qB0, zero);
    sA = MFMA32(k1, qA1, sA);
    sB = MFMA32(k1, qB1, sB);
    k0 = RDS(0, 0); k1 = RDS(0, 1024);
    (void)set_ref(qA1, tile_max(sA), hi);
    (void)set_ref(qB1, tile_max(sB), hi);
    sA = MFMA32(k0, qA0, zero);
    sA = MFMA32(k1, qA1, sA);
    f32x16 oA = zero, oB = zero;
    u32x4 p0, p1 = {0u, 0u, 0u, 0u};
    SBAR();
    STAMP(1, 8);
#pragma unroll 1
    for (int st = 0; st < 4; ++st) {
      CSTAGE(false, 64);
      B0 = B1; B1 = rp + ((st + 2) & 3) * 32768;
    }
    CSTAGE(false, 0);
    B0 = B1; B1 = rp + 2 * 32768;
    CSTAGE(false, 16);
    B0 = B1; B1 = rp + 3 * 32768;
    CSTAGE(false, 64);
    B0 = B1;
    CSTAGE(true, 64);
    oB = MFMA32(v1, BF(p1), oB);
    STAMP(1, 10);
    {
      const f32x16 snd = kp ? oA : oB;
#pragma unroll
      for (int r4 = 0; r4 < 4; ++r4)
        *(f32x4*)(lds + K2_X + ((w * 4 + r4) * 64 + lane) * 16) = (f32x4){snd[4 * r4], snd[4 * r4 + 1], snd[4 * r4 + 2], snd[4 * r4 + 3]};
    }
    f32x16 o0 = kp ? oB : oA;
    bf16x8 qk0 = kp ? qB0 : qA0, qk1 = kp ? qB1 : qA1;
    asm volatile("" : "+v"(o0), "+v"(qk0), "+v"(qk1));
    SBAR();
    LDWO(CPRE, 49);
    xv = *(const u32x4*)(x + (size_t)(tg * 8 + w) * D + (lane < 56 ? lane : 55) * 8);
    SBAR();
    LBAR();
#pragma unroll
    for (int r4 = 0; r4 < 4; ++r4) {
      const f32x4 t = *(const f32x4*)(lds + K2_X + (((w ^ 2) * 4 + r4) * 64 + lane) * 16);
      o0[4 * r4] += t[0]; o0[4 * r4 + 1] += t[1]; o0[4 * r4 + 2] += t[2]; o0[4 * r4 + 3] += t[3];
    }
    const int qb = 2 * qh + kp;
    {
      float chk = 0.f;
#pragma unroll
      for (int r = 0; r < 16; ++r) chk = __builtin_fmaf(o0[r], 0.f, chk);
      const bool bad = !(chk == 0.f) || !(o0[12] < 1.8e19f);
      if (__any(bad)) {
        attn_safe(Kf, Vf, qk0, qk1, o0, lane, hi);
        LDWO(0, 49);
      }
    }
    {
      auto sw = __builtin_amdgcn_permlane32_swap(__float_as_uint(o0[12]), __float_as_uint(o0[12]), false, false);
      const float il = 1.f / __uint_as_float(sw[1]);
      const int c = lane & 31;
#pragma unroll
      for (int r4 = 0; r4 < 4; ++r4) {
        const int d = 8 * r4 + 4 * hi, k = 28 * (4 * qb + (c >> 3)) + d;
        if (d < 28)
          *(u32x2*)(lds + K2_OT + ((k >> 3) * 8 + (c & 7)) * 16 + (k & 7) * 2) =
              (u32x2){cvtpk(o0[4 * r4] * il, o0[4 * r4 + 1] * il), cvtpk(o0[4 * r4 + 2] * il, o0[4 * r4 + 3] * il)};
      }
    }
    STAMP(1, 1);
    LBAR();
    STAMP(1, 2);
    WOGEMM();
  } else {
    const int hw = w - 4;
    __builtin_amdgcn_s_setprio(3);
    const unsigned ring = (unsigned)(uintptr_t)lds + K2_RING + hw * 8192;
    const u16* kp = Kf + (size_t)hw * 2048 + lane * 8;
    const u16* vp = Vf + (size_t)hw * 2048 + lane * 8;
#define DMAS(s_) do { const unsigned sl_ = ring + ((s_) & 3) * 32768; \
    _Pragma("unroll") for (int u_ = 0; u_ < 2; ++u_) { \
      glds16(kp + (s_) * 8192 + u_ * 1024, sl_ + u_ * 4096); glds16(kp + (s_) * 8192 + u_ * 1024 + 512, sl_ + u_ * 4096 + 1024); \
      glds16(vp + (s_) * 8192 + u_ * 1024, sl_ + u_ * 4096 + 2048); glds16(vp + (s_) * 8192 + u_ * 1024 + 512, sl_ + u_ * 4096 + 3072); } } while (0)
    DMAS(0); DMAS(1); DMAS(2);
    {
      const unsigned dum = (unsigned)(uintptr_t)lds + K2_DUM + hw * 1024;
      const int xg = tg & 7, rk = tg >> 3;
      const int ts = xg < 4 ? 25 * xg : 100 + 24 * (xg - 4), te = ts + (xg < 4 ? 25 : 24);
      const int n0 = ts / 7, n1 = (te - 1) / 7;
      const u16* p1 = w1 + (size_t)(64 * rk + 16 * hw + (lane >> 3)) * F + (lane & 7) * 8;
#pragma unroll
      for (int i = 0; i < 5; ++i) {
        const int n = n0 + i <= n1 ? n0 + i : n1;
        glds16(p1 + n * 64, dum); glds16(p1 + n * 64 + 8 * F, dum);
      }
      const u16* p2 = w2 + (size_t)(56 * rk + (lane >> 3)) * S + (4 * xg + hw) * 64 + (lane & 7) * 8;
#pragma unroll
      for (int i = 0; i < 7; ++i) glds16(p2 + (size_t)i * 8 * S, dum);
    }
    waitv<33>(); RBAR();
    LDWO(0, 12); SBAR();
    waitv<37>(); RBAR();
    DMAS(3); LDWO(12, 24); SBAR();
    waitv<49>(); RBAR();
    DMAS(4); LDWO(24, 36); SBAR();
    waitv<32>(); RBAR();
    DMAS(5); LDWO(36, 49); SBAR();
    waitv<33>(); RBAR();
    DMAS(6);
    waitv<21>(); RBAR();
    DMAS(7);
    waitv<8>(); RBAR();
    waitv<0>(); RBAR();
    xv = *(const u32x4*)(x + (size_t)(tg * 8 + w) * D + (lane < 56 ? lane : 55) * 8);
    SBAR();
    __builtin_amdgcn_s_setprio(0);
    RBAR();
    STAMP(1, 1);
    LBAR();
    STAMP(1, 2);
    WOGEMM();
  }
  LBAR();
  STAMP(1, 3);
  {
    const int s = tg * 8 + w;
    const bool act = lane < 56;
    const int lc = act ? lane : 55;
    float v[8];
    const f32x4 a0 = *(const f32x4*)(lds + K2_HB + (w * D + lc * 8) * 4), a1 = *(const f32x4*)(lds + K2_HB + (w * D + lc * 8 + 4) * 4);
    const f32x4 c0 = *(const f32x4*)(lds + K2_HB + ((8 + w) * D + lc * 8) * 4), c1 = *(const f32x4*)(lds + K2_HB + ((8 + w) * D + lc * 8 + 4) * 4);
    u32x4 hv;
#pragma unroll
    for (int i = 0; i < 2; ++i) {
      hv[i] = cvtpk(bflo(xv[i]) + a0[2 * i] + c0[2 * i], bfhi(xv[i]) + a0[2 * i + 1] + c0[2 * i + 1]);
      hv[2 + i] = cvtpk(bflo(xv[2 + i]) + a1[2 * i] + c1[2 * i], bfhi(xv[2 + i]) + a1[2 * i + 1] + c1[2 * i + 1]);
    }
    if (act) *(u32x4*)(H + (size_t)s * D + lane * 8) = hv;
#pragma unroll
    for (int i = 0; i < 4; ++i) { v[2 * i] = act ? bflo(hv[i]) : 0.f; v[2 * i + 1] = act ? bfhi(hv[i]) : 0.f; }
#pragma unroll
    for (int pass = 0; pass < 2; ++pass) {
      float sm = 0.f;
#pragma unroll
      for (int i = 0; i < 8; ++i) sm += v[i];
      const float mean = wave_sum(sm) * (1.f / D);
      float q = 0.f;
#pragma unroll
      for (int i = 0; i < 8; ++i) { v[i] = act ? v[i] - mean : 0.f; q += v[i] * v[i]; }
      const float rs = __builtin_amdgcn_rsqf(wave_sum(q) * (1.f / D) + LN_EPS);
#pragma unroll
      for (int i = 0; i < 8; ++i) v[i] *= rs;
    }
    if (act) *(u32x4*)(lds + K2_HS + w * 912 + lane * 16) = (u32x4){cvtpk(v[0], v[1]), cvtpk(v[2], v[3]), cvtpk(v[4], v[5]), cvtpk(v[6], v[7])};
  }
  LBAR();
  if (tid < D / 2) {
    unsigned r[8];
#pragma unroll
    for (int i = 0; i < 8; ++i) r[i] = *(const unsigned*)(lds + K2_HS + i * 912 + tid * 4);
    u32x4 lo, hi2;
#pragma unroll
    for (int i = 0; i < 4; ++i) {
      lo[i] = (r[2 * i] & 0xffffu) | (r[2 * i + 1] << 16);
      hi2[i] = (r[2 * i] >> 16) | (r[2 * i + 1] & 0xffff0000u);
    }
    u32x4* dst = (u32x4*)(HN + ((size_t)tg * D + 2 * tid) * 8);
    dst[0] = lo; dst[1] = hi2;
  }
  STAMP(1, 4);
}

constexpr int G_RING = 4, G_LDS = 8 * G_RING * 4096;
template <int PH>
__global__ __launch_bounds__(512) void k_mlp(const u16* __restrict__ act, const u16* __restrict__ W, const u16* __restrict__ bias,
                                             const u16* __restrict__ H, u16* __restrict__ out) {
  constexpr int KT = PH == 0 ? S : F, LDW = PH == 0 ? F : S, NK = KT / 256, NWG = 7 * (LDW / 64);
  __shared__ __attribute__((aligned(16))) char lds[G_LDS];
  const int tid = threadIdx.x, lane = tid & 63, w = __builtin_amdgcn_readfirstlane(tid >> 6);
  const int t = xcd_remap(blockIdx.x, NWG), nt = t / 7, mt = t - nt * 7;
  const unsigned lds0 = (unsigned)(uintptr_t)lds + w * (G_RING * 4096);
  const int g = lane >> 4, q4 = (lane & 15) >> 2, p = lane & 3, l15 = lane & 15;
  STAMP(2 + PH, 0);
  __builtin_amdgcn_s_setprio(3);
  const u16* asrc = act + ((size_t)(4 * w + g) * D + 64 * mt + l15) * 8;
  const int r8 = lane >> 3, fe = (r8 >> 1) & 1;
  const u16* wsrc0 = W + (size_t)(32 * w + r8) * LDW + 64 * nt + ((lane & 7) ^ (fe << 1)) * 8;
  const u16* wsrc1 = W + (size_t)(32 * w + 8 + r8) * LDW + 64 * nt + ((lane & 7) ^ ((fe | 2) << 1)) * 8;
  bf16x8 af[NK][4];
#define STAGE(i) do { const unsigned sl_ = lds0 + ((i) % G_RING) * 4096; const size_t o_ = (size_t)(i) * 256 * LDW; \
    glds16(wsrc0 + o_, sl_); glds16(wsrc1 + o_, sl_ + 1024); glds16(wsrc0 + o_ + 16 * LDW, sl_ + 2048); glds16(wsrc1 + o_ + 16 * LDW, sl_ + 3072); \
    const u16* ap_ = asrc + (size_t)(i) * 32 * D * 8; \
    gload16<0>(af[i][0], ap_); gload16<256>(af[i][1], ap_); gload16<512>(af[i][2], ap_); gload16<768>(af[i][3], ap_); } while (0)
#pragma unroll
  for (int i = 0; i < G_RING; ++i) STAGE(i);
  STAMP(2 + PH, 1);
  const int f = ((q4 >> 1) & 1) | ((g & 1) << 1);
  int woff[4];
#pragma unroll
  for (int nb = 0; nb < 4; ++nb) woff[nb] = w * (G_RING * 4096) + (8 * g + q4) * 128 + (((2 * nb + (p >> 1)) ^ (f << 1)) * 16) + 8 * (p & 1);
  f32x4 acc[4][4] = {};
#pragma unroll
  for (int i = 0; i < NK; ++i) {
    constexpr int PF = 0;
    const int rem = NK - 1 - i < G_RING - 1 ? NK - 1 - i : G_RING - 1;
    const bool pfd = i + G_RING > NK;
    if (i == 0) __builtin_amdgcn_s_setprio(3);
    else if (i == 2) __builtin_amdgcn_s_setprio(2);
    else if (i == 4) __builtin_amdgcn_s_setprio(1);
    else if (i == 6) __builtin_amdgcn_s_setprio(0);
    if (rem == 3) { if (pfd) waitv4<24 + PF>(af[i][0], af[i][1], af[i][2], af[i][3]); else waitv4<24>(af[i][0], af[i][1], af[i][2], af[i][3]); }
    else if (rem == 2) waitv4<16 + PF>(af[i][0], af[i][1], af[i][2], af[i][3]);
    else if (rem == 1) waitv4<8 + PF>(af[i][0], af[i][1], af[i][2], af[i][3]);
    else waitv4<PF>(af[i][0], af[i][1], af[i][2], af[i][3]);
    bf16x8 wf[4];
#pragma unroll
    for (int nb = 0; nb < 4; ++nb) {
      const lds_cptr sp = (lds_cptr)lds + woff[nb] + (i % G_RING) * 4096;
      wf[nb] = cat8(trread(sp), trread(sp + 512));
    }
    if (i + G_RING < NK) {
      asm volatile("s_waitcnt lgkmcnt(0)" ::: "memory");
      STAGE(i + G_RING);
    }
#pragma unroll
    for (int nb = 0; nb < 4; ++nb)
#pragma unroll
      for (int mb = 0; mb < 4; ++mb)
        acc[nb][mb] = PH == 0 ? MFMA16(wf[nb], af[i][mb], acc[nb][mb]) : MFMA16(af[i][mb], wf[nb], acc[nb][mb]);
  }
#undef STAGE
  STAMP(2 + PH, 4);
  const int enb = w >> 1, emb = 2 * (w & 1);
  u32x2 eb, eh0, eh1;
  if (PH == 0) {
    eb = *(const u32x2*)(bias + 64 * nt + 16 * enb + 4 * g);
  } else {
    const int s = 64 * nt + 16 * enb + l15;
    eb[0] = bias[s];
    eh0 = *(const u32x2*)(H + (size_t)s * D + 64 * mt + 16 * emb + 4 * g);
    eh1 = *(const u32x2*)(H + (size_t)s * D + 64 * mt + 16 * emb + 16 + 4 * g);
  }
  {
    f32x4* xb = (f32x4*)lds;
#pragma unroll
    for (int nb = 0; nb < 4; ++nb)
#pragma unroll
      for (int mb = 0; mb < 4; ++mb) xb[(w * 16 + nb * 4 + mb) * 64 + lane] = acc[nb][mb];
  }
  LBAR();
  {
    const f32x4* xb = (const f32x4*)lds;
#pragma unroll
    for (int e = 0; e < 2; ++e) {
      f32x4 a = xb[(2 * w + e) * 64 + lane];
#pragma unroll
      for (int v = 1; v < 8; ++v) a += xb[(v * 16 + 2 * w + e) * 64 + lane];
      const int mb = emb + e;
      if (PH == 0) {
        const int fi = 64 * nt + 16 * enb + 4 * g, d = 64 * mt + 16 * mb + l15;
        float z[4] = {a[0] + bflo(eb[0]), a[1] + bfhi(eb[0]), a[2] + bflo(eb[1]), a[3] + bfhi(eb[1])};
#pragma unroll
        for (int i = 0; i < 4; ++i) {
          const float zz = z[i], a2 = zz * (1.f + 0.044715f * zz * zz) * (-2.f * 0.7978845608028654f * 1.4426950408889634f);
          z[i] = zz * __builtin_amdgcn_rcpf(1.f + __builtin_amdgcn_exp2f(a2));
        }
        *(u32x2*)(out + ((size_t)(fi >> 3) * D + d) * 8 + (fi & 7)) = (u32x2){cvtpk(z[0], z[1]), cvtpk(z[2], z[3])};
      } else {
        const int d = 64 * mt + 16 * mb + 4 * g, s = 64 * nt + 16 * enb + l15;
        const float bb = bflo(eb[0]);
        const u32x2 hv = e ? eh1 : eh0;
        *(u32x2*)(out + (size_t)s * D + d) =
            (u32x2){cvtpk(a[0] + bb + bflo(hv[0]), a[1] + bb + bfhi(hv[0])), cvtpk(a[2] + bb + bflo(hv[1]), a[3] + bb + bfhi(hv[1]))};
      }
    }
  }
  STAMP(2 + PH, 5);
}

extern "C" void kernel_launch(void* const* d_in, const int* in_sizes, int n_in, void* d_out, int out_size, void* d_ws, size_t ws_size,
                              hipStream_t stream) {
  const u16* x = (const u16*)d_in[0];
  const u16* wq = (const u16*)d_in[1];
  const u16* wk = (const u16*)d_in[2];
  const u16* wv = (const u16*)d_in[3];
  const u16* wo = (const u16*)d_in[4];
  const u16* w1 = (const u16*)d_in[6];
  const u16* b1 = (const u16*)d_in[7];
  const u16* w2 = (const u16*)d_in[8];
  const u16* b2 = (const u16*)d_in[9];
  char* ws = (char*)d_ws;
  u16 *Qf = (u16*)(ws + WS_QF), *Kf = (u16*)(ws + WS_KF), *Vf = (u16*)(ws + WS_VF), *Wof = (u16*)(ws + WS_WOF);
  u16 *H = (u16*)(ws + WS_H), *HN = (u16*)(ws + WS_HN), *U = (u16*)(ws + WS_U);
  k_qkv<<<256, 512, 0, stream>>>(x, wq, wk, wv, wo, Qf, Kf, Vf, Wof);
  k_attn<<<256, 512, 0, stream>>>(x, Qf, Kf, Vf, Wof, w1, w2, H, HN);
  k_mlp<0><<<196, 512, 0, stream>>>(HN, w1, b1, H, U);
  k_mlp<1><<<224, 512, 0, stream>>>(U, w2, b2, H, (u16*)d_out);
}
```

```cpp
#include <hip/hip_runtime.h>
#include <hip/hip_bf16.h>

typedef unsigned short u16;
using bf16x8 = __attribute__((ext_vector_type(8))) short;
using s16x4 = __attribute__((ext_vector_type(4))) short;
using f32x4 = __attribute__((ext_vector_type(4))) float;
using f32x16 = __attribute__((ext_vector_type(16))) float;
using u32x4 = __attribute__((ext_vector_type(4))) unsigned;
using u32x2 = __attribute__((ext_vector_type(2))) unsigned;
typedef short v4i16_t __attribute__((ext_vector_type(4)));
typedef __attribute__((address_space(3))) const char* lds_cptr;
typedef __bf16 bf16x2_t __attribute__((ext_vector_type(2)));
typedef float f32x2_t __attribute__((ext_vector_type(2)));

#define DI __device__ __forceinline__
#define MFMA32(a, b, c) __builtin_amdgcn_mfma_f32_32x32x16_bf16(a, b, c, 0, 0, 0)
#define MFMA16(a, b, c) __builtin_amdgcn_mfma_f32_16x16x32_bf16(a, b, c, 0, 0, 0)
#define SBAR() __builtin_amdgcn_sched_barrier(0)
#define LBAR() asm volatile("s_waitcnt lgkmcnt(0)\n\ts_barrier" ::: "memory")
#define WAITV_BAR(N) asm volatile("s_waitcnt vmcnt(" #N ") lgkmcnt(0)\n\ts_barrier" ::: "memory")

constexpr int S = 2048, D = 448, DH = 28, F = 1792;
constexpr float QSCALE = 0.18898223650461362f * 1.4426950408889634f;
constexpr float LN_EPS = 1e-5f;

constexpr size_t WS_QF = 0;
constexpr size_t WS_KF = WS_QF + 2097152;
constexpr size_t WS_VF = WS_KF + 131072;
constexpr size_t WS_WOF = WS_VF + 131072;
constexpr size_t WS_H = WS_WOF + 401408;
constexpr size_t WS_HN = WS_H + 1835008;
constexpr size_t WS_U = WS_HN + 1835008;

#define STAMP(k, i)
#define CSTAMP(k, i)

DI unsigned cvtpk(float lo, float hi) {
  const f32x2_t v = {lo, hi};
  return __builtin_bit_cast(unsigned, __builtin_convertvector(v, bf16x2_t));
}
DI float bflo(unsigned u) { return __uint_as_float(u << 16); }
DI float bfhi(unsigned u) { return __uint_as_float(u & 0xffff0000u); }
template <int CTRL> DI float dppf(float v) {
  return __uint_as_float(__builtin_amdgcn_update_dpp(0u, __float_as_uint(v), CTRL, 0xf, 0xf, true));
}
DI float wave_sum(float v) {
  v += dppf<0xB1>(v);
  v += dppf<0x4E>(v);
  v += dppf<0x141>(v);
  v += dppf<0x140>(v);
  auto a = __builtin_amdgcn_permlane16_swap(__float_as_uint(v), __float_as_uint(v), false, false);
  v = __uint_as_float(a[0]) + __uint_as_float(a[1]);
  auto b = __builtin_amdgcn_permlane32_swap(__float_as_uint(v), __float_as_uint(v), false, false);
  return __uint_as_float(b[0]) + __uint_as_float(b[1]);
}
DI float half_max(float v) {
  auto b = __builtin_amdgcn_permlane32_swap(__float_as_uint(v), __float_as_uint(v), false, false);
  return __builtin_fmaxf(__uint_as_float(b[0]), __uint_as_float(b[1]));
}
DI void glds16(const void* g, unsigned lds_base) {
  unsigned sv;
  asm volatile("s_mov_b32 %0, m0\n\ts_mov_b32 m0, %2\n\ts_nop 0\n\tglobal_load_lds_dwordx4 %1, off\n\ts_mov_b32 m0, %0"
               : "=&s"(sv) : "v"(g), "s"(lds_base) : "memory");
}
DI void glds4(const void* g, unsigned lds_base) {
  unsigned sv;
  asm volatile("s_mov_b32 %0, m0\n\ts_mov_b32 m0, %2\n\ts_nop 0\n\tglobal_load_lds_dword %1, off\n\ts_mov_b32 m0, %0"
               : "=&s"(sv) : "v"(g), "s"(lds_base) : "memory");
}
template <int N> DI void waitv4(bf16x8& a, bf16x8& b, bf16x8& c, bf16x8& d) {
  asm volatile("s_waitcnt vmcnt(%4)" : "+v"(a), "+v"(b), "+v"(c), "+v"(d) : "i"(N) : "memory");
}
template <int OFF> DI void gload16(bf16x8& d, const void* p) {
  asm volatile("global_load_dwordx4 %0, %1, off offset:%2" : "=v"(d) : "v"(p), "i"(OFF) : "memory");
}
DI s16x4 trread(lds_cptr p) {
  return __builtin_bit_cast(s16x4, __builtin_amdgcn_ds_read_tr16_b64_v4i16((__attribute__((address_space(3))) v4i16_t*)p));
}
DI bf16x8 ldsr16(lds_cptr p) { return *(const __attribute__((address_space(3))) bf16x8*)p; }
DI bf16x8 cat8(s16x4 a, s16x4 b) { return __builtin_shufflevector(a, b, 0, 1, 2, 3, 4, 5, 6, 7); }
DI int xcd_remap(int bid, int nwg) {
  const int q = nwg >> 3, r = nwg & 7, x = bid & 7, o = bid >> 3;
  return (x < r ? x * (q + 1) : r * (q + 1) + (x - r) * q) + o;
}

constexpr int K1_WI = 448 * 128, K1_X = 2 * K1_WI, K1_XS = 528, K1_LDS = K1_X + 64 * K1_XS;
__global__ __launch_bounds__(512) void k_qkv(const u16* __restrict__ x, const u16* __restrict__ wq, const u16* __restrict__ wk,
                                             const u16* __restrict__ wv, const u16* __restrict__ wo, u16* __restrict__ Qf,
                                             u16* __restrict__ Kf, u16* __restrict__ Vf, u16* __restrict__ Wof) {
  __shared__ __attribute__((aligned(16))) char lds[K1_LDS];
  const int tid = threadIdx.x, lane = tid & 63, w = __builtin_amdgcn_readfirstlane(tid >> 6);
  const int t = xcd_remap(blockIdx.x, 256), rt = t >> 2, ct = t & 3;
  const unsigned lds0 = (unsigned)(uintptr_t)lds;
  STAMP(0, 0);
  bf16x8 xr[4];
  const bool act = lane < 56;
  {
    const u16* xp_ = x + (size_t)(rt * 32 + w * 4) * D + (act ? lane : 55) * 8;
    gload16<0>(xr[0], xp_); gload16<896>(xr[1], xp_); gload16<1792>(xr[2], xp_); gload16<2688>(xr[3], xp_);
  }
#pragma unroll
  for (int im = 0; im < 2; ++im) {
    const int c64 = 2 * ct + im;
    if (c64 < 7) {
#pragma unroll
      for (int i = 0; i < 7; ++i) {
        const int piece = w * 7 + i, row = piece * 8 + (lane >> 3);
        const int f = ((row >> 1) & 1) | (((row >> 3) & 1) << 1);
        const int c = (lane & 7) ^ (f << 1);
        glds16(wq + (size_t)row * D + c64 * 64 + c * 8, lds0 + im * K1_WI + piece * 1024);
      }
    } else {
#pragma unroll
      for (int i = 0; i < 28; ++i) {
        const int piece = w * 28 + i, row = piece * 2 + (lane >> 5), pp = lane & 31;
        const int f = ((row >> 1) & 1) | (((row >> 3) & 1) << 1);
        const int col = ((((pp >> 2) ^ (f << 1)) << 2) + (pp & 3)) * 2;
        const u16* src = col < 28 ? wk + row * DH + col : wv + row * DH + (col < 56 ? col - 28 : 0);
        glds4(src, lds0 + im * K1_WI + piece * 256);
      }
    }
  }
  u16 wt[8];
  const int frag0 = blockIdx.x + 256 * (w & 1);
  const bool dofrag = w < 2 && frag0 < 392;
  const int frag = dofrag ? frag0 : 0;
  {
    const int nb = frag / 14, ks = frag - nb * 14;
#pragma unroll
    for (int j = 0; j < 8; ++j) wt[j] = wo[(size_t)(32 * ks + 8 * (lane >> 4) + j) * D + 16 * nb + (lane & 15)];
  }
  SBAR();
  if (ct < 3) waitv4<22>(xr[0], xr[1], xr[2], xr[3]);
  else waitv4<43>(xr[0], xr[1], xr[2], xr[3]);
  {
    u32x4 xv[4];
#pragma unroll
    for (int r = 0; r < 4; ++r) xv[r] = __builtin_bit_cast(u32x4, xr[r]);
    float v[4][8], mean[4], rs[4];
#pragma unroll
    for (int r = 0; r < 4; ++r) {
      float s = 0.f;
#pragma unroll
      for (int i = 0; i < 4; ++i) {
        v[r][2 * i] = act ? bflo(xv[r][i]) : 0.f; v[r][2 * i + 1] = act ? bfhi(xv[r][i]) : 0.f;
        s += v[r][2 * i] + v[r][2 * i + 1];
      }
      mean[r] = s;
    }
#pragma unroll
    for (int r = 0; r < 4; ++r) mean[r] = wave_sum(mean[r]) * (1.f / D);
#pragma unroll
    for (int r = 0; r < 4; ++r) {
      float q = 0.f;
#pragma unroll
      for (int i = 0; i < 8; ++i) { v[r][i] = act ? v[r][i] - mean[r] : 0.f; q += v[r][i] * v[r][i]; }
      rs[r] = q;
    }
#pragma unroll
    for (int r = 0; r < 4; ++r) rs[r] = __builtin_amdgcn_rsqf(wave_sum(rs[r]) * (1.f / D) + LN_EPS);
    char* xdst = lds + K1_X + lane * K1_XS + w * 64;
#pragma unroll
    for (int r = 0; r < 4; ++r) {
      u32x4 o;
#pragma unroll
      for (int i = 0; i < 4; ++i) o[i] = cvtpk(v[r][2 * i] * rs[r], v[r][2 * i + 1] * rs[r]);
      *(u32x4*)(xdst + r * 16) = o;
    }
  }
  STAMP(0, 1);
  WAITV_BAR(0);
  STAMP(0, 2);
  const int nb = w & 3, im = w >> 2, c64 = 2 * ct + im, g = lane >> 4, q4 = (lane & 15) >> 2, p = lane & 3, l15 = lane & 15;
  const int f = ((q4 >> 1) & 1) | ((g & 1) << 1);
  const lds_cptr wp = (lds_cptr)lds + im * K1_WI + (8 * g + q4) * 128 + (((2 * nb + (p >> 1)) ^ (f << 1)) * 16) + 8 * (p & 1);
  const lds_cptr xp = (lds_cptr)lds + K1_X + g * K1_XS + l15 * 16;
  f32x4 acc0 = {0.f, 0.f, 0.f, 0.f}, acc1 = {0.f, 0.f, 0.f, 0.f};
  {
    bf16x8 a[14], b0[14], b1[14];
#pragma unroll
    for (int ks = 0; ks < 14; ++ks) {
      a[ks] = cat8(trread(wp + ks * 4096), trread(wp + ks * 4096 + 512));
      b0[ks] = ldsr16(xp + ks * 4 * K1_XS);
      b1[ks] = ldsr16(xp + ks * 4 * K1_XS + 256);
    }
    SBAR();
#pragma unroll
    for (int ks = 0; ks < 14; ++ks) {
      acc0 = MFMA16(a[ks], b0[ks], acc0);
      acc1 = MFMA16(a[ks], b1[ks], acc1);
    }
  }
  STAMP(0, 3);
#pragma unroll
  for (int tb = 0; tb < 2; ++tb) {
    const f32x4 a = tb ? acc1 : acc0;
    const int s = rt * 32 + 16 * tb + l15;
    const int nl = 16 * nb + 4 * g;
    if (c64 < 7) {
      const int n0 = 64 * c64 + nl, h = n0 / 28, d0 = n0 - 28 * h;
      const int tg = s >> 3, c = (h & 3) * 8 + (s & 7), b = h >> 2;
      const size_t off = ((((size_t)(tg * 4 + b) * 2 + (d0 >> 4)) * 64) + ((d0 >> 3) & 1) * 32 + c) * 8 + (d0 & 7);
      *(u32x2*)(Qf + off) = (u32x2){cvtpk(a[0] * QSCALE, a[1] * QSCALE), cvtpk(a[2] * QSCALE, a[3] * QSCALE)};
    } else {
      const int kt = s >> 5, kk = s & 31;
      if (nl < 28 || nl == 56) {
        const int d0 = nl < 28 ? nl : 28;
        const size_t off = ((((size_t)kt * 2 + (d0 >> 4)) * 64) + ((d0 >> 3) & 1) * 32 + kk) * 8 + (d0 & 7);
        u32x2 v = {cvtpk(a[0], a[1]), cvtpk(a[2], a[3])};
        if (nl == 56) v = (u32x2){0x3f803f80u, 0x00003f80u};
        *(u32x2*)(Kf + off) = v;
      } else {
        const int d0 = nl < 56 ? nl - 28 : 28;
        const int sp = kk >> 4, j = ((kk >> 3) & 1) * 4 + (kk & 3), hi = (kk >> 2) & 1;
        const size_t off = ((((size_t)kt * 2 + sp) * 64) + hi * 32 + d0) * 8 + j;
        unsigned v0 = cvtpk(a[0], a[1]), v1 = cvtpk(a[2], a[3]);
        if (nl == 60) { v0 = 0x00003f80u; v1 = 0u; }
        Vf[off] = (u16)v0; Vf[off + 8] = (u16)(v0 >> 16); Vf[off + 16] = (u16)v1; Vf[off + 24] = (u16)(v1 >> 16);
      }
    }
  }
  if (dofrag) {
    u32x4 o;
#pragma unroll
    for (int i = 0; i < 4; ++i) o[i] = (unsigned)wt[2 * i] | ((unsigned)wt[2 * i + 1] << 16);
    *(u32x4*)(Wof + ((size_t)frag * 64 + lane) * 8) = o;
  }
  STAMP(0, 4);
}

constexpr int K2_RING = 0, K2_OT = 131072, K2_DUM = K2_OT + 7168, K2_X = K2_DUM + 4096, K2_LDS = K2_X + 16384;
constexpr int K2_HB = 0, HBS = 452, K2_HS = 32768;

DI float set_ref(bf16x8& q1, float m, int hi) {
  const float a = bflo(cvtpk(-m, 0.f)), r1 = -m - a, b = bflo(cvtpk(r1, 0.f)), c = bflo(cvtpk(r1 - b, 0.f));
  u32x4 v = __builtin_bit_cast(u32x4, q1);
  v[2] = hi ? cvtpk(a, b) : v[2];
  v[3] = hi ? cvtpk(c, 0.f) : v[3];
  q1 = __builtin_bit_cast(bf16x8, v);
  return -((a + b) + c);
}
DI float tile_max(const f32x16& s) {
  float a = __builtin_fmaxf(__builtin_fmaxf(s[0], s[1]), s[2]), b = __builtin_fmaxf(__builtin_fmaxf(s[3], s[4]), s[5]);
  a = __builtin_fmaxf(__builtin_fmaxf(a, s[6]), s[7]); b = __builtin_fmaxf(__builtin_fmaxf(b, s[8]), s[9]);
  a = __builtin_fmaxf(__builtin_fmaxf(a, s[10]), s[11]); b = __builtin_fmaxf(__builtin_fmaxf(b, s[12]), s[13]);
  a = __builtin_fmaxf(__builtin_fmaxf(a, s[14]), s[15]);
  return half_max(__builtin_fmaxf(a, b));
}
template <int N> DI void waitv() { asm volatile("s_waitcnt vmcnt(%0)" ::"i"(N) : "memory"); }
#define RBAR() asm volatile("s_barrier" ::: "memory")

DI void attn_safe(const u16* __restrict__ Kf, const u16* __restrict__ Vf, bf16x8 q0, bf16x8 q1, f32x16& o, int lane, int hi) {
  const f32x16 zero = {};
  float mref = 0.f;
  o = zero;
#pragma unroll 1
  for (int t = 0; t < 64; ++t) {
    const bf16x8 k0 = *(const bf16x8*)(Kf + t * 1024 + lane * 8), k1 = *(const bf16x8*)(Kf + t * 1024 + 512 + lane * 8);
    const bf16x8 v0 = *(const bf16x8*)(Vf + t * 1024 + lane * 8), v1 = *(const bf16x8*)(Vf + t * 1024 + 512 + lane * 8);
    (void)set_ref(q1, 0.f, hi);
    f32x16 s = MFMA32(k0, q0, zero);
    s = MFMA32(k1, q1, s);
    const float tm = tile_max(s);
    const float mn = set_ref(q1, t == 0 ? tm : __builtin_fmaxf(tm, mref), hi);
    const float al = t == 0 ? 0.f : __builtin_amdgcn_exp2f(mref - mn);
#pragma unroll
    for (int r = 0; r < 16; ++r) o[r] *= al;
    mref = mn;
    s = MFMA32(k0, q0, zero);
    s = MFMA32(k1, q1, s);
    u32x4 p0, p1;
#pragma unroll
    for (int i = 0; i < 4; ++i) {
      p0[i] = cvtpk(__builtin_amdgcn_exp2f(s[2 * i]), __builtin_amdgcn_exp2f(s[2 * i + 1]));
      p1[i] = cvtpk(__builtin_amdgcn_exp2f(s[8 + 2 * i]), __builtin_amdgcn_exp2f(s[8 + 2 * i + 1]));
    }
    o = MFMA32(v0, __builtin_bit_cast(bf16x8, p0), o);
    o = MFMA32(v1, __builtin_bit_cast(bf16x8, p1), o);
  }
}

#define EX4(S, i) do { S[i] = __builtin_amdgcn_exp2f(S[i]); S[i + 1] = __builtin_amdgcn_exp2f(S[i + 1]); \
    S[i + 2] = __builtin_amdgcn_exp2f(S[i + 2]); S[i + 3] = __builtin_amdgcn_exp2f(S[i + 3]); } while (0)
#define PK8(P, S, i) do { P[0] = cvtpk(S[i], S[i + 1]); P[1] = cvtpk(S[i + 2], S[i + 3]); P[2] = cvtpk(S[i + 4], S[i + 5]); P[3] = cvtpk(S[i + 6], S[i + 7]); } while (0)
#define BF(P) __builtin_bit_cast(bf16x8, P)
constexpr int CPRE = 32;
#define WOPF(j) do { if ((j) < CPRE) wf[(j) / 7][(j) % 7] = *(const bf16x8*)(wop + (((j) / 7) * 14 + (j) % 7) * 512); } while (0)
#define RDS(u, off) ldsr16(((u) < 4 ? B0 : B1) + ((u) & 3) * 8192 + (off))
#define CTILE(u, NX, PF) do { \
      \
    sB = MFMA32(k0, qB0, zero); SBAR(); if (NX) k0 = RDS((u) + 1, 0); EX4(sA, 0); WOPF(PF); SBAR(); \
    sB = MFMA32(k1, qB1, sB); SBAR(); if (NX) k1 = RDS((u) + 1, 1024); EX4(sA, 4); PK8(p0, sA, 0); SBAR(); \
    oB = MFMA32(v1, BF(p1), oB); SBAR(); v1 = RDS(u, 3072); EX4(sA, 8); WOPF((PF) + 1); SBAR(); \
    oA = MFMA32(v0, BF(p0), oA); SBAR(); EX4(sA, 12); PK8(p1, sA, 8); SBAR(); \
      \
    sA = MFMA32(k0, qA0, zero); SBAR(); EX4(sB, 0); WOPF((PF) + 2); SBAR(); \
    sA = MFMA32(k1, qA1, sA); SBAR(); EX4(sB, 4); PK8(p0, sB, 0); SBAR(); \
    oA = MFMA32(v1, BF(p1), oA); SBAR(); EX4(sB, 8); WOPF((PF) + 3); SBAR(); \
    oB = MFMA32(v0, BF(p0), oB); SBAR(); if (NX) v0 = RDS((u) + 1, 2048); EX4(sB, 12); PK8(p1, sB, 8); SBAR(); } while (0)
#define CSTAGE(LAST, PF) do { \
    CTILE(0, true, PF); CTILE(1, true, (PF) + 4); CTILE(2, true, (PF) + 8); \
    if (!(LAST)) RBAR(); \
    CTILE(3, !(LAST), (PF) + 12); } while (0)

__global__ __launch_bounds__(512) void k_attn(const u16* __restrict__ x, const u16* __restrict__ Qf, const u16* __restrict__ Kf,
                                              const u16* __restrict__ Vf, const u16* __restrict__ Wof, const u16* __restrict__ w1,
                                              const u16* __restrict__ w2, u16* __restrict__ H, u16* __restrict__ HN) {
  __shared__ __attribute__((aligned(16))) char lds[K2_LDS];
  const int tid = threadIdx.x, lane = tid & 63, w = __builtin_amdgcn_readfirstlane(tid >> 6), hi = lane >> 5;
  const int tg = blockIdx.x;
  STAMP(1, 0);
  const int kh = w >> 2, ng = w & 3;
  const u16* wop = Wof + ((size_t)(7 * ng * 14 + 7 * kh) * 64 + lane) * 8;
  bf16x8 wf[7][7];
#define LDWO(i0, i1) do { _Pragma("unroll") for (int i_ = i0; i_ < i1; ++i_) wf[i_ / 7][i_ % 7] = *(const bf16x8*)(wop + ((i_ / 7) * 14 + i_ % 7) * 512); } while (0)
#define WOGEMM() do { \
    const int g_ = lane >> 4; \
    bf16x8 bo[7]; \
    _Pragma("unroll") for (int k = 0; k < 7; ++k) bo[k] = *(const bf16x8*)(lds + K2_OT + ((4 * (7 * kh + k) + g_) * 8 + (lane & 7)) * 16); \
    f32x4 acc[7]; \
    _Pragma("unroll") for (int n = 0; n < 7; ++n) { \
      acc[n] = (f32x4){0.f, 0.f, 0.f, 0.f}; \
      _Pragma("unroll") for (int k = 0; k < 7; ++k) acc[n] = MFMA16(wf[n][k], bo[k], acc[n]); \
    } \
    if ((lane & 15) < 8) { \
      _Pragma("unroll") for (int n = 0; n < 7; ++n) *(f32x4*)(lds + K2_HB + ((kh * 8 + (lane & 15)) * HBS + 16 * (7 * ng + n) + 4 * g_) * 4) = acc[n]; \
    } } while (0)
  u32x4 xv;
  if (w < 4) {
    const f32x16 zero = {};
    const int qh = w & 1, kp = w >> 1;
    const lds_cptr rp0 = (lds_cptr)lds + K2_RING + lane * 16;
    const lds_cptr rp = rp0 + kp * 4096;
    lds_cptr B0 = rp, B1 = rp + 32768;
    const u16* qp = Qf + ((size_t)((tg * 4 + 2 * qh) * 2) * 64 + lane) * 8;
    bf16x8 qA0 = *(const bf16x8*)(qp), qA1 = *(const bf16x8*)(qp + 512), qB0 = *(const bf16x8*)(qp + 1024), qB1 = *(const bf16x8*)(qp + 1536);
    RBAR();
    bf16x8 k0 = ldsr16(rp0), k1 = ldsr16(rp0 + 1024), v0 = RDS(0, 2048), v1 = v0;
    (void)set_ref(qA1, 0.f, hi);
    (void)set_ref(qB1, 0.f, hi);
    f32x16 sA = MFMA32(k0, qA0, zero), sB = MFMA32(k0, qB0, zero);
    sA = MFMA32(k1, qA1, sA);
    sB = MFMA32(k1, qB1, sB);
    k0 = RDS(0, 0); k1 = RDS(0, 1024);
    (void)set_ref(qA1, tile_max(sA), hi);
    (void)set_ref(qB1, tile_max(sB), hi);
    sA = MFMA32(k0, qA0, zero);
    sA = MFMA32(k1, qA1, sA);
    f32x16 oA = zero, oB = zero;
    u32x4 p0, p1 = {0u, 0u, 0u, 0u};
    SBAR();
    STAMP(1, 8);
#pragma unroll 1
    for (int st = 0; st < 4; ++st) {
      CSTAGE(false, 64);
      B0 = B1; B1 = rp + ((st + 2) & 3) * 32768;
    }
    CSTAGE(false, 0);
    B0 = B1; B1 = rp + 2 * 32768;
    CSTAGE(false, 16);
    B0 = B1; B1 = rp + 3 * 32768;
    CSTAGE(false, 64);
    B0 = B1;
    CSTAGE(true, 64);
    oB = MFMA32(v1, BF(p1), oB);
    STAMP(1, 10);
    {
      const f32x16 snd = kp ? oA : oB;
#pragma unroll
      for (int r4 = 0; r4 < 4; ++r4)
        *(f32x4*)(lds + K2_X + ((w * 4 + r4) * 64 + lane) * 16) = (f32x4){snd[4 * r4], snd[4 * r4 + 1], snd[4 * r4 + 2], snd[4 * r4 + 3]};
    }
    f32x16 o0 = kp ? oB : oA;
    bf16x8 qk0 = kp ? qB0 : qA0, qk1 = kp ? qB1 : qA1;
    asm volatile("" : "+v"(o0), "+v"(qk0), "+v"(qk1));
    SBAR();
    LBAR();
#pragma unroll
    for (int r4 = 0; r4 < 4; ++r4) {
      const f32x4 t = *(const f32x4*)(lds + K2_X + (((w ^ 2) * 4 + r4) * 64 + lane) * 16);
      o0[4 * r4] += t[0]; o0[4 * r4 + 1] += t[1]; o0[4 * r4 + 2] += t[2]; o0[4 * r4 + 3] += t[3];
    }
    const int qb = 2 * qh + kp;
    {
      float chk = 0.f;
#pragma unroll
      for (int r = 0; r < 16; ++r) chk = __builtin_fmaf(o0[r], 0.f, chk);
      const bool bad = !(chk == 0.f) || !(o0[12] < 1.8e19f);
      if (__any(bad)) {
        attn_safe(Kf, Vf, qk0, qk1, o0, lane, hi);
        LDWO(0, CPRE);
      }
    }
    {
      auto sw = __builtin_amdgcn_permlane32_swap(__float_as_uint(o0[12]), __float_as_uint(o0[12]), false, false);
      const float il = 1.f / __uint_as_float(sw[1]);
      const int c = lane & 31;
#pragma unroll
      for (int r4 = 0; r4 < 4; ++r4) {
        const int d = 8 * r4 + 4 * hi, k = 28 * (4 * qb + (c >> 3)) + d;
        if (d < 28)
          *(u32x2*)(lds + K2_OT + ((k >> 3) * 8 + (c & 7)) * 16 + (k & 7) * 2) =
              (u32x2){cvtpk(o0[4 * r4] * il, o0[4 * r4 + 1] * il), cvtpk(o0[4 * r4 + 2] * il, o0[4 * r4 + 3] * il)};
      }
    }
    STAMP(1, 1);
    LBAR();
    STAMP(1, 2);
    LDWO(CPRE, 49);
    xv = *(const u32x4*)(x + (size_t)(tg * 8 + w) * D + (lane < 56 ? lane : 55) * 8);
    SBAR();
    WOGEMM();
  } else {
    const int hw = w - 4;
    __builtin_amdgcn_s_setprio(3);
    const unsigned ring = (unsigned)(uintptr_t)lds + K2_RING + hw * 8192;
    const u16* kp = Kf + (size_t)hw * 2048 + lane * 8;
    const u16* vp = Vf + (size_t)hw * 2048 + lane * 8;
#define DMAS(s_) do { const unsigned sl_ = ring + ((s_) & 3) * 32768; \
    _Pragma("unroll") for (int u_ = 0; u_ < 2; ++u_) { \
      glds16(kp + (s_) * 8192 + u_ * 1024, sl_ + u_ * 4096); glds16(kp + (s_) * 8192 + u_ * 1024 + 512, sl_ + u_ * 4096 + 1024); \
      glds16(vp + (s_) * 8192 + u_ * 1024, sl_ + u_ * 4096 + 2048); glds16(vp + (s_) * 8192 + u_ * 1024 + 512, sl_ + u_ * 4096 + 3072); } } while (0)
    const unsigned dum = (unsigned)(uintptr_t)lds + K2_DUM + hw * 1024;
    const int xg = tg & 7, rk = tg >> 3;
    const int ts = xg < 4 ? 25 * xg : 100 + 24 * (xg - 4), te = ts + (xg < 4 ? 25 : 24);
    const int n0 = ts / 7, n1 = (te - 1) / 7;
    const u16* p1 = w1 + (size_t)(64 * rk + 16 * hw + (lane >> 3)) * F + (lane & 7) * 8;
    const u16* p2 = w2 + (size_t)(56 * rk + (lane >> 3)) * S + (4 * xg + hw) * 64 + (lane & 7) * 8;
#define PFD(i0, i1) do { _Pragma("unroll") for (int i_ = i0; i_ < i1; ++i_) { \
      if (i_ < 10) { const int n_ = n0 + (i_ >> 1) <= n1 ? n0 + (i_ >> 1) : n1; glds16(p1 + n_ * 64 + (i_ & 1) * 8 * F, dum); } \
      else glds16(p2 + (size_t)(i_ - 10) * 8 * S, dum); } } while (0)
    DMAS(0); DMAS(1); DMAS(2);
    waitv<16>(); RBAR();
    LDWO(0, 12); SBAR(); PFD(0, 3);
    waitv<23>(); RBAR();
    DMAS(3); LDWO(12, 24); SBAR(); PFD(3, 6);
    waitv<38>(); RBAR();
    DMAS(4); LDWO(24, 36); SBAR(); PFD(6, 9);
    waitv<38>(); RBAR();
    DMAS(5); LDWO(36, 49); SBAR(); PFD(9, 12);
    waitv<39>(); RBAR();
    DMAS(6); PFD(12, 15);
    waitv<27>(); RBAR();
    DMAS(7); PFD(15, 17);
    waitv<13>(); RBAR();
    waitv<2>(); RBAR();
    xv = *(const u32x4*)(x + (size_t)(tg * 8 + w) * D + (lane < 56 ? lane : 55) * 8);
    SBAR();
    __builtin_amdgcn_s_setprio(0);
    RBAR();
    STAMP(1, 1);
    LBAR();
    STAMP(1, 2);
    WOGEMM();
  }
  LBAR();
  STAMP(1, 3);
  {
    const int s = tg * 8 + w;
    const bool act = lane < 56;
    const int lc = act ? lane : 55;
    float v[8];
    const f32x4 a0 = *(const f32x4*)(lds + K2_HB + (w * HBS + lc * 8) * 4), a1 = *(const f32x4*)(lds + K2_HB + (w * HBS + lc * 8 + 4) * 4);
    const f32x4 c0 = *(const f32x4*)(lds + K2_HB + ((8 + w) * HBS + lc * 8) * 4), c1 = *(const f32x4*)(lds + K2_HB + ((8 + w) * HBS + lc * 8 + 4) * 4);
    u32x4 hv;
#pragma unroll
    for (int i = 0; i < 2; ++i) {
      hv[i] = cvtpk(bflo(xv[i]) + a0[2 * i] + c0[2 * i], bfhi(xv[i]) + a0[2 * i + 1] + c0[2 * i + 1]);
      hv[2 + i] = cvtpk(bflo(xv[2 + i]) + a1[2 * i] + c1[2 * i], bfhi(xv[2 + i]) + a1[2 * i + 1] + c1[2 * i + 1]);
    }
    if (act) *(u32x4*)(H + (size_t)s * D + lane * 8) = hv;
#pragma unroll
    for (int i = 0; i < 4; ++i) { v[2 * i] = act ? bflo(hv[i]) : 0.f; v[2 * i + 1] = act ? bfhi(hv[i]) : 0.f; }
#pragma unroll
    for (int pass = 0; pass < 2; ++pass) {
      float sm = 0.f;
#pragma unroll
      for (int i = 0; i < 8; ++i) sm += v[i];
      const float mean = wave_sum(sm) * (1.f / D);
      float q = 0.f;
#pragma unroll
      for (int i = 0; i < 8; ++i) { v[i] = act ? v[i] - mean : 0.f; q += v[i] * v[i]; }
      const float rs = __builtin_amdgcn_rsqf(wave_sum(q) * (1.f / D) + LN_EPS);
#pragma unroll
      for (int i = 0; i < 8; ++i) v[i] *= rs;
    }
    if (act) *(u32x4*)(lds + K2_HS + w * 912 + lane * 16) = (u32x4){cvtpk(v[0], v[1]), cvtpk(v[2], v[3]), cvtpk(v[4], v[5]), cvtpk(v[6], v[7])};
  }
  LBAR();
  if (tid < D / 2) {
    unsigned r[8];
#pragma unroll
    for (int i = 0; i < 8; ++i) r[i] = *(const unsigned*)(lds + K2_HS + i * 912 + tid * 4);
    u32x4 lo, hi2;
#pragma unroll
    for (int i = 0; i < 4; ++i) {
      lo[i] = (r[2 * i] & 0xffffu) | (r[2 * i + 1] << 16);
      hi2[i] = (r[2 * i] >> 16) | (r[2 * i + 1] & 0xffff0000u);
    }
    u32x4* dst = (u32x4*)(HN + ((size_t)tg * D + 2 * tid) * 8);
    dst[0] = lo; dst[1] = hi2;
  }
  STAMP(1, 4);
}

constexpr int G_RING = 4, G_LDS = 8 * G_RING * 4096;
template <int PH>
__global__ __launch_bounds__(512) void k_mlp(const u16* __restrict__ act, const u16* __restrict__ W, const u16* __restrict__ bias,
                                             const u16* __restrict__ H, u16* __restrict__ out) {
  constexpr int KT = PH == 0 ? S : F, LDW = PH == 0 ? F : S, NK = KT / 256, NWG = 7 * (LDW / 64);
  __shared__ __attribute__((aligned(16))) char lds[G_LDS];
  const int tid = threadIdx.x, lane = tid & 63, w = __builtin_amdgcn_readfirstlane(tid >> 6);
  const int t = xcd_remap(blockIdx.x, NWG), nt = t / 7, mt = t - nt * 7;
  const unsigned lds0 = (unsigned)(uintptr_t)lds + w * (G_RING * 4096);
  const int g = lane >> 4, q4 = (lane & 15) >> 2, p = lane & 3, l15 = lane & 15;
  STAMP(2 + PH, 0);
  __builtin_amdgcn_s_setprio(3);
  const u16* asrc = act + ((size_t)(4 * w + g) * D + 64 * mt + l15) * 8;
  const int r8 = lane >> 3, fe = (r8 >> 1) & 1;
  const u16* wsrc0 = W + (size_t)(32 * w + r8) * LDW + 64 * nt + ((lane & 7) ^ (fe << 1)) * 8;
  const u16* wsrc1 = W + (size_t)(32 * w + 8 + r8) * LDW + 64 * nt + ((lane & 7) ^ ((fe | 2) << 1)) * 8;
  bf16x8 af[NK][4];
#define STAGE(i) do { const unsigned sl_ = lds0 + ((i) % G_RING) * 4096; const size_t o_ = (size_t)(i) * 256 * LDW; \
    glds16(wsrc0 + o_, sl_); glds16(wsrc1 + o_, sl_ + 1024); glds16(wsrc0 + o_ + 16 * LDW, sl_ + 2048); glds16(wsrc1 + o_ + 16 * LDW, sl_ + 3072); \
    const u16* ap_ = asrc + (size_t)(i) * 32 * D * 8; \
    gload16<0>(af[i][0], ap_); gload16<256>(af[i][1], ap_); gload16<512>(af[i][2], ap_); gload16<768>(af[i][3], ap_); } while (0)
#pragma unroll
  for (int i = 0; i < G_RING; ++i) STAGE(i);
  STAMP(2 + PH, 1);
  const int f = ((q4 >> 1) & 1) | ((g & 1) << 1);
  int woff[4];
#pragma unroll
  for (int nb = 0; nb < 4; ++nb) woff[nb] = w * (G_RING * 4096) + (8 * g + q4) * 128 + (((2 * nb + (p >> 1)) ^ (f << 1)) * 16) + 8 * (p & 1);
  f32x4 acc[4][4] = {};
#pragma unroll
  for (int i = 0; i < NK; ++i) {
    constexpr int PF = 0;
    const int rem = NK - 1 - i < G_RING - 1 ? NK - 1 - i : G_RING - 1;
    const bool pfd = i + G_RING > NK;
    if (i == 0) __builtin_amdgcn_s_setprio(3);
    else if (i == 2) __builtin_amdgcn_s_setprio(2);
    else if (i == 4) __builtin_amdgcn_s_setprio(1);
    else if (i == 6) __builtin_amdgcn_s_setprio(0);
    if (rem == 3) { if (pfd) waitv4<24 + PF>(af[i][0], af[i][1], af[i][2], af[i][3]); else waitv4<24>(af[i][0], af[i][1], af[i][2], af[i][3]); }
    else if (rem == 2) waitv4<16 + PF>(af[i][0], af[i][1], af[i][2], af[i][3]);
    else if (rem == 1) waitv4<8 + PF>(af[i][0], af[i][1], af[i][2], af[i][3]);
    else waitv4<PF>(af[i][0], af[i][1], af[i][2], af[i][3]);
    bf16x8 wf[4];
#pragma unroll
    for (int nb = 0; nb < 4; ++nb) {
      const lds_cptr sp = (lds_cptr)lds + woff[nb] + (i % G_RING) * 4096;
      wf[nb] = cat8(trread(sp), trread(sp + 512));
    }
    if (i + G_RING < NK) {
      asm volatile("s_waitcnt lgkmcnt(0)" ::: "memory");
      STAGE(i + G_RING);
    }
#pragma unroll
    for (int nb = 0; nb < 4; ++nb)
#pragma unroll
      for (int mb = 0; mb < 4; ++mb)
        acc[nb][mb] = PH == 0 ? MFMA16(wf[nb], af[i][mb], acc[nb][mb]) : MFMA16(af[i][mb], wf[nb], acc[nb][mb]);
  }
#undef STAGE
  STAMP(2 + PH, 4);
  const int enb = w >> 1, emb = 2 * (w & 1);
  u32x2 eb, eh0, eh1;
  if (PH == 0) {
    eb = *(const u32x2*)(bias + 64 * nt + 16 * enb + 4 * g);
  } else {
    const int s = 64 * nt + 16 * enb + l15;
    eb[0] = bias[s];
    eh0 = *(const u32x2*)(H + (size_t)s * D + 64 * mt + 16 * emb + 4 * g);
    eh1 = *(const u32x2*)(H + (size_t)s * D + 64 * mt + 16 * emb + 16 + 4 * g);
  }
  {
    f32x4* xb = (f32x4*)lds;
#pragma unroll
    for (int nb = 0; nb < 4; ++nb)
#pragma unroll
      for (int mb = 0; mb < 4; ++mb) xb[(w * 16 + nb * 4 + mb) * 64 + lane] = acc[nb][mb];
  }
  LBAR();
  {
    const f32x4* xb = (const f32x4*)lds;
#pragma unroll
    for (int e = 0; e < 2; ++e) {
      f32x4 a = xb[(2 * w + e) * 64 + lane];
#pragma unroll
      for (int v = 1; v < 8; ++v) a += xb[(v * 16 + 2 * w + e) * 64 + lane];
      const int mb = emb + e;
      if (PH == 0) {
        const int fi = 64 * nt + 16 * enb + 4 * g, d = 64 * mt + 16 * mb + l15;
        float z[4] = {a[0] + bflo(eb[0]), a[1] + bfhi(eb[0]), a[2] + bflo(eb[1]), a[3] + bfhi(eb[1])};
#pragma unroll
        for (int i = 0; i < 4; ++i) {
          const float zz = z[i], a2 = zz * (1.f + 0.044715f * zz * zz) * (-2.f * 0.7978845608028654f * 1.4426950408889634f);
          z[i] = zz * __builtin_amdgcn_rcpf(1.f + __builtin_amdgcn_exp2f(a2));
        }
        *(u32x2*)(out + ((size_t)(fi >> 3) * D + d) * 8 + (fi & 7)) = (u32x2){cvtpk(z[0], z[1]), cvtpk(z[2], z[3])};
      } else {
        const int d = 64 * mt + 16 * mb + 4 * g, s = 64 * nt + 16 * enb + l15;
        const float bb = bflo(eb[0]);
        const u32x2 hv = e ? eh1 : eh0;
        *(u32x2*)(out + (size_t)s * D + d) =
            (u32x2){cvtpk(a[0] + bb + bflo(hv[0]), a[1] + bb + bfhi(hv[0])), cvtpk(a[2] + bb + bflo(hv[1]), a[3] + bb + bfhi(hv[1]))};
      }
    }
  }
  STAMP(2 + PH, 5);
}

extern "C" void kernel_launch(void* const* d_in, const int* in_sizes, int n_in, void* d_out, int out_size, void* d_ws, size_t ws_size,
                              hipStream_t stream) {
  const u16* x = (const u16*)d_in[0];
  const u16* wq = (const u16*)d_in[1];
  const u16* wk = (const u16*)d_in[2];
  const u16* wv = (const u16*)d_in[3];
  const u16* wo = (const u16*)d_in[4];
  const u16* w1 = (const u16*)d_in[6];
  const u16* b1 = (const u16*)d_in[7];
  const u16* w2 = (const u16*)d_in[8];
  const u16* b2 = (const u16*)d_in[9];
  char* ws = (char*)d_ws;
  u16 *Qf = (u16*)(ws + WS_QF), *Kf = (u16*)(ws + WS_KF), *Vf = (u16*)(ws + WS_VF), *Wof = (u16*)(ws + WS_WOF);
  u16 *H = (u16*)(ws + WS_H), *HN = (u16*)(ws + WS_HN), *U = (u16*)(ws + WS_U);
  k_qkv<<<256, 512, 0, stream>>>(x, wq, wk, wv, wo, Qf, Kf, Vf, Wof);
  k_attn<<<256, 512, 0, stream>>>(x, Qf, Kf, Vf, Wof, w1, w2, H, HN);
  k_mlp<0><<<196, 512, 0, stream>>>(HN, w1, b1, H, U);
  k_mlp<1><<<224, 512, 0, stream>>>(U, w2, b2, H, (u16*)d_out);
}
```

```cpp
#include <hip/hip_runtime.h>
#include <hip/hip_bf16.h>

typedef unsigned short u16;
using bf16x8 = __attribute__((ext_vector_type(8))) short;
using s16x4 = __attribute__((ext_vector_type(4))) short;
using f32x4 = __attribute__((ext_vector_type(4))) float;
using f32x16 = __attribute__((ext_vector_type(16))) float;
using u32x4 = __attribute__((ext_vector_type(4))) unsigned;
using u32x2 = __attribute__((ext_vector_type(2))) unsigned;
typedef short v4i16_t __attribute__((ext_vector_type(4)));
typedef __attribute__((address_space(3))) const char* lds_cptr;
typedef __bf16 bf16x2_t __attribute__((ext_vector_type(2)));
typedef float f32x2_t __attribute__((ext_vector_type(2)));

#define DI __device__ __forceinline__
#define MFMA32(a, b, c) __builtin_amdgcn_mfma_f32_32x32x16_bf16(a, b, c, 0, 0, 0)
#define MFMA16(a, b, c) __builtin_amdgcn_mfma_f32_16x16x32_bf16(a, b, c, 0, 0, 0)
#define SBAR() __builtin_amdgcn_sched_barrier(0)
#define LBAR() asm volatile("s_waitcnt lgkmcnt(0)\n\ts_barrier" ::: "memory")
#define WAITV_BAR(N) asm volatile("s_waitcnt vmcnt(" #N ") lgkmcnt(0)\n\ts_barrier" ::: "memory")

constexpr int S = 2048, D = 448, DH = 28, F = 1792;
constexpr float QSCALE = 0.18898223650461362f * 1.4426950408889634f;
constexpr float LN_EPS = 1e-5f;

constexpr size_t WS_QF = 0;
constexpr size_t WS_KF = WS_QF + 2097152;
constexpr size_t WS_VF = WS_KF + 131072;
constexpr size_t WS_WOF = WS_VF + 131072;
constexpr size_t WS_H = WS_WOF + 401408;
constexpr size_t WS_HN = WS_H + 1835008;
constexpr size_t WS_U = WS_HN + 1835008;

#define STAMP(k, i)
#define CSTAMP(k, i)

DI unsigned cvtpk(float lo, float hi) {
  const f32x2_t v = {lo, hi};
  return __builtin_bit_cast(unsigned, __builtin_convertvector(v, bf16x2_t));
}
DI float bflo(unsigned u) { return __uint_as_float(u << 16); }
DI float bfhi(unsigned u) { return __uint_as_float(u & 0xffff0000u); }
template <int CTRL> DI float dppf(float v) {
  return __uint_as_float(__builtin_amdgcn_update_dpp(0u, __float_as_uint(v), CTRL, 0xf, 0xf, true));
}
DI float wave_sum(float v) {
  v += dppf<0xB1>(v);
  v += dppf<0x4E>(v);
  v += dppf<0x141>(v);
  v += dppf<0x140>(v);
  auto a = __builtin_amdgcn_permlane16_swap(__float_as_uint(v), __float_as_uint(v), false, false);
  v = __uint_as_float(a[0]) + __uint_as_float(a[1]);
  auto b = __builtin_amdgcn_permlane32_swap(__float_as_uint(v), __float_as_uint(v), false, false);
  return __uint_as_float(b[0]) + __uint_as_float(b[1]);
}
DI float half_max(float v) {
  auto b = __builtin_amdgcn_permlane32_swap(__float_as_uint(v), __float_as_uint(v), false, false);
  return __builtin_fmaxf(__uint_as_float(b[0]), __uint_as_float(b[1]));
}
DI void glds16(const void* g, unsigned lds_base) {
  unsigned sv;
  asm volatile("s_mov_b32 %0, m0\n\ts_mov_b32 m0, %2\n\ts_nop 0\n\tglobal_load_lds_dwordx4 %1, off\n\ts_mov_b32 m0, %0"
               : "=&s"(sv) : "v"(g), "s"(lds_base) : "memory");
}
DI void glds4(const void* g, unsigned lds_base) {
  unsigned sv;
  asm volatile("s_mov_b32 %0, m0\n\ts_mov_b32 m0, %2\n\ts_nop 0\n\tglobal_load_lds_dword %1, off\n\ts_mov_b32 m0, %0"
               : "=&s"(sv) : "v"(g), "s"(lds_base) : "memory");
}
template <int N> DI void waitv4(bf16x8& a, bf16x8& b, bf16x8& c, bf16x8& d) {
  asm volatile("s_waitcnt vmcnt(%4)" : "+v"(a), "+v"(b), "+v"(c), "+v"(d) : "i"(N) : "memory");
}
template <int OFF> DI void gload16(bf16x8& d, const void* p) {
  asm volatile("global_load_dwordx4 %0, %1, off offset:%2" : "=v"(d) : "v"(p), "i"(OFF) : "memory");
}
DI s16x4 trread(lds_cptr p) {
  return __builtin_bit_cast(s16x4, __builtin_amdgcn_ds_read_tr16_b64_v4i16((__attribute__((address_space(3))) v4i16_t*)p));
}
DI bf16x8 ldsr16(lds_cptr p) { return *(const __attribute__((address_space(3))) bf16x8*)p; }
DI bf16x8 cat8(s16x4 a, s16x4 b) { return __builtin_shufflevector(a, b, 0, 1, 2, 3, 4, 5, 6, 7); }
DI int xcd_remap(int bid, int nwg) {
  const int q = nwg >> 3, r = nwg & 7, x = bid & 7, o = bid >> 3;
  return (x < r ? x * (q + 1) : r * (q + 1) + (x - r) * q) + o;
}

constexpr int K1_WI = 448 * 128, K1_X = 2 * K1_WI, K1_XS = 528, K1_LDS = K1_X + 64 * K1_XS;
__global__ __launch_bounds__(512) void k_qkv(const u16* __restrict__ x, const u16* __restrict__ wq, const u16* __restrict__ wk,
                                             const u16* __restrict__ wv, const u16* __restrict__ wo, u16* __restrict__ Qf,
                                             u16* __restrict__ Kf, u16* __restrict__ Vf, u16* __restrict__ Wof) {
  __shared__ __attribute__((aligned(16))) char lds[K1_LDS];
  const int tid = threadIdx.x, lane = tid & 63, w = __builtin_amdgcn_readfirstlane(tid >> 6);
  const int t = xcd_remap(blockIdx.x, 256), rt = t >> 2, ct = t & 3;
  const unsigned lds0 = (unsigned)(uintptr_t)lds;
  STAMP(0, 0);
  bf16x8 xr[4];
  const bool act = lane < 56;
  {
    const u16* xp_ = x + (size_t)(rt * 32 + w * 4) * D + (act ? lane : 55) * 8;
    gload16<0>(xr[0], xp_); gload16<896>(xr[1], xp_); gload16<1792>(xr[2], xp_); gload16<2688>(xr[3], xp_);
  }
#pragma unroll
  for (int im = 0; im < 2; ++im) {
    const int c64 = 2 * ct + im;
    if (c64 < 7) {
#pragma unroll
      for (int i = 0; i < 7; ++i) {
        const int piece = w * 7 + i, row = piece * 8 + (lane >> 3);
        const int f = ((row >> 1) & 1) | (((row >> 3) & 1) << 1);
        const int c = (lane & 7) ^ (f << 1);
        glds16(wq + (size_t)row * D + c64 * 64 + c * 8, lds0 + im * K1_WI + piece * 1024);
      }
    } else {
#pragma unroll
      for (int i = 0; i < 7; ++i) {
        const int pc = w * 7 + i < 50 ? w * 7 + i : 49, mat = pc >= 25, piece = pc - 25 * mat;
        const int e = piece * 512 + lane * 8;
        glds16((mat ? wv : wk) + (e < 448 * 28 ? e : 448 * 28 - 8), lds0 + im * K1_WI + mat * 25600 + piece * 1024);
      }
    }
  }
  u16 wt[8];
  const int frag0 = blockIdx.x + 256 * (w & 1);
  const bool dofrag = w < 2 && frag0 < 392;
  const int frag = dofrag ? frag0 : 0;
  {
    const int nb = frag / 14, ks = frag - nb * 14;
#pragma unroll
    for (int j = 0; j < 8; ++j) wt[j] = wo[(size_t)(32 * ks + 8 * (lane >> 4) + j) * D + 16 * nb + (lane & 15)];
  }
  SBAR();
  waitv4<22>(xr[0], xr[1], xr[2], xr[3]);
  {
    u32x4 xv[4];
#pragma unroll
    for (int r = 0; r < 4; ++r) xv[r] = __builtin_bit_cast(u32x4, xr[r]);
    float v[4][8], mean[4], rs[4];
#pragma unroll
    for (int r = 0; r < 4; ++r) {
      float s = 0.f;
#pragma unroll
      for (int i = 0; i < 4; ++i) {
        v[r][2 * i] = act ? bflo(xv[r][i]) : 0.f; v[r][2 * i + 1] = act ? bfhi(xv[r][i]) : 0.f;
        s += v[r][2 * i] + v[r][2 * i + 1];
      }
      mean[r] = s;
    }
#pragma unroll
    for (int r = 0; r < 4; ++r) mean[r] = wave_sum(mean[r]) * (1.f / D);
#pragma unroll
    for (int r = 0; r < 4; ++r) {
      float q = 0.f;
#pragma unroll
      for (int i = 0; i < 8; ++i) { v[r][i] = act ? v[r][i] - mean[r] : 0.f; q += v[r][i] * v[r][i]; }
      rs[r] = q;
    }
#pragma unroll
    for (int r = 0; r < 4; ++r) rs[r] = __builtin_amdgcn_rsqf(wave_sum(rs[r]) * (1.f / D) + LN_EPS);
    char* xdst = lds + K1_X + lane * K1_XS + w * 64;
#pragma unroll
    for (int r = 0; r < 4; ++r) {
      u32x4 o;
#pragma unroll
      for (int i = 0; i < 4; ++i) o[i] = cvtpk(v[r][2 * i] * rs[r], v[r][2 * i + 1] * rs[r]);
      *(u32x4*)(xdst + r * 16) = o;
    }
  }
  STAMP(0, 1);
  WAITV_BAR(0);
  STAMP(0, 2);
  const int nb = w & 3, im = w >> 2, c64 = 2 * ct + im, g = lane >> 4, q4 = (lane & 15) >> 2, p = lane & 3, l15 = lane & 15;
  const int f = ((q4 >> 1) & 1) | ((g & 1) << 1);
  const bool kv = c64 == 7;
  const int rs_ = kv ? 56 : 128;
  const lds_cptr wp = (lds_cptr)lds + im * K1_WI + (8 * g + q4) * rs_ +
                      (kv ? (nb >> 1) * 25600 + (nb & 1) * 32 + 8 * p : (((2 * nb + (p >> 1)) ^ (f << 1)) * 16) + 8 * (p & 1));
  const lds_cptr xp = (lds_cptr)lds + K1_X + g * K1_XS + l15 * 16;
  f32x4 acc0 = {0.f, 0.f, 0.f, 0.f}, acc1 = {0.f, 0.f, 0.f, 0.f};
  {
    bf16x8 a[14], b0[14], b1[14];
#pragma unroll
    for (int ks = 0; ks < 14; ++ks) {
      a[ks] = cat8(trread(wp + ks * 32 * rs_), trread(wp + ks * 32 * rs_ + 4 * rs_));
      b0[ks] = ldsr16(xp + ks * 4 * K1_XS);
      b1[ks] = ldsr16(xp + ks * 4 * K1_XS + 256);
    }
    SBAR();
#pragma unroll
    for (int ks = 0; ks < 14; ++ks) {
      acc0 = MFMA16(a[ks], b0[ks], acc0);
      acc1 = MFMA16(a[ks], b1[ks], acc1);
    }
  }
  STAMP(0, 3);
#pragma unroll
  for (int tb = 0; tb < 2; ++tb) {
    const f32x4 a = tb ? acc1 : acc0;
    const int s = rt * 32 + 16 * tb + l15;
    const int nl = 16 * nb + 4 * g;
    if (c64 < 7) {
      const int n0 = 64 * c64 + nl, h = n0 / 28, d0 = n0 - 28 * h;
      const int tg = s >> 3, c = (h & 3) * 8 + (s & 7), b = h >> 2;
      const size_t off = ((((size_t)(tg * 4 + b) * 2 + (d0 >> 4)) * 64) + ((d0 >> 3) & 1) * 32 + c) * 8 + (d0 & 7);
      *(u32x2*)(Qf + off) = (u32x2){cvtpk(a[0] * QSCALE, a[1] * QSCALE), cvtpk(a[2] * QSCALE, a[3] * QSCALE)};
    } else {
      const int kt = s >> 5, kk = s & 31;
      const int d0 = 16 * (nb & 1) + 4 * g;
      if (nb < 2) {
        const size_t off = ((((size_t)kt * 2 + (d0 >> 4)) * 64) + ((d0 >> 3) & 1) * 32 + kk) * 8 + (d0 & 7);
        u32x2 v = {cvtpk(a[0], a[1]), cvtpk(a[2], a[3])};
        if (d0 == 28) v = (u32x2){0x3f803f80u, 0x00003f80u};
        *(u32x2*)(Kf + off) = v;
      } else {
        const int sp = kk >> 4, j = ((kk >> 3) & 1) * 4 + (kk & 3), hi = (kk >> 2) & 1;
        const size_t off = ((((size_t)kt * 2 + sp) * 64) + hi * 32 + d0) * 8 + j;
        unsigned v0 = cvtpk(a[0], a[1]), v1 = cvtpk(a[2], a[3]);
        if (d0 == 28) { v0 = 0x00003f80u; v1 = 0u; }
        Vf[off] = (u16)v0; Vf[off + 8] = (u16)(v0 >> 16); Vf[off + 16] = (u16)v1; Vf[off + 24] = (u16)(v1 >> 16);
      }
    }
  }
  if (dofrag) {
    u32x4 o;
#pragma unroll
    for (int i = 0; i < 4; ++i) o[i] = (unsigned)wt[2 * i] | ((unsigned)wt[2 * i + 1] << 16);
    *(u32x4*)(Wof + ((size_t)frag * 64 + lane) * 8) = o;
  }
  STAMP(0, 4);
}

constexpr int K2_RING = 0, K2_OT = 131072, K2_DUM = K2_OT + 7168, K2_X = K2_DUM + 4096, K2_LDS = K2_X + 16384;
constexpr int K2_HB = 0, HBS = 452, K2_HS = 32768;

DI float set_ref(bf16x8& q1, float m, int hi) {
  const float a = bflo(cvtpk(-m, 0.f)), r1 = -m - a, b = bflo(cvtpk(r1, 0.f)), c = bflo(cvtpk(r1 - b, 0.f));
  u32x4 v = __builtin_bit_cast(u32x4, q1);
  v[2] = hi ? cvtpk(a, b) : v[2];
  v[3] = hi ? cvtpk(c, 0.f) : v[3];
  q1 = __builtin_bit_cast(bf16x8, v);
  return -((a + b) + c);
}
DI float tile_max(const f32x16& s) {
  float a = __builtin_fmaxf(__builtin_fmaxf(s[0], s[1]), s[2]), b = __builtin_fmaxf(__builtin_fmaxf(s[3], s[4]), s[5]);
  a = __builtin_fmaxf(__builtin_fmaxf(a, s[6]), s[7]); b = __builtin_fmaxf(__builtin_fmaxf(b, s[8]), s[9]);
  a = __builtin_fmaxf(__builtin_fmaxf(a, s[10]), s[11]); b = __builtin_fmaxf(__builtin_fmaxf(b, s[12]), s[13]);
  a = __builtin_fmaxf(__builtin_fmaxf(a, s[14]), s[15]);
  return half_max(__builtin_fmaxf(a, b));
}
template <int N> DI void waitv() { asm volatile("s_waitcnt vmcnt(%0)" ::"i"(N) : "memory"); }
#define RBAR() asm volatile("s_barrier" ::: "memory")

DI void attn_safe(const u16* __restrict__ Kf, const u16* __restrict__ Vf, bf16x8 q0, bf16x8 q1, f32x16& o, int lane, int hi) {
  const f32x16 zero = {};
  float mref = 0.f;
  o = zero;
#pragma unroll 1
  for (int t = 0; t < 64; ++t) {
    const bf16x8 k0 = *(const bf16x8*)(Kf + t * 1024 + lane * 8), k1 = *(const bf16x8*)(Kf + t * 1024 + 512 + lane * 8);
    const bf16x8 v0 = *(const bf16x8*)(Vf + t * 1024 + lane * 8), v1 = *(const bf16x8*)(Vf + t * 1024 + 512 + lane * 8);
    (void)set_ref(q1, 0.f, hi);
    f32x16 s = MFMA32(k0, q0, zero);
    s = MFMA32(k1, q1, s);
    const float tm = tile_max(s);
    const float mn = set_ref(q1, t == 0 ? tm : __builtin_fmaxf(tm, mref), hi);
    const float al = t == 0 ? 0.f : __builtin_amdgcn_exp2f(mref - mn);
#pragma unroll
    for (int r = 0; r < 16; ++r) o[r] *= al;
    mref = mn;
    s = MFMA32(k0, q0, zero);
    s = MFMA32(k1, q1, s);
    u32x4 p0, p1;
#pragma unroll
    for (int i = 0; i < 4; ++i) {
      p0[i] = cvtpk(__builtin_amdgcn_exp2f(s[2 * i]), __builtin_amdgcn_exp2f(s[2 * i + 1]));
      p1[i] = cvtpk(__builtin_amdgcn_exp2f(s[8 + 2 * i]), __builtin_amdgcn_exp2f(s[8 + 2 * i + 1]));
    }
    o = MFMA32(v0, __builtin_bit_cast(bf16x8, p0), o);
    o = MFMA32(v1, __builtin_bit_cast(bf16x8, p1), o);
  }
}

#define EX4(S, i) do { S[i] = __builtin_amdgcn_exp2f(S[i]); S[i + 1] = __builtin_amdgcn_exp2f(S[i + 1]); \
    S[i + 2] = __builtin_amdgcn_exp2f(S[i + 2]); S[i + 3] = __builtin_amdgcn_exp2f(S[i + 3]); } while (0)
#define PK8(P, S, i) do { P[0] = cvtpk(S[i], S[i + 1]); P[1] = cvtpk(S[i + 2], S[i + 3]); P[2] = cvtpk(S[i + 4], S[i + 5]); P[3] = cvtpk(S[i + 6], S[i + 7]); } while (0)
#define BF(P) __builtin_bit_cast(bf16x8, P)
constexpr int CPRE = 32;
#define WOPF(j) do { if ((j) < CPRE) wf[(j) / 7][(j) % 7] = *(const bf16x8*)(wop + (((j) / 7) * 14 + (j) % 7) * 512); } while (0)
#define RDS(u, off) ldsr16(((u) < 4 ? B0 : B1) + ((u) & 3) * 8192 + (off))
#define CTILE(u, NX, PF) do { \
      \
    sB = MFMA32(k0, qB0, zero); SBAR(); if (NX) k0 = RDS((u) + 1, 0); EX4(sA, 0); WOPF(PF); SBAR(); \
    sB = MFMA32(k1, qB1, sB); SBAR(); if (NX) k1 = RDS((u) + 1, 1024); EX4(sA, 4); PK8(p0, sA, 0); SBAR(); \
    oB = MFMA32(v1, BF(p1), oB); SBAR(); v1 = RDS(u, 3072); EX4(sA, 8); WOPF((PF) + 1); SBAR(); \
    oA = MFMA32(v0, BF(p0), oA); SBAR(); EX4(sA, 12); PK8(p1, sA, 8); SBAR(); \
      \
    sA = MFMA32(k0, qA0, zero); SBAR(); EX4(sB, 0); WOPF((PF) + 2); SBAR(); \
    sA = MFMA32(k1, qA1, sA); SBAR(); EX4(sB, 4); PK8(p0, sB, 0); SBAR(); \
    oA = MFMA32(v1, BF(p1), oA); SBAR(); EX4(sB, 8); WOPF((PF) + 3); SBAR(); \
    oB = MFMA32(v0, BF(p0), oB); SBAR(); if (NX) v0 = RDS((u) + 1, 2048); EX4(sB, 12); PK8(p1, sB, 8); SBAR(); } while (0)
#define CSTAGE(LAST, PF) do { \
    CTILE(0, true, PF); CTILE(1, true, (PF) + 4); CTILE(2, true, (PF) + 8); \
    if (!(LAST)) RBAR(); \
    CTILE(3, !(LAST), (PF) + 12); } while (0)

__global__ __launch_bounds__(512) void k_attn(const u16* __restrict__ x, const u16* __restrict__ Qf, const u16* __restrict__ Kf,
                                              const u16* __restrict__ Vf, const u16* __restrict__ Wof, const u16* __restrict__ w1,
                                              const u16* __restrict__ w2, u16* __restrict__ H, u16* __restrict__ HN) {
  __shared__ __attribute__((aligned(16))) char lds[K2_LDS];
  const int tid = threadIdx.x, lane = tid & 63, w = __builtin_amdgcn_readfirstlane(tid >> 6), hi = lane >> 5;
  const int tg = blockIdx.x;
  STAMP(1, 0);
  const int kh = w >> 2, ng = w & 3;
  const u16* wop = Wof + ((size_t)(7 * ng * 14 + 7 * kh) * 64 + lane) * 8;
  bf16x8 wf[7][7];
#define LDWO(i0, i1) do { _Pragma("unroll") for (int i_ = i0; i_ < i1; ++i_) wf[i_ / 7][i_ % 7] = *(const bf16x8*)(wop + ((i_ / 7) * 14 + i_ % 7) * 512); } while (0)
#define WOGEMM() do { \
    const int g_ = lane >> 4; \
    bf16x8 bo[7]; \
    _Pragma("unroll") for (int k = 0; k < 7; ++k) bo[k] = *(const bf16x8*)(lds + K2_OT + ((4 * (7 * kh + k) + g_) * 8 + (lane & 7)) * 16); \
    f32x4 acc[7]; \
    _Pragma("unroll") for (int n = 0; n < 7; ++n) { \
      acc[n] = (f32x4){0.f, 0.f, 0.f, 0.f}; \
      _Pragma("unroll") for (int k = 0; k < 7; ++k) acc[n] = MFMA16(wf[n][k], bo[k], acc[n]); \
    } \
    if ((lane & 15) < 8) { \
      _Pragma("unroll") for (int n = 0; n < 7; ++n) *(f32x4*)(lds + K2_HB + ((kh * 8 + (lane & 15)) * HBS + 16 * (7 * ng + n) + 4 * g_) * 4) = acc[n]; \
    } } while (0)
  u32x4 xv;
  if (w < 4) {
    const f32x16 zero = {};
    const int qh = w & 1, kp = w >> 1;
    const lds_cptr rp0 = (lds_cptr)lds + K2_RING + lane * 16;
    const lds_cptr rp = rp0 + kp * 4096;
    lds_cptr B0 = rp, B1 = rp + 32768;
    const u16* qp = Qf + ((size_t)((tg * 4 + 2 * qh) * 2) * 64 + lane) * 8;
    bf16x8 qA0 = *(const bf16x8*)(qp), qA1 = *(const bf16x8*)(qp + 512), qB0 = *(const bf16x8*)(qp + 1024), qB1 = *(const bf16x8*)(qp + 1536);
    RBAR();
    bf16x8 k0 = ldsr16(rp0), k1 = ldsr16(rp0 + 1024), v0 = RDS(0, 2048), v1 = v0;
    (void)set_ref(qA1, 0.f, hi);
    (void)set_ref(qB1, 0.f, hi);
    f32x16 sA = MFMA32(k0, qA0, zero), sB = MFMA32(k0, qB0, zero);
    sA = MFMA32(k1, qA1, sA);
    sB = MFMA32(k1, qB1, sB);
    k0 = RDS(0, 0); k1 = RDS(0, 1024);
    (void)set_ref(qA1, tile_max(sA), hi);
    (void)set_ref(qB1, tile_max(sB), hi);
    sA = MFMA32(k0, qA0, zero);
    sA = MFMA32(k1, qA1, sA);
    f32x16 oA = zero, oB = zero;
    u32x4 p0, p1 = {0u, 0u, 0u, 0u};
    SBAR();
    STAMP(1, 8);
#pragma unroll 1
    for (int st = 0; st < 4; ++st) {
      CSTAGE(false, 64);
      B0 = B1; B1 = rp + ((st + 2) & 3) * 32768;
    }
    CSTAGE(false, 0);
    B0 = B1; B1 = rp + 2 * 32768;
    CSTAGE(false, 16);
    B0 = B1; B1 = rp + 3 * 32768;
    CSTAGE(false, 64);
    B0 = B1;
    CSTAGE(true, 64);
    oB = MFMA32(v1, BF(p1), oB);
    STAMP(1, 10);
    {
      const f32x16 snd = kp ? oA : oB;
#pragma unroll
      for (int r4 = 0; r4 < 4; ++r4)
        *(f32x4*)(lds + K2_X + ((w * 4 + r4) * 64 + lane) * 16) = (f32x4){snd[4 * r4], snd[4 * r4 + 1], snd[4 * r4 + 2], snd[4 * r4 + 3]};
    }
    f32x16 o0 = kp ? oB : oA;
    bf16x8 qk0 = kp ? qB0 : qA0, qk1 = kp ? qB1 : qA1;
    asm volatile("" : "+v"(o0), "+v"(qk0), "+v"(qk1));
    SBAR();
    LBAR();
#pragma unroll
    for (int r4 = 0; r4 < 4; ++r4) {
      const f32x4 t = *(const f32x4*)(lds + K2_X + (((w ^ 2) * 4 + r4) * 64 + lane) * 16);
      o0[4 * r4] += t[0]; o0[4 * r4 + 1] += t[1]; o0[4 * r4 + 2] += t[2]; o0[4 * r4 + 3] += t[3];
    }
    const int qb = 2 * qh + kp;
    {
      float chk = 0.f;
#pragma unroll
      for (int r = 0; r < 16; ++r) chk = __builtin_fmaf(o0[r], 0.f, chk);
      const bool bad = !(chk == 0.f) || !(o0[12] < 1.8e19f);
      if (__any(bad)) {
        attn_safe(Kf, Vf, qk0, qk1, o0, lane, hi);
        LDWO(0, CPRE);
      }
    }
    {
      auto sw = __builtin_amdgcn_permlane32_swap(__float_as_uint(o0[12]), __float_as_uint(o0[12]), false, false);
      const float il = 1.f / __uint_as_float(sw[1]);
      const int c = lane & 31;
#pragma unroll
      for (int r4 = 0; r4 < 4; ++r4) {
        const int d = 8 * r4 + 4 * hi, k = 28 * (4 * qb + (c >> 3)) + d;
        if (d < 28)
          *(u32x2*)(lds + K2_OT + ((k >> 3) * 8 + (c & 7)) * 16 + (k & 7) * 2) =
              (u32x2){cvtpk(o0[4 * r4] * il, o0[4 * r4 + 1] * il), cvtpk(o0[4 * r4 + 2] * il, o0[4 * r4 + 3] * il)};
      }
    }
    STAMP(1, 1);
    LBAR();
    STAMP(1, 2);
    LDWO(CPRE, 49);
    xv = *(const u32x4*)(x + (size_t)(tg * 8 + w) * D + (lane < 56 ? lane : 55) * 8);
    SBAR();
    WOGEMM();
  } else {
    const int hw = w - 4;
    __builtin_amdgcn_s_setprio(3);
    const unsigned ring = (unsigned)(uintptr_t)lds + K2_RING + hw * 8192;
    const u16* kp = Kf + (size_t)hw * 2048 + lane * 8;
    const u16* vp = Vf + (size_t)hw * 2048 + lane * 8;
#define DMAS(s_) do { const unsigned sl_ = ring + ((s_) & 3) * 32768; \
    _Pragma("unroll") for (int u_ = 0; u_ < 2; ++u_) { \
      glds16(kp + (s_) * 8192 + u_ * 1024, sl_ + u_ * 4096); glds16(kp + (s_) * 8192 + u_ * 1024 + 512, sl_ + u_ * 4096 + 1024); \
      glds16(vp + (s_) * 8192 + u_ * 1024, sl_ + u_ * 4096 + 2048); glds16(vp + (s_) * 8192 + u_ * 1024 + 512, sl_ + u_ * 4096 + 3072); } } while (0)
    const unsigned dum = (unsigned)(uintptr_t)lds + K2_DUM + hw * 1024;
    const int xg = tg & 7, rk = tg >> 3;
    const int ts = xg < 4 ? 25 * xg : 100 + 24 * (xg - 4), te = ts + (xg < 4 ? 25 : 24);
    const int n0 = ts / 7, n1 = (te - 1) / 7;
    const int nb0 = n0 + 3 <= 27 ? n0 : 24;
    const u16* p1 = w1 + (size_t)(64 * rk + 16 * hw + (lane >> 5)) * F + nb0 * 64 + (lane & 31) * 8;
    const u16* p1b = w1 + (size_t)(64 * rk + 16 * hw + (lane >> 3)) * F + n1 * 64 + (lane & 7) * 8;
    const u16* p2 = w2 + (size_t)(56 * rk + 14 * hw + (lane >> 5)) * S + 4 * xg * 64 + (lane & 31) * 8;
#define PFD(i0, i1) do { _Pragma("unroll") for (int i_ = i0; i_ < i1; ++i_) { \
      if (i_ < 8) glds16(p1 + (size_t)i_ * 2 * F, dum); \
      else if (i_ < 10) glds16(p1b + (size_t)(i_ - 8) * 8 * F, dum); \
      else glds16(p2 + (size_t)(i_ - 10) * 2 * S, dum); } } while (0)
    DMAS(0); DMAS(1); DMAS(2);
    waitv<16>(); RBAR();
    LDWO(0, 12); SBAR(); PFD(0, 3);
    waitv<23>(); RBAR();
    DMAS(3); LDWO(12, 24); SBAR(); PFD(3, 6);
    waitv<38>(); RBAR();
    DMAS(4); LDWO(24, 36); SBAR(); PFD(6, 9);
    waitv<38>(); RBAR();
    DMAS(5); LDWO(36, 49); SBAR(); PFD(9, 12);
    waitv<39>(); RBAR();
    DMAS(6); PFD(12, 15);
    waitv<27>(); RBAR();
    DMAS(7); PFD(15, 17);
    waitv<13>(); RBAR();
    waitv<2>(); RBAR();
    xv = *(const u32x4*)(x + (size_t)(tg * 8 + w) * D + (lane < 56 ? lane : 55) * 8);
    SBAR();
    __builtin_amdgcn_s_setprio(0);
    RBAR();
    STAMP(1, 1);
    LBAR();
    STAMP(1, 2);
    WOGEMM();
  }
  LBAR();
  STAMP(1, 3);
  {
    const int s = tg * 8 + w;
    const bool act = lane < 56;
    const int lc = act ? lane : 55;
    float v[8];
    const f32x4 a0 = *(const f32x4*)(lds + K2_HB + (w * HBS + lc * 8) * 4), a1 = *(const f32x4*)(lds + K2_HB + (w * HBS + lc * 8 + 4) * 4);
    const f32x4 c0 = *(const f32x4*)(lds + K2_HB + ((8 + w) * HBS + lc * 8) * 4), c1 = *(const f32x4*)(lds + K2_HB + ((8 + w) * HBS + lc * 8 + 4) * 4);
    u32x4 hv;
#pragma unroll
    for (int i = 0; i < 2; ++i) {
      hv[i] = cvtpk(bflo(xv[i]) + a0[2 * i] + c0[2 * i], bfhi(xv[i]) + a0[2 * i + 1] + c0[2 * i + 1]);
      hv[2 + i] = cvtpk(bflo(xv[2 + i]) + a1[2 * i] + c1[2 * i], bfhi(xv[2 + i]) + a1[2 * i + 1] + c1[2 * i + 1]);
    }
    if (act) *(u32x4*)(H + (size_t)s * D + lane * 8) = hv;
#pragma unroll
    for (int i = 0; i < 4; ++i) { v[2 * i] = act ? bflo(hv[i]) : 0.f; v[2 * i + 1] = act ? bfhi(hv[i]) : 0.f; }
#pragma unroll
    for (int pass = 0; pass < 2; ++pass) {
      float sm = 0.f;
#pragma unroll
      for (int i = 0; i < 8; ++i) sm += v[i];
      const float mean = wave_sum(sm) * (1.f / D);
      float q = 0.f;
#pragma unroll
      for (int i = 0; i < 8; ++i) { v[i] = act ? v[i] - mean : 0.f; q += v[i] * v[i]; }
      const float rs = __builtin_amdgcn_rsqf(wave_sum(q) * (1.f / D) + LN_EPS);
#pragma unroll
      for (int i = 0; i < 8; ++i) v[i] *= rs;
    }
    if (act) *(u32x4*)(lds + K2_HS + w * 912 + lane * 16) = (u32x4){cvtpk(v[0], v[1]), cvtpk(v[2], v[3]), cvtpk(v[4], v[5]), cvtpk(v[6], v[7])};
  }
  LBAR();
  if (tid < D / 2) {
    unsigned r[8];
#pragma unroll
    for (int i = 0; i < 8; ++i) r[i] = *(const unsigned*)(lds + K2_HS + i * 912 + tid * 4);
    u32x4 lo, hi2;
#pragma unroll
    for (int i = 0; i < 4; ++i) {
      lo[i] = (r[2 * i] & 0xffffu) | (r[2 * i + 1] << 16);
      hi2[i] = (r[2 * i] >> 16) | (r[2 * i + 1] & 0xffff0000u);
    }
    u32x4* dst = (u32x4*)(HN + ((size_t)tg * D + 2 * tid) * 8);
    dst[0] = lo; dst[1] = hi2;
  }
  STAMP(1, 4);
}

constexpr int G_RING = 4, G_LDS = 8 * G_RING * 4096;
template <int PH>
__global__ __launch_bounds__(512) void k_mlp(const u16* __restrict__ act, const u16* __restrict__ W, const u16* __restrict__ bias,
                                             const u16* __restrict__ H, u16* __restrict__ out) {
  constexpr int KT = PH == 0 ? S : F, LDW = PH == 0 ? F : S, NK = KT / 256, NWG = 7 * (LDW / 64);
  __shared__ __attribute__((aligned(16))) char lds[G_LDS];
  const int tid = threadIdx.x, lane = tid & 63, w = __builtin_amdgcn_readfirstlane(tid >> 6);
  const int t = xcd_remap(blockIdx.x, NWG), nt = t / 7, mt = t - nt * 7;
  const unsigned lds0 = (unsigned)(uintptr_t)lds + w * (G_RING * 4096);
  const int g = lane >> 4, q4 = (lane & 15) >> 2, p = lane & 3, l15 = lane & 15;
  STAMP(2 + PH, 0);
  __builtin_amdgcn_s_setprio(3);
  const u16* asrc = act + ((size_t)(4 * w + g) * D + 64 * mt + l15) * 8;
  const int r8 = lane >> 3, fe = (r8 >> 1) & 1;
  const u16* wsrc0 = W + (size_t)(32 * w + r8) * LDW + 64 * nt + ((lane & 7) ^ (fe << 1)) * 8;
  const u16* wsrc1 = W + (size_t)(32 * w + 8 + r8) * LDW + 64 * nt + ((lane & 7) ^ ((fe | 2) << 1)) * 8;
  bf16x8 af[NK][4];
#define STAGE(i) do { const unsigned sl_ = lds0 + ((i) % G_RING) * 4096; const size_t o_ = (size_t)(i) * 256 * LDW; \
    glds16(wsrc0 + o_, sl_); glds16(wsrc1 + o_, sl_ + 1024); glds16(wsrc0 + o_ + 16 * LDW, sl_ + 2048); glds16(wsrc1 + o_ + 16 * LDW, sl_ + 3072); \
    const u16* ap_ = asrc + (size_t)(i) * 32 * D * 8; \
    gload16<0>(af[i][0], ap_); gload16<256>(af[i][1], ap_); gload16<512>(af[i][2], ap_); gload16<768>(af[i][3], ap_); } while (0)
#pragma unroll
  for (int i = 0; i < G_RING; ++i) STAGE(i);
  STAMP(2 + PH, 1);
  const int f = ((q4 >> 1) & 1) | ((g & 1) << 1);
  int woff[4];
#pragma unroll
  for (int nb = 0; nb < 4; ++nb) woff[nb] = w * (G_RING * 4096) + (8 * g + q4) * 128 + (((2 * nb + (p >> 1)) ^ (f << 1)) * 16) + 8 * (p & 1);
  f32x4 acc[4][4] = {};
#pragma unroll
  for (int i = 0; i < NK; ++i) {
    constexpr int PF = 0;
    const int rem = NK - 1 - i < G_RING - 1 ? NK - 1 - i : G_RING - 1;
    const bool pfd = i + G_RING > NK;
    if (i == 0) __builtin_amdgcn_s_setprio(3);
    else if (i == 2) __builtin_amdgcn_s_setprio(2);
    else if (i == 4) __builtin_amdgcn_s_setprio(1);
    else if (i == 6) __builtin_amdgcn_s_setprio(0);
    if (rem == 3) { if (pfd) waitv4<24 + PF>(af[i][0], af[i][1], af[i][2], af[i][3]); else waitv4<24>(af[i][0], af[i][1], af[i][2], af[i][3]); }
    else if (rem == 2) waitv4<16 + PF>(af[i][0], af[i][1], af[i][2], af[i][3]);
    else if (rem == 1) waitv4<8 + PF>(af[i][0], af[i][1], af[i][2], af[i][3]);
    else waitv4<PF>(af[i][0], af[i][1], af[i][2], af[i][3]);
    bf16x8 wf[4];
#pragma unroll
    for (int nb = 0; nb < 4; ++nb) {
      const lds_cptr sp = (lds_cptr)lds + woff[nb] + (i % G_RING) * 4096;
      wf[nb] = cat8(trread(sp), trread(sp + 512));
    }
    if (i + G_RING < NK) {
      asm volatile("s_waitcnt lgkmcnt(0)" ::: "memory");
      STAGE(i + G_RING);
    }
#pragma unroll
    for (int nb = 0; nb < 4; ++nb)
#pragma unroll
      for (int mb = 0; mb < 4; ++mb)
        acc[nb][mb] = PH == 0 ? MFMA16(wf[nb], af[i][mb], acc[nb][mb]) : MFMA16(af[i][mb], wf[nb], acc[nb][mb]);
  }
#undef STAGE
  STAMP(2 + PH, 4);
  const int enb = w >> 1, emb = 2 * (w & 1);
  u32x2 eb, eh0, eh1;
  if (PH == 0) {
    eb = *(const u32x2*)(bias + 64 * nt + 16 * enb + 4 * g);
  } else {
    const int s = 64 * nt + 16 * enb + l15;
    eb[0] = bias[s];
    eh0 = *(const u32x2*)(H + (size_t)s * D + 64 * mt + 16 * emb + 4 * g);
    eh1 = *(const u32x2*)(H + (size_t)s * D + 64 * mt + 16 * emb + 16 + 4 * g);
  }
  {
    f32x4* xb = (f32x4*)lds;
#pragma unroll
    for (int nb = 0; nb < 4; ++nb)
#pragma unroll
      for (int mb = 0; mb < 4; ++mb) xb[(w * 16 + nb * 4 + mb) * 64 + lane] = acc[nb][mb];
  }
  LBAR();
  {
    const f32x4* xb = (const f32x4*)lds;
#pragma unroll
    for (int e = 0; e < 2; ++e) {
      f32x4 a = xb[(2 * w + e) * 64 + lane];
#pragma unroll
      for (int v = 1; v < 8; ++v) a += xb[(v * 16 + 2 * w + e) * 64 + lane];
      const int mb = emb + e;
      if (PH == 0) {
        const int fi = 64 * nt + 16 * enb + 4 * g, d = 64 * mt + 16 * mb + l15;
        float z[4] = {a[0] + bflo(eb[0]), a[1] + bfhi(eb[0]), a[2] + bflo(eb[1]), a[3] + bfhi(eb[1])};
#pragma unroll
        for (int i = 0; i < 4; ++i) {
          const float zz = z[i], a2 = zz * (1.f + 0.044715f * zz * zz) * (-2.f * 0.7978845608028654f * 1.4426950408889634f);
          z[i] = zz * __builtin_amdgcn_rcpf(1.f + __builtin_amdgcn_exp2f(a2));
        }
        *(u32x2*)(out + ((size_t)(fi >> 3) * D + d) * 8 + (fi & 7)) = (u32x2){cvtpk(z[0], z[1]), cvtpk(z[2], z[3])};
      } else {
        const int d = 64 * mt + 16 * mb + 4 * g, s = 64 * nt + 16 * enb + l15;
        const float bb = bflo(eb[0]);
        const u32x2 hv = e ? eh1 : eh0;
        *(u32x2*)(out + (size_t)s * D + d) =
            (u32x2){cvtpk(a[0] + bb + bflo(hv[0]), a[1] + bb + bfhi(hv[0])), cvtpk(a[2] + bb + bflo(hv[1]), a[3] + bb + bfhi(hv[1]))};
      }
    }
  }
  STAMP(2 + PH, 5);
}

extern "C" void kernel_launch(void* const* d_in, const int* in_sizes, int n_in, void* d_out, int out_size, void* d_ws, size_t ws_size,
                              hipStream_t stream) {
  const u16* x = (const u16*)d_in[0];
  const u16* wq = (const u16*)d_in[1];
  const u16* wk = (const u16*)d_in[2];
  const u16* wv = (const u16*)d_in[3];
  const u16* wo = (const u16*)d_in[4];
  const u16* w1 = (const u16*)d_in[6];
  const u16* b1 = (const u16*)d_in[7];
  const u16* w2 = (const u16*)d_in[8];
  const u16* b2 = (const u16*)d_in[9];
  char* ws = (char*)d_ws;
  u16 *Qf = (u16*)(ws + WS_QF), *Kf = (u16*)(ws + WS_KF), *Vf = (u16*)(ws + WS_VF), *Wof = (u16*)(ws + WS_WOF);
  u16 *H = (u16*)(ws + WS_H), *HN = (u16*)(ws + WS_HN), *U = (u16*)(ws + WS_U);
  k_qkv<<<256, 512, 0, stream>>>(x, wq, wk, wv, wo, Qf, Kf, Vf, Wof);
  k_attn<<<256, 512, 0, stream>>>(x, Qf, Kf, Vf, Wof, w1, w2, H, HN);
  k_mlp<0><<<196, 512, 0, stream>>>(HN, w1, b1, H, U);
  k_mlp<1><<<224, 512, 0, stream>>>(U, w2, b2, H, (u16*)d_out);
}
```

```cpp
#include <hip/hip_runtime.h>
#include <hip/hip_bf16.h>

typedef unsigned short u16;
using bf16x8 = __attribute__((ext_vector_type(8))) short;
using s16x4 = __attribute__((ext_vector_type(4))) short;
using f32x4 = __attribute__((ext_vector_type(4))) float;
using f32x16 = __attribute__((ext_vector_type(16))) float;
using u32x4 = __attribute__((ext_vector_type(4))) unsigned;
using u32x2 = __attribute__((ext_vector_type(2))) unsigned;
typedef short v4i16_t __attribute__((ext_vector_type(4)));
typedef __attribute__((address_space(3))) const char* lds_cptr;
typedef __bf16 bf16x2_t __attribute__((ext_vector_type(2)));
typedef float f32x2_t __attribute__((ext_vector_type(2)));

#define DI __device__ __forceinline__
#define MFMA32(a, b, c) __builtin_amdgcn_mfma_f32_32x32x16_bf16(a, b, c, 0, 0, 0)
#define MFMA16(a, b, c) __builtin_amdgcn_mfma_f32_16x16x32_bf16(a, b, c, 0, 0, 0)
#define SBAR() __builtin_amdgcn_sched_barrier(0)
#define LBAR() asm volatile("s_waitcnt lgkmcnt(0)\n\ts_barrier" ::: "memory")
#define WAITV_BAR(N) asm volatile("s_waitcnt vmcnt(" #N ") lgkmcnt(0)\n\ts_barrier" ::: "memory")

constexpr int S = 2048, D = 448, DH = 28, F = 1792;
constexpr float QSCALE = 0.18898223650461362f * 1.4426950408889634f;
constexpr float LN_EPS = 1e-5f;

constexpr size_t WS_QF = 0;
constexpr size_t WS_KF = WS_QF + 2097152;
constexpr size_t WS_VF = WS_KF + 131072;
constexpr size_t WS_WOF = WS_VF + 131072;
constexpr size_t WS_H = WS_WOF + 401408;
constexpr size_t WS_HN = WS_H + 1835008;
constexpr size_t WS_U = WS_HN + 1835008;

#define STAMP(k, i)
#define CSTAMP(k, i)

DI unsigned cvtpk(float lo, float hi) {
  const f32x2_t v = {lo, hi};
  return __builtin_bit_cast(unsigned, __builtin_convertvector(v, bf16x2_t));
}
DI float bflo(unsigned u) { return __uint_as_float(u << 16); }
DI float bfhi(unsigned u) { return __uint_as_float(u & 0xffff0000u); }
template <int CTRL> DI float dppf(float v) {
  return __uint_as_float(__builtin_amdgcn_update_dpp(0u, __float_as_uint(v), CTRL, 0xf, 0xf, true));
}
DI float wave_sum(float v) {
  v += dppf<0xB1>(v);
  v += dppf<0x4E>(v);
  v += dppf<0x141>(v);
  v += dppf<0x140>(v);
  auto a = __builtin_amdgcn_permlane16_swap(__float_as_uint(v), __float_as_uint(v), false, false);
  v = __uint_as_float(a[0]) + __uint_as_float(a[1]);
  auto b = __builtin_amdgcn_permlane32_swap(__float_as_uint(v), __float_as_uint(v), false, false);
  return __uint_as_float(b[0]) + __uint_as_float(b[1]);
}
DI float half_max(float v) {
  auto b = __builtin_amdgcn_permlane32_swap(__float_as_uint(v), __float_as_uint(v), false, false);
  return __builtin_fmaxf(__uint_as_float(b[0]), __uint_as_float(b[1]));
}
DI void glds16(const void* g, unsigned lds_base) {
  unsigned sv;
  asm volatile("s_mov_b32 %0, m0\n\ts_mov_b32 m0, %2\n\ts_nop 0\n\tglobal_load_lds_dwordx4 %1, off\n\ts_mov_b32 m0, %0"
               : "=&s"(sv) : "v"(g), "s"(lds_base) : "memory");
}
DI void glds16s(const void* sbase, unsigned voff, unsigned lds_base) {
  unsigned sv;
  asm volatile("s_mov_b32 %0, m0\n\ts_mov_b32 m0, %3\n\ts_nop 0\n\tglobal_load_lds_dwordx4 %1, %2\n\ts_mov_b32 m0, %0"
               : "=&s"(sv) : "v"(voff), "s"(sbase), "s"(lds_base) : "memory");
}
DI void glds4(const void* g, unsigned lds_base) {
  unsigned sv;
  asm volatile("s_mov_b32 %0, m0\n\ts_mov_b32 m0, %2\n\ts_nop 0\n\tglobal_load_lds_dword %1, off\n\ts_mov_b32 m0, %0"
               : "=&s"(sv) : "v"(g), "s"(lds_base) : "memory");
}
template <int N> DI void waitv4(bf16x8& a, bf16x8& b, bf16x8& c, bf16x8& d) {
  asm volatile("s_waitcnt vmcnt(%4)" : "+v"(a), "+v"(b), "+v"(c), "+v"(d) : "i"(N) : "memory");
}
template <int OFF> DI void gload16(bf16x8& d, const void* p) {
  asm volatile("global_load_dwordx4 %0, %1, off offset:%2" : "=v"(d) : "v"(p), "i"(OFF) : "memory");
}
DI s16x4 trread(lds_cptr p) {
  return __builtin_bit_cast(s16x4, __builtin_amdgcn_ds_read_tr16_b64_v4i16((__attribute__((address_space(3))) v4i16_t*)p));
}
DI bf16x8 ldsr16(lds_cptr p) { return *(const __attribute__((address_space(3))) bf16x8*)p; }
DI bf16x8 cat8(s16x4 a, s16x4 b) { return __builtin_shufflevector(a, b, 0, 1, 2, 3, 4, 5, 6, 7); }
DI int xcd_remap(int bid, int nwg) {
  const int q = nwg >> 3, r = nwg & 7, x = bid & 7, o = bid >> 3;
  return (x < r ? x * (q + 1) : r * (q + 1) + (x - r) * q) + o;
}

constexpr int K1_WI = 448 * 128, K1_X = 2 * K1_WI, K1_XS = 528, K1_LDS = K1_X + 64 * K1_XS;
__global__ __launch_bounds__(512) void k_qkv(const u16* __restrict__ x, const u16* __restrict__ wq, const u16* __restrict__ wk,
                                             const u16* __restrict__ wv, const u16* __restrict__ wo, u16* __restrict__ Qf,
                                             u16* __restrict__ Kf, u16* __restrict__ Vf, u16* __restrict__ Wof) {
  __shared__ __attribute__((aligned(16))) char lds[K1_LDS];
  const int tid = threadIdx.x, lane = tid & 63, w = __builtin_amdgcn_readfirstlane(tid >> 6);
  const int t = xcd_remap(blockIdx.x, 256), rt = t >> 2, ct = t & 3;
  const unsigned lds0 = (unsigned)(uintptr_t)lds;
  STAMP(0, 0);
  bf16x8 xr[4];
  const bool act = lane < 56;
  {
    const u16* xp_ = x + (size_t)(rt * 32 + w * 4) * D + (act ? lane : 55) * 8;
    gload16<0>(xr[0], xp_); gload16<896>(xr[1], xp_); gload16<1792>(xr[2], xp_); gload16<2688>(xr[3], xp_);
  }
#pragma unroll
  for (int im = 0; im < 2; ++im) {
    const int c64 = 2 * ct + im;
    if (c64 < 7) {
#pragma unroll
      for (int i = 0; i < 7; ++i) {
        const int piece = w * 7 + i, row = piece * 8 + (lane >> 3);
        const int f = ((row >> 1) & 1) | (((row >> 3) & 1) << 1);
        const int c = (lane & 7) ^ (f << 1);
        glds16(wq + (size_t)row * D + c64 * 64 + c * 8, lds0 + im * K1_WI + piece * 1024);
      }
    } else {
#pragma unroll
      for (int i = 0; i < 7; ++i) {
        const int pc = w * 7 + i < 50 ? w * 7 + i : 49, mat = pc >= 25, piece = pc - 25 * mat;
        const int e = piece * 512 + lane * 8;
        glds16((mat ? wv : wk) + (e < 448 * 28 ? e : 448 * 28 - 8), lds0 + im * K1_WI + mat * 25600 + piece * 1024);
      }
    }
  }
  u16 wt[8];
  const int frag0 = blockIdx.x + 256 * (w & 1);
  const bool dofrag = w < 2 && frag0 < 392;
  const int frag = dofrag ? frag0 : 0;
  {
    const int nb = frag / 14, ks = frag - nb * 14;
#pragma unroll
    for (int j = 0; j < 8; ++j) wt[j] = wo[(size_t)(32 * ks + 8 * (lane >> 4) + j) * D + 16 * nb + (lane & 15)];
  }
  SBAR();
  waitv4<22>(xr[0], xr[1], xr[2], xr[3]);
  {
    u32x4 xv[4];
#pragma unroll
    for (int r = 0; r < 4; ++r) xv[r] = __builtin_bit_cast(u32x4, xr[r]);
    float v[4][8], mean[4], rs[4];
#pragma unroll
    for (int r = 0; r < 4; ++r) {
      float s = 0.f;
#pragma unroll
      for (int i = 0; i < 4; ++i) {
        v[r][2 * i] = act ? bflo(xv[r][i]) : 0.f; v[r][2 * i + 1] = act ? bfhi(xv[r][i]) : 0.f;
        s += v[r][2 * i] + v[r][2 * i + 1];
      }
      mean[r] = s;
    }
#pragma unroll
    for (int r = 0; r < 4; ++r) mean[r] = wave_sum(mean[r]) * (1.f / D);
#pragma unroll
    for (int r = 0; r < 4; ++r) {
      float q = 0.f;
#pragma unroll
      for (int i = 0; i < 8; ++i) { v[r][i] = act ? v[r][i] - mean[r] : 0.f; q += v[r][i] * v[r][i]; }
      rs[r] = q;
    }
#pragma unroll
    for (int r = 0; r < 4; ++r) rs[r] = __builtin_amdgcn_rsqf(wave_sum(rs[r]) * (1.f / D) + LN_EPS);
    char* xdst = lds + K1_X + lane * K1_XS + w * 64;
#pragma unroll
    for (int r = 0; r < 4; ++r) {
      u32x4 o;
#pragma unroll
      for (int i = 0; i < 4; ++i) o[i] = cvtpk(v[r][2 * i] * rs[r], v[r][2 * i + 1] * rs[r]);
      *(u32x4*)(xdst + r * 16) = o;
    }
  }
  STAMP(0, 1);
  WAITV_BAR(0);
  STAMP(0, 2);
  const int nb = w & 3, im = w >> 2, c64 = 2 * ct + im, g = lane >> 4, q4 = (lane & 15) >> 2, p = lane & 3, l15 = lane & 15;
  const int f = ((q4 >> 1) & 1) | ((g & 1) << 1);
  const bool kv = c64 == 7;
  const int rs_ = kv ? 56 : 128;
  const lds_cptr wp = (lds_cptr)lds + im * K1_WI + (8 * g + q4) * rs_ +
                      (kv ? (nb >> 1) * 25600 + (nb & 1) * 32 + 8 * p : (((2 * nb + (p >> 1)) ^ (f << 1)) * 16) + 8 * (p & 1));
  const lds_cptr xp = (lds_cptr)lds + K1_X + g * K1_XS + l15 * 16;
  f32x4 acc0 = {0.f, 0.f, 0.f, 0.f}, acc1 = {0.f, 0.f, 0.f, 0.f};
  {
    bf16x8 a[14], b0[14], b1[14];
#pragma unroll
    for (int ks = 0; ks < 14; ++ks) {
      a[ks] = cat8(trread(wp + ks * 32 * rs_), trread(wp + ks * 32 * rs_ + 4 * rs_));
      b0[ks] = ldsr16(xp + ks * 4 * K1_XS);
      b1[ks] = ldsr16(xp + ks * 4 * K1_XS + 256);
    }
    SBAR();
#pragma unroll
    for (int ks = 0; ks < 14; ++ks) {
      acc0 = MFMA16(a[ks], b0[ks], acc0);
      acc1 = MFMA16(a[ks], b1[ks], acc1);
    }
  }
  STAMP(0, 3);
#pragma unroll
  for (int tb = 0; tb < 2; ++tb) {
    const f32x4 a = tb ? acc1 : acc0;
    const int s = rt * 32 + 16 * tb + l15;
    const int nl = 16 * nb + 4 * g;
    if (c64 < 7) {
      const int n0 = 64 * c64 + nl, h = n0 / 28, d0 = n0 - 28 * h;
      const int tg = s >> 3, c = (h & 3) * 8 + (s & 7), b = h >> 2;
      const size_t off = ((((size_t)(tg * 4 + b) * 2 + (d0 >> 4)) * 64) + ((d0 >> 3) & 1) * 32 + c) * 8 + (d0 & 7);
      *(u32x2*)(Qf + off) = (u32x2){cvtpk(a[0] * QSCALE, a[1] * QSCALE), cvtpk(a[2] * QSCALE, a[3] * QSCALE)};
    } else {
      const int kt = s >> 5, kk = s & 31;
      const int d0 = 16 * (nb & 1) + 4 * g;
      if (nb < 2) {
        const size_t off = ((((size_t)kt * 2 + (d0 >> 4)) * 64) + ((d0 >> 3) & 1) * 32 + kk) * 8 + (d0 & 7);
        u32x2 v = {cvtpk(a[0], a[1]), cvtpk(a[2], a[3])};
        if (d0 == 28) v = (u32x2){0x3f803f80u, 0x00003f80u};
        *(u32x2*)(Kf + off) = v;
      } else {
        const int sp = kk >> 4, j = ((kk >> 3) & 1) * 4 + (kk & 3), hi = (kk >> 2) & 1;
        const size_t off = ((((size_t)kt * 2 + sp) * 64) + hi * 32 + d0) * 8 + j;
        unsigned v0 = cvtpk(a[0], a[1]), v1 = cvtpk(a[2], a[3]);
        if (d0 == 28) { v0 = 0x00003f80u; v1 = 0u; }
        Vf[off] = (u16)v0; Vf[off + 8] = (u16)(v0 >> 16); Vf[off + 16] = (u16)v1; Vf[off + 24] = (u16)(v1 >> 16);
      }
    }
  }
  if (dofrag) {
    u32x4 o;
#pragma unroll
    for (int i = 0; i < 4; ++i) o[i] = (unsigned)wt[2 * i] | ((unsigned)wt[2 * i + 1] << 16);
    *(u32x4*)(Wof + ((size_t)frag * 64 + lane) * 8) = o;
  }
  STAMP(0, 4);
}

constexpr int K2_RING = 0, K2_OT = 131072, K2_DUM = K2_OT + 7168, K2_X = K2_DUM + 4096, K2_LDS = K2_X + 16384;
constexpr int K2_HB = 0, HBS = 452, K2_HS = 32768;

DI float set_ref(bf16x8& q1, float m, int hi) {
  const float a = bflo(cvtpk(-m, 0.f)), r1 = -m - a, b = bflo(cvtpk(r1, 0.f)), c = bflo(cvtpk(r1 - b, 0.f));
  u32x4 v = __builtin_bit_cast(u32x4, q1);
  v[2] = hi ? cvtpk(a, b) : v[2];
  v[3] = hi ? cvtpk(c, 0.f) : v[3];
  q1 = __builtin_bit_cast(bf16x8, v);
  return -((a + b) + c);
}
DI float tile_max(const f32x16& s) {
  float a = __builtin_fmaxf(__builtin_fmaxf(s[0], s[1]), s[2]), b = __builtin_fmaxf(__builtin_fmaxf(s[3], s[4]), s[5]);
  a = __builtin_fmaxf(__builtin_fmaxf(a, s[6]), s[7]); b = __builtin_fmaxf(__builtin_fmaxf(b, s[8]), s[9]);
  a = __builtin_fmaxf(__builtin_fmaxf(a, s[10]), s[11]); b = __builtin_fmaxf(__builtin_fmaxf(b, s[12]), s[13]);
  a = __builtin_fmaxf(__builtin_fmaxf(a, s[14]), s[15]);
  return half_max(__builtin_fmaxf(a, b));
}
template <int N> DI void waitv() { asm volatile("s_waitcnt vmcnt(%0)" ::"i"(N) : "memory"); }
#define RBAR() asm volatile("s_barrier" ::: "memory")

DI void attn_safe(const u16* __restrict__ Kf, const u16* __restrict__ Vf, bf16x8 q0, bf16x8 q1, f32x16& o, int lane, int hi) {
  const f32x16 zero = {};
  float mref = 0.f;
  o = zero;
#pragma unroll 1
  for (int t = 0; t < 64; ++t) {
    const bf16x8 k0 = *(const bf16x8*)(Kf + t * 1024 + lane * 8), k1 = *(const bf16x8*)(Kf + t * 1024 + 512 + lane * 8);
    const bf16x8 v0 = *(const bf16x8*)(Vf + t * 1024 + lane * 8), v1 = *(const bf16x8*)(Vf + t * 1024 + 512 + lane * 8);
    (void)set_ref(q1, 0.f, hi);
    f32x16 s = MFMA32(k0, q0, zero);
    s = MFMA32(k1, q1, s);
    const float tm = tile_max(s);
    const float mn = set_ref(q1, t == 0 ? tm : __builtin_fmaxf(tm, mref), hi);
    const float al = t == 0 ? 0.f : __builtin_amdgcn_exp2f(mref - mn);
#pragma unroll
    for (int r = 0; r < 16; ++r) o[r] *= al;
    mref = mn;
    s = MFMA32(k0, q0, zero);
    s = MFMA32(k1, q1, s);
    u32x4 p0, p1;
#pragma unroll
    for (int i = 0; i < 4; ++i) {
      p0[i] = cvtpk(__builtin_amdgcn_exp2f(s[2 * i]), __builtin_amdgcn_exp2f(s[2 * i + 1]));
      p1[i] = cvtpk(__builtin_amdgcn_exp2f(s[8 + 2 * i]), __builtin_amdgcn_exp2f(s[8 + 2 * i + 1]));
    }
    o = MFMA32(v0, __builtin_bit_cast(bf16x8, p0), o);
    o = MFMA32(v1, __builtin_bit_cast(bf16x8, p1), o);
  }
}

#define EX4(S, i) do { S[i] = __builtin_amdgcn_exp2f(S[i]); S[i + 1] = __builtin_amdgcn_exp2f(S[i + 1]); \
    S[i + 2] = __builtin_amdgcn_exp2f(S[i + 2]); S[i + 3] = __builtin_amdgcn_exp2f(S[i + 3]); } while (0)
#define PK8(P, S, i) do { P[0] = cvtpk(S[i], S[i + 1]); P[1] = cvtpk(S[i + 2], S[i + 3]); P[2] = cvtpk(S[i + 4], S[i + 5]); P[3] = cvtpk(S[i + 6], S[i + 7]); } while (0)
#define BF(P) __builtin_bit_cast(bf16x8, P)
constexpr int CPRE = 32;
#define WOPF(j) do { if ((j) < CPRE) wf[(j) / 7][(j) % 7] = WOFR(j); } while (0)
#define RDS(u, off) ldsr16(((u) < 4 ? B0 : B1) + ((u) & 3) * 8192 + (off))
#define CTILE(u, NX, PF) do { \
      \
    sB = MFMA32(k0, qB0, zero); SBAR(); if (NX) k0 = RDS((u) + 1, 0); EX4(sA, 0); WOPF(PF); SBAR(); \
    sB = MFMA32(k1, qB1, sB); SBAR(); if (NX) k1 = RDS((u) + 1, 1024); EX4(sA, 4); PK8(p0, sA, 0); SBAR(); \
    oB = MFMA32(v1, BF(p1), oB); SBAR(); v1 = RDS(u, 3072); EX4(sA, 8); WOPF((PF) + 1); SBAR(); \
    oA = MFMA32(v0, BF(p0), oA); SBAR(); EX4(sA, 12); PK8(p1, sA, 8); SBAR(); \
      \
    sA = MFMA32(k0, qA0, zero); SBAR(); EX4(sB, 0); WOPF((PF) + 2); SBAR(); \
    sA = MFMA32(k1, qA1, sA); SBAR(); EX4(sB, 4); PK8(p0, sB, 0); SBAR(); \
    oA = MFMA32(v1, BF(p1), oA); SBAR(); EX4(sB, 8); WOPF((PF) + 3); SBAR(); \
    oB = MFMA32(v0, BF(p0), oB); SBAR(); if (NX) v0 = RDS((u) + 1, 2048); EX4(sB, 12); PK8(p1, sB, 8); SBAR(); } while (0)
#define CSTAGE(LAST, PF) do { \
    CTILE(0, true, PF); CTILE(1, true, (PF) + 4); CTILE(2, true, (PF) + 8); \
    if (!(LAST)) RBAR(); \
    CTILE(3, !(LAST), (PF) + 12); } while (0)

__global__ __launch_bounds__(512) void k_attn(const u16* __restrict__ x, const u16* __restrict__ Qf, const u16* __restrict__ Kf,
                                              const u16* __restrict__ Vf, const u16* __restrict__ Wof, const u16* __restrict__ w1,
                                              const u16* __restrict__ w2, u16* __restrict__ H, u16* __restrict__ HN) {
  __shared__ __attribute__((aligned(16))) char lds[K2_LDS];
  const int tid = threadIdx.x, lane = tid & 63, w = __builtin_amdgcn_readfirstlane(tid >> 6), hi = lane >> 5;
  const int tg = blockIdx.x;
  STAMP(1, 0);
  const int kh = w >> 2, ng = w & 3;
  const char* wob = (const char*)(Wof + (size_t)(7 * ng * 14 + 7 * kh) * 512);
  const unsigned wl = lane * 16;
  bf16x8 wf[7][7];
#define WOFR(i_) (*(const bf16x8*)(wob + (((i_) / 7) * 14 + (i_) % 7) * 1024 + wl))
#define LDWO(i0, i1) do { _Pragma("unroll") for (int i_ = i0; i_ < i1; ++i_) wf[i_ / 7][i_ % 7] = WOFR(i_); } while (0)
#define WOGEMM() do { \
    const int g_ = lane >> 4; \
    bf16x8 bo[7]; \
    _Pragma("unroll") for (int k = 0; k < 7; ++k) bo[k] = *(const bf16x8*)(lds + K2_OT + ((4 * (7 * kh + k) + g_) * 8 + (lane & 7)) * 16); \
    f32x4 acc[7]; \
    _Pragma("unroll") for (int n = 0; n < 7; ++n) { \
      acc[n] = (f32x4){0.f, 0.f, 0.f, 0.f}; \
      _Pragma("unroll") for (int k = 0; k < 7; ++k) acc[n] = MFMA16(wf[n][k], bo[k], acc[n]); \
    } \
    if ((lane & 15) < 8) { \
      _Pragma("unroll") for (int n = 0; n < 7; ++n) *(f32x4*)(lds + K2_HB + ((kh * 8 + (lane & 15)) * HBS + 16 * (7 * ng + n) + 4 * g_) * 4) = acc[n]; \
    } } while (0)
  u32x4 xv;
  if (w < 4) {
    const f32x16 zero = {};
    const int qh = w & 1, kp = w >> 1;
    const lds_cptr rp0 = (lds_cptr)lds + K2_RING + lane * 16;
    const lds_cptr rp = rp0 + kp * 4096;
    lds_cptr B0 = rp, B1 = rp + 32768;
    const u16* qp = Qf + ((size_t)((tg * 4 + 2 * qh) * 2) * 64 + lane) * 8;
    bf16x8 qA0 = *(const bf16x8*)(qp), qA1 = *(const bf16x8*)(qp + 512), qB0 = *(const bf16x8*)(qp + 1024), qB1 = *(const bf16x8*)(qp + 1536);
    RBAR();
    bf16x8 k0 = ldsr16(rp0), k1 = ldsr16(rp0 + 1024), v0 = RDS(0, 2048), v1 = v0;
    (void)set_ref(qA1, 0.f, hi);
    (void)set_ref(qB1, 0.f, hi);
    f32x16 sA = MFMA32(k0, qA0, zero), sB = MFMA32(k0, qB0, zero);
    sA = MFMA32(k1, qA1, sA);
    sB = MFMA32(k1, qB1, sB);
    k0 = RDS(0, 0); k1 = RDS(0, 1024);
    (void)set_ref(qA1, tile_max(sA), hi);
    (void)set_ref(qB1, tile_max(sB), hi);
    sA = MFMA32(k0, qA0, zero);
    sA = MFMA32(k1, qA1, sA);
    f32x16 oA = zero, oB = zero;
    u32x4 p0, p1 = {0u, 0u, 0u, 0u};
    SBAR();
    STAMP(1, 8);
#pragma unroll 1
    for (int st = 0; st < 4; ++st) {
      CSTAGE(false, 64);
      B0 = B1; B1 = rp + ((st + 2) & 3) * 32768;
    }
    CSTAGE(false, 0);
    B0 = B1; B1 = rp + 2 * 32768;
    CSTAGE(false, 16);
    B0 = B1; B1 = rp + 3 * 32768;
    CSTAGE(false, 64);
    B0 = B1;
    CSTAGE(true, 64);
    oB = MFMA32(v1, BF(p1), oB);
    STAMP(1, 10);
    {
      const f32x16 snd = kp ? oA : oB;
#pragma unroll
      for (int r4 = 0; r4 < 4; ++r4)
        *(f32x4*)(lds + K2_X + ((w * 4 + r4) * 64 + lane) * 16) = (f32x4){snd[4 * r4], snd[4 * r4 + 1], snd[4 * r4 + 2], snd[4 * r4 + 3]};
    }
    f32x16 o0 = kp ? oB : oA;
    bf16x8 qk0 = kp ? qB0 : qA0, qk1 = kp ? qB1 : qA1;
    asm volatile("" : "+v"(o0), "+v"(qk0), "+v"(qk1));
    SBAR();
    LBAR();
#pragma unroll
    for (int r4 = 0; r4 < 4; ++r4) {
      const f32x4 t = *(const f32x4*)(lds + K2_X + (((w ^ 2) * 4 + r4) * 64 + lane) * 16);
      o0[4 * r4] += t[0]; o0[4 * r4 + 1] += t[1]; o0[4 * r4 + 2] += t[2]; o0[4 * r4 + 3] += t[3];
    }
    const int qb = 2 * qh + kp;
    {
      float chk = 0.f;
#pragma unroll
      for (int r = 0; r < 16; ++r) chk = __builtin_fmaf(o0[r], 0.f, chk);
      const bool bad = !(chk == 0.f) || !(o0[12] < 1.8e19f);
      if (__any(bad)) {
        attn_safe(Kf, Vf, qk0, qk1, o0, lane, hi);
        LDWO(0, CPRE);
      }
    }
    {
      auto sw = __builtin_amdgcn_permlane32_swap(__float_as_uint(o0[12]), __float_as_uint(o0[12]), false, false);
      const float il = 1.f / __uint_as_float(sw[1]);
      const int c = lane & 31;
#pragma unroll
      for (int r4 = 0; r4 < 4; ++r4) {
        const int d = 8 * r4 + 4 * hi, k = 28 * (4 * qb + (c >> 3)) + d;
        if (d < 28)
          *(u32x2*)(lds + K2_OT + ((k >> 3) * 8 + (c & 7)) * 16 + (k & 7) * 2) =
              (u32x2){cvtpk(o0[4 * r4] * il, o0[4 * r4 + 1] * il), cvtpk(o0[4 * r4 + 2] * il, o0[4 * r4 + 3] * il)};
      }
    }
    STAMP(1, 1);
    LBAR();
    STAMP(1, 2);
    LDWO(CPRE, 49);
    xv = *(const u32x4*)(x + (size_t)(tg * 8 + w) * D + (lane < 56 ? lane : 55) * 8);
    SBAR();
    WOGEMM();
  } else {
    const int hw = w - 4;
    __builtin_amdgcn_s_setprio(3);
    const unsigned ring = (unsigned)(uintptr_t)lds + K2_RING + hw * 8192;
    const u16* kp = Kf + (size_t)hw * 2048;
    const u16* vp = Vf + (size_t)hw * 2048;
    const unsigned l16 = lane * 16;
#define DMAS(s_) do { const unsigned sl_ = ring + ((s_) & 3) * 32768; \
    _Pragma("unroll") for (int u_ = 0; u_ < 2; ++u_) { \
      glds16s(kp + (s_) * 8192 + u_ * 1024, l16, sl_ + u_ * 4096); glds16s(kp + (s_) * 8192 + u_ * 1024 + 512, l16, sl_ + u_ * 4096 + 1024); \
      glds16s(vp + (s_) * 8192 + u_ * 1024, l16, sl_ + u_ * 4096 + 2048); glds16s(vp + (s_) * 8192 + u_ * 1024 + 512, l16, sl_ + u_ * 4096 + 3072); } } while (0)
    const unsigned dum = (unsigned)(uintptr_t)lds + K2_DUM + hw * 1024;
    const int xg = tg & 7, rk = tg >> 3;
    const int ts = xg < 4 ? 25 * xg : 100 + 24 * (xg - 4), te = ts + (xg < 4 ? 25 : 24);
    const int n0 = ts / 7, n1 = (te - 1) / 7;
    const int nb0 = n0 + 3 <= 27 ? n0 : 24;
    const u16* p1 = w1 + (size_t)(64 * rk + 16 * hw) * F + nb0 * 64;
    const u16* p1b = w1 + (size_t)(64 * rk + 16 * hw) * F + n1 * 64;
    const u16* p2 = w2 + (size_t)(56 * rk + 14 * hw) * S + 4 * xg * 64;
    const unsigned o1 = ((lane >> 5) * F + (lane & 31) * 8) * 2, o1b = ((lane >> 3) * F + (lane & 7) * 8) * 2, o2 = ((lane >> 5) * S + (lane & 31) * 8) * 2;
#define PFD(i0, i1) do { _Pragma("unroll") for (int i_ = i0; i_ < i1; ++i_) { \
      if (i_ < 8) glds16s(p1 + (size_t)i_ * 2 * F, o1, dum); \
      else if (i_ < 10) glds16s(p1b + (size_t)(i_ - 8) * 8 * F, o1b, dum); \
      else glds16s(p2 + (size_t)(i_ - 10) * 2 * S, o2, dum); } } while (0)
    DMAS(0); DMAS(1); DMAS(2);
    waitv<16>(); RBAR();
    LDWO(0, 12); SBAR(); PFD(0, 3);
    waitv<23>(); RBAR();
    DMAS(3); LDWO(12, 24); SBAR(); PFD(3, 6);
    waitv<38>(); RBAR();
    DMAS(4); LDWO(24, 36); SBAR(); PFD(6, 9);
    waitv<38>(); RBAR();
    DMAS(5); LDWO(36, 49); SBAR(); PFD(9, 12);
    waitv<39>(); RBAR();
    DMAS(6); PFD(12, 15);
    waitv<27>(); RBAR();
    DMAS(7); PFD(15, 17);
    waitv<13>(); RBAR();
    waitv<2>(); RBAR();
    xv = *(const u32x4*)(x + (size_t)(tg * 8 + w) * D + (lane < 56 ? lane : 55) * 8);
    SBAR();
    __builtin_amdgcn_s_setprio(0);
    RBAR();
    STAMP(1, 1);
    LBAR();
    STAMP(1, 2);
    WOGEMM();
  }
  LBAR();
  STAMP(1, 3);
  {
    const int s = tg * 8 + w;
    const bool act = lane < 56;
    const int lc = act ? lane : 55;
    float v[8];
    const f32x4 a0 = *(const f32x4*)(lds + K2_HB + (w * HBS + lc * 8) * 4), a1 = *(const f32x4*)(lds + K2_HB + (w * HBS + lc * 8 + 4) * 4);
    const f32x4 c0 = *(const f32x4*)(lds + K2_HB + ((8 + w) * HBS + lc * 8) * 4), c1 = *(const f32x4*)(lds + K2_HB + ((8 + w) * HBS + lc * 8 + 4) * 4);
    u32x4 hv;
#pragma unroll
    for (int i = 0; i < 2; ++i) {
      hv[i] = cvtpk(bflo(xv[i]) + a0[2 * i] + c0[2 * i], bfhi(xv[i]) + a0[2 * i + 1] + c0[2 * i + 1]);
      hv[2 + i] = cvtpk(bflo(xv[2 + i]) + a1[2 * i] + c1[2 * i], bfhi(xv[2 + i]) + a1[2 * i + 1] + c1[2 * i + 1]);
    }
    if (act) *(u32x4*)(H + (size_t)s * D + lane * 8) = hv;
#pragma unroll
    for (int i = 0; i < 4; ++i) { v[2 * i] = act ? bflo(hv[i]) : 0.f; v[2 * i + 1] = act ? bfhi(hv[i]) : 0.f; }
#pragma unroll
    for (int pass = 0; pass < 2; ++pass) {
      float sm = 0.f;
#pragma unroll
      for (int i = 0; i < 8; ++i) sm += v[i];
      const float mean = wave_sum(sm) * (1.f / D);
      float q = 0.f;
#pragma unroll
      for (int i = 0; i < 8; ++i) { v[i] = act ? v[i] - mean : 0.f; q += v[i] * v[i]; }
      const float rs = __builtin_amdgcn_rsqf(wave_sum(q) * (1.f / D) + LN_EPS);
#pragma unroll
      for (int i = 0; i < 8; ++i) v[i] *= rs;
    }
    if (act) *(u32x4*)(lds + K2_HS + w * 912 + lane * 16) = (u32x4){cvtpk(v[0], v[1]), cvtpk(v[2], v[3]), cvtpk(v[4], v[5]), cvtpk(v[6], v[7])};
  }
  LBAR();
  if (tid < D / 2) {
    unsigned r[8];
#pragma unroll
    for (int i = 0; i < 8; ++i) r[i] = *(const unsigned*)(lds + K2_HS + i * 912 + tid * 4);
    u32x4 lo, hi2;
#pragma unroll
    for (int i = 0; i < 4; ++i) {
      lo[i] = (r[2 * i] & 0xffffu) | (r[2 * i + 1] << 16);
      hi2[i] = (r[2 * i] >> 16) | (r[2 * i + 1] & 0xffff0000u);
    }
    u32x4* dst = (u32x4*)(HN + ((size_t)tg * D + 2 * tid) * 8);
    dst[0] = lo; dst[1] = hi2;
  }
  STAMP(1, 4);
}

constexpr int G_RING = 4, G_LDS = 8 * G_RING * 4096;
template <int PH>
__global__ __launch_bounds__(512) void k_mlp(const u16* __restrict__ act, const u16* __restrict__ W, const u16* __restrict__ bias,
                                             const u16* __restrict__ H, u16* __restrict__ out) {
  constexpr int KT = PH == 0 ? S : F, LDW = PH == 0 ? F : S, NK = KT / 256, NWG = 7 * (LDW / 64);
  __shared__ __attribute__((aligned(16))) char lds[G_LDS];
  const int tid = threadIdx.x, lane = tid & 63, w = __builtin_amdgcn_readfirstlane(tid >> 6);
  const int t = xcd_remap(blockIdx.x, NWG), nt = t / 7, mt = t - nt * 7;
  const unsigned lds0 = (unsigned)(uintptr_t)lds + w * (G_RING * 4096);
  const int g = lane >> 4, q4 = (lane & 15) >> 2, p = lane & 3, l15 = lane & 15;
  STAMP(2 + PH, 0);
  __builtin_amdgcn_s_setprio(3);
  const int enb = w >> 1, emb = 2 * (w & 1);
  u32x2 eb, eh0, eh1;
  if (PH == 0) {
    eb = *(const u32x2*)(bias + 64 * nt + 16 * enb + 4 * g);
  } else {
    const int s = 64 * nt + 16 * enb + l15;
    eb[0] = bias[s];
    eh0 = *(const u32x2*)(H + (size_t)s * D + 64 * mt + 16 * emb + 4 * g);
    eh1 = *(const u32x2*)(H + (size_t)s * D + 64 * mt + 16 * emb + 16 + 4 * g);
  }
  SBAR();
  const u16* asrc = act + ((size_t)(4 * w + g) * D + 64 * mt + l15) * 8;
  const int r8 = lane >> 3, fe = (r8 >> 1) & 1;
  const u16* wsrc0 = W + (size_t)(32 * w + r8) * LDW + 64 * nt + ((lane & 7) ^ (fe << 1)) * 8;
  const u16* wsrc1 = W + (size_t)(32 * w + 8 + r8) * LDW + 64 * nt + ((lane & 7) ^ ((fe | 2) << 1)) * 8;
  bf16x8 af[NK][4];
#define STAGE(i) do { const unsigned sl_ = lds0 + ((i) % G_RING) * 4096; const size_t o_ = (size_t)(i) * 256 * LDW; \
    glds16(wsrc0 + o_, sl_); glds16(wsrc1 + o_, sl_ + 1024); glds16(wsrc0 + o_ + 16 * LDW, sl_ + 2048); glds16(wsrc1 + o_ + 16 * LDW, sl_ + 3072); \
    const u16* ap_ = asrc + (size_t)(i) * 32 * D * 8; \
    gload16<0>(af[i][0], ap_); gload16<256>(af[i][1], ap_); gload16<512>(af[i][2], ap_); gload16<768>(af[i][3], ap_); } while (0)
#pragma unroll
  for (int i = 0; i < G_RING; ++i) STAGE(i);
  STAMP(2 + PH, 1);
  const int f = ((q4 >> 1) & 1) | ((g & 1) << 1);
  int woff[4];
#pragma unroll
  for (int nb = 0; nb < 4; ++nb) woff[nb] = w * (G_RING * 4096) + (8 * g + q4) * 128 + (((2 * nb + (p >> 1)) ^ (f << 1)) * 16) + 8 * (p & 1);
  f32x4 acc[4][4] = {};
#pragma unroll
  for (int i = 0; i < NK; ++i) {
    constexpr int PF = 0;
    const int rem = NK - 1 - i < G_RING - 1 ? NK - 1 - i : G_RING - 1;
    const bool pfd = i + G_RING > NK;
    if (i == 0) __builtin_amdgcn_s_setprio(3);
    else if (i == 2) __builtin_amdgcn_s_setprio(2);
    else if (i == 4) __builtin_amdgcn_s_setprio(1);
    else if (i == 6) __builtin_amdgcn_s_setprio(0);
    if (rem == 3) { if (pfd) waitv4<24 + PF>(af[i][0], af[i][1], af[i][2], af[i][3]); else waitv4<24>(af[i][0], af[i][1], af[i][2], af[i][3]); }
    else if (rem == 2) waitv4<16 + PF>(af[i][0], af[i][1], af[i][2], af[i][3]);
    else if (rem == 1) waitv4<8 + PF>(af[i][0], af[i][1], af[i][2], af[i][3]);
    else waitv4<PF>(af[i][0], af[i][1], af[i][2], af[i][3]);
    bf16x8 wf[4];
#pragma unroll
    for (int nb = 0; nb < 4; ++nb) {
      const lds_cptr sp = (lds_cptr)lds + woff[nb] + (i % G_RING) * 4096;
      wf[nb] = cat8(trread(sp), trread(sp + 512));
    }
    if (i + G_RING < NK) {
      asm volatile("s_waitcnt lgkmcnt(0)" ::: "memory");
      STAGE(i + G_RING);
    }
#pragma unroll
    for (int nb = 0; nb < 4; ++nb)
#pragma unroll
      for (int mb = 0; mb < 4; ++mb)
        acc[nb][mb] = PH == 0 ? MFMA16(wf[nb], af[i][mb], acc[nb][mb]) : MFMA16(af[i][mb], wf[nb], acc[nb][mb]);
  }
#undef STAGE
  STAMP(2 + PH, 4);
  {
    f32x4* xb = (f32x4*)lds;
#pragma unroll
    for (int nb = 0; nb < 4; ++nb)
#pragma unroll
      for (int mb = 0; mb < 4; ++mb) xb[(w * 16 + nb * 4 + mb) * 64 + lane] = acc[nb][mb];
  }
  LBAR();
  {
    const f32x4* xb = (const f32x4*)lds;
#pragma unroll
    for (int e = 0; e < 2; ++e) {
      f32x4 a = xb[(2 * w + e) * 64 + lane];
#pragma unroll
      for (int v = 1; v < 8; ++v) a += xb[(v * 16 + 2 * w + e) * 64 + lane];
      const int mb = emb + e;
      if (PH == 0) {
        const int fi = 64 * nt + 16 * enb + 4 * g, d = 64 * mt + 16 * mb + l15;
        float z[4] = {a[0] + bflo(eb[0]), a[1] + bfhi(eb[0]), a[2] + bflo(eb[1]), a[3] + bfhi(eb[1])};
#pragma unroll
        for (int i = 0; i < 4; ++i) {
          const float zz = z[i], a2 = zz * (1.f + 0.044715f * zz * zz) * (-2.f * 0.7978845608028654f * 1.4426950408889634f);
          z[i] = zz * __builtin_amdgcn_rcpf(1.f + __builtin_amdgcn_exp2f(a2));
        }
        *(u32x2*)(out + ((size_t)(fi >> 3) * D + d) * 8 + (fi & 7)) = (u32x2){cvtpk(z[0], z[1]), cvtpk(z[2], z[3])};
      } else {
        const int d = 64 * mt + 16 * mb + 4 * g, s = 64 * nt + 16 * enb + l15;
        const float bb = bflo(eb[0]);
        const u32x2 hv = e ? eh1 : eh0;
        *(u32x2*)(out + (size_t)s * D + d) =
            (u32x2){cvtpk(a[0] + bb + bflo(hv[0]), a[1] + bb + bfhi(hv[0])), cvtpk(a[2] + bb + bflo(hv[1]), a[3] + bb + bfhi(hv[1]))};
      }
    }
  }
  STAMP(2 + PH, 5);
}

extern "C" void kernel_launch(void* const* d_in, const int* in_sizes, int n_in, void* d_out, int out_size, void* d_ws, size_t ws_size,
                              hipStream_t stream) {
  const u16* x = (const u16*)d_in[0];
  const u16* wq = (const u16*)d_in[1];
  const u16* wk = (const u16*)d_in[2];
  const u16* wv = (const u16*)d_in[3];
  const u16* wo = (const u16*)d_in[4];
  const u16* w1 = (const u16*)d_in[6];
  const u16* b1 = (const u16*)d_in[7];
  const u16* w2 = (const u16*)d_in[8];
  const u16* b2 = (const u16*)d_in[9];
  char* ws = (char*)d_ws;
  u16 *Qf = (u16*)(ws + WS_QF), *Kf = (u16*)(ws + WS_KF), *Vf = (u16*)(ws + WS_VF), *Wof = (u16*)(ws + WS_WOF);
  u16 *H = (u16*)(ws + WS_H), *HN = (u16*)(ws + WS_HN), *U = (u16*)(ws + WS_U);
  k_qkv<<<256, 512, 0, stream>>>(x, wq, wk, wv, wo, Qf, Kf, Vf, Wof);
  k_attn<<<256, 512, 0, stream>>>(x, Qf, Kf, Vf, Wof, w1, w2, H, HN);
  k_mlp<0><<<196, 512, 0, stream>>>(HN, w1, b1, H, U);
  k_mlp<1><<<224, 512, 0, stream>>>(U, w2, b2, H, (u16*)d_out);
}
```

```cpp
#include <hip/hip_runtime.h>
#include <hip/hip_bf16.h>

typedef unsigned short u16;
using bf16x8 = __attribute__((ext_vector_type(8))) short;
using s16x4 = __attribute__((ext_vector_type(4))) short;
using f32x4 = __attribute__((ext_vector_type(4))) float;
using f32x16 = __attribute__((ext_vector_type(16))) float;
using u32x4 = __attribute__((ext_vector_type(4))) unsigned;
using u32x2 = __attribute__((ext_vector_type(2))) unsigned;
typedef short v4i16_t __attribute__((ext_vector_type(4)));
typedef __attribute__((address_space(3))) const char* lds_cptr;
typedef __bf16 bf16x2_t __attribute__((ext_vector_type(2)));
typedef float f32x2_t __attribute__((ext_vector_type(2)));

#define DI __device__ __forceinline__
#define MFMA32(a, b, c) __builtin_amdgcn_mfma_f32_32x32x16_bf16(a, b, c, 0, 0, 0)
#define MFMA16(a, b, c) __builtin_amdgcn_mfma_f32_16x16x32_bf16(a, b, c, 0, 0, 0)
#define SBAR() __builtin_amdgcn_sched_barrier(0)
#define LBAR() asm volatile("s_waitcnt lgkmcnt(0)\n\ts_barrier" ::: "memory")
#define WAITV_BAR(N) asm volatile("s_waitcnt vmcnt(" #N ") lgkmcnt(0)\n\ts_barrier" ::: "memory")

constexpr int S = 2048, D = 448, DH = 28, F = 1792;
constexpr float QSCALE = 0.18898223650461362f * 1.4426950408889634f;
constexpr float LN_EPS = 1e-5f;

constexpr size_t WS_QF = 0;
constexpr size_t WS_KF = WS_QF + 2097152;
constexpr size_t WS_VF = WS_KF + 131072;
constexpr size_t WS_WOF = WS_VF + 131072;
constexpr size_t WS_H = WS_WOF + 401408;
constexpr size_t WS_HN = WS_H + 1835008;
constexpr size_t WS_U = WS_HN + 1835008;

#define STAMP(k, i)
#define CSTAMP(k, i)

DI unsigned cvtpk(float lo, float hi) {
  const f32x2_t v = {lo, hi};
  return __builtin_bit_cast(unsigned, __builtin_convertvector(v, bf16x2_t));
}
DI float bflo(unsigned u) { return __uint_as_float(u << 16); }
DI float bfhi(unsigned u) { return __uint_as_float(u & 0xffff0000u); }
template <int CTRL> DI float dppf(float v) {
  return __uint_as_float(__builtin_amdgcn_update_dpp(0u, __float_as_uint(v), CTRL, 0xf, 0xf, true));
}
DI float wave_sum(float v) {
  v += dppf<0xB1>(v);
  v += dppf<0x4E>(v);
  v += dppf<0x141>(v);
  v += dppf<0x140>(v);
  auto a = __builtin_amdgcn_permlane16_swap(__float_as_uint(v), __float_as_uint(v), false, false);
  v = __uint_as_float(a[0]) + __uint_as_float(a[1]);
  auto b = __builtin_amdgcn_permlane32_swap(__float_as_uint(v), __float_as_uint(v), false, false);
  return __uint_as_float(b[0]) + __uint_as_float(b[1]);
}
DI float half_max(float v) {
  auto b = __builtin_amdgcn_permlane32_swap(__float_as_uint(v), __float_as_uint(v), false, false);
  return __builtin_fmaxf(__uint_as_float(b[0]), __uint_as_float(b[1]));
}
DI void glds16(const void* g, unsigned lds_base) {
  unsigned sv;
  asm volatile("s_mov_b32 %0, m0\n\ts_mov_b32 m0, %2\n\ts_nop 0\n\tglobal_load_lds_dwordx4 %1, off\n\ts_mov_b32 m0, %0"
               : "=&s"(sv) : "v"(g), "s"(lds_base) : "memory");
}
DI void glds16s(const void* sbase, unsigned voff, unsigned lds_base) {
  unsigned sv;
  asm volatile("s_mov_b32 %0, m0\n\ts_mov_b32 m0, %3\n\ts_nop 0\n\tglobal_load_lds_dwordx4 %1, %2\n\ts_mov_b32 m0, %0"
               : "=&s"(sv) : "v"(voff), "s"(sbase), "s"(lds_base) : "memory");
}
DI void glds4(const void* g, unsigned lds_base) {
  unsigned sv;
  asm volatile("s_mov_b32 %0, m0\n\ts_mov_b32 m0, %2\n\ts_nop 0\n\tglobal_load_lds_dword %1, off\n\ts_mov_b32 m0, %0"
               : "=&s"(sv) : "v"(g), "s"(lds_base) : "memory");
}
template <int N> DI void waitv4(bf16x8& a, bf16x8& b, bf16x8& c, bf16x8& d) {
  asm volatile("s_waitcnt vmcnt(%4)" : "+v"(a), "+v"(b), "+v"(c), "+v"(d) : "i"(N) : "memory");
}
template <int OFF> DI void gload16(bf16x8& d, const void* p) {
  asm volatile("global_load_dwordx4 %0, %1, off offset:%2" : "=v"(d) : "v"(p), "i"(OFF) : "memory");
}
DI s16x4 trread(lds_cptr p) {
  return __builtin_bit_cast(s16x4, __builtin_amdgcn_ds_read_tr16_b64_v4i16((__attribute__((address_space(3))) v4i16_t*)p));
}
DI bf16x8 ldsr16(lds_cptr p) { return *(const __attribute__((address_space(3))) bf16x8*)p; }
DI bf16x8 cat8(s16x4 a, s16x4 b) { return __builtin_shufflevector(a, b, 0, 1, 2, 3, 4, 5, 6, 7); }
DI int xcd_remap(int bid, int nwg) {
  const int q = nwg >> 3, r = nwg & 7, x = bid & 7, o = bid >> 3;
  return (x < r ? x * (q + 1) : r * (q + 1) + (x - r) * q) + o;
}

constexpr int K1_WI = 448 * 128, K1_X = 2 * K1_WI, K1_XS = 528, K1_LDS = K1_X + 64 * K1_XS;
__global__ __launch_bounds__(512) void k_qkv(const u16* __restrict__ x, const u16* __restrict__ wq, const u16* __restrict__ wk,
                                             const u16* __restrict__ wv, const u16* __restrict__ wo, u16* __restrict__ Qf,
                                             u16* __restrict__ Kf, u16* __restrict__ Vf, u16* __restrict__ Wof) {
  __shared__ __attribute__((aligned(16))) char lds[K1_LDS];
  const int tid = threadIdx.x, lane = tid & 63, w = __builtin_amdgcn_readfirstlane(tid >> 6);
  const int t = xcd_remap(blockIdx.x, 256), rt = t >> 2, ct = t & 3;
  const unsigned lds0 = (unsigned)(uintptr_t)lds;
  STAMP(0, 0);
  bf16x8 xr[4];
  const bool act = lane < 56;
  {
    const u16* xp_ = x + (size_t)(rt * 32 + w * 4) * D + (act ? lane : 55) * 8;
    gload16<0>(xr[0], xp_); gload16<896>(xr[1], xp_); gload16<1792>(xr[2], xp_); gload16<2688>(xr[3], xp_);
  }
#pragma unroll
  for (int im = 0; im < 2; ++im) {
    const int c64 = 2 * ct + im;
    if (c64 < 7) {
#pragma unroll
      for (int i = 0; i < 7; ++i) {
        const int piece = w * 7 + i, row = piece * 8 + (lane >> 3);
        const int f = ((row >> 1) & 1) | (((row >> 3) & 1) << 1);
        const int c = (lane & 7) ^ (f << 1);
        glds16(wq + (size_t)row * D + c64 * 64 + c * 8, lds0 + im * K1_WI + piece * 1024);
      }
    } else {
#pragma unroll
      for (int i = 0; i < 7; ++i) {
        const int pc = w * 7 + i < 50 ? w * 7 + i : 49, mat = pc >= 25, piece = pc - 25 * mat;
        const int e = piece * 512 + lane * 8;
        glds16((mat ? wv : wk) + (e < 448 * 28 ? e : 448 * 28 - 8), lds0 + im * K1_WI + mat * 25600 + piece * 1024);
      }
    }
  }
  u16 wt[8];
  const int frag0 = blockIdx.x + 256 * (w & 1);
  const bool dofrag = w < 2 && frag0 < 392;
  const int frag = dofrag ? frag0 : 0;
  {
    const int nb = frag / 14, ks = frag - nb * 14;
#pragma unroll
    for (int j = 0; j < 8; ++j) wt[j] = wo[(size_t)(32 * ks + 8 * (lane >> 4) + j) * D + 16 * nb + (lane & 15)];
  }
  SBAR();
  waitv4<22>(xr[0], xr[1], xr[2], xr[3]);
  {
    u32x4 xv[4];
#pragma unroll
    for (int r = 0; r < 4; ++r) xv[r] = __builtin_bit_cast(u32x4, xr[r]);
    float v[4][8], mean[4], rs[4];
#pragma unroll
    for (int r = 0; r < 4; ++r) {
      float s = 0.f;
#pragma unroll
      for (int i = 0; i < 4; ++i) {
        v[r][2 * i] = act ? bflo(xv[r][i]) : 0.f; v[r][2 * i + 1] = act ? bfhi(xv[r][i]) : 0.f;
        s += v[r][2 * i] + v[r][2 * i + 1];
      }
      mean[r] = s;
    }
#pragma unroll
    for (int r = 0; r < 4; ++r) mean[r] = wave_sum(mean[r]) * (1.f / D);
#pragma unroll
    for (int r = 0; r < 4; ++r) {
      float q = 0.f;
#pragma unroll
      for (int i = 0; i < 8; ++i) { v[r][i] = act ? v[r][i] - mean[r] : 0.f; q += v[r][i] * v[r][i]; }
      rs[r] = q;
    }
#pragma unroll
    for (int r = 0; r < 4; ++r) rs[r] = __builtin_amdgcn_rsqf(wave_sum(rs[r]) * (1.f / D) + LN_EPS);
    char* xdst = lds + K1_X + lane * K1_XS + w * 64;
#pragma unroll
    for (int r = 0; r < 4; ++r) {
      u32x4 o;
#pragma unroll
      for (int i = 0; i < 4; ++i) o[i] = cvtpk(v[r][2 * i] * rs[r], v[r][2 * i + 1] * rs[r]);
      *(u32x4*)(xdst + r * 16) = o;
    }
  }
  STAMP(0, 1);
  WAITV_BAR(0);
  STAMP(0, 2);
  const int nb = w & 3, im = w >> 2, c64 = 2 * ct + im, g = lane >> 4, q4 = (lane & 15) >> 2, p = lane & 3, l15 = lane & 15;
  const int f = ((q4 >> 1) & 1) | ((g & 1) << 1);
  const bool kv = c64 == 7;
  const int rs_ = kv ? 56 : 128;
  const lds_cptr wp = (lds_cptr)lds + im * K1_WI + (8 * g + q4) * rs_ +
                      (kv ? (nb >> 1) * 25600 + (nb & 1) * 32 + 8 * p : (((2 * nb + (p >> 1)) ^ (f << 1)) * 16) + 8 * (p & 1));
  const lds_cptr xp = (lds_cptr)lds + K1_X + g * K1_XS + l15 * 16;
  f32x4 acc0 = {0.f, 0.f, 0.f, 0.f}, acc1 = {0.f, 0.f, 0.f, 0.f};
  {
    bf16x8 a[14], b0[14], b1[14];
#pragma unroll
    for (int ks = 0; ks < 14; ++ks) {
      a[ks] = cat8(trread(wp + ks * 32 * rs_), trread(wp + ks * 32 * rs_ + 4 * rs_));
      b0[ks] = ldsr16(xp + ks * 4 * K1_XS);
      b1[ks] = ldsr16(xp + ks * 4 * K1_XS + 256);
    }
    SBAR();
#pragma unroll
    for (int ks = 0; ks < 14; ++ks) {
      acc0 = MFMA16(a[ks], b0[ks], acc0);
      acc1 = MFMA16(a[ks], b1[ks], acc1);
    }
  }
  STAMP(0, 3);
#pragma unroll
  for (int tb = 0; tb < 2; ++tb) {
    const f32x4 a = tb ? acc1 : acc0;
    const int s = rt * 32 + 16 * tb + l15;
    const int nl = 16 * nb + 4 * g;
    if (c64 < 7) {
      const int n0 = 64 * c64 + nl, h = n0 / 28, d0 = n0 - 28 * h;
      const int tg = s >> 3, c = (h & 3) * 8 + (s & 7), b = h >> 2;
      const size_t off = ((((size_t)(tg * 4 + b) * 2 + (d0 >> 4)) * 64) + ((d0 >> 3) & 1) * 32 + c) * 8 + (d0 & 7);
      *(u32x2*)(Qf + off) = (u32x2){cvtpk(a[0] * QSCALE, a[1] * QSCALE), cvtpk(a[2] * QSCALE, a[3] * QSCALE)};
    } else {
      const int kt = s >> 5, kk = s & 31;
      const int d0 = 16 * (nb & 1) + 4 * g;
      if (nb < 2) {
        const size_t off = ((((size_t)kt * 2 + (d0 >> 4)) * 64) + ((d0 >> 3) & 1) * 32 + kk) * 8 + (d0 & 7);
        u32x2 v = {cvtpk(a[0], a[1]), cvtpk(a[2], a[3])};
        if (d0 == 28) v = (u32x2){0x3f803f80u, 0x00003f80u};
        *(u32x2*)(Kf + off) = v;
      } else {
        const int sp = kk >> 4, j = ((kk >> 3) & 1) * 4 + (kk & 3), hi = (kk >> 2) & 1;
        const size_t off = ((((size_t)kt * 2 + sp) * 64) + hi * 32 + d0) * 8 + j;
        unsigned v0 = cvtpk(a[0], a[1]), v1 = cvtpk(a[2], a[3]);
        if (d0 == 28) { v0 = 0x00003f80u; v1 = 0u; }
        Vf[off] = (u16)v0; Vf[off + 8] = (u16)(v0 >> 16); Vf[off + 16] = (u16)v1; Vf[off + 24] = (u16)(v1 >> 16);
      }
    }
  }
  if (dofrag) {
    u32x4 o;
#pragma unroll
    for (int i = 0; i < 4; ++i) o[i] = (unsigned)wt[2 * i] | ((unsigned)wt[2 * i + 1] << 16);
    *(u32x4*)(Wof + ((size_t)frag * 64 + lane) * 8) = o;
  }
  STAMP(0, 4);
}

constexpr int K2_RING = 0, K2_OT = 131072, K2_DUM = K2_OT + 7168, K2_X = K2_DUM + 4096, K2_LDS = K2_X + 16384;
constexpr int K2_HB = 65536, HBS = 452, K2_HS = 98304;

DI float set_ref(bf16x8& q1, float m, int hi) {
  const float a = bflo(cvtpk(-m, 0.f)), r1 = -m - a, b = bflo(cvtpk(r1, 0.f)), c = bflo(cvtpk(r1 - b, 0.f));
  u32x4 v = __builtin_bit_cast(u32x4, q1);
  v[2] = hi ? cvtpk(a, b) : v[2];
  v[3] = hi ? cvtpk(c, 0.f) : v[3];
  q1 = __builtin_bit_cast(bf16x8, v);
  return -((a + b) + c);
}
DI float tile_max(const f32x16& s) {
  float a = __builtin_fmaxf(__builtin_fmaxf(s[0], s[1]), s[2]), b = __builtin_fmaxf(__builtin_fmaxf(s[3], s[4]), s[5]);
  a = __builtin_fmaxf(__builtin_fmaxf(a, s[6]), s[7]); b = __builtin_fmaxf(__builtin_fmaxf(b, s[8]), s[9]);
  a = __builtin_fmaxf(__builtin_fmaxf(a, s[10]), s[11]); b = __builtin_fmaxf(__builtin_fmaxf(b, s[12]), s[13]);
  a = __builtin_fmaxf(__builtin_fmaxf(a, s[14]), s[15]);
  return half_max(__builtin_fmaxf(a, b));
}
template <int N> DI void waitv() { asm volatile("s_waitcnt vmcnt(%0)" ::"i"(N) : "memory"); }
#define RBAR() asm volatile("s_barrier" ::: "memory")

DI void attn_safe(const u16* __restrict__ Kf, const u16* __restrict__ Vf, bf16x8 q0, bf16x8 q1, f32x16& o, int lane, int hi) {
  const f32x16 zero = {};
  float mref = 0.f;
  o = zero;
#pragma unroll 1
  for (int t = 0; t < 64; ++t) {
    const bf16x8 k0 = *(const bf16x8*)(Kf + t * 1024 + lane * 8), k1 = *(const bf16x8*)(Kf + t * 1024 + 512 + lane * 8);
    const bf16x8 v0 = *(const bf16x8*)(Vf + t * 1024 + lane * 8), v1 = *(const bf16x8*)(Vf + t * 1024 + 512 + lane * 8);
    (void)set_ref(q1, 0.f, hi);
    f32x16 s = MFMA32(k0, q0, zero);
    s = MFMA32(k1, q1, s);
    const float tm = tile_max(s);
    const float mn = set_ref(q1, t == 0 ? tm : __builtin_fmaxf(tm, mref), hi);
    const float al = t == 0 ? 0.f : __builtin_amdgcn_exp2f(mref - mn);
#pragma unroll
    for (int r = 0; r < 16; ++r) o[r] *= al;
    mref = mn;
    s = MFMA32(k0, q0, zero);
    s = MFMA32(k1, q1, s);
    u32x4 p0, p1;
#pragma unroll
    for (int i = 0; i < 4; ++i) {
      p0[i] = cvtpk(__builtin_amdgcn_exp2f(s[2 * i]), __builtin_amdgcn_exp2f(s[2 * i + 1]));
      p1[i] = cvtpk(__builtin_amdgcn_exp2f(s[8 + 2 * i]), __builtin_amdgcn_exp2f(s[8 + 2 * i + 1]));
    }
    o = MFMA32(v0, __builtin_bit_cast(bf16x8, p0), o);
    o = MFMA32(v1, __builtin_bit_cast(bf16x8, p1), o);
  }
}

#define EX4(S, i) do { S[i] = __builtin_amdgcn_exp2f(S[i]); S[i + 1] = __builtin_amdgcn_exp2f(S[i + 1]); \
    S[i + 2] = __builtin_amdgcn_exp2f(S[i + 2]); S[i + 3] = __builtin_amdgcn_exp2f(S[i + 3]); } while (0)
#define PK8(P, S, i) do { P[0] = cvtpk(S[i], S[i + 1]); P[1] = cvtpk(S[i + 2], S[i + 3]); P[2] = cvtpk(S[i + 4], S[i + 5]); P[3] = cvtpk(S[i + 6], S[i + 7]); } while (0)
#define BF(P) __builtin_bit_cast(bf16x8, P)
constexpr int CPRE = 0;
#define WOPF(j) do { if ((j) < CPRE) wf[(j) / 7][(j) % 7] = WOFR(j); } while (0)
#define RDS(u, off) ldsr16(((u) < 4 ? B0 : B1) + ((u) & 3) * 8192 + (off))
#define CTILE(u, NX, PF) do { \
      \
    sB = MFMA32(k0, qB0, zero); SBAR(); if (NX) k0 = RDS((u) + 1, 0); EX4(sA, 0); WOPF(PF); SBAR(); \
    sB = MFMA32(k1, qB1, sB); SBAR(); if (NX) k1 = RDS((u) + 1, 1024); EX4(sA, 4); PK8(p0, sA, 0); SBAR(); \
    oB = MFMA32(v1, BF(p1), oB); SBAR(); v1 = RDS(u, 3072); EX4(sA, 8); WOPF((PF) + 1); SBAR(); \
    oA = MFMA32(v0, BF(p0), oA); SBAR(); EX4(sA, 12); PK8(p1, sA, 8); SBAR(); \
      \
    sA = MFMA32(k0, qA0, zero); SBAR(); EX4(sB, 0); WOPF((PF) + 2); SBAR(); \
    sA = MFMA32(k1, qA1, sA); SBAR(); EX4(sB, 4); PK8(p0, sB, 0); SBAR(); \
    oA = MFMA32(v1, BF(p1), oA); SBAR(); EX4(sB, 8); WOPF((PF) + 3); SBAR(); \
    oB = MFMA32(v0, BF(p0), oB); SBAR(); if (NX) v0 = RDS((u) + 1, 2048); EX4(sB, 12); PK8(p1, sB, 8); SBAR(); } while (0)
#define CSTAGE(LAST, PF) do { \
    CTILE(0, true, PF); CTILE(1, true, (PF) + 4); CTILE(2, true, (PF) + 8); \
    if (!(LAST)) RBAR(); \
    CTILE(3, !(LAST), (PF) + 12); } while (0)

__global__ __launch_bounds__(512) void k_attn(const u16* __restrict__ x, const u16* __restrict__ Qf, const u16* __restrict__ Kf,
                                              const u16* __restrict__ Vf, const u16* __restrict__ Wof, const u16* __restrict__ w1,
                                              const u16* __restrict__ w2, u16* __restrict__ H, u16* __restrict__ HN) {
  __shared__ __attribute__((aligned(16))) char lds[K2_LDS];
  const int tid = threadIdx.x, lane = tid & 63, w = __builtin_amdgcn_readfirstlane(tid >> 6), hi = lane >> 5;
  const int tg = blockIdx.x;
  STAMP(1, 0);
  const int kh = w >> 2, ng = w & 3;
  const char* wob = (const char*)(Wof + (size_t)(7 * ng * 14 + 7 * kh) * 512);
  const unsigned wl = lane * 16;
  bf16x8 wf[7][7];
#define WOFR(i_) (*(const bf16x8*)(wob + (((i_) / 7) * 14 + (i_) % 7) * 1024 + wl))
#define LDWO(i0, i1) do { _Pragma("unroll") for (int i_ = i0; i_ < i1; ++i_) wf[i_ / 7][i_ % 7] = WOFR(i_); } while (0)
#define BOF(k) (*(const bf16x8*)(lds + K2_OT + ((4 * (k) + (lane >> 4)) * 8 + (lane & 7)) * 16))
#define HBST(acc) do { if ((lane & 15) < 8) { \
      _Pragma("unroll") for (int n = 0; n < 7; ++n) *(f32x4*)(lds + K2_HB + ((kh * 8 + (lane & 15)) * HBS + 16 * (7 * ng + n) + 4 * (lane >> 4)) * 4) = acc[n]; \
    } } while (0)
  u32x4 xv;
  if (w < 4) {
    const f32x16 zero = {};
    const int qh = w & 1, kp = w >> 1;
    const lds_cptr rp0 = (lds_cptr)lds + K2_RING + lane * 16;
    const lds_cptr rp = rp0 + kp * 4096;
    lds_cptr B0 = rp, B1 = rp + 32768;
    const u16* qp = Qf + ((size_t)((tg * 4 + 2 * qh) * 2) * 64 + lane) * 8;
    bf16x8 qA0 = *(const bf16x8*)(qp), qA1 = *(const bf16x8*)(qp + 512), qB0 = *(const bf16x8*)(qp + 1024), qB1 = *(const bf16x8*)(qp + 1536);
    RBAR();
    bf16x8 k0 = ldsr16(rp0), k1 = ldsr16(rp0 + 1024), v0 = RDS(0, 2048), v1 = v0;
    (void)set_ref(qA1, 0.f, hi);
    (void)set_ref(qB1, 0.f, hi);
    f32x16 sA = MFMA32(k0, qA0, zero), sB = MFMA32(k0, qB0, zero);
    sA = MFMA32(k1, qA1, sA);
    sB = MFMA32(k1, qB1, sB);
    k0 = RDS(0, 0); k1 = RDS(0, 1024);
    (void)set_ref(qA1, tile_max(sA), hi);
    (void)set_ref(qB1, tile_max(sB), hi);
    sA = MFMA32(k0, qA0, zero);
    sA = MFMA32(k1, qA1, sA);
    f32x16 oA = zero, oB = zero;
    u32x4 p0, p1 = {0u, 0u, 0u, 0u};
    SBAR();
    STAMP(1, 8);
#pragma unroll 1
    for (int st = 0; st < 4; ++st) {
      CSTAGE(false, 64);
      B0 = B1; B1 = rp + ((st + 2) & 3) * 32768;
    }
    CSTAGE(false, 0);
    B0 = B1; B1 = rp + 2 * 32768;
    CSTAGE(false, 16);
    B0 = B1; B1 = rp + 3 * 32768;
    CSTAGE(false, 64);
    B0 = B1;
    CSTAGE(true, 64);
    oB = MFMA32(v1, BF(p1), oB);
    STAMP(1, 10);
    {
      const f32x16 snd = kp ? oA : oB;
#pragma unroll
      for (int r4 = 0; r4 < 4; ++r4)
        *(f32x4*)(lds + K2_X + ((w * 4 + r4) * 64 + lane) * 16) = (f32x4){snd[4 * r4], snd[4 * r4 + 1], snd[4 * r4 + 2], snd[4 * r4 + 3]};
    }
    f32x16 o0 = kp ? oB : oA;
    bf16x8 qk0 = kp ? qB0 : qA0, qk1 = kp ? qB1 : qA1;
    asm volatile("" : "+v"(o0), "+v"(qk0), "+v"(qk1));
    SBAR();
    LBAR();
#pragma unroll
    for (int r4 = 0; r4 < 4; ++r4) {
      const f32x4 t = *(const f32x4*)(lds + K2_X + (((w ^ 2) * 4 + r4) * 64 + lane) * 16);
      o0[4 * r4] += t[0]; o0[4 * r4 + 1] += t[1]; o0[4 * r4 + 2] += t[2]; o0[4 * r4 + 3] += t[3];
    }
    const int qb = 2 * qh + kp;
    {
      float chk = 0.f;
#pragma unroll
      for (int r = 0; r < 16; ++r) chk = __builtin_fmaf(o0[r], 0.f, chk);
      const bool bad = !(chk == 0.f) || !(o0[12] < 1.8e19f);
      if (__any(bad)) {
        attn_safe(Kf, Vf, qk0, qk1, o0, lane, hi);
      }
    }
    {
      auto sw = __builtin_amdgcn_permlane32_swap(__float_as_uint(o0[12]), __float_as_uint(o0[12]), false, false);
      const float il = 1.f / __uint_as_float(sw[1]);
      const int c = lane & 31;
#pragma unroll
      for (int r4 = 0; r4 < 4; ++r4) {
        const int d = 8 * r4 + 4 * hi, k = 28 * (4 * qb + (c >> 3)) + d;
        if (d < 28)
          *(u32x2*)(lds + K2_OT + ((k >> 3) * 8 + (c & 7)) * 16 + (k & 7) * 2) =
              (u32x2){cvtpk(o0[4 * r4] * il, o0[4 * r4 + 1] * il), cvtpk(o0[4 * r4 + 2] * il, o0[4 * r4 + 3] * il)};
      }
    }
    STAMP(1, 1);
    LBAR();
    STAMP(1, 2);
    LDWO(16, 39);
    xv = *(const u32x4*)(x + (size_t)(tg * 8 + w) * D + (lane < 56 ? lane : 55) * 8);
    SBAR();
    {
      bf16x8 bo[7];
#pragma unroll
      for (int k = 0; k < 7; ++k) bo[k] = BOF(k);
#pragma unroll
      for (int i = 0; i < 16; ++i) wf[i / 7][i % 7] = *(const bf16x8*)(lds + K2_RING + (i >> 3) * 32768 + w * 8192 + (i & 7) * 1024 + lane * 16);
      f32x4 acc[7];
#pragma unroll
      for (int n = 0; n < 7; ++n) acc[n] = (f32x4){0.f, 0.f, 0.f, 0.f};
#pragma unroll
      for (int i = 0; i < 39; ++i) acc[i / 7] = MFMA16(wf[i / 7][i % 7], bo[i % 7], acc[i / 7]);
      HBST(acc);
    }
  } else {
    const int hw = w - 4;
    __builtin_amdgcn_s_setprio(3);
    const unsigned ring = (unsigned)(uintptr_t)lds + K2_RING + hw * 8192;
    const u16* kp = Kf + (size_t)hw * 2048;
    const u16* vp = Vf + (size_t)hw * 2048;
    const unsigned l16 = lane * 16;
#define DMAS(s_) do { const unsigned sl_ = ring + ((s_) & 3) * 32768; \
    _Pragma("unroll") for (int u_ = 0; u_ < 2; ++u_) { \
      glds16s(kp + (s_) * 8192 + u_ * 1024, l16, sl_ + u_ * 4096); glds16s(kp + (s_) * 8192 + u_ * 1024 + 512, l16, sl_ + u_ * 4096 + 1024); \
      glds16s(vp + (s_) * 8192 + u_ * 1024, l16, sl_ + u_ * 4096 + 2048); glds16s(vp + (s_) * 8192 + u_ * 1024 + 512, l16, sl_ + u_ * 4096 + 3072); } } while (0)
    const unsigned dum = (unsigned)(uintptr_t)lds + K2_DUM + hw * 1024;
    const int xg = tg & 7, rk = tg >> 3;
    const int ts = xg < 4 ? 25 * xg : 100 + 24 * (xg - 4), te = ts + (xg < 4 ? 25 : 24);
    const int n0 = ts / 7, n1 = (te - 1) / 7;
    const int nb0 = n0 + 3 <= 27 ? n0 : 24;
    const u16* p1 = w1 + (size_t)(64 * rk + 16 * hw) * F + nb0 * 64;
    const u16* p1b = w1 + (size_t)(64 * rk + 16 * hw) * F + n1 * 64;
    const u16* p2 = w2 + (size_t)(56 * rk + 14 * hw) * S + 4 * xg * 64;
    const unsigned o1 = ((lane >> 5) * F + (lane & 31) * 8) * 2, o1b = ((lane >> 3) * F + (lane & 7) * 8) * 2, o2 = ((lane >> 5) * S + (lane & 31) * 8) * 2;
#define PFD(i0, i1) do { _Pragma("unroll") for (int i_ = i0; i_ < i1; ++i_) { \
      if (i_ < 8) glds16s(p1 + (size_t)i_ * 2 * F, o1, dum); \
      else if (i_ < 10) glds16s(p1b + (size_t)(i_ - 8) * 8 * F, o1b, dum); \
      else glds16s(p2 + (size_t)(i_ - 10) * 2 * S, o2, dum); } } while (0)
    DMAS(0); DMAS(1); DMAS(2);
    waitv<16>(); RBAR();
    LDWO(0, 12); SBAR(); PFD(0, 3);
    waitv<23>(); RBAR();
    DMAS(3); LDWO(12, 24); SBAR(); PFD(3, 6);
    waitv<38>(); RBAR();
    DMAS(4); LDWO(24, 36); SBAR(); PFD(6, 9);
    waitv<38>(); RBAR();
    DMAS(5); LDWO(36, 49); SBAR(); PFD(9, 12);
    waitv<39>(); RBAR();
    bf16x8 wx[10];
    DMAS(6);
#pragma unroll
    for (int i = 0; i < 10; ++i) wx[i] = *(const bf16x8*)(wob - 7 * 1024 + (((39 + i) / 7) * 14 + (39 + i) % 7) * 1024 + wl);
    SBAR(); PFD(12, 15);
    waitv<37>(); RBAR();
    DMAS(7); PFD(15, 17);
    waitv<23>(); RBAR();
#pragma unroll
    for (int i = 0; i < 8; ++i) glds16s(wob - 7 * 1024 + ((i / 7) * 14 + i % 7) * 1024, wl, ring + i * 1024);
    waitv<10>(); RBAR();
#pragma unroll
    for (int i = 8; i < 16; ++i) glds16s(wob - 7 * 1024 + ((i / 7) * 14 + i % 7) * 1024, wl, ring + 32768 + (i - 8) * 1024);
    xv = *(const u32x4*)(x + (size_t)(tg * 8 + w) * D + (lane < 56 ? lane : 55) * 8);
    SBAR();
    __builtin_amdgcn_s_setprio(0);
    RBAR();
    waitv<1>();
    STAMP(1, 1);
    LBAR();
    STAMP(1, 2);
    {
      f32x4 acc[7];
      {
        bf16x8 bo[7];
#pragma unroll
        for (int k = 0; k < 7; ++k) bo[k] = BOF(k);
        acc[5] = (f32x4){0.f, 0.f, 0.f, 0.f}; acc[6] = acc[5];
#pragma unroll
        for (int i = 0; i < 10; ++i) acc[(39 + i) / 7] = MFMA16(wx[i], bo[(39 + i) % 7], acc[(39 + i) / 7]);
      }
      SBAR();
      bf16x8 bo[7];
#pragma unroll
      for (int k = 0; k < 7; ++k) bo[k] = BOF(7 + k);
#pragma unroll
      for (int n = 0; n < 7; ++n) {
        if (n < 5) acc[n] = (f32x4){0.f, 0.f, 0.f, 0.f};
#pragma unroll
        for (int k = 0; k < 7; ++k) acc[n] = MFMA16(wf[n][k], bo[k], acc[n]);
      }
      HBST(acc);
    }
  }
  LBAR();
  STAMP(1, 3);
  {
    const int s = tg * 8 + w;
    const bool act = lane < 56;
    const int lc = act ? lane : 55;
    float v[8];
    const f32x4 a0 = *(const f32x4*)(lds + K2_HB + (w * HBS + lc * 8) * 4), a1 = *(const f32x4*)(lds + K2_HB + (w * HBS + lc * 8 + 4) * 4);
    const f32x4 c0 = *(const f32x4*)(lds + K2_HB + ((8 + w) * HBS + lc * 8) * 4), c1 = *(const f32x4*)(lds + K2_HB + ((8 + w) * HBS + lc * 8 + 4) * 4);
    u32x4 hv;
#pragma unroll
    for (int i = 0; i < 2; ++i) {
      hv[i] = cvtpk(bflo(xv[i]) + a0[2 * i] + c0[2 * i], bfhi(xv[i]) + a0[2 * i + 1] + c0[2 * i + 1]);
      hv[2 + i] = cvtpk(bflo(xv[2 + i]) + a1[2 * i] + c1[2 * i], bfhi(xv[2 + i]) + a1[2 * i + 1] + c1[2 * i + 1]);
    }
    if (act) *(u32x4*)(H + (size_t)s * D + lane * 8) = hv;
#pragma unroll
    for (int i = 0; i < 4; ++i) { v[2 * i] = act ? bflo(hv[i]) : 0.f; v[2 * i + 1] = act ? bfhi(hv[i]) : 0.f; }
#pragma unroll
    for (int pass = 0; pass < 2; ++pass) {
      float sm = 0.f;
#pragma unroll
      for (int i = 0; i < 8; ++i) sm += v[i];
      const float mean = wave_sum(sm) * (1.f / D);
      float q = 0.f;
#pragma unroll
      for (int i = 0; i < 8; ++i) { v[i] = act ? v[i] - mean : 0.f; q += v[i] * v[i]; }
      const float rs = __builtin_amdgcn_rsqf(wave_sum(q) * (1.f / D) + LN_EPS);
#pragma unroll
      for (int i = 0; i < 8; ++i) v[i] *= rs;
    }
    if (act) *(u32x4*)(lds + K2_HS + w * 912 + lane * 16) = (u32x4){cvtpk(v[0], v[1]), cvtpk(v[2], v[3]), cvtpk(v[4], v[5]), cvtpk(v[6], v[7])};
  }
  LBAR();
  if (tid < D / 2) {
    unsigned r[8];
#pragma unroll
    for (int i = 0; i < 8; ++i) r[i] = *(const unsigned*)(lds + K2_HS + i * 912 + tid * 4);
    u32x4 lo, hi2;
#pragma unroll
    for (int i = 0; i < 4; ++i) {
      lo[i] = (r[2 * i] & 0xffffu) | (r[2 * i + 1] << 16);
      hi2[i] = (r[2 * i] >> 16) | (r[2 * i + 1] & 0xffff0000u);
    }
    u32x4* dst = (u32x4*)(HN + ((size_t)tg * D + 2 * tid) * 8);
    dst[0] = lo; dst[1] = hi2;
  }
  STAMP(1, 4);
}

constexpr int G_RING = 4, G_LDS = 8 * G_RING * 4096;
template <int PH>
__global__ __launch_bounds__(512) void k_mlp(const u16* __restrict__ act, const u16* __restrict__ W, const u16* __restrict__ bias,
                                             const u16* __restrict__ H, u16* __restrict__ out) {
  constexpr int KT = PH == 0 ? S : F, LDW = PH == 0 ? F : S, NK = KT / 256, NWG = 7 * (LDW / 64);
  __shared__ __attribute__((aligned(16))) char lds[G_LDS];
  const int tid = threadIdx.x, lane = tid & 63, w = __builtin_amdgcn_readfirstlane(tid >> 6);
  const int t = xcd_remap(blockIdx.x, NWG), nt = t / 7, mt = t - nt * 7;
  const unsigned lds0 = (unsigned)(uintptr_t)lds + w * (G_RING * 4096);
  const int g = lane >> 4, q4 = (lane & 15) >> 2, p = lane & 3, l15 = lane & 15;
  STAMP(2 + PH, 0);
  __builtin_amdgcn_s_setprio(3);
  const int enb = w >> 1, emb = 2 * (w & 1);
  u32x2 eb, eh0, eh1;
  if (PH == 0) {
    eb = *(const u32x2*)(bias + 64 * nt + 16 * enb + 4 * g);
  } else {
    const int s = 64 * nt + 16 * enb + l15;
    eb[0] = bias[s];
    eh0 = *(const u32x2*)(H + (size_t)s * D + 64 * mt + 16 * emb + 4 * g);
    eh1 = *(const u32x2*)(H + (size_t)s * D + 64 * mt + 16 * emb + 16 + 4 * g);
  }
  SBAR();
  const u16* asrc = act + ((size_t)(4 * w + g) * D + 64 * mt + l15) * 8;
  const int r8 = lane >> 3, fe = (r8 >> 1) & 1;
  const u16* wsrc0 = W + (size_t)(32 * w + r8) * LDW + 64 * nt + ((lane & 7) ^ (fe << 1)) * 8;
  const u16* wsrc1 = W + (size_t)(32 * w + 8 + r8) * LDW + 64 * nt + ((lane & 7) ^ ((fe | 2) << 1)) * 8;
  bf16x8 af[NK][4];
#define STAGE(i) do { const unsigned sl_ = lds0 + ((i) % G_RING) * 4096; const size_t o_ = (size_t)(i) * 256 * LDW; \
    glds16(wsrc0 + o_, sl_); glds16(wsrc1 + o_, sl_ + 1024); glds16(wsrc0 + o_ + 16 * LDW, sl_ + 2048); glds16(wsrc1 + o_ + 16 * LDW, sl_ + 3072); \
    const u16* ap_ = asrc + (size_t)(i) * 32 * D * 8; \
    gload16<0>(af[i][0], ap_); gload16<256>(af[i][1], ap_); gload16<512>(af[i][2], ap_); gload16<768>(af[i][3], ap_); } while (0)
#pragma unroll
  for (int i = 0; i < G_RING; ++i) STAGE(i);
  STAMP(2 + PH, 1);
  const int f = ((q4 >> 1) & 1) | ((g & 1) << 1);
  int woff[4];
#pragma unroll
  for (int nb = 0; nb < 4; ++nb) woff[nb] = w * (G_RING * 4096) + (8 * g + q4) * 128 + (((2 * nb + (p >> 1)) ^ (f << 1)) * 16) + 8 * (p & 1);
  f32x4 acc[4][4] = {};
#pragma unroll
  for (int i = 0; i < NK; ++i) {
    constexpr int PF = 0;
    const int rem = NK - 1 - i < G_RING - 1 ? NK - 1 - i : G_RING - 1;
    const bool pfd = i + G_RING > NK;
    if (i == 0) __builtin_amdgcn_s_setprio(3);
    else if (i == 2) __builtin_amdgcn_s_setprio(2);
    else if (i == 4) __builtin_amdgcn_s_setprio(1);
    else if (i == 6) __builtin_amdgcn_s_setprio(0);
    if (rem == 3) { if (pfd) waitv4<24 + PF>(af[i][0], af[i][1], af[i][2], af[i][3]); else waitv4<24>(af[i][0], af[i][1], af[i][2], af[i][3]); }
    else if (rem == 2) waitv4<16 + PF>(af[i][0], af[i][1], af[i][2], af[i][3]);
    else if (rem == 1) waitv4<8 + PF>(af[i][0], af[i][1], af[i][2], af[i][3]);
    else waitv4<PF>(af[i][0], af[i][1], af[i][2], af[i][3]);
    bf16x8 wf[4];
#pragma unroll
    for (int nb = 0; nb < 4; ++nb) {
      const lds_cptr sp = (lds_cptr)lds + woff[nb] + (i % G_RING) * 4096;
      wf[nb] = cat8(trread(sp), trread(sp + 512));
    }
    if (i + G_RING < NK) {
      asm volatile("s_waitcnt lgkmcnt(0)" ::: "memory");
      STAGE(i + G_RING);
    }
#pragma unroll
    for (int nb = 0; nb < 4; ++nb)
#pragma unroll
      for (int mb = 0; mb < 4; ++mb)
        acc[nb][mb] = PH == 0 ? MFMA16(wf[nb], af[i][mb], acc[nb][mb]) : MFMA16(af[i][mb], wf[nb], acc[nb][mb]);
  }
#undef STAGE
  STAMP(2 + PH, 4);
  {
    f32x4* xb = (f32x4*)lds;
#pragma unroll
    for (int nb = 0; nb < 4; ++nb)
#pragma unroll
      for (int mb = 0; mb < 4; ++mb) xb[(w * 16 + nb * 4 + mb) * 64 + lane] = acc[nb][mb];
  }
  LBAR();
  {
    const f32x4* xb = (const f32x4*)lds;
#pragma unroll
    for (int e = 0; e < 2; ++e) {
      f32x4 a = xb[(2 * w + e) * 64 + lane];
#pragma unroll
      for (int v = 1; v < 8; ++v) a += xb[(v * 16 + 2 * w + e) * 64 + lane];
      const int mb = emb + e;
      if (PH == 0) {
        const int fi = 64 * nt + 16 * enb + 4 * g, d = 64 * mt + 16 * mb + l15;
        float z[4] = {a[0] + bflo(eb[0]), a[1] + bfhi(eb[0]), a[2] + bflo(eb[1]), a[3] + bfhi(eb[1])};
#pragma unroll
        for (int i = 0; i < 4; ++i) {
          const float zz = z[i], a2 = zz * (1.f + 0.044715f * zz * zz) * (-2.f * 0.7978845608028654f * 1.4426950408889634f);
          z[i] = zz * __builtin_amdgcn_rcpf(1.f + __builtin_amdgcn_exp2f(a2));
        }
        *(u32x2*)(out + ((size_t)(fi >> 3) * D + d) * 8 + (fi & 7)) = (u32x2){cvtpk(z[0], z[1]), cvtpk(z[2], z[3])};
      } else {
        const int d = 64 * mt + 16 * mb + 4 * g, s = 64 * nt + 16 * enb + l15;
        const float bb = bflo(eb[0]);
        const u32x2 hv = e ? eh1 : eh0;
        *(u32x2*)(out + (size_t)s * D + d) =
            (u32x2){cvtpk(a[0] + bb + bflo(hv[0]), a[1] + bb + bfhi(hv[0])), cvtpk(a[2] + bb + bflo(hv[1]), a[3] + bb + bfhi(hv[1]))};
      }
    }
  }
  STAMP(2 + PH, 5);
}

extern "C" void kernel_launch(void* const* d_in, const int* in_sizes, int n_in, void* d_out, int out_size, void* d_ws, size_t ws_size,
                              hipStream_t stream) {
  const u16* x = (const u16*)d_in[0];
  const u16* wq = (const u16*)d_in[1];
  const u16* wk = (const u16*)d_in[2];
  const u16* wv = (const u16*)d_in[3];
  const u16* wo = (const u16*)d_in[4];
  const u16* w1 = (const u16*)d_in[6];
  const u16* b1 = (const u16*)d_in[7];
  const u16* w2 = (const u16*)d_in[8];
  const u16* b2 = (const u16*)d_in[9];
  char* ws = (char*)d_ws;
  u16 *Qf = (u16*)(ws + WS_QF), *Kf = (u16*)(ws + WS_KF), *Vf = (u16*)(ws + WS_VF), *Wof = (u16*)(ws + WS_WOF);
  u16 *H = (u16*)(ws + WS_H), *HN = (u16*)(ws + WS_HN), *U = (u16*)(ws + WS_U);
  k_qkv<<<256, 512, 0, stream>>>(x, wq, wk, wv, wo, Qf, Kf, Vf, Wof);
  k_attn<<<256, 512, 0, stream>>>(x, Qf, Kf, Vf, Wof, w1, w2, H, HN);
  k_mlp<0><<<196, 512, 0, stream>>>(HN, w1, b1, H, U);
  k_mlp<1><<<224, 512, 0, stream>>>(U, w2, b2, H, (u16*)d_out);
}
```

```cpp
#include <hip/hip_runtime.h>
#include <hip/hip_bf16.h>

typedef unsigned short u16;
using bf16x8 = __attribute__((ext_vector_type(8))) short;
using s16x4 = __attribute__((ext_vector_type(4))) short;
using f32x4 = __attribute__((ext_vector_type(4))) float;
using f32x16 = __attribute__((ext_vector_type(16))) float;
using u32x4 = __attribute__((ext_vector_type(4))) unsigned;
using u32x2 = __attribute__((ext_vector_type(2))) unsigned;
typedef short v4i16_t __attribute__((ext_vector_type(4)));
typedef __attribute__((address_space(3))) const char* lds_cptr;
typedef __bf16 bf16x2_t __attribute__((ext_vector_type(2)));
typedef float f32x2_t __attribute__((ext_vector_type(2)));

#define DI __device__ __forceinline__
#define MFMA32(a, b, c) __builtin_amdgcn_mfma_f32_32x32x16_bf16(a, b, c, 0, 0, 0)
#define MFMA16(a, b, c) __builtin_amdgcn_mfma_f32_16x16x32_bf16(a, b, c, 0, 0, 0)
#define SBAR() __builtin_amdgcn_sched_barrier(0)
#define LBAR() asm volatile("s_waitcnt lgkmcnt(0)\n\ts_barrier" ::: "memory")
#define WAITV_BAR(N) asm volatile("s_waitcnt vmcnt(" #N ") lgkmcnt(0)\n\ts_barrier" ::: "memory")

constexpr int S = 2048, D = 448, DH = 28, F = 1792;
constexpr float QSCALE = 0.18898223650461362f * 1.4426950408889634f;
constexpr float LN_EPS = 1e-5f;

constexpr size_t WS_QF = 0;
constexpr size_t WS_KF = WS_QF + 2097152;
constexpr size_t WS_VF = WS_KF + 131072;
constexpr size_t WS_WOF = WS_VF + 131072;
constexpr size_t WS_H = WS_WOF + 401408;
constexpr size_t WS_HN = WS_H + 1835008;
constexpr size_t WS_U = WS_HN + 1835008;
constexpr size_t WS_CNT = WS_U + 1605632;

#define STAMP(k, i)
#define CSTAMP(k, i)

DI unsigned cvtpk(float lo, float hi) {
  const f32x2_t v = {lo, hi};
  return __builtin_bit_cast(unsigned, __builtin_convertvector(v, bf16x2_t));
}
DI float bflo(unsigned u) { return __uint_as_float(u << 16); }
DI float bfhi(unsigned u) { return __uint_as_float(u & 0xffff0000u); }
template <int CTRL> DI float dppf(float v) {
  return __uint_as_float(__builtin_amdgcn_update_dpp(0u, __float_as_uint(v), CTRL, 0xf, 0xf, true));
}
DI float wave_sum(float v) {
  v += dppf<0xB1>(v);
  v += dppf<0x4E>(v);
  v += dppf<0x141>(v);
  v += dppf<0x140>(v);
  auto a = __builtin_amdgcn_permlane16_swap(__float_as_uint(v), __float_as_uint(v), false, false);
  v = __uint_as_float(a[0]) + __uint_as_float(a[1]);
  auto b = __builtin_amdgcn_permlane32_swap(__float_as_uint(v), __float_as_uint(v), false, false);
  return __uint_as_float(b[0]) + __uint_as_float(b[1]);
}
DI float half_max(float v) {
  auto b = __builtin_amdgcn_permlane32_swap(__float_as_uint(v), __float_as_uint(v), false, false);
  return __builtin_fmaxf(__uint_as_float(b[0]), __uint_as_float(b[1]));
}
DI void glds16(const void* g, unsigned lds_base) {
  unsigned sv;
  asm volatile("s_mov_b32 %0, m0\n\ts_mov_b32 m0, %2\n\ts_nop 0\n\tglobal_load_lds_dwordx4 %1, off\n\ts_mov_b32 m0, %0"
               : "=&s"(sv) : "v"(g), "s"(lds_base) : "memory");
}
DI void glds16s(const void* sbase, unsigned voff, unsigned lds_base) {
  unsigned sv;
  asm volatile("s_mov_b32 %0, m0\n\ts_mov_b32 m0, %3\n\ts_nop 0\n\tglobal_load_lds_dwordx4 %1, %2\n\ts_mov_b32 m0, %0"
               : "=&s"(sv) : "v"(voff), "s"(sbase), "s"(lds_base) : "memory");
}
DI void glds4(const void* g, unsigned lds_base) {
  unsigned sv;
  asm volatile("s_mov_b32 %0, m0\n\ts_mov_b32 m0, %2\n\ts_nop 0\n\tglobal_load_lds_dword %1, off\n\ts_mov_b32 m0, %0"
               : "=&s"(sv) : "v"(g), "s"(lds_base) : "memory");
}
template <int N> DI void waitv4(bf16x8& a, bf16x8& b, bf16x8& c, bf16x8& d) {
  asm volatile("s_waitcnt vmcnt(%4)" : "+v"(a), "+v"(b), "+v"(c), "+v"(d) : "i"(N) : "memory");
}
template <int OFF> DI void gload16(bf16x8& d, const void* p) {
  asm volatile("global_load_dwordx4 %0, %1, off offset:%2" : "=v"(d) : "v"(p), "i"(OFF) : "memory");
}
DI s16x4 trread(lds_cptr p) {
  return __builtin_bit_cast(s16x4, __builtin_amdgcn_ds_read_tr16_b64_v4i16((__attribute__((address_space(3))) v4i16_t*)p));
}
DI bf16x8 ldsr16(lds_cptr p) { return *(const __attribute__((address_space(3))) bf16x8*)p; }
DI bf16x8 cat8(s16x4 a, s16x4 b) { return __builtin_shufflevector(a, b, 0, 1, 2, 3, 4, 5, 6, 7); }
DI int xcd_remap(int bid, int nwg) {
  const int q = nwg >> 3, r = nwg & 7, x = bid & 7, o = bid >> 3;
  return (x < r ? x * (q + 1) : r * (q + 1) + (x - r) * q) + o;
}

constexpr int K1_WI = 448 * 128, K1_X = 2 * K1_WI, K1_XS = 528, K1_LDS = K1_X + 64 * K1_XS;
__global__ __launch_bounds__(512) void k_qkv(const u16* __restrict__ x, const u16* __restrict__ wq, const u16* __restrict__ wk,
                                             const u16* __restrict__ wv, const u16* __restrict__ wo, u16* __restrict__ Qf,
                                             u16* __restrict__ Kf, u16* __restrict__ Vf, u16* __restrict__ Wof, unsigned* __restrict__ cnt) {
  __shared__ __attribute__((aligned(16))) char lds[K1_LDS];
  const int tid = threadIdx.x, lane = tid & 63, w = __builtin_amdgcn_readfirstlane(tid >> 6);
  const int t = xcd_remap(blockIdx.x, 256), rt = t >> 2, ct = t & 3;
  const unsigned lds0 = (unsigned)(uintptr_t)lds;
  STAMP(0, 0);
  bf16x8 xr[4];
  const bool act = lane < 56;
  {
    const u16* xp_ = x + (size_t)(rt * 32 + w * 4) * D + (act ? lane : 55) * 8;
    gload16<0>(xr[0], xp_); gload16<896>(xr[1], xp_); gload16<1792>(xr[2], xp_); gload16<2688>(xr[3], xp_);
  }
#pragma unroll
  for (int im = 0; im < 2; ++im) {
    const int c64 = 2 * ct + im;
    if (c64 < 7) {
#pragma unroll
      for (int i = 0; i < 7; ++i) {
        const int piece = w * 7 + i, row = piece * 8 + (lane >> 3);
        const int f = ((row >> 1) & 1) | (((row >> 3) & 1) << 1);
        const int c = (lane & 7) ^ (f << 1);
        glds16(wq + (size_t)row * D + c64 * 64 + c * 8, lds0 + im * K1_WI + piece * 1024);
      }
    } else {
#pragma unroll
      for (int i = 0; i < 7; ++i) {
        const int pc = w * 7 + i < 50 ? w * 7 + i : 49, mat = pc >= 25, piece = pc - 25 * mat;
        const int e = piece * 512 + lane * 8;
        glds16((mat ? wv : wk) + (e < 448 * 28 ? e : 448 * 28 - 8), lds0 + im * K1_WI + mat * 25600 + piece * 1024);
      }
    }
  }
  u16 wt[8];
  const int frag0 = blockIdx.x + 256 * (w & 1);
  const bool dofrag = w < 2 && frag0 < 392;
  const int frag = dofrag ? frag0 : 0;
  {
    const int nb = frag / 14, ks = frag - nb * 14;
#pragma unroll
    for (int j = 0; j < 8; ++j) wt[j] = wo[(size_t)(32 * ks + 8 * (lane >> 4) + j) * D + 16 * nb + (lane & 15)];
  }
  SBAR();
  waitv4<22>(xr[0], xr[1], xr[2], xr[3]);
  {
    u32x4 xv[4];
#pragma unroll
    for (int r = 0; r < 4; ++r) xv[r] = __builtin_bit_cast(u32x4, xr[r]);
    float v[4][8], mean[4], rs[4];
#pragma unroll
    for (int r = 0; r < 4; ++r) {
      float s = 0.f;
#pragma unroll
      for (int i = 0; i < 4; ++i) {
        v[r][2 * i] = act ? bflo(xv[r][i]) : 0.f; v[r][2 * i + 1] = act ? bfhi(xv[r][i]) : 0.f;
        s += v[r][2 * i] + v[r][2 * i + 1];
      }
      mean[r] = s;
    }
#pragma unroll
    for (int r = 0; r < 4; ++r) mean[r] = wave_sum(mean[r]) * (1.f / D);
#pragma unroll
    for (int r = 0; r < 4; ++r) {
      float q = 0.f;
#pragma unroll
      for (int i = 0; i < 8; ++i) { v[r][i] = act ? v[r][i] - mean[r] : 0.f; q += v[r][i] * v[r][i]; }
      rs[r] = q;
    }
#pragma unroll
    for (int r = 0; r < 4; ++r) rs[r] = __builtin_amdgcn_rsqf(wave_sum(rs[r]) * (1.f / D) + LN_EPS);
    char* xdst = lds + K1_X + lane * K1_XS + w * 64;
#pragma unroll
    for (int r = 0; r < 4; ++r) {
      u32x4 o;
#pragma unroll
      for (int i = 0; i < 4; ++i) o[i] = cvtpk(v[r][2 * i] * rs[r], v[r][2 * i + 1] * rs[r]);
      *(u32x4*)(xdst + r * 16) = o;
    }
  }
  STAMP(0, 1);
  WAITV_BAR(0);
  STAMP(0, 2);
  const int nb = w & 3, im = w >> 2, c64 = 2 * ct + im, g = lane >> 4, q4 = (lane & 15) >> 2, p = lane & 3, l15 = lane & 15;
  const int f = ((q4 >> 1) & 1) | ((g & 1) << 1);
  const bool kv = c64 == 7;
  const int rs_ = kv ? 56 : 128;
  const lds_cptr wp = (lds_cptr)lds + im * K1_WI + (8 * g + q4) * rs_ +
                      (kv ? (nb >> 1) * 25600 + (nb & 1) * 32 + 8 * p : (((2 * nb + (p >> 1)) ^ (f << 1)) * 16) + 8 * (p & 1));
  const lds_cptr xp = (lds_cptr)lds + K1_X + g * K1_XS + l15 * 16;
  f32x4 acc0 = {0.f, 0.f, 0.f, 0.f}, acc1 = {0.f, 0.f, 0.f, 0.f};
  {
    bf16x8 a[14], b0[14], b1[14];
#pragma unroll
    for (int ks = 0; ks < 14; ++ks) {
      a[ks] = cat8(trread(wp + ks * 32 * rs_), trread(wp + ks * 32 * rs_ + 4 * rs_));
      b0[ks] = ldsr16(xp + ks * 4 * K1_XS);
      b1[ks] = ldsr16(xp + ks * 4 * K1_XS + 256);
    }
    SBAR();
#pragma unroll
    for (int ks = 0; ks < 14; ++ks) {
      acc0 = MFMA16(a[ks], b0[ks], acc0);
      acc1 = MFMA16(a[ks], b1[ks], acc1);
    }
  }
  STAMP(0, 3);
#pragma unroll
  for (int tb = 0; tb < 2; ++tb) {
    const f32x4 a = tb ? acc1 : acc0;
    const int s = rt * 32 + 16 * tb + l15;
    const int nl = 16 * nb + 4 * g;
    if (c64 < 7) {
      const int n0 = 64 * c64 + nl, h = n0 / 28, d0 = n0 - 28 * h;
      const int tg = s >> 3, c = (h & 3) * 8 + (s & 7), b = h >> 2;
      const size_t off = ((((size_t)(tg * 4 + b) * 2 + (d0 >> 4)) * 64) + ((d0 >> 3) & 1) * 32 + c) * 8 + (d0 & 7);
      *(u32x2*)(Qf + off) = (u32x2){cvtpk(a[0] * QSCALE, a[1] * QSCALE), cvtpk(a[2] * QSCALE, a[3] * QSCALE)};
    } else {
      const int kt = s >> 5, kk = s & 31;
      const int d0 = 16 * (nb & 1) + 4 * g;
      if (nb < 2) {
        const size_t off = ((((size_t)kt * 2 + (d0 >> 4)) * 64) + ((d0 >> 3) & 1) * 32 + kk) * 8 + (d0 & 7);
        u32x2 v = {cvtpk(a[0], a[1]), cvtpk(a[2], a[3])};
        if (d0 == 28) v = (u32x2){0x3f803f80u, 0x00003f80u};
        *(u32x2*)(Kf + off) = v;
      } else {
        const int sp = kk >> 4, j = ((kk >> 3) & 1) * 4 + (kk & 3), hi = (kk >> 2) & 1;
        const size_t off = ((((size_t)kt * 2 + sp) * 64) + hi * 32 + d0) * 8 + j;
        unsigned v0 = cvtpk(a[0], a[1]), v1 = cvtpk(a[2], a[3]);
        if (d0 == 28) { v0 = 0x00003f80u; v1 = 0u; }
        Vf[off] = (u16)v0; Vf[off + 8] = (u16)(v0 >> 16); Vf[off + 16] = (u16)v1; Vf[off + 24] = (u16)(v1 >> 16);
      }
    }
  }
  if (dofrag) {
    u32x4 o;
#pragma unroll
    for (int i = 0; i < 4; ++i) o[i] = (unsigned)wt[2 * i] | ((unsigned)wt[2 * i + 1] << 16);
    *(u32x4*)(Wof + ((size_t)frag * 64 + lane) * 8) = o;
  }
  if (blockIdx.x == 0) { cnt[tid] = 0u; cnt[512 + tid] = 0u; }
  STAMP(0, 4);
}

constexpr int K2_RING = 0, K2_OT = 131072, K2_DUM = K2_OT + 7168, K2_X = K2_DUM + 4096, K2_LDS = K2_X + 16384;
constexpr int K2_HB = 65536, HBS = 452, K2_HS = 98304;

DI float set_ref(bf16x8& q1, float m, int hi) {
  const float a = bflo(cvtpk(-m, 0.f)), r1 = -m - a, b = bflo(cvtpk(r1, 0.f)), c = bflo(cvtpk(r1 - b, 0.f));
  u32x4 v = __builtin_bit_cast(u32x4, q1);
  v[2] = hi ? cvtpk(a, b) : v[2];
  v[3] = hi ? cvtpk(c, 0.f) : v[3];
  q1 = __builtin_bit_cast(bf16x8, v);
  return -((a + b) + c);
}
DI float tile_max(const f32x16& s) {
  float a = __builtin_fmaxf(__builtin_fmaxf(s[0], s[1]), s[2]), b = __builtin_fmaxf(__builtin_fmaxf(s[3], s[4]), s[5]);
  a = __builtin_fmaxf(__builtin_fmaxf(a, s[6]), s[7]); b = __builtin_fmaxf(__builtin_fmaxf(b, s[8]), s[9]);
  a = __builtin_fmaxf(__builtin_fmaxf(a, s[10]), s[11]); b = __builtin_fmaxf(__builtin_fmaxf(b, s[12]), s[13]);
  a = __builtin_fmaxf(__builtin_fmaxf(a, s[14]), s[15]);
  return half_max(__builtin_fmaxf(a, b));
}
template <int N> DI void waitv() { asm volatile("s_waitcnt vmcnt(%0)" ::"i"(N) : "memory"); }
#define RBAR() asm volatile("s_barrier" ::: "memory")

DI void attn_safe(const u16* __restrict__ Kf, const u16* __restrict__ Vf, bf16x8 q0, bf16x8 q1, f32x16& o, int lane, int hi) {
  const f32x16 zero = {};
  float mref = 0.f;
  o = zero;
#pragma unroll 1
  for (int t = 0; t < 64; ++t) {
    const bf16x8 k0 = *(const bf16x8*)(Kf + t * 1024 + lane * 8), k1 = *(const bf16x8*)(Kf + t * 1024 + 512 + lane * 8);
    const bf16x8 v0 = *(const bf16x8*)(Vf + t * 1024 + lane * 8), v1 = *(const bf16x8*)(Vf + t * 1024 + 512 + lane * 8);
    (void)set_ref(q1, 0.f, hi);
    f32x16 s = MFMA32(k0, q0, zero);
    s = MFMA32(k1, q1, s);
    const float tm = tile_max(s);
    const float mn = set_ref(q1, t == 0 ? tm : __builtin_fmaxf(tm, mref), hi);
    const float al = t == 0 ? 0.f : __builtin_amdgcn_exp2f(mref - mn);
#pragma unroll
    for (int r = 0; r < 16; ++r) o[r] *= al;
    mref = mn;
    s = MFMA32(k0, q0, zero);
    s = MFMA32(k1, q1, s);
    u32x4 p0, p1;
#pragma unroll
    for (int i = 0; i < 4; ++i) {
      p0[i] = cvtpk(__builtin_amdgcn_exp2f(s[2 * i]), __builtin_amdgcn_exp2f(s[2 * i + 1]));
      p1[i] = cvtpk(__builtin_amdgcn_exp2f(s[8 + 2 * i]), __builtin_amdgcn_exp2f(s[8 + 2 * i + 1]));
    }
    o = MFMA32(v0, __builtin_bit_cast(bf16x8, p0), o);
    o = MFMA32(v1, __builtin_bit_cast(bf16x8, p1), o);
  }
}

#define EX4(S, i) do { S[i] = __builtin_amdgcn_exp2f(S[i]); S[i + 1] = __builtin_amdgcn_exp2f(S[i + 1]); \
    S[i + 2] = __builtin_amdgcn_exp2f(S[i + 2]); S[i + 3] = __builtin_amdgcn_exp2f(S[i + 3]); } while (0)
#define PK8(P, S, i) do { P[0] = cvtpk(S[i], S[i + 1]); P[1] = cvtpk(S[i + 2], S[i + 3]); P[2] = cvtpk(S[i + 4], S[i + 5]); P[3] = cvtpk(S[i + 6], S[i + 7]); } while (0)
#define BF(P) __builtin_bit_cast(bf16x8, P)
constexpr int CPRE = 0;
#define WOPF(j) do { if ((j) < CPRE) wf[(j) / 7][(j) % 7] = WOFR(j); } while (0)
#define RDS(u, off) ldsr16(((u) < 4 ? B0 : B1) + ((u) & 3) * 8192 + (off))
#define CTILE(u, NX, PF) do { \
      \
    sB = MFMA32(k0, qB0, zero); SBAR(); if (NX) k0 = RDS((u) + 1, 0); EX4(sA, 0); WOPF(PF); SBAR(); \
    sB = MFMA32(k1, qB1, sB); SBAR(); if (NX) k1 = RDS((u) + 1, 1024); EX4(sA, 4); PK8(p0, sA, 0); SBAR(); \
    oB = MFMA32(v1, BF(p1), oB); SBAR(); v1 = RDS(u, 3072); EX4(sA, 8); WOPF((PF) + 1); SBAR(); \
    oA = MFMA32(v0, BF(p0), oA); SBAR(); EX4(sA, 12); PK8(p1, sA, 8); SBAR(); \
      \
    sA = MFMA32(k0, qA0, zero); SBAR(); EX4(sB, 0); WOPF((PF) + 2); SBAR(); \
    sA = MFMA32(k1, qA1, sA); SBAR(); EX4(sB, 4); PK8(p0, sB, 0); SBAR(); \
    oA = MFMA32(v1, BF(p1), oA); SBAR(); EX4(sB, 8); WOPF((PF) + 3); SBAR(); \
    oB = MFMA32(v0, BF(p0), oB); SBAR(); if (NX) v0 = RDS((u) + 1, 2048); EX4(sB, 12); PK8(p1, sB, 8); SBAR(); } while (0)
#define CSTAGE(LAST, PF) do { \
    CTILE(0, true, PF); CTILE(1, true, (PF) + 4); CTILE(2, true, (PF) + 8); \
    if (!(LAST)) RBAR(); \
    CTILE(3, !(LAST), (PF) + 12); } while (0)

__global__ __launch_bounds__(512) void k_attn(const u16* __restrict__ x, const u16* __restrict__ Qf, const u16* __restrict__ Kf,
                                              const u16* __restrict__ Vf, const u16* __restrict__ Wof, const u16* __restrict__ w1,
                                              const u16* __restrict__ w2, u16* __restrict__ H, u16* __restrict__ HN) {
  __shared__ __attribute__((aligned(16))) char lds[K2_LDS];
  const int tid = threadIdx.x, lane = tid & 63, w = __builtin_amdgcn_readfirstlane(tid >> 6), hi = lane >> 5;
  const int tg = blockIdx.x;
  STAMP(1, 0);
  const int kh = w >> 2, ng = w & 3;
  const char* wob = (const char*)(Wof + (size_t)(7 * ng * 14 + 7 * kh) * 512);
  const unsigned wl = lane * 16;
  bf16x8 wf[7][7];
#define WOFR(i_) (*(const bf16x8*)(wob + (((i_) / 7) * 14 + (i_) % 7) * 1024 + wl))
#define LDWO(i0, i1) do { _Pragma("unroll") for (int i_ = i0; i_ < i1; ++i_) wf[i_ / 7][i_ % 7] = WOFR(i_); } while (0)
#define BOF(k) (*(const bf16x8*)(lds + K2_OT + ((4 * (k) + (lane >> 4)) * 8 + (lane & 7)) * 16))
#define HBST(acc) do { if ((lane & 15) < 8) { \
      _Pragma("unroll") for (int n = 0; n < 7; ++n) *(f32x4*)(lds + K2_HB + ((kh * 8 + (lane & 15)) * HBS + 16 * (7 * ng + n) + 4 * (lane >> 4)) * 4) = acc[n]; \
    } } while (0)
  u32x4 xv;
  if (w < 4) {
    const f32x16 zero = {};
    const int qh = w & 1, kp = w >> 1;
    const lds_cptr rp0 = (lds_cptr)lds + K2_RING + lane * 16;
    const lds_cptr rp = rp0 + kp * 4096;
    lds_cptr B0 = rp, B1 = rp + 32768;
    const u16* qp = Qf + ((size_t)((tg * 4 + 2 * qh) * 2) * 64 + lane) * 8;
    bf16x8 qA0 = *(const bf16x8*)(qp), qA1 = *(const bf16x8*)(qp + 512), qB0 = *(const bf16x8*)(qp + 1024), qB1 = *(const bf16x8*)(qp + 1536);
    RBAR();
    bf16x8 k0 = ldsr16(rp0), k1 = ldsr16(rp0 + 1024), v0 = RDS(0, 2048), v1 = v0;
    (void)set_ref(qA1, 0.f, hi);
    (void)set_ref(qB1, 0.f, hi);
    f32x16 sA = MFMA32(k0, qA0, zero), sB = MFMA32(k0, qB0, zero);
    sA = MFMA32(k1, qA1, sA);
    sB = MFMA32(k1, qB1, sB);
    k0 = RDS(0, 0); k1 = RDS(0, 1024);
    (void)set_ref(qA1, tile_max(sA), hi);
    (void)set_ref(qB1, tile_max(sB), hi);
    sA = MFMA32(k0, qA0, zero);
    sA = MFMA32(k1, qA1, sA);
    f32x16 oA = zero, oB = zero;
    u32x4 p0, p1 = {0u, 0u, 0u, 0u};
    SBAR();
    STAMP(1, 8);
#pragma unroll 1
    for (int st = 0; st < 4; ++st) {
      CSTAGE(false, 64);
      B0 = B1; B1 = rp + ((st + 2) & 3) * 32768;
    }
    CSTAGE(false, 0);
    B0 = B1; B1 = rp + 2 * 32768;
    CSTAGE(false, 16);
    B0 = B1; B1 = rp + 3 * 32768;
    CSTAGE(false, 64);
    B0 = B1;
    CSTAGE(true, 64);
    oB = MFMA32(v1, BF(p1), oB);
    STAMP(1, 10);
    {
      const f32x16 snd = kp ? oA : oB;
#pragma unroll
      for (int r4 = 0; r4 < 4; ++r4)
        *(f32x4*)(lds + K2_X + ((w * 4 + r4) * 64 + lane) * 16) = (f32x4){snd[4 * r4], snd[4 * r4 + 1], snd[4 * r4 + 2], snd[4 * r4 + 3]};
    }
    f32x16 o0 = kp ? oB : oA;
    bf16x8 qk0 = kp ? qB0 : qA0, qk1 = kp ? qB1 : qA1;
    asm volatile("" : "+v"(o0), "+v"(qk0), "+v"(qk1));
    SBAR();
    LBAR();
#pragma unroll
    for (int r4 = 0; r4 < 4; ++r4) {
      const f32x4 t = *(const f32x4*)(lds + K2_X + (((w ^ 2) * 4 + r4) * 64 + lane) * 16);
      o0[4 * r4] += t[0]; o0[4 * r4 + 1] += t[1]; o0[4 * r4 + 2] += t[2]; o0[4 * r4 + 3] += t[3];
    }
    const int qb = 2 * qh + kp;
    {
      float chk = 0.f;
#pragma unroll
      for (int r = 0; r < 16; ++r) chk = __builtin_fmaf(o0[r], 0.f, chk);
      const bool bad = !(chk == 0.f) || !(o0[12] < 1.8e19f);
      if (__any(bad)) {
        attn_safe(Kf, Vf, qk0, qk1, o0, lane, hi);
      }
    }
    {
      auto sw = __builtin_amdgcn_permlane32_swap(__float_as_uint(o0[12]), __float_as_uint(o0[12]), false, false);
      const float il = 1.f / __uint_as_float(sw[1]);
      const int c = lane & 31;
#pragma unroll
      for (int r4 = 0; r4 < 4; ++r4) {
        const int d = 8 * r4 + 4 * hi, k = 28 * (4 * qb + (c >> 3)) + d;
        if (d < 28)
          *(u32x2*)(lds + K2_OT + ((k >> 3) * 8 + (c & 7)) * 16 + (k & 7) * 2) =
              (u32x2){cvtpk(o0[4 * r4] * il, o0[4 * r4 + 1] * il), cvtpk(o0[4 * r4 + 2] * il, o0[4 * r4 + 3] * il)};
      }
    }
    STAMP(1, 1);
    LBAR();
    STAMP(1, 2);
    LDWO(16, 39);
    xv = *(const u32x4*)(x + (size_t)(tg * 8 + w) * D + (lane < 56 ? lane : 55) * 8);
    SBAR();
    {
      bf16x8 bo[7];
#pragma unroll
      for (int k = 0; k < 7; ++k) bo[k] = BOF(k);
#pragma unroll
      for (int i = 0; i < 16; ++i) wf[i / 7][i % 7] = *(const bf16x8*)(lds + K2_RING + (i >> 3) * 32768 + w * 8192 + (i & 7) * 1024 + lane * 16);
      f32x4 acc[7];
#pragma unroll
      for (int n = 0; n < 7; ++n) acc[n] = (f32x4){0.f, 0.f, 0.f, 0.f};
#pragma unroll
      for (int i = 0; i < 39; ++i) acc[i / 7] = MFMA16(wf[i / 7][i % 7], bo[i % 7], acc[i / 7]);
      HBST(acc);
    }
  } else {
    const int hw = w - 4;
    __builtin_amdgcn_s_setprio(3);
    const unsigned ring = (unsigned)(uintptr_t)lds + K2_RING + hw * 8192;
    const u16* kp = Kf + (size_t)hw * 2048;
    const u16* vp = Vf + (size_t)hw * 2048;
    const unsigned l16 = lane * 16;
#define DMAS(s_) do { const unsigned sl_ = ring + ((s_) & 3) * 32768; \
    _Pragma("unroll") for (int u_ = 0; u_ < 2; ++u_) { \
      glds16s(kp + (s_) * 8192 + u_ * 1024, l16, sl_ + u_ * 4096); glds16s(kp + (s_) * 8192 + u_ * 1024 + 512, l16, sl_ + u_ * 4096 + 1024); \
      glds16s(vp + (s_) * 8192 + u_ * 1024, l16, sl_ + u_ * 4096 + 2048); glds16s(vp + (s_) * 8192 + u_ * 1024 + 512, l16, sl_ + u_ * 4096 + 3072); } } while (0)
    const unsigned dum = (unsigned)(uintptr_t)lds + K2_DUM + hw * 1024;
    const int xg = tg & 7, rk = tg >> 3;
    const int ts = xg < 4 ? 25 * xg : 100 + 24 * (xg - 4), te = ts + (xg < 4 ? 25 : 24);
    const int n0 = ts / 7, n1 = (te - 1) / 7;
    const int nb0 = n0 + 3 <= 27 ? n0 : 24;
    const u16* p1 = w1 + (size_t)(64 * rk + 16 * hw) * F + nb0 * 64;
    const u16* p1b = w1 + (size_t)(64 * rk + 16 * hw) * F + n1 * 64;
    const u16* p2 = w2 + (size_t)(56 * rk + 14 * hw) * S + 4 * xg * 64;
    const unsigned o1 = ((lane >> 5) * F + (lane & 31) * 8) * 2, o1b = ((lane >> 3) * F + (lane & 7) * 8) * 2, o2 = ((lane >> 5) * S + (lane & 31) * 8) * 2;
#define PFD(i0, i1) do { _Pragma("unroll") for (int i_ = i0; i_ < i1; ++i_) { \
      if (i_ < 8) glds16s(p1 + (size_t)i_ * 2 * F, o1, dum); \
      else if (i_ < 10) glds16s(p1b + (size_t)(i_ - 8) * 8 * F, o1b, dum); \
      else glds16s(p2 + (size_t)(i_ - 10) * 2 * S, o2, dum); } } while (0)
    DMAS(0); DMAS(1); DMAS(2);
    waitv<16>(); RBAR();
    LDWO(0, 12); SBAR(); PFD(0, 3);
    waitv<23>(); RBAR();
    DMAS(3); LDWO(12, 24); SBAR(); PFD(3, 6);
    waitv<38>(); RBAR();
    DMAS(4); LDWO(24, 36); SBAR(); PFD(6, 9);
    waitv<38>(); RBAR();
    DMAS(5); LDWO(36, 49); SBAR(); PFD(9, 12);
    waitv<39>(); RBAR();
    bf16x8 wx[10];
    DMAS(6);
#pragma unroll
    for (int i = 0; i < 10; ++i) wx[i] = *(const bf16x8*)(wob - 7 * 1024 + (((39 + i) / 7) * 14 + (39 + i) % 7) * 1024 + wl);
    SBAR(); PFD(12, 15);
    waitv<37>(); RBAR();
    DMAS(7); PFD(15, 17);
    waitv<23>(); RBAR();
#pragma unroll
    for (int i = 0; i < 8; ++i) glds16s(wob - 7 * 1024 + ((i / 7) * 14 + i % 7) * 1024, wl, ring + i * 1024);
    waitv<10>(); RBAR();
#pragma unroll
    for (int i = 8; i < 16; ++i) glds16s(wob - 7 * 1024 + ((i / 7) * 14 + i % 7) * 1024, wl, ring + 32768 + (i - 8) * 1024);
    xv = *(const u32x4*)(x + (size_t)(tg * 8 + w) * D + (lane < 56 ? lane : 55) * 8);
    SBAR();
    __builtin_amdgcn_s_setprio(0);
    RBAR();
    waitv<1>();
    STAMP(1, 1);
    LBAR();
    STAMP(1, 2);
    {
      f32x4 acc[7];
      {
        bf16x8 bo[7];
#pragma unroll
        for (int k = 0; k < 7; ++k) bo[k] = BOF(k);
        acc[5] = (f32x4){0.f, 0.f, 0.f, 0.f}; acc[6] = acc[5];
#pragma unroll
        for (int i = 0; i < 10; ++i) acc[(39 + i) / 7] = MFMA16(wx[i], bo[(39 + i) % 7], acc[(39 + i) / 7]);
      }
      SBAR();
      bf16x8 bo[7];
#pragma unroll
      for (int k = 0; k < 7; ++k) bo[k] = BOF(7 + k);
#pragma unroll
      for (int n = 0; n < 7; ++n) {
        if (n < 5) acc[n] = (f32x4){0.f, 0.f, 0.f, 0.f};
#pragma unroll
        for (int k = 0; k < 7; ++k) acc[n] = MFMA16(wf[n][k], bo[k], acc[n]);
      }
      HBST(acc);
    }
  }
  LBAR();
  STAMP(1, 3);
  {
    const int s = tg * 8 + w;
    const bool act = lane < 56;
    const int lc = act ? lane : 55;
    float v[8];
    const f32x4 a0 = *(const f32x4*)(lds + K2_HB + (w * HBS + lc * 8) * 4), a1 = *(const f32x4*)(lds + K2_HB + (w * HBS + lc * 8 + 4) * 4);
    const f32x4 c0 = *(const f32x4*)(lds + K2_HB + ((8 + w) * HBS + lc * 8) * 4), c1 = *(const f32x4*)(lds + K2_HB + ((8 + w) * HBS + lc * 8 + 4) * 4);
    u32x4 hv;
#pragma unroll
    for (int i = 0; i < 2; ++i) {
      hv[i] = cvtpk(bflo(xv[i]) + a0[2 * i] + c0[2 * i], bfhi(xv[i]) + a0[2 * i + 1] + c0[2 * i + 1]);
      hv[2 + i] = cvtpk(bflo(xv[2 + i]) + a1[2 * i] + c1[2 * i], bfhi(xv[2 + i]) + a1[2 * i + 1] + c1[2 * i + 1]);
    }
    if (act) *(u32x4*)(H + (size_t)s * D + lane * 8) = hv;
#pragma unroll
    for (int i = 0; i < 4; ++i) { v[2 * i] = act ? bflo(hv[i]) : 0.f; v[2 * i + 1] = act ? bfhi(hv[i]) : 0.f; }
#pragma unroll
    for (int pass = 0; pass < 2; ++pass) {
      float sm = 0.f;
#pragma unroll
      for (int i = 0; i < 8; ++i) sm += v[i];
      const float mean = wave_sum(sm) * (1.f / D);
      float q = 0.f;
#pragma unroll
      for (int i = 0; i < 8; ++i) { v[i] = act ? v[i] - mean : 0.f; q += v[i] * v[i]; }
      const float rs = __builtin_amdgcn_rsqf(wave_sum(q) * (1.f / D) + LN_EPS);
#pragma unroll
      for (int i = 0; i < 8; ++i) v[i] *= rs;
    }
    if (act) *(u32x4*)(lds + K2_HS + w * 912 + lane * 16) = (u32x4){cvtpk(v[0], v[1]), cvtpk(v[2], v[3]), cvtpk(v[4], v[5]), cvtpk(v[6], v[7])};
  }
  LBAR();
  if (tid < D / 2) {
    unsigned r[8];
#pragma unroll
    for (int i = 0; i < 8; ++i) r[i] = *(const unsigned*)(lds + K2_HS + i * 912 + tid * 4);
    u32x4 lo, hi2;
#pragma unroll
    for (int i = 0; i < 4; ++i) {
      lo[i] = (r[2 * i] & 0xffffu) | (r[2 * i + 1] << 16);
      hi2[i] = (r[2 * i] >> 16) | (r[2 * i + 1] & 0xffff0000u);
    }
    u32x4* dst = (u32x4*)(HN + ((size_t)tg * D + 2 * tid) * 8);
    dst[0] = lo; dst[1] = hi2;
  }
  STAMP(1, 4);
}

constexpr int G_RING = 4, G_LDS = 8 * G_RING * 4096;
typedef __attribute__((address_space(1))) unsigned gu32;
typedef __attribute__((address_space(1))) unsigned long long gu64;
#define RLX_AGENT __ATOMIC_RELAXED, __HIP_MEMORY_SCOPE_AGENT
template <int OFF> DI void gload16c(bf16x8& d, const void* p) {
  asm volatile("global_load_dwordx4 %0, %1, off offset:%2 sc1" : "=v"(d) : "v"(p), "i"(OFF) : "memory");
}
template <int PH, bool DEP>
DI void mlp_tile(char* lds, int t, const u16* __restrict__ act, const u16* __restrict__ W, const u16* __restrict__ bias,
                 const u16* __restrict__ H, u16* __restrict__ out, unsigned* cnt, unsigned want) {
  constexpr int KT = PH == 0 ? S : F, LDW = PH == 0 ? F : S, NK = KT / 256, NR = NK - G_RING;
  const int tid = threadIdx.x, lane = tid & 63, w = __builtin_amdgcn_readfirstlane(tid >> 6);
  const int nt = t / 7, mt = t - nt * 7;
  const unsigned lds0 = (unsigned)(uintptr_t)lds + w * (G_RING * 4096);
  const int g = lane >> 4, q4 = (lane & 15) >> 2, p = lane & 3, l15 = lane & 15;
  STAMP(2 + PH, 0);
  const int enb = w >> 1, emb = 2 * (w & 1);
  u32x2 eb, eh0, eh1;
  if (PH == 0) {
    eb = *(const u32x2*)(bias + 64 * nt + 16 * enb + 4 * g);
  } else {
    const int s = 64 * nt + 16 * enb + l15;
    eb[0] = bias[s];
    eh0 = *(const u32x2*)(H + (size_t)s * D + 64 * mt + 16 * emb + 4 * g);
    eh1 = *(const u32x2*)(H + (size_t)s * D + 64 * mt + 16 * emb + 16 + 4 * g);
  }
  SBAR();
  const u16* asrc = act + ((size_t)(4 * w + g) * D + 64 * mt + l15) * 8;
  const int r8 = lane >> 3, fe = (r8 >> 1) & 1;
  const u16* wsrc0 = W + (size_t)(32 * w + r8) * LDW + 64 * nt + ((lane & 7) ^ (fe << 1)) * 8;
  const u16* wsrc1 = W + (size_t)(32 * w + 8 + r8) * LDW + 64 * nt + ((lane & 7) ^ ((fe | 2) << 1)) * 8;
  const int f = ((q4 >> 1) & 1) | ((g & 1) << 1);
  int woff[4];
#pragma unroll
  for (int nb = 0; nb < 4; ++nb) woff[nb] = w * (G_RING * 4096) + (8 * g + q4) * 128 + (((2 * nb + (p >> 1)) ^ (f << 1)) * 16) + 8 * (p & 1);
  bf16x8 af[NK][4];
  f32x4 acc[4][4] = {};
#define STAGE_W(i) do { const unsigned sl_ = lds0 + ((i) % G_RING) * 4096; const size_t o_ = (size_t)(i) * 256 * LDW; \
    glds16(wsrc0 + o_, sl_); glds16(wsrc1 + o_, sl_ + 1024); glds16(wsrc0 + o_ + 16 * LDW, sl_ + 2048); glds16(wsrc1 + o_ + 16 * LDW, sl_ + 3072); } while (0)
#define LOAD_A(i) do { const u16* ap_ = asrc + (size_t)(i) * 32 * D * 8; \
    if (DEP) { gload16c<0>(af[i][0], ap_); gload16c<256>(af[i][1], ap_); gload16c<512>(af[i][2], ap_); gload16c<768>(af[i][3], ap_); } \
    else { gload16<0>(af[i][0], ap_); gload16<256>(af[i][1], ap_); gload16<512>(af[i][2], ap_); gload16<768>(af[i][3], ap_); } } while (0)
#define RDW(WF, i) do { _Pragma("unroll") for (int nb_ = 0; nb_ < 4; ++nb_) { const lds_cptr sp_ = (lds_cptr)lds + woff[nb_] + ((i) % G_RING) * 4096; \
      WF[nb_] = cat8(trread(sp_), trread(sp_ + 512)); } } while (0)
#define MMA(WF, i) do { _Pragma("unroll") for (int nb_ = 0; nb_ < 4; ++nb_) _Pragma("unroll") for (int mb_ = 0; mb_ < 4; ++mb_) \
      acc[nb_][mb_] = PH == 0 ? MFMA16(WF[nb_], af[i][mb_], acc[nb_][mb_]) : MFMA16(af[i][mb_], WF[nb_], acc[nb_][mb_]); } while (0)
  if (!DEP) {
    __builtin_amdgcn_s_setprio(3);
#pragma unroll
    for (int i = 0; i < G_RING; ++i) { STAGE_W(i); LOAD_A(i); }
    STAMP(2 + PH, 1);
#pragma unroll
    for (int i = 0; i < NK; ++i) {
      const int rem = NK - 1 - i < G_RING - 1 ? NK - 1 - i : G_RING - 1;
      if (i == 2) __builtin_amdgcn_s_setprio(2);
      else if (i == 4) __builtin_amdgcn_s_setprio(1);
      else if (i == 6) __builtin_amdgcn_s_setprio(0);
      if (rem == 3) waitv4<24>(af[i][0], af[i][1], af[i][2], af[i][3]);
      else if (rem == 2) waitv4<16>(af[i][0], af[i][1], af[i][2], af[i][3]);
      else if (rem == 1) waitv4<8>(af[i][0], af[i][1], af[i][2], af[i][3]);
      else waitv4<0>(af[i][0], af[i][1], af[i][2], af[i][3]);
      bf16x8 wf[4];
      RDW(wf, i);
      if (i + G_RING < NK) {
        asm volatile("s_waitcnt lgkmcnt(0)" ::: "memory");
        STAGE_W(i + G_RING); LOAD_A(i + G_RING);
      }
      MMA(wf, i);
    }
  } else {
    bf16x8 wr[NR][4];
#pragma unroll
    for (int j = 0; j < NR; ++j) STAGE_W(G_RING + j);
    if (NR < G_RING) STAGE_W(G_RING - 1);
#pragma unroll
    for (int j = 0; j < NR; ++j) {
      waitv<12>();
      RDW(wr[j], G_RING + j);
      asm volatile("s_waitcnt lgkmcnt(0)" ::: "memory");
      STAGE_W(j);
    }
    STAMP(2 + PH, 1);
    if (w == 0) {
      for (unsigned spins = 0; spins < (1u << 20); ++spins) {
        if ((unsigned)__builtin_amdgcn_readfirstlane(__hip_atomic_load((gu32*)cnt, RLX_AGENT)) >= want) break;
        __builtin_amdgcn_s_sleep(1);
      }
    }
    asm volatile("s_barrier" ::: "memory");
    STAMP(2 + PH, 2);
#pragma unroll
    for (int j = 0; j < NK; ++j) LOAD_A((j + G_RING) % NK);
#pragma unroll
    for (int j = 0; j < NK; ++j) {
      constexpr int dummy = 0; (void)dummy;
      const int i = (j + G_RING) % NK;
      if (NK - 1 - j == 7) waitv4<28>(af[i][0], af[i][1], af[i][2], af[i][3]);
      else if (NK - 1 - j == 6) waitv4<24>(af[i][0], af[i][1], af[i][2], af[i][3]);
      else if (NK - 1 - j == 5) waitv4<20>(af[i][0], af[i][1], af[i][2], af[i][3]);
      else if (NK - 1 - j == 4) waitv4<16>(af[i][0], af[i][1], af[i][2], af[i][3]);
      else if (NK - 1 - j == 3) waitv4<12>(af[i][0], af[i][1], af[i][2], af[i][3]);
      else if (NK - 1 - j == 2) waitv4<8>(af[i][0], af[i][1], af[i][2], af[i][3]);
      else if (NK - 1 - j == 1) waitv4<4>(af[i][0], af[i][1], af[i][2], af[i][3]);
      else waitv4<0>(af[i][0], af[i][1], af[i][2], af[i][3]);
      if (j < NR) {
        MMA(wr[j], i);
      } else {
        bf16x8 wf[4];
        RDW(wf, i);
        MMA(wf, i);
      }
    }
  }
#undef STAGE_W
#undef LOAD_A
#undef RDW
#undef MMA
  STAMP(2 + PH, 4);
  {
    f32x4* xb = (f32x4*)lds;
#pragma unroll
    for (int nb = 0; nb < 4; ++nb)
#pragma unroll
      for (int mb = 0; mb < 4; ++mb) xb[(w * 16 + nb * 4 + mb) * 64 + lane] = acc[nb][mb];
  }
  LBAR();
  {
    const f32x4* xb = (const f32x4*)lds;
#pragma unroll
    for (int e = 0; e < 2; ++e) {
      f32x4 a = xb[(2 * w + e) * 64 + lane];
#pragma unroll
      for (int v = 1; v < 8; ++v) a += xb[(v * 16 + 2 * w + e) * 64 + lane];
      const int mb = emb + e;
      if (PH == 0) {
        const int fi = 64 * nt + 16 * enb + 4 * g, d = 64 * mt + 16 * mb + l15;
        float z[4] = {a[0] + bflo(eb[0]), a[1] + bfhi(eb[0]), a[2] + bflo(eb[1]), a[3] + bfhi(eb[1])};
#pragma unroll
        for (int i = 0; i < 4; ++i) {
          const float zz = z[i], a2 = zz * (1.f + 0.044715f * zz * zz) * (-2.f * 0.7978845608028654f * 1.4426950408889634f);
          z[i] = zz * __builtin_amdgcn_rcpf(1.f + __builtin_amdgcn_exp2f(a2));
        }
        const unsigned long long v = (unsigned long long)cvtpk(z[0], z[1]) | ((unsigned long long)cvtpk(z[2], z[3]) << 32);
        __hip_atomic_store((gu64*)(out + ((size_t)(fi >> 3) * D + d) * 8 + (fi & 7)), v, RLX_AGENT);
      } else {
        const int d = 64 * mt + 16 * mb + 4 * g, s = 64 * nt + 16 * enb + l15;
        const float bb = bflo(eb[0]);
        const u32x2 hv = e ? eh1 : eh0;
        *(u32x2*)(out + (size_t)s * D + d) =
            (u32x2){cvtpk(a[0] + bb + bflo(hv[0]), a[1] + bb + bfhi(hv[0])), cvtpk(a[2] + bb + bflo(hv[1]), a[3] + bb + bfhi(hv[1]))};
      }
    }
  }
  STAMP(2 + PH, 5);
}

__global__ __launch_bounds__(512) void k_mlp(const u16* __restrict__ HN, const u16* __restrict__ w1, const u16* __restrict__ b1,
                                             const u16* __restrict__ w2, const u16* __restrict__ b2, const u16* __restrict__ H,
                                             u16* U, u16* __restrict__ out, unsigned* cnt) {
  __shared__ __attribute__((aligned(16))) char lds[G_LDS];
  if (blockIdx.x < 196) {
    const int t = xcd_remap(blockIdx.x, 196);
    mlp_tile<0, false>(lds, t, HN, w1, b1, H, U, cnt, 0u);
    asm volatile("s_waitcnt vmcnt(0) lgkmcnt(0)\n\ts_barrier" ::: "memory");
    if (threadIdx.x == 0) __hip_atomic_fetch_add((gu32*)(cnt + (t % 7) * 64), 1u, RLX_AGENT);
  }
  const int t2 = xcd_remap(blockIdx.x, 224);
  mlp_tile<1, true>(lds, t2, U, w2, b2, H, out, cnt + (t2 % 7) * 64, 28u);
}

extern "C" void kernel_launch(void* const* d_in, const int* in_sizes, int n_in, void* d_out, int out_size, void* d_ws, size_t ws_size,
                              hipStream_t stream) {
  const u16* x = (const u16*)d_in[0];
  const u16* wq = (const u16*)d_in[1];
  const u16* wk = (const u16*)d_in[2];
  const u16* wv = (const u16*)d_in[3];
  const u16* wo = (const u16*)d_in[4];
  const u16* w1 = (const u16*)d_in[6];
  const u16* b1 = (const u16*)d_in[7];
  const u16* w2 = (const u16*)d_in[8];
  const u16* b2 = (const u16*)d_in[9];
  char* ws = (char*)d_ws;
  u16 *Qf = (u16*)(ws + WS_QF), *Kf = (u16*)(ws + WS_KF), *Vf = (u16*)(ws + WS_VF), *Wof = (u16*)(ws + WS_WOF);
  u16 *H = (u16*)(ws + WS_H), *HN = (u16*)(ws + WS_HN), *U = (u16*)(ws + WS_U);
  unsigned* cnt = (unsigned*)(ws + WS_CNT);
  k_qkv<<<256, 512, 0, stream>>>(x, wq, wk, wv, wo, Qf, Kf, Vf, Wof, cnt);
  k_attn<<<256, 512, 0, stream>>>(x, Qf, Kf, Vf, Wof, w1, w2, H, HN);
  k_mlp<<<224, 512, 0, stream>>>(HN, w1, b1, w2, b2, H, U, (u16*)d_out, cnt);
}
```

```cpp
#include <hip/hip_runtime.h>
#include <hip/hip_bf16.h>

typedef unsigned short u16;
using bf16x8 = __attribute__((ext_vector_type(8))) short;
using s16x4 = __attribute__((ext_vector_type(4))) short;
using f32x4 = __attribute__((ext_vector_type(4))) float;
using f32x16 = __attribute__((ext_vector_type(16))) float;
using u32x4 = __attribute__((ext_vector_type(4))) unsigned;
using u32x2 = __attribute__((ext_vector_type(2))) unsigned;
typedef short v4i16_t __attribute__((ext_vector_type(4)));
typedef __attribute__((address_space(3))) const char* lds_cptr;
typedef __bf16 bf16x2_t __attribute__((ext_vector_type(2)));
typedef float f32x2_t __attribute__((ext_vector_type(2)));

#define DI __device__ __forceinline__
#define MFMA32(a, b, c) __builtin_amdgcn_mfma_f32_32x32x16_bf16(a, b, c, 0, 0, 0)
#define MFMA16(a, b, c) __builtin_amdgcn_mfma_f32_16x16x32_bf16(a, b, c, 0, 0, 0)
#define SBAR() __builtin_amdgcn_sched_barrier(0)
#define LBAR() asm volatile("s_waitcnt lgkmcnt(0)\n\ts_barrier" ::: "memory")
#define WAITV_BAR(N) asm volatile("s_waitcnt vmcnt(" #N ") lgkmcnt(0)\n\ts_barrier" ::: "memory")

constexpr int S = 2048, D = 448, DH = 28, F = 1792;
constexpr float QSCALE = 0.18898223650461362f * 1.4426950408889634f;
constexpr float LN_EPS = 1e-5f;

constexpr size_t WS_QF = 0;
constexpr size_t WS_KF = WS_QF + 2097152;
constexpr size_t WS_VF = WS_KF + 131072;
constexpr size_t WS_WOF = WS_VF + 131072;
constexpr size_t WS_H = WS_WOF + 401408;
constexpr size_t WS_HN = WS_H + 1835008;
constexpr size_t WS_U = WS_HN + 1835008;
constexpr size_t WS_CNT = WS_U + 1605632;

#define STAMP(k, i)
#define CSTAMP(k, i)

DI unsigned cvtpk(float lo, float hi) {
  const f32x2_t v = {lo, hi};
  return __builtin_bit_cast(unsigned, __builtin_convertvector(v, bf16x2_t));
}
DI float bflo(unsigned u) { return __uint_as_float(u << 16); }
DI float bfhi(unsigned u) { return __uint_as_float(u & 0xffff0000u); }
template <int CTRL> DI float dppf(float v) {
  return __uint_as_float(__builtin_amdgcn_update_dpp(0u, __float_as_uint(v), CTRL, 0xf, 0xf, true));
}
DI float wave_sum(float v) {
  v += dppf<0xB1>(v);
  v += dppf<0x4E>(v);
  v += dppf<0x141>(v);
  v += dppf<0x140>(v);
  auto a = __builtin_amdgcn_permlane16_swap(__float_as_uint(v), __float_as_uint(v), false, false);
  v = __uint_as_float(a[0]) + __uint_as_float(a[1]);
  auto b = __builtin_amdgcn_permlane32_swap(__float_as_uint(v), __float_as_uint(v), false, false);
  return __uint_as_float(b[0]) + __uint_as_float(b[1]);
}
DI float half_max(float v) {
  auto b = __builtin_amdgcn_permlane32_swap(__float_as_uint(v), __float_as_uint(v), false, false);
  return __builtin_fmaxf(__uint_as_float(b[0]), __uint_as_float(b[1]));
}
DI void glds16(const void* g, unsigned lds_base) {
  unsigned sv;
  asm volatile("s_mov_b32 %0, m0\n\ts_mov_b32 m0, %2\n\ts_nop 0\n\tglobal_load_lds_dwordx4 %1, off\n\ts_mov_b32 m0, %0"
               : "=&s"(sv) : "v"(g), "s"(lds_base) : "memory");
}
DI void glds16s(const void* sbase, unsigned voff, unsigned lds_base) {
  unsigned sv;
  asm volatile("s_mov_b32 %0, m0\n\ts_mov_b32 m0, %3\n\ts_nop 0\n\tglobal_load_lds_dwordx4 %1, %2\n\ts_mov_b32 m0, %0"
               : "=&s"(sv) : "v"(voff), "s"(sbase), "s"(lds_base) : "memory");
}
DI void glds4(const void* g, unsigned lds_base) {
  unsigned sv;
  asm volatile("s_mov_b32 %0, m0\n\ts_mov_b32 m0, %2\n\ts_nop 0\n\tglobal_load_lds_dword %1, off\n\ts_mov_b32 m0, %0"
               : "=&s"(sv) : "v"(g), "s"(lds_base) : "memory");
}
template <int N> DI void waitv4(bf16x8& a, bf16x8& b, bf16x8& c, bf16x8& d) {
  asm volatile("s_waitcnt vmcnt(%4)" : "+v"(a), "+v"(b), "+v"(c), "+v"(d) : "i"(N) : "memory");
}
template <int OFF> DI void gload16(bf16x8& d, const void* p) {
  asm volatile("global_load_dwordx4 %0, %1, off offset:%2" : "=v"(d) : "v"(p), "i"(OFF) : "memory");
}
DI s16x4 trread(lds_cptr p) {
  return __builtin_bit_cast(s16x4, __builtin_amdgcn_ds_read_tr16_b64_v4i16((__attribute__((address_space(3))) v4i16_t*)p));
}
DI bf16x8 ldsr16(lds_cptr p) { return *(const __attribute__((address_space(3))) bf16x8*)p; }
DI bf16x8 cat8(s16x4 a, s16x4 b) { return __builtin_shufflevector(a, b, 0, 1, 2, 3, 4, 5, 6, 7); }
DI int xcd_remap(int bid, int nwg) {
  const int q = nwg >> 3, r = nwg & 7, x = bid & 7, o = bid >> 3;
  return (x < r ? x * (q + 1) : r * (q + 1) + (x - r) * q) + o;
}

constexpr int K1_WI = 448 * 128, K1_X = 2 * K1_WI, K1_XS = 528, K1_LDS = K1_X + 64 * K1_XS;
__global__ __launch_bounds__(512) void k_qkv(const u16* __restrict__ x, const u16* __restrict__ wq, const u16* __restrict__ wk,
                                             const u16* __restrict__ wv, const u16* __restrict__ wo, u16* __restrict__ Qf,
                                             u16* __restrict__ Kf, u16* __restrict__ Vf, u16* __restrict__ Wof, unsigned* __restrict__ cnt) {
  __shared__ __attribute__((aligned(16))) char lds[K1_LDS];
  const int tid = threadIdx.x, lane = tid & 63, w = __builtin_amdgcn_readfirstlane(tid >> 6);
  const int t = xcd_remap(blockIdx.x, 256), rt = t >> 2, ct = t & 3;
  const unsigned lds0 = (unsigned)(uintptr_t)lds;
  STAMP(0, 0);
  bf16x8 xr[4];
  const bool act = lane < 56;
  {
    const u16* xp_ = x + (size_t)(rt * 32 + w * 4) * D + (act ? lane : 55) * 8;
    gload16<0>(xr[0], xp_); gload16<896>(xr[1], xp_); gload16<1792>(xr[2], xp_); gload16<2688>(xr[3], xp_);
  }
#pragma unroll
  for (int im = 0; im < 2; ++im) {
    const int c64 = 2 * ct + im;
    if (c64 < 7) {
#pragma unroll
      for (int i = 0; i < 7; ++i) {
        const int piece = w * 7 + i, row = piece * 8 + (lane >> 3);
        const int f = ((row >> 1) & 1) | (((row >> 3) & 1) << 1);
        const int c = (lane & 7) ^ (f << 1);
        glds16(wq + (size_t)row * D + c64 * 64 + c * 8, lds0 + im * K1_WI + piece * 1024);
      }
    } else {
#pragma unroll
      for (int i = 0; i < 7; ++i) {
        const int pc = w * 7 + i < 50 ? w * 7 + i : 49, mat = pc >= 25, piece = pc - 25 * mat;
        const int e = piece * 512 + lane * 8;
        glds16((mat ? wv : wk) + (e < 448 * 28 ? e : 448 * 28 - 8), lds0 + im * K1_WI + mat * 25600 + piece * 1024);
      }
    }
  }
  u16 wt[8];
  const int frag0 = blockIdx.x + 256 * (w & 1);
  const bool dofrag = w < 2 && frag0 < 392;
  const int frag = dofrag ? frag0 : 0;
  {
    const int nb = frag / 14, ks = frag - nb * 14;
#pragma unroll
    for (int j = 0; j < 8; ++j) wt[j] = wo[(size_t)(32 * ks + 8 * (lane >> 4) + j) * D + 16 * nb + (lane & 15)];
  }
  SBAR();
  waitv4<22>(xr[0], xr[1], xr[2], xr[3]);
  {
    u32x4 xv[4];
#pragma unroll
    for (int r = 0; r < 4; ++r) xv[r] = __builtin_bit_cast(u32x4, xr[r]);
    float v[4][8], mean[4], rs[4];
#pragma unroll
    for (int r = 0; r < 4; ++r) {
      float s = 0.f;
#pragma unroll
      for (int i = 0; i < 4; ++i) {
        v[r][2 * i] = act ? bflo(xv[r][i]) : 0.f; v[r][2 * i + 1] = act ? bfhi(xv[r][i]) : 0.f;
        s += v[r][2 * i] + v[r][2 * i + 1];
      }
      mean[r] = s;
    }
#pragma unroll
    for (int r = 0; r < 4; ++r) mean[r] = wave_sum(mean[r]) * (1.f / D);
#pragma unroll
    for (int r = 0; r < 4; ++r) {
      float q = 0.f;
#pragma unroll
      for (int i = 0; i < 8; ++i) { v[r][i] = act ? v[r][i] - mean[r] : 0.f; q += v[r][i] * v[r][i]; }
      rs[r] = q;
    }
#pragma unroll
    for (int r = 0; r < 4; ++r) rs[r] = __builtin_amdgcn_rsqf(wave_sum(rs[r]) * (1.f / D) + LN_EPS);
    char* xdst = lds + K1_X + lane * K1_XS + w * 64;
#pragma unroll
    for (int r = 0; r < 4; ++r) {
      u32x4 o;
#pragma unroll
      for (int i = 0; i < 4; ++i) o[i] = cvtpk(v[r][2 * i] * rs[r], v[r][2 * i + 1] * rs[r]);
      *(u32x4*)(xdst + r * 16) = o;
    }
  }
  STAMP(0, 1);
  WAITV_BAR(0);
  STAMP(0, 2);
  const int nb = w & 3, im = w >> 2, c64 = 2 * ct + im, g = lane >> 4, q4 = (lane & 15) >> 2, p = lane & 3, l15 = lane & 15;
  const int f = ((q4 >> 1) & 1) | ((g & 1) << 1);
  const bool kv = c64 == 7;
  const int rs_ = kv ? 56 : 128;
  const lds_cptr wp = (lds_cptr)lds + im * K1_WI + (8 * g + q4) * rs_ +
                      (kv ? (nb >> 1) * 25600 + (nb & 1) * 32 + 8 * p : (((2 * nb + (p >> 1)) ^ (f << 1)) * 16) + 8 * (p & 1));
  const lds_cptr xp = (lds_cptr)lds + K1_X + g * K1_XS + l15 * 16;
  f32x4 acc0 = {0.f, 0.f, 0.f, 0.f}, acc1 = {0.f, 0.f, 0.f, 0.f};
  {
    bf16x8 a[14], b0[14], b1[14];
#pragma unroll
    for (int ks = 0; ks < 14; ++ks) {
      a[ks] = cat8(trread(wp + ks * 32 * rs_), trread(wp + ks * 32 * rs_ + 4 * rs_));
      b0[ks] = ldsr16(xp + ks * 4 * K1_XS);
      b1[ks] = ldsr16(xp + ks * 4 * K1_XS + 256);
    }
    SBAR();
#pragma unroll
    for (int ks = 0; ks < 14; ++ks) {
      acc0 = MFMA16(a[ks], b0[ks], acc0);
      acc1 = MFMA16(a[ks], b1[ks], acc1);
    }
  }
  STAMP(0, 3);
#pragma unroll
  for (int tb = 0; tb < 2; ++tb) {
    const f32x4 a = tb ? acc1 : acc0;
    const int s = rt * 32 + 16 * tb + l15;
    const int nl = 16 * nb + 4 * g;
    if (c64 < 7) {
      const int n0 = 64 * c64 + nl, h = n0 / 28, d0 = n0 - 28 * h;
      const int tg = s >> 3, c = (h & 3) * 8 + (s & 7), b = h >> 2;
      const size_t off = ((((size_t)(tg * 4 + b) * 2 + (d0 >> 4)) * 64) + ((d0 >> 3) & 1) * 32 + c) * 8 + (d0 & 7);
      *(u32x2*)(Qf + off) = (u32x2){cvtpk(a[0] * QSCALE, a[1] * QSCALE), cvtpk(a[2] * QSCALE, a[3] * QSCALE)};
    } else {
      const int kt = s >> 5, kk = s & 31;
      const int d0 = 16 * (nb & 1) + 4 * g;
      if (nb < 2) {
        const size_t off = ((((size_t)kt * 2 + (d0 >> 4)) * 64) + ((d0 >> 3) & 1) * 32 + kk) * 8 + (d0 & 7);
        u32x2 v = {cvtpk(a[0], a[1]), cvtpk(a[2], a[3])};
        if (d0 == 28) v = (u32x2){0x3f803f80u, 0x00003f80u};
        *(u32x2*)(Kf + off) = v;
      } else {
        const int sp = kk >> 4, j = ((kk >> 3) & 1) * 4 + (kk & 3), hi = (kk >> 2) & 1;
        const size_t off = ((((size_t)kt * 2 + sp) * 64) + hi * 32 + d0) * 8 + j;
        unsigned v0 = cvtpk(a[0], a[1]), v1 = cvtpk(a[2], a[3]);
        if (d0 == 28) { v0 = 0x00003f80u; v1 = 0u; }
        Vf[off] = (u16)v0; Vf[off + 8] = (u16)(v0 >> 16); Vf[off + 16] = (u16)v1; Vf[off + 24] = (u16)(v1 >> 16);
      }
    }
  }
  if (dofrag) {
    u32x4 o;
#pragma unroll
    for (int i = 0; i < 4; ++i) o[i] = (unsigned)wt[2 * i] | ((unsigned)wt[2 * i + 1] << 16);
    *(u32x4*)(Wof + ((size_t)frag * 64 + lane) * 8) = o;
  }
  if (blockIdx.x == 0) { cnt[tid] = 0u; cnt[512 + tid] = 0u; }
  STAMP(0, 4);
}

constexpr int K2_RING = 0, K2_OT = 131072, K2_DUM = K2_OT + 7168, K2_X = K2_DUM + 4096, K2_LDS = K2_X + 16384;
constexpr int K2_HB = 65536, HBS = 452, K2_HS = 98304;

DI float set_ref(bf16x8& q1, float m, int hi) {
  const float a = bflo(cvtpk(-m, 0.f)), r1 = -m - a, b = bflo(cvtpk(r1, 0.f)), c = bflo(cvtpk(r1 - b, 0.f));
  u32x4 v = __builtin_bit_cast(u32x4, q1);
  v[2] = hi ? cvtpk(a, b) : v[2];
  v[3] = hi ? cvtpk(c, 0.f) : v[3];
  q1 = __builtin_bit_cast(bf16x8, v);
  return -((a + b) + c);
}
DI float tile_max(const f32x16& s) {
  float a = __builtin_fmaxf(__builtin_fmaxf(s[0], s[1]), s[2]), b = __builtin_fmaxf(__builtin_fmaxf(s[3], s[4]), s[5]);
  a = __builtin_fmaxf(__builtin_fmaxf(a, s[6]), s[7]); b = __builtin_fmaxf(__builtin_fmaxf(b, s[8]), s[9]);
  a = __builtin_fmaxf(__builtin_fmaxf(a, s[10]), s[11]); b = __builtin_fmaxf(__builtin_fmaxf(b, s[12]), s[13]);
  a = __builtin_fmaxf(__builtin_fmaxf(a, s[14]), s[15]);
  return half_max(__builtin_fmaxf(a, b));
}
template <int N> DI void waitv() { asm volatile("s_waitcnt vmcnt(%0)" ::"i"(N) : "memory"); }
#define RBAR() asm volatile("s_barrier" ::: "memory")

DI void attn_safe(const u16* __restrict__ Kf, const u16* __restrict__ Vf, bf16x8 q0, bf16x8 q1, f32x16& o, int lane, int hi) {
  const f32x16 zero = {};
  float mref = 0.f;
  o = zero;
#pragma unroll 1
  for (int t = 0; t < 64; ++t) {
    const bf16x8 k0 = *(const bf16x8*)(Kf + t * 1024 + lane * 8), k1 = *(const bf16x8*)(Kf + t * 1024 + 512 + lane * 8);
    const bf16x8 v0 = *(const bf16x8*)(Vf + t * 1024 + lane * 8), v1 = *(const bf16x8*)(Vf + t * 1024 + 512 + lane * 8);
    (void)set_ref(q1, 0.f, hi);
    f32x16 s = MFMA32(k0, q0, zero);
    s = MFMA32(k1, q1, s);
    const float tm = tile_max(s);
    const float mn = set_ref(q1, t == 0 ? tm : __builtin_fmaxf(tm, mref), hi);
    const float al = t == 0 ? 0.f : __builtin_amdgcn_exp2f(mref - mn);
#pragma unroll
    for (int r = 0; r < 16; ++r) o[r] *= al;
    mref = mn;
    s = MFMA32(k0, q0, zero);
    s = MFMA32(k1, q1, s);
    u32x4 p0, p1;
#pragma unroll
    for (int i = 0; i < 4; ++i) {
      p0[i] = cvtpk(__builtin_amdgcn_exp2f(s[2 * i]), __builtin_amdgcn_exp2f(s[2 * i + 1]));
      p1[i] = cvtpk(__builtin_amdgcn_exp2f(s[8 + 2 * i]), __builtin_amdgcn_exp2f(s[8 + 2 * i + 1]));
    }
    o = MFMA32(v0, __builtin_bit_cast(bf16x8, p0), o);
    o = MFMA32(v1, __builtin_bit_cast(bf16x8, p1), o);
  }
}

#define EX4(S, i) do { S[i] = __builtin_amdgcn_exp2f(S[i]); S[i + 1] = __builtin_amdgcn_exp2f(S[i + 1]); \
    S[i + 2] = __builtin_amdgcn_exp2f(S[i + 2]); S[i + 3] = __builtin_amdgcn_exp2f(S[i + 3]); } while (0)
#define PK8(P, S, i) do { P[0] = cvtpk(S[i], S[i + 1]); P[1] = cvtpk(S[i + 2], S[i + 3]); P[2] = cvtpk(S[i + 4], S[i + 5]); P[3] = cvtpk(S[i + 6], S[i + 7]); } while (0)
#define BF(P) __builtin_bit_cast(bf16x8, P)
constexpr int CPRE = 0;
#define WOPF(j) do { if ((j) < CPRE) wf[(j) / 7][(j) % 7] = WOFR(j); } while (0)
#define RDS(u, off) ldsr16(((u) < 4 ? B0 : B1) + ((u) & 3) * 8192 + (off))
#define CTILE(u, NX, PF) do { \
      \
    sB = MFMA32(k0, qB0, zero); SBAR(); if (NX) k0 = RDS((u) + 1, 0); EX4(sA, 0); WOPF(PF); SBAR(); \
    sB = MFMA32(k1, qB1, sB); SBAR(); if (NX) k1 = RDS((u) + 1, 1024); EX4(sA, 4); PK8(p0, sA, 0); SBAR(); \
    oB = MFMA32(v1, BF(p1), oB); SBAR(); v1 = RDS(u, 3072); EX4(sA, 8); WOPF((PF) + 1); SBAR(); \
    oA = MFMA32(v0, BF(p0), oA); SBAR(); EX4(sA, 12); PK8(p1, sA, 8); SBAR(); \
      \
    sA = MFMA32(k0, qA0, zero); SBAR(); EX4(sB, 0); WOPF((PF) + 2); SBAR(); \
    sA = MFMA32(k1, qA1, sA); SBAR(); EX4(sB, 4); PK8(p0, sB, 0); SBAR(); \
    oA = MFMA32(v1, BF(p1), oA); SBAR(); EX4(sB, 8); WOPF((PF) + 3); SBAR(); \
    oB = MFMA32(v0, BF(p0), oB); SBAR(); if (NX) v0 = RDS((u) + 1, 2048); EX4(sB, 12); PK8(p1, sB, 8); SBAR(); } while (0)
#define CSTAGE(LAST, PF) do { \
    CTILE(0, true, PF); CTILE(1, true, (PF) + 4); CTILE(2, true, (PF) + 8); \
    if (!(LAST)) RBAR(); \
    CTILE(3, !(LAST), (PF) + 12); } while (0)

__global__ __launch_bounds__(512) void k_attn(const u16* __restrict__ x, const u16* __restrict__ Qf, const u16* __restrict__ Kf,
                                              const u16* __restrict__ Vf, const u16* __restrict__ Wof, const u16* __restrict__ w1,
                                              const u16* __restrict__ w2, u16* __restrict__ H, u16* __restrict__ HN) {
  __shared__ __attribute__((aligned(16))) char lds[K2_LDS];
  const int tid = threadIdx.x, lane = tid & 63, w = __builtin_amdgcn_readfirstlane(tid >> 6), hi = lane >> 5;
  const int tg = blockIdx.x;
  STAMP(1, 0);
  const int kh = w >> 2, ng = w & 3;
  const char* wob = (const char*)(Wof + (size_t)(7 * ng * 14 + 7 * kh) * 512);
  const unsigned wl = lane * 16;
  bf16x8 wf[7][7];
#define WOFR(i_) (*(const bf16x8*)(wob + (((i_) / 7) * 14 + (i_) % 7) * 1024 + wl))
#define LDWO(i0, i1) do { _Pragma("unroll") for (int i_ = i0; i_ < i1; ++i_) wf[i_ / 7][i_ % 7] = WOFR(i_); } while (0)
#define BOF(k) (*(const bf16x8*)(lds + K2_OT + ((4 * (k) + (lane >> 4)) * 8 + (lane & 7)) * 16))
#define HBST(acc) do { if ((lane & 15) < 8) { \
      _Pragma("unroll") for (int n = 0; n < 7; ++n) *(f32x4*)(lds + K2_HB + ((kh * 8 + (lane & 15)) * HBS + 16 * (7 * ng + n) + 4 * (lane >> 4)) * 4) = acc[n]; \
    } } while (0)
  u32x4 xv;
  if (w < 4) {
    const f32x16 zero = {};
    const int qh = w & 1, kp = w >> 1;
    const lds_cptr rp0 = (lds_cptr)lds + K2_RING + lane * 16;
    const lds_cptr rp = rp0 + kp * 4096;
    lds_cptr B0 = rp, B1 = rp + 32768;
    const u16* qp = Qf + ((size_t)((tg * 4 + 2 * qh) * 2) * 64 + lane) * 8;
    bf16x8 qA0 = *(const bf16x8*)(qp), qA1 = *(const bf16x8*)(qp + 512), qB0 = *(const bf16x8*)(qp + 1024), qB1 = *(const bf16x8*)(qp + 1536);
    RBAR();
    bf16x8 k0 = ldsr16(rp0), k1 = ldsr16(rp0 + 1024), v0 = RDS(0, 2048), v1 = v0;
    (void)set_ref(qA1, 0.f, hi);
    (void)set_ref(qB1, 0.f, hi);
    f32x16 sA = MFMA32(k0, qA0, zero), sB = MFMA32(k0, qB0, zero);
    sA = MFMA32(k1, qA1, sA);
    sB = MFMA32(k1, qB1, sB);
    k0 = RDS(0, 0); k1 = RDS(0, 1024);
    (void)set_ref(qA1, tile_max(sA), hi);
    (void)set_ref(qB1, tile_max(sB), hi);
    sA = MFMA32(k0, qA0, zero);
    sA = MFMA32(k1, qA1, sA);
    f32x16 oA = zero, oB = zero;
    u32x4 p0, p1 = {0u, 0u, 0u, 0u};
    SBAR();
    STAMP(1, 8);
#pragma unroll 1
    for (int st = 0; st < 4; ++st) {
      CSTAGE(false, 64);
      B0 = B1; B1 = rp + ((st + 2) & 3) * 32768;
    }
    CSTAGE(false, 0);
    B0 = B1; B1 = rp + 2 * 32768;
    CSTAGE(false, 16);
    B0 = B1; B1 = rp + 3 * 32768;
    CSTAGE(false, 64);
    B0 = B1;
    CSTAGE(true, 64);
    oB = MFMA32(v1, BF(p1), oB);
    STAMP(1, 10);
    {
      const f32x16 snd = kp ? oA : oB;
#pragma unroll
      for (int r4 = 0; r4 < 4; ++r4)
        *(f32x4*)(lds + K2_X + ((w * 4 + r4) * 64 + lane) * 16) = (f32x4){snd[4 * r4], snd[4 * r4 + 1], snd[4 * r4 + 2], snd[4 * r4 + 3]};
    }
    f32x16 o0 = kp ? oB : oA;
    bf16x8 qk0 = kp ? qB0 : qA0, qk1 = kp ? qB1 : qA1;
    asm volatile("" : "+v"(o0), "+v"(qk0), "+v"(qk1));
    SBAR();
    LBAR();
#pragma unroll
    for (int r4 = 0; r4 < 4; ++r4) {
      const f32x4 t = *(const f32x4*)(lds + K2_X + (((w ^ 2) * 4 + r4) * 64 + lane) * 16);
      o0[4 * r4] += t[0]; o0[4 * r4 + 1] += t[1]; o0[4 * r4 + 2] += t[2]; o0[4 * r4 + 3] += t[3];
    }
    const int qb = 2 * qh + kp;
    {
      float chk = 0.f;
#pragma unroll
      for (int r = 0; r < 16; ++r) chk = __builtin_fmaf(o0[r], 0.f, chk);
      const bool bad = !(chk == 0.f) || !(o0[12] < 1.8e19f);
      if (__any(bad)) {
        attn_safe(Kf, Vf, qk0, qk1, o0, lane, hi);
      }
    }
    {
      auto sw = __builtin_amdgcn_permlane32_swap(__float_as_uint(o0[12]), __float_as_uint(o0[12]), false, false);
      const float il = 1.f / __uint_as_float(sw[1]);
      const int c = lane & 31;
#pragma unroll
      for (int r4 = 0; r4 < 4; ++r4) {
        const int d = 8 * r4 + 4 * hi, k = 28 * (4 * qb + (c >> 3)) + d;
        if (d < 28)
          *(u32x2*)(lds + K2_OT + ((k >> 3) * 8 + (c & 7)) * 16 + (k & 7) * 2) =
              (u32x2){cvtpk(o0[4 * r4] * il, o0[4 * r4 + 1] * il), cvtpk(o0[4 * r4 + 2] * il, o0[4 * r4 + 3] * il)};
      }
    }
    STAMP(1, 1);
    LBAR();
    STAMP(1, 2);
    LDWO(16, 39);
    xv = *(const u32x4*)(x + (size_t)(tg * 8 + w) * D + (lane < 56 ? lane : 55) * 8);
    SBAR();
    {
      bf16x8 bo[7];
#pragma unroll
      for (int k = 0; k < 7; ++k) bo[k] = BOF(k);
#pragma unroll
      for (int i = 0; i < 16; ++i) wf[i / 7][i % 7] = *(const bf16x8*)(lds + K2_RING + (i >> 3) * 32768 + w * 8192 + (i & 7) * 1024 + lane * 16);
      f32x4 acc[7];
#pragma unroll
      for (int n = 0; n < 7; ++n) acc[n] = (f32x4){0.f, 0.f, 0.f, 0.f};
#pragma unroll
      for (int i = 0; i < 39; ++i) acc[i / 7] = MFMA16(wf[i / 7][i % 7], bo[i % 7], acc[i / 7]);
      HBST(acc);
    }
  } else {
    const int hw = w - 4;
    __builtin_amdgcn_s_setprio(3);
    const unsigned ring = (unsigned)(uintptr_t)lds + K2_RING + hw * 8192;
    const u16* kp = Kf + (size_t)hw * 2048;
    const u16* vp = Vf + (size_t)hw * 2048;
    const unsigned l16 = lane * 16;
#define DMAS(s_) do { const unsigned sl_ = ring + ((s_) & 3) * 32768; \
    _Pragma("unroll") for (int u_ = 0; u_ < 2; ++u_) { \
      glds16s(kp + (s_) * 8192 + u_ * 1024, l16, sl_ + u_ * 4096); glds16s(kp + (s_) * 8192 + u_ * 1024 + 512, l16, sl_ + u_ * 4096 + 1024); \
      glds16s(vp + (s_) * 8192 + u_ * 1024, l16, sl_ + u_ * 4096 + 2048); glds16s(vp + (s_) * 8192 + u_ * 1024 + 512, l16, sl_ + u_ * 4096 + 3072); } } while (0)
    const unsigned dum = (unsigned)(uintptr_t)lds + K2_DUM + hw * 1024;
    const int xg = tg & 7, rk = tg >> 3;
    const int ts = xg < 4 ? 25 * xg : 100 + 24 * (xg - 4), te = ts + (xg < 4 ? 25 : 24);
    const int n0 = ts / 7, n1 = (te - 1) / 7;
    const int nb0 = n0 + 3 <= 27 ? n0 : 24;
    const u16* p1 = w1 + (size_t)(64 * rk + 16 * hw) * F + nb0 * 64;
    const u16* p1b = w1 + (size_t)(64 * rk + 16 * hw) * F + n1 * 64;
    const u16* p2 = w2 + (size_t)(56 * rk + 14 * hw) * S + 4 * xg * 64;
    const unsigned o1 = ((lane >> 5) * F + (lane & 31) * 8) * 2, o1b = ((lane >> 3) * F + (lane & 7) * 8) * 2, o2 = ((lane >> 5) * S + (lane & 31) * 8) * 2;
#define PFD(i0, i1) do { _Pragma("unroll") for (int i_ = i0; i_ < i1; ++i_) { \
      if (i_ < 8) glds16s(p1 + (size_t)i_ * 2 * F, o1, dum); \
      else if (i_ < 10) glds16s(p1b + (size_t)(i_ - 8) * 8 * F, o1b, dum); \
      else glds16s(p2 + (size_t)(i_ - 10) * 2 * S, o2, dum); } } while (0)
    DMAS(0); DMAS(1); DMAS(2);
    waitv<16>(); RBAR();
    LDWO(0, 12); SBAR(); PFD(0, 3);
    waitv<23>(); RBAR();
    DMAS(3); LDWO(12, 24); SBAR(); PFD(3, 6);
    waitv<38>(); RBAR();
    DMAS(4); LDWO(24, 36); SBAR(); PFD(6, 9);
    waitv<38>(); RBAR();
    DMAS(5); LDWO(36, 49); SBAR(); PFD(9, 12);
    waitv<39>(); RBAR();
    bf16x8 wx[10];
    DMAS(6);
#pragma unroll
    for (int i = 0; i < 10; ++i) wx[i] = *(const bf16x8*)(wob - 7 * 1024 + (((39 + i) / 7) * 14 + (39 + i) % 7) * 1024 + wl);
    SBAR(); PFD(12, 15);
    waitv<37>(); RBAR();
    DMAS(7); PFD(15, 17);
    waitv<23>(); RBAR();
#pragma unroll
    for (int i = 0; i < 8; ++i) glds16s(wob - 7 * 1024 + ((i / 7) * 14 + i % 7) * 1024, wl, ring + i * 1024);
    waitv<10>(); RBAR();
#pragma unroll
    for (int i = 8; i < 16; ++i) glds16s(wob - 7 * 1024 + ((i / 7) * 14 + i % 7) * 1024, wl, ring + 32768 + (i - 8) * 1024);
    xv = *(const u32x4*)(x + (size_t)(tg * 8 + w) * D + (lane < 56 ? lane : 55) * 8);
    SBAR();
    __builtin_amdgcn_s_setprio(0);
    RBAR();
    waitv<1>();
    STAMP(1, 1);
    LBAR();
    STAMP(1, 2);
    {
      f32x4 acc[7];
      {
        bf16x8 bo[7];
#pragma unroll
        for (int k = 0; k < 7; ++k) bo[k] = BOF(k);
        acc[5] = (f32x4){0.f, 0.f, 0.f, 0.f}; acc[6] = acc[5];
#pragma unroll
        for (int i = 0; i < 10; ++i) acc[(39 + i) / 7] = MFMA16(wx[i], bo[(39 + i) % 7], acc[(39 + i) / 7]);
      }
      SBAR();
      bf16x8 bo[7];
#pragma unroll
      for (int k = 0; k < 7; ++k) bo[k] = BOF(7 + k);
#pragma unroll
      for (int n = 0; n < 7; ++n) {
        if (n < 5) acc[n] = (f32x4){0.f, 0.f, 0.f, 0.f};
#pragma unroll
        for (int k = 0; k < 7; ++k) acc[n] = MFMA16(wf[n][k], bo[k], acc[n]);
      }
      HBST(acc);
    }
  }
  LBAR();
  STAMP(1, 3);
  {
    const int s = tg * 8 + w;
    const bool act = lane < 56;
    const int lc = act ? lane : 55;
    float v[8];
    const f32x4 a0 = *(const f32x4*)(lds + K2_HB + (w * HBS + lc * 8) * 4), a1 = *(const f32x4*)(lds + K2_HB + (w * HBS + lc * 8 + 4) * 4);
    const f32x4 c0 = *(const f32x4*)(lds + K2_HB + ((8 + w) * HBS + lc * 8) * 4), c1 = *(const f32x4*)(lds + K2_HB + ((8 + w) * HBS + lc * 8 + 4) * 4);
    u32x4 hv;
#pragma unroll
    for (int i = 0; i < 2; ++i) {
      hv[i] = cvtpk(bflo(xv[i]) + a0[2 * i] + c0[2 * i], bfhi(xv[i]) + a0[2 * i + 1] + c0[2 * i + 1]);
      hv[2 + i] = cvtpk(bflo(xv[2 + i]) + a1[2 * i] + c1[2 * i], bfhi(xv[2 + i]) + a1[2 * i + 1] + c1[2 * i + 1]);
    }
    if (act) *(u32x4*)(H + (size_t)s * D + lane * 8) = hv;
#pragma unroll
    for (int i = 0; i < 4; ++i) { v[2 * i] = act ? bflo(hv[i]) : 0.f; v[2 * i + 1] = act ? bfhi(hv[i]) : 0.f; }
#pragma unroll
    for (int pass = 0; pass < 2; ++pass) {
      float sm = 0.f;
#pragma unroll
      for (int i = 0; i < 8; ++i) sm += v[i];
      const float mean = wave_sum(sm) * (1.f / D);
      float q = 0.f;
#pragma unroll
      for (int i = 0; i < 8; ++i) { v[i] = act ? v[i] - mean : 0.f; q += v[i] * v[i]; }
      const float rs = __builtin_amdgcn_rsqf(wave_sum(q) * (1.f / D) + LN_EPS);
#pragma unroll
      for (int i = 0; i < 8; ++i) v[i] *= rs;
    }
    if (act) *(u32x4*)(lds + K2_HS + w * 912 + lane * 16) = (u32x4){cvtpk(v[0], v[1]), cvtpk(v[2], v[3]), cvtpk(v[4], v[5]), cvtpk(v[6], v[7])};
  }
  LBAR();
  if (tid < D / 2) {
    unsigned r[8];
#pragma unroll
    for (int i = 0; i < 8; ++i) r[i] = *(const unsigned*)(lds + K2_HS + i * 912 + tid * 4);
    u32x4 lo, hi2;
#pragma unroll
    for (int i = 0; i < 4; ++i) {
      lo[i] = (r[2 * i] & 0xffffu) | (r[2 * i + 1] << 16);
      hi2[i] = (r[2 * i] >> 16) | (r[2 * i + 1] & 0xffff0000u);
    }
    u32x4* dst = (u32x4*)(HN + ((size_t)tg * D + 2 * tid) * 8);
    dst[0] = lo; dst[1] = hi2;
  }
  STAMP(1, 4);
}

constexpr int G_RING = 4, G_LDS = 8 * G_RING * 4096;
typedef __attribute__((address_space(1))) unsigned gu32;
typedef __attribute__((address_space(1))) unsigned long long gu64;
#define RLX_AGENT __ATOMIC_RELAXED, __HIP_MEMORY_SCOPE_AGENT
template <int OFF> DI void gload16c(bf16x8& d, const void* p) {
  asm volatile("global_load_dwordx4 %0, %1, off offset:%2 sc1" : "=v"(d) : "v"(p), "i"(OFF) : "memory");
}
template <int PH, bool DEP>
DI void mlp_tile(char* lds, int t, const u16* __restrict__ act, const u16* __restrict__ W, const u16* __restrict__ bias,
                 const u16* __restrict__ H, u16* __restrict__ out, unsigned* cnt, unsigned want, bf16x8 (&wr)[3][4],
                 const u16* __restrict__ W2, int nt2, bool have) {
  constexpr int KT = PH == 0 ? S : F, LDW = PH == 0 ? F : S, NK = KT / 256, NR = NK - G_RING;
  const int tid = threadIdx.x, lane = tid & 63, w = __builtin_amdgcn_readfirstlane(tid >> 6);
  const int nt = t / 7, mt = t - nt * 7;
  const unsigned lds0 = (unsigned)(uintptr_t)lds + w * (G_RING * 4096);
  const int g = lane >> 4, q4 = (lane & 15) >> 2, p = lane & 3, l15 = lane & 15;
  STAMP(2 + PH, 0);
  const int enb = w >> 1, emb = 2 * (w & 1);
  u32x2 eb, eh0, eh1;
  if (PH == 0) {
    eb = *(const u32x2*)(bias + 64 * nt + 16 * enb + 4 * g);
  } else {
    const int s = 64 * nt + 16 * enb + l15;
    eb[0] = bias[s];
    eh0 = *(const u32x2*)(H + (size_t)s * D + 64 * mt + 16 * emb + 4 * g);
    eh1 = *(const u32x2*)(H + (size_t)s * D + 64 * mt + 16 * emb + 16 + 4 * g);
  }
  SBAR();
  const u16* asrc = act + ((size_t)(4 * w + g) * D + 64 * mt + l15) * 8;
  const int r8 = lane >> 3, fe = (r8 >> 1) & 1;
  const u16* wsrc0 = W + (size_t)(32 * w + r8) * LDW + 64 * nt + ((lane & 7) ^ (fe << 1)) * 8;
  const u16* wsrc1 = W + (size_t)(32 * w + 8 + r8) * LDW + 64 * nt + ((lane & 7) ^ ((fe | 2) << 1)) * 8;
  const int f = ((q4 >> 1) & 1) | ((g & 1) << 1);
  int woff[4];
#pragma unroll
  for (int nb = 0; nb < 4; ++nb) woff[nb] = w * (G_RING * 4096) + (8 * g + q4) * 128 + (((2 * nb + (p >> 1)) ^ (f << 1)) * 16) + 8 * (p & 1);
  bf16x8 af[NK][4];
  f32x4 acc[4][4] = {};
#define STAGE_W(i) do { const unsigned sl_ = lds0 + ((i) % G_RING) * 4096; const size_t o_ = (size_t)(i) * 256 * LDW; \
    glds16(wsrc0 + o_, sl_); glds16(wsrc1 + o_, sl_ + 1024); glds16(wsrc0 + o_ + 16 * LDW, sl_ + 2048); glds16(wsrc1 + o_ + 16 * LDW, sl_ + 3072); } while (0)
#define LOAD_A(i) do { const u16* ap_ = asrc + (size_t)(i) * 32 * D * 8; \
    if (DEP) { gload16c<0>(af[i][0], ap_); gload16c<256>(af[i][1], ap_); gload16c<512>(af[i][2], ap_); gload16c<768>(af[i][3], ap_); } \
    else { gload16<0>(af[i][0], ap_); gload16<256>(af[i][1], ap_); gload16<512>(af[i][2], ap_); gload16<768>(af[i][3], ap_); } } while (0)
#define RDW(WF, i) do { _Pragma("unroll") for (int nb_ = 0; nb_ < 4; ++nb_) { const lds_cptr sp_ = (lds_cptr)lds + woff[nb_] + ((i) % G_RING) * 4096; \
      WF[nb_] = cat8(trread(sp_), trread(sp_ + 512)); } } while (0)
#define MMA(WF, i) do { _Pragma("unroll") for (int nb_ = 0; nb_ < 4; ++nb_) _Pragma("unroll") for (int mb_ = 0; mb_ < 4; ++mb_) \
      acc[nb_][mb_] = PH == 0 ? MFMA16(WF[nb_], af[i][mb_], acc[nb_][mb_]) : MFMA16(af[i][mb_], WF[nb_], acc[nb_][mb_]); } while (0)
  if (!DEP) {
    __builtin_amdgcn_s_setprio(3);
#pragma unroll
    for (int i = 0; i < G_RING; ++i) { STAGE_W(i); LOAD_A(i); }
    STAMP(2 + PH, 1);
#pragma unroll
    for (int i = 0; i < NK; ++i) {
      const int rem = NK - 1 - i < G_RING - 1 ? NK - 1 - i : G_RING - 1;
      if (i == 2) __builtin_amdgcn_s_setprio(2);
      else if (i == 4) __builtin_amdgcn_s_setprio(1);
      else if (i == 6) __builtin_amdgcn_s_setprio(0);
      if (rem == 3) waitv4<24>(af[i][0], af[i][1], af[i][2], af[i][3]);
      else if (rem == 2) waitv4<16 + 4>(af[i][0], af[i][1], af[i][2], af[i][3]);
      else if (rem == 1) waitv4<8 + 8>(af[i][0], af[i][1], af[i][2], af[i][3]);
      else waitv4<12>(af[i][0], af[i][1], af[i][2], af[i][3]);
      bf16x8 wf[4];
      RDW(wf, i);
      if (i + G_RING < NK) {
        asm volatile("s_waitcnt lgkmcnt(0)" ::: "memory");
        STAGE_W(i + G_RING); LOAD_A(i + G_RING);
      } else if (i + 1 < NK) {
        asm volatile("s_waitcnt lgkmcnt(0)" ::: "memory");
        const int j = i + G_RING - NK;
        const unsigned sl_ = lds0 + (i % G_RING) * 4096;
        const size_t o_ = (size_t)(4 + j) * 256 * S;
        const u16* a0 = W2 + (size_t)(32 * w + r8) * S + 64 * nt2 + ((lane & 7) ^ (fe << 1)) * 8 + o_;
        const u16* a1 = W2 + (size_t)(32 * w + 8 + r8) * S + 64 * nt2 + ((lane & 7) ^ ((fe | 2) << 1)) * 8 + o_;
        glds16(a0, sl_); glds16(a1, sl_ + 1024); glds16(a0 + 16 * S, sl_ + 2048); glds16(a1 + 16 * S, sl_ + 3072);
      }
      MMA(wf, i);
    }
    waitv<0>();
#pragma unroll
    for (int j = 0; j < 3; ++j) RDW(wr[j], NK - G_RING + j);
    asm volatile("s_waitcnt lgkmcnt(0)" ::: "memory");
  } else {
    static_assert(!DEP || NR == 3, "wr holds three k-steps");
    if (have) {
#pragma unroll
      for (int j = 0; j < G_RING; ++j) STAGE_W(j);
    } else {
#pragma unroll
      for (int j = 0; j < NR; ++j) STAGE_W(G_RING + j);
      STAGE_W(G_RING - 1);
#pragma unroll
      for (int j = 0; j < NR; ++j) {
        waitv<12>();
        RDW(wr[j], G_RING + j);
        asm volatile("s_waitcnt lgkmcnt(0)" ::: "memory");
        STAGE_W(j);
      }
    }
    STAMP(2 + PH, 1);
    if (w == 0) {
      for (unsigned spins = 0; spins < (1u << 20); ++spins) {
        if ((unsigned)__builtin_amdgcn_readfirstlane(__hip_atomic_load((gu32*)cnt, RLX_AGENT)) >= want) break;
        __builtin_amdgcn_s_sleep(1);
      }
    }
    asm volatile("s_barrier" ::: "memory");
    STAMP(2 + PH, 2);
#pragma unroll
    for (int j = 0; j < NK; ++j) LOAD_A((j + G_RING) % NK);
#pragma unroll
    for (int j = 0; j < NK; ++j) {
      constexpr int dummy = 0; (void)dummy;
      const int i = (j + G_RING) % NK;
      if (NK - 1 - j == 7) waitv4<28>(af[i][0], af[i][1], af[i][2], af[i][3]);
      else if (NK - 1 - j == 6) waitv4<24>(af[i][0], af[i][1], af[i][2], af[i][3]);
      else if (NK - 1 - j == 5) waitv4<20>(af[i][0], af[i][1], af[i][2], af[i][3]);
      else if (NK - 1 - j == 4) waitv4<16>(af[i][0], af[i][1], af[i][2], af[i][3]);
      else if (NK - 1 - j == 3) waitv4<12>(af[i][0], af[i][1], af[i][2], af[i][3]);
      else if (NK - 1 - j == 2) waitv4<8>(af[i][0], af[i][1], af[i][2], af[i][3]);
      else if (NK - 1 - j == 1) waitv4<4>(af[i][0], af[i][1], af[i][2], af[i][3]);
      else waitv4<0>(af[i][0], af[i][1], af[i][2], af[i][3]);
      if (j < NR) {
        MMA(wr[j], i);
      } else {
        bf16x8 wf[4];
        RDW(wf, i);
        MMA(wf, i);
      }
    }
  }
#undef STAGE_W
#undef LOAD_A
#undef RDW
#undef MMA
  STAMP(2 + PH, 4);
  {
    f32x4* xb = (f32x4*)lds;
#pragma unroll
    for (int nb = 0; nb < 4; ++nb)
#pragma unroll
      for (int mb = 0; mb < 4; ++mb) xb[(w * 16 + nb * 4 + mb) * 64 + lane] = acc[nb][mb];
  }
  LBAR();
  {
    const f32x4* xb = (const f32x4*)lds;
#pragma unroll
    for (int e = 0; e < 2; ++e) {
      f32x4 a = xb[(2 * w + e) * 64 + lane];
#pragma unroll
      for (int v = 1; v < 8; ++v) a += xb[(v * 16 + 2 * w + e) * 64 + lane];
      const int mb = emb + e;
      if (PH == 0) {
        const int fi = 64 * nt + 16 * enb + 4 * g, d = 64 * mt + 16 * mb + l15;
        float z[4] = {a[0] + bflo(eb[0]), a[1] + bfhi(eb[0]), a[2] + bflo(eb[1]), a[3] + bfhi(eb[1])};
#pragma unroll
        for (int i = 0; i < 4; ++i) {
          const float zz = z[i], a2 = zz * (1.f + 0.044715f * zz * zz) * (-2.f * 0.7978845608028654f * 1.4426950408889634f);
          z[i] = zz * __builtin_amdgcn_rcpf(1.f + __builtin_amdgcn_exp2f(a2));
        }
        const unsigned long long v = (unsigned long long)cvtpk(z[0], z[1]) | ((unsigned long long)cvtpk(z[2], z[3]) << 32);
        __hip_atomic_store((gu64*)(out + ((size_t)(fi >> 3) * D + d) * 8 + (fi & 7)), v, RLX_AGENT);
      } else {
        const int d = 64 * mt + 16 * mb + 4 * g, s = 64 * nt + 16 * enb + l15;
        const float bb = bflo(eb[0]);
        const u32x2 hv = e ? eh1 : eh0;
        *(u32x2*)(out + (size_t)s * D + d) =
            (u32x2){cvtpk(a[0] + bb + bflo(hv[0]), a[1] + bb + bfhi(hv[0])), cvtpk(a[2] + bb + bflo(hv[1]), a[3] + bb + bfhi(hv[1]))};
      }
    }
  }
  STAMP(2 + PH, 5);
}

__global__ __launch_bounds__(512) void k_mlp(const u16* __restrict__ HN, const u16* __restrict__ w1, const u16* __restrict__ b1,
                                             const u16* __restrict__ w2, const u16* __restrict__ b2, const u16* __restrict__ H,
                                             u16* U, u16* __restrict__ out, unsigned* cnt) {
  __shared__ __attribute__((aligned(16))) char lds[G_LDS];
  const int t2 = xcd_remap(blockIdx.x, 224);
  bf16x8 wr[3][4];
  if (blockIdx.x < 196) {
    const int t = xcd_remap(blockIdx.x, 196);
    mlp_tile<0, false>(lds, t, HN, w1, b1, H, U, cnt, 0u, wr, w2, t2 / 7, false);
    asm volatile("s_waitcnt vmcnt(0) lgkmcnt(0)\n\ts_barrier" ::: "memory");
    if (threadIdx.x == 0) __hip_atomic_fetch_add((gu32*)(cnt + (t % 7) * 64), 1u, RLX_AGENT);
  }
  mlp_tile<1, true>(lds, t2, U, w2, b2, H, out, cnt + (t2 % 7) * 64, 28u, wr, w2, 0, blockIdx.x < 196);
}

extern "C" void kernel_launch(void* const* d_in, const int* in_sizes, int n_in, void* d_out, int out_size, void* d_ws, size_t ws_size,
                              hipStream_t stream) {
  const u16* x = (const u16*)d_in[0];
  const u16* wq = (const u16*)d_in[1];
  const u16* wk = (const u16*)d_in[2];
  const u16* wv = (const u16*)d_in[3];
  const u16* wo = (const u16*)d_in[4];
  const u16* w1 = (const u16*)d_in[6];
  const u16* b1 = (const u16*)d_in[7];
  const u16* w2 = (const u16*)d_in[8];
  const u16* b2 = (const u16*)d_in[9];
  char* ws = (char*)d_ws;
  u16 *Qf = (u16*)(ws + WS_QF), *Kf = (u16*)(ws + WS_KF), *Vf = (u16*)(ws + WS_VF), *Wof = (u16*)(ws + WS_WOF);
  u16 *H = (u16*)(ws + WS_H), *HN = (u16*)(ws + WS_HN), *U = (u16*)(ws + WS_U);
  unsigned* cnt = (unsigned*)(ws + WS_CNT);
  k_qkv<<<256, 512, 0, stream>>>(x, wq, wk, wv, wo, Qf, Kf, Vf, Wof, cnt);
  k_attn<<<256, 512, 0, stream>>>(x, Qf, Kf, Vf, Wof, w1, w2, H, HN);
  k_mlp<<<224, 512, 0, stream>>>(HN, w1, b1, w2, b2, H, U, (u16*)d_out, cnt);
}
```

```cpp
#include <hip/hip_runtime.h>
#include <hip/hip_bf16.h>

typedef unsigned short u16;
using bf16x8 = __attribute__((ext_vector_type(8))) short;
using s16x4 = __attribute__((ext_vector_type(4))) short;
using f32x4 = __attribute__((ext_vector_type(4))) float;
using f32x16 = __attribute__((ext_vector_type(16))) float;
using u32x4 = __attribute__((ext_vector_type(4))) unsigned;
using u32x2 = __attribute__((ext_vector_type(2))) unsigned;
typedef short v4i16_t __attribute__((ext_vector_type(4)));
typedef __attribute__((address_space(3))) const char* lds_cptr;
typedef __bf16 bf16x2_t __attribute__((ext_vector_type(2)));
typedef float f32x2_t __attribute__((ext_vector_type(2)));

#define DI __device__ __forceinline__
#define MFMA32(a, b, c) __builtin_amdgcn_mfma_f32_32x32x16_bf16(a, b, c, 0, 0, 0)
#define MFMA16(a, b, c) __builtin_amdgcn_mfma_f32_16x16x32_bf16(a, b, c, 0, 0, 0)
#define SBAR() __builtin_amdgcn_sched_barrier(0)
#define LBAR() asm volatile("s_waitcnt lgkmcnt(0)\n\ts_barrier" ::: "memory")
#define WAITV_BAR(N) asm volatile("s_waitcnt vmcnt(" #N ") lgkmcnt(0)\n\ts_barrier" ::: "memory")

constexpr int S = 2048, D = 448, DH = 28, F = 1792;
constexpr float QSCALE = 0.18898223650461362f * 1.4426950408889634f;
constexpr float LN_EPS = 1e-5f;

constexpr size_t WS_QF = 0;
constexpr size_t WS_KF = WS_QF + 2097152;
constexpr size_t WS_VF = WS_KF + 131072;
constexpr size_t WS_WOF = WS_VF + 131072;
constexpr size_t WS_H = WS_WOF + 401408;
constexpr size_t WS_HN = WS_H + 1835008;
constexpr size_t WS_U = WS_HN + 1835008;
constexpr size_t WS_CNT = WS_U + 1605632;

#define STAMP(k, i)
#define CSTAMP(k, i)

DI unsigned cvtpk(float lo, float hi) {
  const f32x2_t v = {lo, hi};
  return __builtin_bit_cast(unsigned, __builtin_convertvector(v, bf16x2_t));
}
DI float bflo(unsigned u) { return __uint_as_float(u << 16); }
DI float bfhi(unsigned u) { return __uint_as_float(u & 0xffff0000u); }
template <int CTRL> DI float dppf(float v) {
  return __uint_as_float(__builtin_amdgcn_update_dpp(0u, __float_as_uint(v), CTRL, 0xf, 0xf, true));
}
DI float wave_sum(float v) {
  v += dppf<0xB1>(v);
  v += dppf<0x4E>(v);
  v += dppf<0x141>(v);
  v += dppf<0x140>(v);
  auto a = __builtin_amdgcn_permlane16_swap(__float_as_uint(v), __float_as_uint(v), false, false);
  v = __uint_as_float(a[0]) + __uint_as_float(a[1]);
  auto b = __builtin_amdgcn_permlane32_swap(__float_as_uint(v), __float_as_uint(v), false, false);
  return __uint_as_float(b[0]) + __uint_as_float(b[1]);
}
DI float half_max(float v) {
  auto b = __builtin_amdgcn_permlane32_swap(__float_as_uint(v), __float_as_uint(v), false, false);
  return __builtin_fmaxf(__uint_as_float(b[0]), __uint_as_float(b[1]));
}
DI void glds16(const void* g, unsigned lds_base) {
  unsigned sv;
  asm volatile("s_mov_b32 %0, m0\n\ts_mov_b32 m0, %2\n\ts_nop 0\n\tglobal_load_lds_dwordx4 %1, off\n\ts_mov_b32 m0, %0"
               : "=&s"(sv) : "v"(g), "s"(lds_base) : "memory");
}
DI void glds16s(const void* sbase, unsigned voff, unsigned lds_base) {
  unsigned sv;
  asm volatile("s_mov_b32 %0, m0\n\ts_mov_b32 m0, %3\n\ts_nop 0\n\tglobal_load_lds_dwordx4 %1, %2\n\ts_mov_b32 m0, %0"
               : "=&s"(sv) : "v"(voff), "s"(sbase), "s"(lds_base) : "memory");
}
DI void glds4(const void* g, unsigned lds_base) {
  unsigned sv;
  asm volatile("s_mov_b32 %0, m0\n\ts_mov_b32 m0, %2\n\ts_nop 0\n\tglobal_load_lds_dword %1, off\n\ts_mov_b32 m0, %0"
               : "=&s"(sv) : "v"(g), "s"(lds_base) : "memory");
}
template <int N> DI void waitv4(bf16x8& a, bf16x8& b, bf16x8& c, bf16x8& d) {
  asm volatile("s_waitcnt vmcnt(%4)" : "+v"(a), "+v"(b), "+v"(c), "+v"(d) : "i"(N) : "memory");
}
template <int OFF> DI void gload16(bf16x8& d, const void* p) {
  asm volatile("global_load_dwordx4 %0, %1, off offset:%2" : "=v"(d) : "v"(p), "i"(OFF) : "memory");
}
DI s16x4 trread(lds_cptr p) {
  return __builtin_bit_cast(s16x4, __builtin_amdgcn_ds_read_tr16_b64_v4i16((__attribute__((address_space(3))) v4i16_t*)p));
}
DI bf16x8 ldsr16(lds_cptr p) { return *(const __attribute__((address_space(3))) bf16x8*)p; }
DI bf16x8 cat8(s16x4 a, s16x4 b) { return __builtin_shufflevector(a, b, 0, 1, 2, 3, 4, 5, 6, 7); }
DI int xcd_remap(int bid, int nwg) {
  const int q = nwg >> 3, r = nwg & 7, x = bid & 7, o = bid >> 3;
  return (x < r ? x * (q + 1) : r * (q + 1) + (x - r) * q) + o;
}

constexpr int K1_WI = 448 * 128, K1_X = 2 * K1_WI, K1_XS = 528, K1_LDS = K1_X + 64 * K1_XS;
__global__ __launch_bounds__(512) void k_qkv(const u16* __restrict__ x, const u16* __restrict__ wq, const u16* __restrict__ wk,
                                             const u16* __restrict__ wv, const u16* __restrict__ wo, u16* __restrict__ Qf,
                                             u16* __restrict__ Kf, u16* __restrict__ Vf, u16* __restrict__ Wof, unsigned* __restrict__ cnt) {
  __shared__ __attribute__((aligned(16))) char lds[K1_LDS];
  const int tid = threadIdx.x, lane = tid & 63, w = __builtin_amdgcn_readfirstlane(tid >> 6);
  const int t = xcd_remap(blockIdx.x, 256), rt = t >> 2, ct = t & 3;
  const unsigned lds0 = (unsigned)(uintptr_t)lds;
  STAMP(0, 0);
  bf16x8 xr[4];
  const bool act = lane < 56;
  {
    const u16* xp_ = x + (size_t)(rt * 32 + w * 4) * D + (act ? lane : 55) * 8;
    gload16<0>(xr[0], xp_); gload16<896>(xr[1], xp_); gload16<1792>(xr[2], xp_); gload16<2688>(xr[3], xp_);
  }
#pragma unroll
  for (int im = 0; im < 2; ++im) {
    const int c64 = 2 * ct + im;
    if (c64 < 7) {
#pragma unroll
      for (int i = 0; i < 7; ++i) {
        const int piece = w * 7 + i, row = piece * 8 + (lane >> 3);
        const int f = ((row >> 1) & 1) | (((row >> 3) & 1) << 1);
        const int c = (lane & 7) ^ (f << 1);
        glds16(wq + (size_t)row * D + c64 * 64 + c * 8, lds0 + im * K1_WI + piece * 1024);
      }
    } else {
#pragma unroll
      for (int i = 0; i < 7; ++i) {
        const int pc = w * 7 + i < 50 ? w * 7 + i : 49, mat = pc >= 25, piece = pc - 25 * mat;
        const int e = piece * 512 + lane * 8;
        glds16((mat ? wv : wk) + (e < 448 * 28 ? e : 448 * 28 - 8), lds0 + im * K1_WI + mat * 25600 + piece * 1024);
      }
    }
  }
  u16 wt[8];
  const int frag0 = blockIdx.x + 256 * (w & 1);
  const bool dofrag = w < 2 && frag0 < 392;
  const int frag = dofrag ? frag0 : 0;
  {
    const int nb = frag / 14, ks = frag - nb * 14;
#pragma unroll
    for (int j = 0; j < 8; ++j) wt[j] = wo[(size_t)(32 * ks + 8 * (lane >> 4) + j) * D + 16 * nb + (lane & 15)];
  }
  SBAR();
  waitv4<22>(xr[0], xr[1], xr[2], xr[3]);
  {
    u32x4 xv[4];
#pragma unroll
    for (int r = 0; r < 4; ++r) xv[r] = __builtin_bit_cast(u32x4, xr[r]);
    float v[4][8], mean[4], rs[4];
#pragma unroll
    for (int r = 0; r < 4; ++r) {
      float s = 0.f;
#pragma unroll
      for (int i = 0; i < 4; ++i) {
        v[r][2 * i] = act ? bflo(xv[r][i]) : 0.f; v[r][2 * i + 1] = act ? bfhi(xv[r][i]) : 0.f;
        s += v[r][2 * i] + v[r][2 * i + 1];
      }
      mean[r] = s;
    }
#pragma unroll
    for (int r = 0; r < 4; ++r) mean[r] = wave_sum(mean[r]) * (1.f / D);
#pragma unroll
    for (int r = 0; r < 4; ++r) {
      float q = 0.f;
#pragma unroll
      for (int i = 0; i < 8; ++i) { v[r][i] = act ? v[r][i] - mean[r] : 0.f; q += v[r][i] * v[r][i]; }
      rs[r] = q;
    }
#pragma unroll
    for (int r = 0; r < 4; ++r) rs[r] = __builtin_amdgcn_rsqf(wave_sum(rs[r]) * (1.f / D) + LN_EPS);
    char* xdst = lds + K1_X + lane * K1_XS + w * 64;
#pragma unroll
    for (int r = 0; r < 4; ++r) {
      u32x4 o;
#pragma unroll
      for (int i = 0; i < 4; ++i) o[i] = cvtpk(v[r][2 * i] * rs[r], v[r][2 * i + 1] * rs[r]);
      *(u32x4*)(xdst + r * 16) = o;
    }
  }
  STAMP(0, 1);
  WAITV_BAR(0);
  STAMP(0, 2);
  const int nb = w & 3, im = w >> 2, c64 = 2 * ct + im, g = lane >> 4, q4 = (lane & 15) >> 2, p = lane & 3, l15 = lane & 15;
  const int f = ((q4 >> 1) & 1) | ((g & 1) << 1);
  const bool kv = c64 == 7;
  const int rs_ = kv ? 56 : 128;
  const lds_cptr wp = (lds_cptr)lds + im * K1_WI + (8 * g + q4) * rs_ +
                      (kv ? (nb >> 1) * 25600 + (nb & 1) * 32 + 8 * p : (((2 * nb + (p >> 1)) ^ (f << 1)) * 16) + 8 * (p & 1));
  const lds_cptr xp = (lds_cptr)lds + K1_X + g * K1_XS + l15 * 16;
  f32x4 acc0 = {0.f, 0.f, 0.f, 0.f}, acc1 = {0.f, 0.f, 0.f, 0.f};
  {
    bf16x8 a[14], b0[14], b1[14];
#pragma unroll
    for (int ks = 0; ks < 14; ++ks) {
      a[ks] = cat8(trread(wp + ks * 32 * rs_), trread(wp + ks * 32 * rs_ + 4 * rs_));
      b0[ks] = ldsr16(xp + ks * 4 * K1_XS);
      b1[ks] = ldsr16(xp + ks * 4 * K1_XS + 256);
    }
    SBAR();
#pragma unroll
    for (int ks = 0; ks < 14; ++ks) {
      acc0 = MFMA16(a[ks], b0[ks], acc0);
      acc1 = MFMA16(a[ks], b1[ks], acc1);
    }
  }
  STAMP(0, 3);
#pragma unroll
  for (int tb = 0; tb < 2; ++tb) {
    const f32x4 a = tb ? acc1 : acc0;
    const int s = rt * 32 + 16 * tb + l15;
    const int nl = 16 * nb + 4 * g;
    if (c64 < 7) {
      const int n0 = 64 * c64 + nl, h = n0 / 28, d0 = n0 - 28 * h;
      const int tg = s >> 3, c = (h & 3) * 8 + (s & 7), b = h >> 2;
      const size_t off = ((((size_t)(tg * 4 + b) * 2 + (d0 >> 4)) * 64) + ((d0 >> 3) & 1) * 32 + c) * 8 + (d0 & 7);
      *(u32x2*)(Qf + off) = (u32x2){cvtpk(a[0] * QSCALE, a[1] * QSCALE), cvtpk(a[2] * QSCALE, a[3] * QSCALE)};
    } else {
      const int kt = s >> 5, kk = s & 31;
      const int d0 = 16 * (nb & 1) + 4 * g;
      if (nb < 2) {
        const size_t off = ((((size_t)kt * 2 + (d0 >> 4)) * 64) + ((d0 >> 3) & 1) * 32 + kk) * 8 + (d0 & 7);
        u32x2 v = {cvtpk(a[0], a[1]), cvtpk(a[2], a[3])};
        if (d0 == 28) v = (u32x2){0x3f803f80u, 0x00003f80u};
        *(u32x2*)(Kf + off) = v;
      } else {
        const int sp = kk >> 4, j = ((kk >> 3) & 1) * 4 + (kk & 3), hi = (kk >> 2) & 1;
        const size_t off = ((((size_t)kt * 2 + sp) * 64) + hi * 32 + d0) * 8 + j;
        unsigned v0 = cvtpk(a[0], a[1]), v1 = cvtpk(a[2], a[3]);
        if (d0 == 28) { v0 = 0x00003f80u; v1 = 0u; }
        Vf[off] = (u16)v0; Vf[off + 8] = (u16)(v0 >> 16); Vf[off + 16] = (u16)v1; Vf[off + 24] = (u16)(v1 >> 16);
      }
    }
  }
  if (dofrag) {
    u32x4 o;
#pragma unroll
    for (int i = 0; i < 4; ++i) o[i] = (unsigned)wt[2 * i] | ((unsigned)wt[2 * i + 1] << 16);
    *(u32x4*)(Wof + ((size_t)frag * 64 + lane) * 8) = o;
  }
  if (blockIdx.x == 0) { cnt[tid] = 0u; cnt[512 + tid] = 0u; }
  STAMP(0, 4);
}

constexpr int K2_RING = 0, K2_OT = 131072, K2_DUM = K2_OT + 7168, K2_X = K2_DUM + 4096, K2_LDS = K2_X + 16384;
constexpr int K2_HB = 65536, HBS = 452, K2_HS = K2_X;

DI float set_ref(bf16x8& q1, float m, int hi) {
  const float a = bflo(cvtpk(-m, 0.f)), r1 = -m - a, b = bflo(cvtpk(r1, 0.f)), c = bflo(cvtpk(r1 - b, 0.f));
  u32x4 v = __builtin_bit_cast(u32x4, q1);
  v[2] = hi ? cvtpk(a, b) : v[2];
  v[3] = hi ? cvtpk(c, 0.f) : v[3];
  q1 = __builtin_bit_cast(bf16x8, v);
  return -((a + b) + c);
}
DI float tile_max(const f32x16& s) {
  float a = __builtin_fmaxf(__builtin_fmaxf(s[0], s[1]), s[2]), b = __builtin_fmaxf(__builtin_fmaxf(s[3], s[4]), s[5]);
  a = __builtin_fmaxf(__builtin_fmaxf(a, s[6]), s[7]); b = __builtin_fmaxf(__builtin_fmaxf(b, s[8]), s[9]);
  a = __builtin_fmaxf(__builtin_fmaxf(a, s[10]), s[11]); b = __builtin_fmaxf(__builtin_fmaxf(b, s[12]), s[13]);
  a = __builtin_fmaxf(__builtin_fmaxf(a, s[14]), s[15]);
  return half_max(__builtin_fmaxf(a, b));
}
template <int N> DI void waitv() { asm volatile("s_waitcnt vmcnt(%0)" ::"i"(N) : "memory"); }
#define RBAR() asm volatile("s_barrier" ::: "memory")

DI void attn_safe(const u16* __restrict__ Kf, const u16* __restrict__ Vf, bf16x8 q0, bf16x8 q1, f32x16& o, int lane, int hi) {
  const f32x16 zero = {};
  float mref = 0.f;
  o = zero;
#pragma unroll 1
  for (int t = 0; t < 64; ++t) {
    const bf16x8 k0 = *(const bf16x8*)(Kf + t * 1024 + lane * 8), k1 = *(const bf16x8*)(Kf + t * 1024 + 512 + lane * 8);
    const bf16x8 v0 = *(const bf16x8*)(Vf + t * 1024 + lane * 8), v1 = *(const bf16x8*)(Vf + t * 1024 + 512 + lane * 8);
    (void)set_ref(q1, 0.f, hi);
    f32x16 s = MFMA32(k0, q0, zero);
    s = MFMA32(k1, q1, s);
    const float tm = tile_max(s);
    const float mn = set_ref(q1, t == 0 ? tm : __builtin_fmaxf(tm, mref), hi);
    const float al = t == 0 ? 0.f : __builtin_amdgcn_exp2f(mref - mn);
#pragma unroll
    for (int r = 0; r < 16; ++r) o[r] *= al;
    mref = mn;
    s = MFMA32(k0, q0, zero);
    s = MFMA32(k1, q1, s);
    u32x4 p0, p1;
#pragma unroll
    for (int i = 0; i < 4; ++i) {
      p0[i] = cvtpk(__builtin_amdgcn_exp2f(s[2 * i]), __builtin_amdgcn_exp2f(s[2 * i + 1]));
      p1[i] = cvtpk(__builtin_amdgcn_exp2f(s[8 + 2 * i]), __builtin_amdgcn_exp2f(s[8 + 2 * i + 1]));
    }
    o = MFMA32(v0, __builtin_bit_cast(bf16x8, p0), o);
    o = MFMA32(v1, __builtin_bit_cast(bf16x8, p1), o);
  }
}

#define EX4(S, i) do { S[i] = __builtin_amdgcn_exp2f(S[i]); S[i + 1] = __builtin_amdgcn_exp2f(S[i + 1]); \
    S[i + 2] = __builtin_amdgcn_exp2f(S[i + 2]); S[i + 3] = __builtin_amdgcn_exp2f(S[i + 3]); } while (0)
#define PK8(P, S, i) do { P[0] = cvtpk(S[i], S[i + 1]); P[1] = cvtpk(S[i + 2], S[i + 3]); P[2] = cvtpk(S[i + 4], S[i + 5]); P[3] = cvtpk(S[i + 6], S[i + 7]); } while (0)
#define BF(P) __builtin_bit_cast(bf16x8, P)
constexpr int CPRE = 0;
#define WOPF(j) do { if ((j) < CPRE) wf[(j) / 7][(j) % 7] = WOFR(j); } while (0)
#define RDS(u, off) ldsr16(((u) < 4 ? B0 : B1) + ((u) & 3) * 8192 + (off))
#define CTILE(u, NX, PF) do { \
      \
    sB = MFMA32(k0, qB0, zero); SBAR(); if (NX) k0 = RDS((u) + 1, 0); EX4(sA, 0); WOPF(PF); SBAR(); \
    sB = MFMA32(k1, qB1, sB); SBAR(); if (NX) k1 = RDS((u) + 1, 1024); EX4(sA, 4); PK8(p0, sA, 0); SBAR(); \
    oB = MFMA32(v1, BF(p1), oB); SBAR(); v1 = RDS(u, 3072); EX4(sA, 8); WOPF((PF) + 1); SBAR(); \
    oA = MFMA32(v0, BF(p0), oA); SBAR(); EX4(sA, 12); PK8(p1, sA, 8); SBAR(); \
      \
    sA = MFMA32(k0, qA0, zero); SBAR(); EX4(sB, 0); WOPF((PF) + 2); SBAR(); \
    sA = MFMA32(k1, qA1, sA); SBAR(); EX4(sB, 4); PK8(p0, sB, 0); SBAR(); \
    oA = MFMA32(v1, BF(p1), oA); SBAR(); EX4(sB, 8); WOPF((PF) + 3); SBAR(); \
    oB = MFMA32(v0, BF(p0), oB); SBAR(); if (NX) v0 = RDS((u) + 1, 2048); EX4(sB, 12); PK8(p1, sB, 8); SBAR(); } while (0)
#define CSTAGE(LAST, PF) do { \
    CTILE(0, true, PF); CTILE(1, true, (PF) + 4); CTILE(2, true, (PF) + 8); \
    if (!(LAST)) RBAR(); \
    CTILE(3, !(LAST), (PF) + 12); } while (0)

DI void st16c(void* p, u32x4 v) {
  asm volatile("global_store_dwordx4 %0, %1, off sc1\n\ts_nop 1" ::"v"(p), "v"(v) : "memory");
}
DI void attn_phase(char* lds, const u16* __restrict__ x, const u16* __restrict__ Qf, const u16* __restrict__ Kf,
                   const u16* __restrict__ Vf, const u16* __restrict__ Wof, const u16* __restrict__ w1,
                   const u16* __restrict__ w2, u16* H, u16* HN) {
  const int tid = threadIdx.x, lane = tid & 63, w = __builtin_amdgcn_readfirstlane(tid >> 6), hi = lane >> 5;
  const int tg = blockIdx.x;
  STAMP(1, 0);
  const int kh = w >> 2, ng = w & 3;
  const char* wob = (const char*)(Wof + (size_t)(7 * ng * 14 + 7 * kh) * 512);
  const unsigned wl = lane * 16;
  bf16x8 wf[7][7];
#define WOFR(i_) (*(const bf16x8*)(wob + (((i_) / 7) * 14 + (i_) % 7) * 1024 + wl))
#define LDWO(i0, i1) do { _Pragma("unroll") for (int i_ = i0; i_ < i1; ++i_) wf[i_ / 7][i_ % 7] = WOFR(i_); } while (0)
#define BOF(k) (*(const bf16x8*)(lds + K2_OT + ((4 * (k) + (lane >> 4)) * 8 + (lane & 7)) * 16))
#define HBST(acc) do { if ((lane & 15) < 8) { \
      _Pragma("unroll") for (int n = 0; n < 7; ++n) *(f32x4*)(lds + K2_HB + ((kh * 8 + (lane & 15)) * HBS + 16 * (7 * ng + n) + 4 * (lane >> 4)) * 4) = acc[n]; \
    } } while (0)
  u32x4 xv;
  if (w < 4) {
    const f32x16 zero = {};
    const int qh = w & 1, kp = w >> 1;
    const lds_cptr rp0 = (lds_cptr)lds + K2_RING + lane * 16;
    const lds_cptr rp = rp0 + kp * 4096;
    lds_cptr B0 = rp, B1 = rp + 32768;
    const u16* qp = Qf + ((size_t)((tg * 4 + 2 * qh) * 2) * 64 + lane) * 8;
    bf16x8 qA0 = *(const bf16x8*)(qp), qA1 = *(const bf16x8*)(qp + 512), qB0 = *(const bf16x8*)(qp + 1024), qB1 = *(const bf16x8*)(qp + 1536);
    RBAR();
    bf16x8 k0 = ldsr16(rp0), k1 = ldsr16(rp0 + 1024), v0 = RDS(0, 2048), v1 = v0;
    (void)set_ref(qA1, 0.f, hi);
    (void)set_ref(qB1, 0.f, hi);
    f32x16 sA = MFMA32(k0, qA0, zero), sB = MFMA32(k0, qB0, zero);
    sA = MFMA32(k1, qA1, sA);
    sB = MFMA32(k1, qB1, sB);
    k0 = RDS(0, 0); k1 = RDS(0, 1024);
    (void)set_ref(qA1, tile_max(sA), hi);
    (void)set_ref(qB1, tile_max(sB), hi);
    sA = MFMA32(k0, qA0, zero);
    sA = MFMA32(k1, qA1, sA);
    f32x16 oA = zero, oB = zero;
    u32x4 p0, p1 = {0u, 0u, 0u, 0u};
    SBAR();
    STAMP(1, 8);
#pragma unroll 1
    for (int st = 0; st < 4; ++st) {
      CSTAGE(false, 64);
      B0 = B1; B1 = rp + ((st + 2) & 3) * 32768;
    }
    CSTAGE(false, 0);
    B0 = B1; B1 = rp + 2 * 32768;
    CSTAGE(false, 16);
    B0 = B1; B1 = rp + 3 * 32768;
    CSTAGE(false, 64);
    B0 = B1;
    CSTAGE(true, 64);
    oB = MFMA32(v1, BF(p1), oB);
    STAMP(1, 10);
    {
      const f32x16 snd = kp ? oA : oB;
#pragma unroll
      for (int r4 = 0; r4 < 4; ++r4)
        *(f32x4*)(lds + K2_X + ((w * 4 + r4) * 64 + lane) * 16) = (f32x4){snd[4 * r4], snd[4 * r4 + 1], snd[4 * r4 + 2], snd[4 * r4 + 3]};
    }
    f32x16 o0 = kp ? oB : oA;
    bf16x8 qk0 = kp ? qB0 : qA0, qk1 = kp ? qB1 : qA1;
    asm volatile("" : "+v"(o0), "+v"(qk0), "+v"(qk1));
    SBAR();
    LBAR();
#pragma unroll
    for (int r4 = 0; r4 < 4; ++r4) {
      const f32x4 t = *(const f32x4*)(lds + K2_X + (((w ^ 2) * 4 + r4) * 64 + lane) * 16);
      o0[4 * r4] += t[0]; o0[4 * r4 + 1] += t[1]; o0[4 * r4 + 2] += t[2]; o0[4 * r4 + 3] += t[3];
    }
    const int qb = 2 * qh + kp;
    {
      float chk = 0.f;
#pragma unroll
      for (int r = 0; r < 16; ++r) chk = __builtin_fmaf(o0[r], 0.f, chk);
      const bool bad = !(chk == 0.f) || !(o0[12] < 1.8e19f);
      if (__any(bad)) {
        attn_safe(Kf, Vf, qk0, qk1, o0, lane, hi);
      }
    }
    {
      auto sw = __builtin_amdgcn_permlane32_swap(__float_as_uint(o0[12]), __float_as_uint(o0[12]), false, false);
      const float il = 1.f / __uint_as_float(sw[1]);
      const int c = lane & 31;
#pragma unroll
      for (int r4 = 0; r4 < 4; ++r4) {
        const int d = 8 * r4 + 4 * hi, k = 28 * (4 * qb + (c >> 3)) + d;
        if (d < 28)
          *(u32x2*)(lds + K2_OT + ((k >> 3) * 8 + (c & 7)) * 16 + (k & 7) * 2) =
              (u32x2){cvtpk(o0[4 * r4] * il, o0[4 * r4 + 1] * il), cvtpk(o0[4 * r4 + 2] * il, o0[4 * r4 + 3] * il)};
      }
    }
    STAMP(1, 1);
    LBAR();
    STAMP(1, 2);
    LDWO(16, 39);
    xv = *(const u32x4*)(x + (size_t)(tg * 8 + w) * D + (lane < 56 ? lane : 55) * 8);
    SBAR();
    {
      bf16x8 bo[7];
#pragma unroll
      for (int k = 0; k < 7; ++k) bo[k] = BOF(k);
#pragma unroll
      for (int i = 0; i < 16; ++i) wf[i / 7][i % 7] = *(const bf16x8*)(lds + K2_RING + (i >> 3) * 32768 + w * 8192 + (i & 7) * 1024 + lane * 16);
      f32x4 acc[7];
#pragma unroll
      for (int n = 0; n < 7; ++n) acc[n] = (f32x4){0.f, 0.f, 0.f, 0.f};
#pragma unroll
      for (int i = 0; i < 39; ++i) acc[i / 7] = MFMA16(wf[i / 7][i % 7], bo[i % 7], acc[i / 7]);
      HBST(acc);
    }
  } else {
    const int hw = w - 4;
    __builtin_amdgcn_s_setprio(3);
    const unsigned ring = (unsigned)(uintptr_t)lds + K2_RING + hw * 8192;
    const u16* kp = Kf + (size_t)hw * 2048;
    const u16* vp = Vf + (size_t)hw * 2048;
    const unsigned l16 = lane * 16;
#define DMAS(s_) do { const unsigned sl_ = ring + ((s_) & 3) * 32768; \
    _Pragma("unroll") for (int u_ = 0; u_ < 2; ++u_) { \
      glds16s(kp + (s_) * 8192 + u_ * 1024, l16, sl_ + u_ * 4096); glds16s(kp + (s_) * 8192 + u_ * 1024 + 512, l16, sl_ + u_ * 4096 + 1024); \
      glds16s(vp + (s_) * 8192 + u_ * 1024, l16, sl_ + u_ * 4096 + 2048); glds16s(vp + (s_) * 8192 + u_ * 1024 + 512, l16, sl_ + u_ * 4096 + 3072); } } while (0)
    const unsigned dum = (unsigned)(uintptr_t)lds + K2_DUM + hw * 1024;
    const int xg = tg & 7, rk = tg >> 3;
    const int ts = xg < 4 ? 25 * xg : 100 + 24 * (xg - 4), te = ts + (xg < 4 ? 25 : 24);
    const int n0 = ts / 7, n1 = (te - 1) / 7;
    const int nb0 = n0 + 3 <= 27 ? n0 : 24;
    const u16* p1 = w1 + (size_t)(64 * rk + 16 * hw) * F + nb0 * 64;
    const u16* p1b = w1 + (size_t)(64 * rk + 16 * hw) * F + n1 * 64;
    const u16* p2 = w2 + (size_t)(56 * rk + 14 * hw) * S + 4 * xg * 64;
    const unsigned o1 = ((lane >> 5) * F + (lane & 31) * 8) * 2, o1b = ((lane >> 3) * F + (lane & 7) * 8) * 2, o2 = ((lane >> 5) * S + (lane & 31) * 8) * 2;
#define PFD(i0, i1) do { _Pragma("unroll") for (int i_ = i0; i_ < i1; ++i_) { \
      if (i_ < 8) glds16s(p1 + (size_t)i_ * 2 * F, o1, dum); \
      else if (i_ < 10) glds16s(p1b + (size_t)(i_ - 8) * 8 * F, o1b, dum); \
      else glds16s(p2 + (size_t)(i_ - 10) * 2 * S, o2, dum); } } while (0)
    DMAS(0); DMAS(1); DMAS(2);
    waitv<16>(); RBAR();
    LDWO(0, 12); SBAR(); PFD(0, 3);
    waitv<23>(); RBAR();
    DMAS(3); LDWO(12, 24); SBAR(); PFD(3, 6);
    waitv<38>(); RBAR();
    DMAS(4); LDWO(24, 36); SBAR(); PFD(6, 9);
    waitv<38>(); RBAR();
    DMAS(5); LDWO(36, 49); SBAR(); PFD(9, 12);
    waitv<39>(); RBAR();
    bf16x8 wx[10];
    DMAS(6);
#pragma unroll
    for (int i = 0; i < 10; ++i) wx[i] = *(const bf16x8*)(wob - 7 * 1024 + (((39 + i) / 7) * 14 + (39 + i) % 7) * 1024 + wl);
    SBAR(); PFD(12, 15);
    waitv<37>(); RBAR();
    DMAS(7); PFD(15, 17);
    waitv<23>(); RBAR();
#pragma unroll
    for (int i = 0; i < 8; ++i) glds16s(wob - 7 * 1024 + ((i / 7) * 14 + i % 7) * 1024, wl, ring + i * 1024);
    waitv<10>(); RBAR();
#pragma unroll
    for (int i = 8; i < 16; ++i) glds16s(wob - 7 * 1024 + ((i / 7) * 14 + i % 7) * 1024, wl, ring + 32768 + (i - 8) * 1024);
    xv = *(const u32x4*)(x + (size_t)(tg * 8 + w) * D + (lane < 56 ? lane : 55) * 8);
    SBAR();
    __builtin_amdgcn_s_setprio(0);
    RBAR();
    waitv<1>();
    STAMP(1, 1);
    LBAR();
    STAMP(1, 2);
    {
      f32x4 acc[7];
      {
        bf16x8 bo[7];
#pragma unroll
        for (int k = 0; k < 7; ++k) bo[k] = BOF(k);
        acc[5] = (f32x4){0.f, 0.f, 0.f, 0.f}; acc[6] = acc[5];
#pragma unroll
        for (int i = 0; i < 10; ++i) acc[(39 + i) / 7] = MFMA16(wx[i], bo[(39 + i) % 7], acc[(39 + i) / 7]);
      }
      SBAR();
      bf16x8 bo[7];
#pragma unroll
      for (int k = 0; k < 7; ++k) bo[k] = BOF(7 + k);
#pragma unroll
      for (int n = 0; n < 7; ++n) {
        if (n < 5) acc[n] = (f32x4){0.f, 0.f, 0.f, 0.f};
#pragma unroll
        for (int k = 0; k < 7; ++k) acc[n] = MFMA16(wf[n][k], bo[k], acc[n]);
      }
      HBST(acc);
    }
  }
  LBAR();
  STAMP(1, 3);
  {
    const int s = tg * 8 + w;
    const bool act = lane < 56;
    const int lc = act ? lane : 55;
    float v[8];
    const f32x4 a0 = *(const f32x4*)(lds + K2_HB + (w * HBS + lc * 8) * 4), a1 = *(const f32x4*)(lds + K2_HB + (w * HBS + lc * 8 + 4) * 4);
    const f32x4 c0 = *(const f32x4*)(lds + K2_HB + ((8 + w) * HBS + lc * 8) * 4), c1 = *(const f32x4*)(lds + K2_HB + ((8 + w) * HBS + lc * 8 + 4) * 4);
    u32x4 hv;
#pragma unroll
    for (int i = 0; i < 2; ++i) {
      hv[i] = cvtpk(bflo(xv[i]) + a0[2 * i] + c0[2 * i], bfhi(xv[i]) + a0[2 * i + 1] + c0[2 * i + 1]);
      hv[2 + i] = cvtpk(bflo(xv[2 + i]) + a1[2 * i] + c1[2 * i], bfhi(xv[2 + i]) + a1[2 * i + 1] + c1[2 * i + 1]);
    }
    if (act) st16c(H + (size_t)s * D + lane * 8, hv);
#pragma unroll
    for (int i = 0; i < 4; ++i) { v[2 * i] = act ? bflo(hv[i]) : 0.f; v[2 * i + 1] = act ? bfhi(hv[i]) : 0.f; }
#pragma unroll
    for (int pass = 0; pass < 2; ++pass) {
      float sm = 0.f;
#pragma unroll
      for (int i = 0; i < 8; ++i) sm += v[i];
      const float mean = wave_sum(sm) * (1.f / D);
      float q = 0.f;
#pragma unroll
      for (int i = 0; i < 8; ++i) { v[i] = act ? v[i] - mean : 0.f; q += v[i] * v[i]; }
      const float rs = __builtin_amdgcn_rsqf(wave_sum(q) * (1.f / D) + LN_EPS);
#pragma unroll
      for (int i = 0; i < 8; ++i) v[i] *= rs;
    }
    if (act) *(u32x4*)(lds + K2_HS + w * 912 + lane * 16) = (u32x4){cvtpk(v[0], v[1]), cvtpk(v[2], v[3]), cvtpk(v[4], v[5]), cvtpk(v[6], v[7])};
  }
  LBAR();
  if (tid < D / 2) {
    unsigned r[8];
#pragma unroll
    for (int i = 0; i < 8; ++i) r[i] = *(const unsigned*)(lds + K2_HS + i * 912 + tid * 4);
    u32x4 lo, hi2;
#pragma unroll
    for (int i = 0; i < 4; ++i) {
      lo[i] = (r[2 * i] & 0xffffu) | (r[2 * i + 1] << 16);
      hi2[i] = (r[2 * i] >> 16) | (r[2 * i + 1] & 0xffff0000u);
    }
    u32x4* dst = (u32x4*)(HN + ((size_t)tg * D + 2 * tid) * 8);
    st16c(dst, lo); st16c(dst + 1, hi2);
  }
  STAMP(1, 4);
}

constexpr int G_RING = 4, G_LDS = 8 * G_RING * 4096;
typedef __attribute__((address_space(1))) unsigned gu32;
typedef __attribute__((address_space(1))) unsigned long long gu64;
#define RLX_AGENT __ATOMIC_RELAXED, __HIP_MEMORY_SCOPE_AGENT
template <int OFF> DI void gload16c(bf16x8& d, const void* p) {
  asm volatile("global_load_dwordx4 %0, %1, off offset:%2 sc1" : "=v"(d) : "v"(p), "i"(OFF) : "memory");
}
template <int PH, int NSH>
DI void mlp_tile(char* lds, int t, const u16* act, const u16* __restrict__ W, const u16* __restrict__ bias, const u16* H, u16* out,
                 unsigned* cnt, unsigned want) {
  constexpr int KT = PH == 0 ? S : F, LDW = PH == 0 ? F : S, NK = KT / 256, NR = NK - G_RING, LA = 5;
  const int tid = threadIdx.x, lane = tid & 63, w = __builtin_amdgcn_readfirstlane(tid >> 6);
  const int nt = t / 7, mt = t - nt * 7;
  const unsigned lds0 = (unsigned)(uintptr_t)lds + w * (G_RING * 4096);
  const int g = lane >> 4, q4 = (lane & 15) >> 2, p = lane & 3, l15 = lane & 15;
  STAMP(2 + PH, 0);
  const int enb = w >> 1, emb = 2 * (w & 1);
  u32x2 eb;
  if (PH == 0) eb = *(const u32x2*)(bias + 64 * nt + 16 * enb + 4 * g);
  else eb[0] = bias[64 * nt + 16 * enb + l15];
  SBAR();
  const u16* asrc = act + ((size_t)(4 * w + g) * D + 64 * mt + l15) * 8;
  const int r8 = lane >> 3, fe = (r8 >> 1) & 1;
  const u16* wsrc0 = W + (size_t)(32 * w + r8) * LDW + 64 * nt + ((lane & 7) ^ (fe << 1)) * 8;
  const u16* wsrc1 = W + (size_t)(32 * w + 8 + r8) * LDW + 64 * nt + ((lane & 7) ^ ((fe | 2) << 1)) * 8;
  const int f = ((q4 >> 1) & 1) | ((g & 1) << 1);
  int woff[4];
#pragma unroll
  for (int nb = 0; nb < 4; ++nb) woff[nb] = w * (G_RING * 4096) + (8 * g + q4) * 128 + (((2 * nb + (p >> 1)) ^ (f << 1)) * 16) + 8 * (p & 1);
  bf16x8 af[NK][4];
  f32x4 acc[4][4] = {};
#define STAGE_W(i) do { const unsigned sl_ = lds0 + ((i) % G_RING) * 4096; const size_t o_ = (size_t)(i) * 256 * LDW; \
    glds16(wsrc0 + o_, sl_); glds16(wsrc1 + o_, sl_ + 1024); glds16(wsrc0 + o_ + 16 * LDW, sl_ + 2048); glds16(wsrc1 + o_ + 16 * LDW, sl_ + 3072); } while (0)
#define LOAD_A(i) do { const u16* ap_ = asrc + (size_t)(i) * 32 * D * 8; \
    gload16c<0>(af[i][0], ap_); gload16c<256>(af[i][1], ap_); gload16c<512>(af[i][2], ap_); gload16c<768>(af[i][3], ap_); } while (0)
#define RDW(WF, i) do { _Pragma("unroll") for (int nb_ = 0; nb_ < 4; ++nb_) { const lds_cptr sp_ = (lds_cptr)lds + woff[nb_] + ((i) % G_RING) * 4096; \
      WF[nb_] = cat8(trread(sp_), trread(sp_ + 512)); } } while (0)
#define MMA(WF, i) do { _Pragma("unroll") for (int nb_ = 0; nb_ < 4; ++nb_) _Pragma("unroll") for (int mb_ = 0; mb_ < 4; ++mb_) \
      acc[nb_][mb_] = PH == 0 ? MFMA16(WF[nb_], af[i][mb_], acc[nb_][mb_]) : MFMA16(af[i][mb_], WF[nb_], acc[nb_][mb_]); } while (0)
  bf16x8 wr[NR][4];
#pragma unroll
  for (int j = 0; j < NR; ++j) STAGE_W(G_RING + j);
  if (NR < G_RING) STAGE_W(G_RING - 1);
#pragma unroll
  for (int j = 0; j < NR; ++j) {
    waitv<12>();
    RDW(wr[j], G_RING + j);
    asm volatile("s_waitcnt lgkmcnt(0)" ::: "memory");
    STAGE_W(j);
  }
  STAMP(2 + PH, 1);
  if (w == 0) {
    gu32* cp = (gu32*)(cnt + (lane < NSH ? lane : 0) * 64);
    for (unsigned spins = 0; spins < (1u << 20); ++spins) {
      if (__all(__hip_atomic_load(cp, RLX_AGENT) >= want)) break;
      __builtin_amdgcn_s_sleep(1);
    }
  }
  asm volatile("s_barrier" ::: "memory");
  STAMP(2 + PH, 2);
  unsigned long long eh0 = 0, eh1 = 0;
  if (PH == 1) {
    const int s = 64 * nt + 16 * enb + l15;
    eh0 = __hip_atomic_load((gu64*)(H + (size_t)s * D + 64 * mt + 16 * emb + 4 * g), RLX_AGENT);
    eh1 = __hip_atomic_load((gu64*)(H + (size_t)s * D + 64 * mt + 16 * emb + 16 + 4 * g), RLX_AGENT);
  }
  SBAR();
#pragma unroll
  for (int j = 0; j < LA; ++j) LOAD_A((j + G_RING) % NK);
#pragma unroll
  for (int j = 0; j < NK; ++j) {
    const int i = (j + G_RING) % NK;
    const int rem = NK - 1 - j < LA - 1 ? NK - 1 - j : LA - 1;
    if (rem == 4) waitv4<16>(af[i][0], af[i][1], af[i][2], af[i][3]);
    else if (rem == 3) waitv4<12>(af[i][0], af[i][1], af[i][2], af[i][3]);
    else if (rem == 2) waitv4<8>(af[i][0], af[i][1], af[i][2], af[i][3]);
    else if (rem == 1) waitv4<4>(af[i][0], af[i][1], af[i][2], af[i][3]);
    else waitv4<0>(af[i][0], af[i][1], af[i][2], af[i][3]);
    if (j < NR) {
      MMA(wr[j], i);
    } else {
      bf16x8 wf[4];
      RDW(wf, i);
      MMA(wf, i);
    }
    SBAR();
    if (j + LA < NK) LOAD_A((j + LA + G_RING) % NK);
  }
#undef STAGE_W
#undef LOAD_A
#undef RDW
#undef MMA
  STAMP(2 + PH, 4);
  {
    f32x4* xb = (f32x4*)lds;
#pragma unroll
    for (int nb = 0; nb < 4; ++nb)
#pragma unroll
      for (int mb = 0; mb < 4; ++mb) xb[(w * 16 + nb * 4 + mb) * 64 + lane] = acc[nb][mb];
  }
  LBAR();
  {
    const f32x4* xb = (const f32x4*)lds;
#pragma unroll
    for (int e = 0; e < 2; ++e) {
      f32x4 a = xb[(2 * w + e) * 64 + lane];
#pragma unroll
      for (int v = 1; v < 8; ++v) a += xb[(v * 16 + 2 * w + e) * 64 + lane];
      const int mb = emb + e;
      if (PH == 0) {
        const int fi = 64 * nt + 16 * enb + 4 * g, d = 64 * mt + 16 * mb + l15;
        float z[4] = {a[0] + bflo(eb[0]), a[1] + bfhi(eb[0]), a[2] + bflo(eb[1]), a[3] + bfhi(eb[1])};
#pragma unroll
        for (int i = 0; i < 4; ++i) {
          const float zz = z[i], a2 = zz * (1.f + 0.044715f * zz * zz) * (-2.f * 0.7978845608028654f * 1.4426950408889634f);
          z[i] = zz * __builtin_amdgcn_rcpf(1.f + __builtin_amdgcn_exp2f(a2));
        }
        const unsigned long long v = (unsigned long long)cvtpk(z[0], z[1]) | ((unsigned long long)cvtpk(z[2], z[3]) << 32);
        __hip_atomic_store((gu64*)(out + ((size_t)(fi >> 3) * D + d) * 8 + (fi & 7)), v, RLX_AGENT);
      } else {
        const int d = 64 * mt + 16 * mb + 4 * g, s = 64 * nt + 16 * enb + l15;
        const float bb = bflo(eb[0]);
        const unsigned long long hv = e ? eh1 : eh0;
        const unsigned h0 = (unsigned)hv, h1 = (unsigned)(hv >> 32);
        *(u32x2*)(out + (size_t)s * D + d) =
            (u32x2){cvtpk(a[0] + bb + bflo(h0), a[1] + bb + bfhi(h0)), cvtpk(a[2] + bb + bflo(h1), a[3] + bb + bfhi(h1))};
      }
    }
  }
  STAMP(2 + PH, 5);
}
#define PUBLISH(c) do { asm volatile("s_waitcnt vmcnt(0) lgkmcnt(0)\n\ts_barrier" ::: "memory"); \
    if (threadIdx.x == 0) __hip_atomic_fetch_add((gu32*)(c), 1u, RLX_AGENT); } while (0)

__global__ __launch_bounds__(512) void k_main(const u16* __restrict__ x, const u16* __restrict__ Qf, const u16* __restrict__ Kf,
                                              const u16* __restrict__ Vf, const u16* __restrict__ Wof, const u16* __restrict__ w1,
                                              const u16* __restrict__ b1, const u16* __restrict__ w2, const u16* __restrict__ b2,
                                              u16* H, u16* HN, u16* U, u16* out, unsigned* cnt) {
  __shared__ __attribute__((aligned(16))) char lds[K2_LDS];
  attn_phase(lds, x, Qf, Kf, Vf, Wof, w1, w2, H, HN);
  PUBLISH(cnt + (8 + (blockIdx.x & 7)) * 64);
  if (blockIdx.x < 196) {
    const int t = xcd_remap(blockIdx.x, 196);
    mlp_tile<0, 8>(lds, t, HN, w1, b1, H, U, cnt + 8 * 64, 32u);
    PUBLISH(cnt + (t % 7) * 64);
  }
  if (blockIdx.x < 224) {
    const int t2 = xcd_remap(blockIdx.x, 224);
    mlp_tile<1, 1>(lds, t2, U, w2, b2, H, out, cnt + (t2 % 7) * 64, 28u);
  }
}

extern "C" void kernel_launch(void* const* d_in, const int* in_sizes, int n_in, void* d_out, int out_size, void* d_ws, size_t ws_size,
                              hipStream_t stream) {
  const u16* x = (const u16*)d_in[0];
  const u16* wq = (const u16*)d_in[1];
  const u16* wk = (const u16*)d_in[2];
  const u16* wv = (const u16*)d_in[3];
  const u16* wo = (const u16*)d_in[4];
  const u16* w1 = (const u16*)d_in[6];
  const u16* b1 = (const u16*)d_in[7];
  const u16* w2 = (const u16*)d_in[8];
  const u16* b2 = (const u16*)d_in[9];
  char* ws = (char*)d_ws;
  u16 *Qf = (u16*)(ws + WS_QF), *Kf = (u16*)(ws + WS_KF), *Vf = (u16*)(ws + WS_VF), *Wof = (u16*)(ws + WS_WOF);
  u16 *H = (u16*)(ws + WS_H), *HN = (u16*)(ws + WS_HN), *U = (u16*)(ws + WS_U);
  unsigned* cnt = (unsigned*)(ws + WS_CNT);
  k_qkv<<<256, 512, 0, stream>>>(x, wq, wk, wv, wo, Qf, Kf, Vf, Wof, cnt);
  k_main<<<256, 512, 0, stream>>>(x, Qf, Kf, Vf, Wof, w1, b1, w2, b2, H, HN, U, (u16*)d_out, cnt);
}
```

```cpp
#include <hip/hip_runtime.h>
#include <hip/hip_bf16.h>

typedef unsigned short u16;
using bf16x8 = __attribute__((ext_vector_type(8))) short;
using s16x4 = __attribute__((ext_vector_type(4))) short;
using f32x4 = __attribute__((ext_vector_type(4))) float;
using f32x16 = __attribute__((ext_vector_type(16))) float;
using u32x4 = __attribute__((ext_vector_type(4))) unsigned;
using u32x2 = __attribute__((ext_vector_type(2))) unsigned;
typedef short v4i16_t __attribute__((ext_vector_type(4)));
typedef __attribute__((address_space(3))) const char* lds_cptr;
typedef __bf16 bf16x2_t __attribute__((ext_vector_type(2)));
typedef float f32x2_t __attribute__((ext_vector_type(2)));

#define DI __device__ __forceinline__
#define MFMA32(a, b, c) __builtin_amdgcn_mfma_f32_32x32x16_bf16(a, b, c, 0, 0, 0)
#define MFMA16(a, b, c) __builtin_amdgcn_mfma_f32_16x16x32_bf16(a, b, c, 0, 0, 0)
#define SBAR() __builtin_amdgcn_sched_barrier(0)
#define LBAR() asm volatile("s_waitcnt lgkmcnt(0)\n\ts_barrier" ::: "memory")
#define WAITV_BAR(N) asm volatile("s_waitcnt vmcnt(" #N ") lgkmcnt(0)\n\ts_barrier" ::: "memory")

constexpr int S = 2048, D = 448, DH = 28, F = 1792;
constexpr float QSCALE = 0.18898223650461362f * 1.4426950408889634f;
constexpr float LN_EPS = 1e-5f;

constexpr size_t WS_QF = 0;
constexpr size_t WS_KF = WS_QF + 2097152;
constexpr size_t WS_VF = WS_KF + 131072;
constexpr size_t WS_WOF = WS_VF + 131072;
constexpr size_t WS_H = WS_WOF + 401408;
constexpr size_t WS_HN = WS_H + 1835008;
constexpr size_t WS_U = WS_HN + 1835008;
constexpr size_t WS_CNT = WS_U + 1605632;

#define STAMP(k, i)
#define CSTAMP(k, i)

DI unsigned cvtpk(float lo, float hi) {
  const f32x2_t v = {lo, hi};
  return __builtin_bit_cast(unsigned, __builtin_convertvector(v, bf16x2_t));
}
DI float bflo(unsigned u) { return __uint_as_float(u << 16); }
DI float bfhi(unsigned u) { return __uint_as_float(u & 0xffff0000u); }
template <int CTRL> DI float dppf(float v) {
  return __uint_as_float(__builtin_amdgcn_update_dpp(0u, __float_as_uint(v), CTRL, 0xf, 0xf, true));
}
DI float wave_sum(float v) {
  v += dppf<0xB1>(v);
  v += dppf<0x4E>(v);
  v += dppf<0x141>(v);
  v += dppf<0x140>(v);
  auto a = __builtin_amdgcn_permlane16_swap(__float_as_uint(v), __float_as_uint(v), false, false);
  v = __uint_as_float(a[0]) + __uint_as_float(a[1]);
  auto b = __builtin_amdgcn_permlane32_swap(__float_as_uint(v), __float_as_uint(v), false, false);
  return __uint_as_float(b[0]) + __uint_as_float(b[1]);
}
DI float half_max(float v) {
  auto b = __builtin_amdgcn_permlane32_swap(__float_as_uint(v), __float_as_uint(v), false, false);
  return __builtin_fmaxf(__uint_as_float(b[0]), __uint_as_float(b[1]));
}
DI void glds16(const void* g, unsigned lds_base) {
  unsigned sv;
  asm volatile("s_mov_b32 %0, m0\n\ts_mov_b32 m0, %2\n\ts_nop 0\n\tglobal_load_lds_dwordx4 %1, off\n\ts_mov_b32 m0, %0"
               : "=&s"(sv) : "v"(g), "s"(lds_base) : "memory");
}
DI void glds16s(const void* sbase, unsigned voff, unsigned lds_base) {
  unsigned sv;
  asm volatile("s_mov_b32 %0, m0\n\ts_mov_b32 m0, %3\n\ts_nop 0\n\tglobal_load_lds_dwordx4 %1, %2\n\ts_mov_b32 m0, %0"
               : "=&s"(sv) : "v"(voff), "s"(sbase), "s"(lds_base) : "memory");
}
DI void glds4(const void* g, unsigned lds_base) {
  unsigned sv;
  asm volatile("s_mov_b32 %0, m0\n\ts_mov_b32 m0, %2\n\ts_nop 0\n\tglobal_load_lds_dword %1, off\n\ts_mov_b32 m0, %0"
               : "=&s"(sv) : "v"(g), "s"(lds_base) : "memory");
}
template <int N> DI void waitv4(bf16x8& a, bf16x8& b, bf16x8& c, bf16x8& d) {
  asm volatile("s_waitcnt vmcnt(%4)" : "+v"(a), "+v"(b), "+v"(c), "+v"(d) : "i"(N) : "memory");
}
template <int OFF> DI void gload16(bf16x8& d, const void* p) {
  asm volatile("global_load_dwordx4 %0, %1, off offset:%2" : "=v"(d) : "v"(p), "i"(OFF) : "memory");
}
DI s16x4 trread(lds_cptr p) {
  return __builtin_bit_cast(s16x4, __builtin_amdgcn_ds_read_tr16_b64_v4i16((__attribute__((address_space(3))) v4i16_t*)p));
}
DI bf16x8 ldsr16(lds_cptr p) { return *(const __attribute__((address_space(3))) bf16x8*)p; }
DI bf16x8 cat8(s16x4 a, s16x4 b) { return __builtin_shufflevector(a, b, 0, 1, 2, 3, 4, 5, 6, 7); }
DI int xcd_remap(int bid, int nwg) {
  const int q = nwg >> 3, r = nwg & 7, x = bid & 7, o = bid >> 3;
  return (x < r ? x * (q + 1) : r * (q + 1) + (x - r) * q) + o;
}

constexpr int K1_WI = 448 * 128, K1_X = 2 * K1_WI, K1_XS = 528, K1_DUM = K1_X + 64 * K1_XS, K1_LDS = K1_DUM + 1024;
constexpr int KMAIN_KB = 28;
__global__ __launch_bounds__(512) void k_qkv(const u16* __restrict__ x, const u16* __restrict__ wq, const u16* __restrict__ wk,
                                             const u16* __restrict__ wv, const u16* __restrict__ wo, u16* __restrict__ Qf,
                                             u16* __restrict__ Kf, u16* __restrict__ Vf, u16* __restrict__ Wof, unsigned* __restrict__ cnt) {
  __shared__ __attribute__((aligned(16))) char lds[K1_LDS];
  const int tid = threadIdx.x, lane = tid & 63, w = __builtin_amdgcn_readfirstlane(tid >> 6);
  const int t = xcd_remap(blockIdx.x, 256), rt = t >> 2, ct = t & 3;
  const unsigned lds0 = (unsigned)(uintptr_t)lds;
  STAMP(0, 0);
  bf16x8 xr[4];
  const bool act = lane < 56;
  {
    const u16* xp_ = x + (size_t)(rt * 32 + w * 4) * D + (act ? lane : 55) * 8;
    gload16<0>(xr[0], xp_); gload16<896>(xr[1], xp_); gload16<1792>(xr[2], xp_); gload16<2688>(xr[3], xp_);
  }
#pragma unroll
  for (int im = 0; im < 2; ++im) {
    const int c64 = 2 * ct + im;
    if (c64 < 7) {
#pragma unroll
      for (int i = 0; i < 7; ++i) {
        const int piece = w * 7 + i, row = piece * 8 + (lane >> 3);
        const int f = ((row >> 1) & 1) | (((row >> 3) & 1) << 1);
        const int c = (lane & 7) ^ (f << 1);
        glds16(wq + (size_t)row * D + c64 * 64 + c * 8, lds0 + im * K1_WI + piece * 1024);
      }
    } else {
#pragma unroll
      for (int i = 0; i < 7; ++i) {
        const int pc = w * 7 + i < 50 ? w * 7 + i : 49, mat = pc >= 25, piece = pc - 25 * mat;
        const int e = piece * 512 + lane * 8;
        glds16((mat ? wv : wk) + (e < 448 * 28 ? e : 448 * 28 - 8), lds0 + im * K1_WI + mat * 25600 + piece * 1024);
      }
    }
  }
  u16 wt[8];
  const int frag0 = blockIdx.x + 256 * (w & 1);
  const bool dofrag = w < 2 && frag0 < 392;
  const int frag = dofrag ? frag0 : 0;
  {
    const int nb = frag / 14, ks = frag - nb * 14;
#pragma unroll
    for (int j = 0; j < 8; ++j) wt[j] = wo[(size_t)(32 * ks + 8 * (lane >> 4) + j) * D + 16 * nb + (lane & 15)];
  }
  SBAR();
  waitv4<22>(xr[0], xr[1], xr[2], xr[3]);
  {
    u32x4 xv[4];
#pragma unroll
    for (int r = 0; r < 4; ++r) xv[r] = __builtin_bit_cast(u32x4, xr[r]);
    float v[4][8], mean[4], rs[4];
#pragma unroll
    for (int r = 0; r < 4; ++r) {
      float s = 0.f;
#pragma unroll
      for (int i = 0; i < 4; ++i) {
        v[r][2 * i] = act ? bflo(xv[r][i]) : 0.f; v[r][2 * i + 1] = act ? bfhi(xv[r][i]) : 0.f;
        s += v[r][2 * i] + v[r][2 * i + 1];
      }
      mean[r] = s;
    }
#pragma unroll
    for (int r = 0; r < 4; ++r) mean[r] = wave_sum(mean[r]) * (1.f / D);
#pragma unroll
    for (int r = 0; r < 4; ++r) {
      float q = 0.f;
#pragma unroll
      for (int i = 0; i < 8; ++i) { v[r][i] = act ? v[r][i] - mean[r] : 0.f; q += v[r][i] * v[r][i]; }
      rs[r] = q;
    }
#pragma unroll
    for (int r = 0; r < 4; ++r) rs[r] = __builtin_amdgcn_rsqf(wave_sum(rs[r]) * (1.f / D) + LN_EPS);
    char* xdst = lds + K1_X + lane * K1_XS + w * 64;
#pragma unroll
    for (int r = 0; r < 4; ++r) {
      u32x4 o;
#pragma unroll
      for (int i = 0; i < 4; ++i) o[i] = cvtpk(v[r][2 * i] * rs[r], v[r][2 * i + 1] * rs[r]);
      *(u32x4*)(xdst + r * 16) = o;
    }
  }
  STAMP(0, 1);
  WAITV_BAR(0);
  STAMP(0, 2);
  if (w == 7 && (blockIdx.x >> 3) < KMAIN_KB) {
    unsigned long long pc;
    int off;
    asm volatile("s_getpc_b64 %0\n1:\n\ts_mov_b32 %1, k_main-1b" : "=s"(pc), "=s"(off));
    glds16s((const char*)(pc + (long long)off) + (blockIdx.x >> 3) * 1024, lane * 16, lds0 + K1_DUM);
  }
  const int nb = w & 3, im = w >> 2, c64 = 2 * ct + im, g = lane >> 4, q4 = (lane & 15) >> 2, p = lane & 3, l15 = lane & 15;
  const int f = ((q4 >> 1) & 1) | ((g & 1) << 1);
  const bool kv = c64 == 7;
  const int rs_ = kv ? 56 : 128;
  const lds_cptr wp = (lds_cptr)lds + im * K1_WI + (8 * g + q4) * rs_ +
                      (kv ? (nb >> 1) * 25600 + (nb & 1) * 32 + 8 * p : (((2 * nb + (p >> 1)) ^ (f << 1)) * 16) + 8 * (p & 1));
  const lds_cptr xp = (lds_cptr)lds + K1_X + g * K1_XS + l15 * 16;
  f32x4 acc0 = {0.f, 0.f, 0.f, 0.f}, acc1 = {0.f, 0.f, 0.f, 0.f};
  {
    bf16x8 a[14], b0[14], b1[14];
#pragma unroll
    for (int ks = 0; ks < 14; ++ks) {
      a[ks] = cat8(trread(wp + ks * 32 * rs_), trread(wp + ks * 32 * rs_ + 4 * rs_));
      b0[ks] = ldsr16(xp + ks * 4 * K1_XS);
      b1[ks] = ldsr16(xp + ks * 4 * K1_XS + 256);
    }
    SBAR();
#pragma unroll
    for (int ks = 0; ks < 14; ++ks) {
      acc0 = MFMA16(a[ks], b0[ks], acc0);
      acc1 = MFMA16(a[ks], b1[ks], acc1);
    }
  }
  STAMP(0, 3);
#pragma unroll
  for (int tb = 0; tb < 2; ++tb) {
    const f32x4 a = tb ? acc1 : acc0;
    const int s = rt * 32 + 16 * tb + l15;
    const int nl = 16 * nb + 4 * g;
    if (c64 < 7) {
      const int n0 = 64 * c64 + nl, h = n0 / 28, d0 = n0 - 28 * h;
      const int tg = s >> 3, c = (h & 3) * 8 + (s & 7), b = h >> 2;
      const size_t off = ((((size_t)(tg * 4 + b) * 2 + (d0 >> 4)) * 64) + ((d0 >> 3) & 1) * 32 + c) * 8 + (d0 & 7);
      *(u32x2*)(Qf + off) = (u32x2){cvtpk(a[0] * QSCALE, a[1] * QSCALE), cvtpk(a[2] * QSCALE, a[3] * QSCALE)};
    } else {
      const int kt = s >> 5, kk = s & 31;
      const int d0 = 16 * (nb & 1) + 4 * g;
      if (nb < 2) {
        const size_t off = ((((size_t)kt * 2 + (d0 >> 4)) * 64) + ((d0 >> 3) & 1) * 32 + kk) * 8 + (d0 & 7);
        u32x2 v = {cvtpk(a[0], a[1]), cvtpk(a[2], a[3])};
        if (d0 == 28) v = (u32x2){0x3f803f80u, 0x00003f80u};
        *(u32x2*)(Kf + off) = v;
      } else {
        const int sp = kk >> 4, j = ((kk >> 3) & 1) * 4 + (kk & 3), hi = (kk >> 2) & 1;
        const size_t off = ((((size_t)kt * 2 + sp) * 64) + hi * 32 + d0) * 8 + j;
        unsigned v0 = cvtpk(a[0], a[1]), v1 = cvtpk(a[2], a[3]);
        if (d0 == 28) { v0 = 0x00003f80u; v1 = 0u; }
        Vf[off] = (u16)v0; Vf[off + 8] = (u16)(v0 >> 16); Vf[off + 16] = (u16)v1; Vf[off + 24] = (u16)(v1 >> 16);
      }
    }
  }
  if (dofrag) {
    u32x4 o;
#pragma unroll
    for (int i = 0; i < 4; ++i) o[i] = (unsigned)wt[2 * i] | ((unsigned)wt[2 * i + 1] << 16);
    *(u32x4*)(Wof + ((size_t)frag * 64 + lane) * 8) = o;
  }
  if (blockIdx.x == 0) { cnt[tid] = 0u; cnt[512 + tid] = 0u; }
  if (w == 7) asm volatile("s_waitcnt vmcnt(0)" ::: "memory");
  STAMP(0, 4);
}

constexpr int K2_RING = 0, K2_OT = 131072, K2_DUM = K2_OT + 7168, K2_X = K2_DUM + 4096, K2_LDS = K2_X + 16384;
constexpr int K2_HB = 65536, HBS = 452, K2_HS = K2_X;

DI float set_ref(bf16x8& q1, float m, int hi) {
  const float a = bflo(cvtpk(-m, 0.f)), r1 = -m - a, b = bflo(cvtpk(r1, 0.f)), c = bflo(cvtpk(r1 - b, 0.f));
  u32x4 v = __builtin_bit_cast(u32x4, q1);
  v[2] = hi ? cvtpk(a, b) : v[2];
  v[3] = hi ? cvtpk(c, 0.f) : v[3];
  q1 = __builtin_bit_cast(bf16x8, v);
  return -((a + b) + c);
}
DI float tile_max(const f32x16& s) {
  float a = __builtin_fmaxf(__builtin_fmaxf(s[0], s[1]), s[2]), b = __builtin_fmaxf(__builtin_fmaxf(s[3], s[4]), s[5]);
  a = __builtin_fmaxf(__builtin_fmaxf(a, s[6]), s[7]); b = __builtin_fmaxf(__builtin_fmaxf(b, s[8]), s[9]);
  a = __builtin_fmaxf(__builtin_fmaxf(a, s[10]), s[11]); b = __builtin_fmaxf(__builtin_fmaxf(b, s[12]), s[13]);
  a = __builtin_fmaxf(__builtin_fmaxf(a, s[14]), s[15]);
  return half_max(__builtin_fmaxf(a, b));
}
template <int N> DI void waitv() { asm volatile("s_waitcnt vmcnt(%0)" ::"i"(N) : "memory"); }
#define RBAR() asm volatile("s_barrier" ::: "memory")

DI void attn_safe(const u16* __restrict__ Kf, const u16* __restrict__ Vf, bf16x8 q0, bf16x8 q1, f32x16& o, int lane, int hi) {
  const f32x16 zero = {};
  float mref = 0.f;
  o = zero;
#pragma unroll 1
  for (int t = 0; t < 64; ++t) {
    const bf16x8 k0 = *(const bf16x8*)(Kf + t * 1024 + lane * 8), k1 = *(const bf16x8*)(Kf + t * 1024 + 512 + lane * 8);
    const bf16x8 v0 = *(const bf16x8*)(Vf + t * 1024 + lane * 8), v1 = *(const bf16x8*)(Vf + t * 1024 + 512 + lane * 8);
    (void)set_ref(q1, 0.f, hi);
    f32x16 s = MFMA32(k0, q0, zero);
    s = MFMA32(k1, q1, s);
    const float tm = tile_max(s);
    const float mn = set_ref(q1, t == 0 ? tm : __builtin_fmaxf(tm, mref), hi);
    const float al = t == 0 ? 0.f : __builtin_amdgcn_exp2f(mref - mn);
#pragma unroll
    for (int r = 0; r < 16; ++r) o[r] *= al;
    mref = mn;
    s = MFMA32(k0, q0, zero);
    s = MFMA32(k1, q1, s);
    u32x4 p0, p1;
#pragma unroll
    for (int i = 0; i < 4; ++i) {
      p0[i] = cvtpk(__builtin_amdgcn_exp2f(s[2 * i]), __builtin_amdgcn_exp2f(s[2 * i + 1]));
      p1[i] = cvtpk(__builtin_amdgcn_exp2f(s[8 + 2 * i]), __builtin_amdgcn_exp2f(s[8 + 2 * i + 1]));
    }
    o = MFMA32(v0, __builtin_bit_cast(bf16x8, p0), o);
    o = MFMA32(v1, __builtin_bit_cast(bf16x8, p1), o);
  }
}

#define EX4(S, i) do { S[i] = __builtin_amdgcn_exp2f(S[i]); S[i + 1] = __builtin_amdgcn_exp2f(S[i + 1]); \
    S[i + 2] = __builtin_amdgcn_exp2f(S[i + 2]); S[i + 3] = __builtin_amdgcn_exp2f(S[i + 3]); } while (0)
#define PK8(P, S, i) do { P[0] = cvtpk(S[i], S[i + 1]); P[1] = cvtpk(S[i + 2], S[i + 3]); P[2] = cvtpk(S[i + 4], S[i + 5]); P[3] = cvtpk(S[i + 6], S[i + 7]); } while (0)
#define BF(P) __builtin_bit_cast(bf16x8, P)
constexpr int CPRE = 0;
#define WOPF(j) do { if ((j) < CPRE) wf[(j) / 7][(j) % 7] = WOFR(j); } while (0)
#define RDS(u, off) ldsr16(((u) < 4 ? B0 : B1) + ((u) & 3) * 8192 + (off))
#define CTILE(u, NX, PF) do { \
      \
    sB = MFMA32(k0, qB0, zero); SBAR(); if (NX) k0 = RDS((u) + 1, 0); EX4(sA, 0); WOPF(PF); SBAR(); \
    sB = MFMA32(k1, qB1, sB); SBAR(); if (NX) k1 = RDS((u) + 1, 1024); EX4(sA, 4); PK8(p0, sA, 0); SBAR(); \
    oB = MFMA32(v1, BF(p1), oB); SBAR(); v1 = RDS(u, 3072); EX4(sA, 8); WOPF((PF) + 1); SBAR(); \
    oA = MFMA32(v0, BF(p0), oA); SBAR(); EX4(sA, 12); PK8(p1, sA, 8); SBAR(); \
      \
    sA = MFMA32(k0, qA0, zero); SBAR(); EX4(sB, 0); WOPF((PF) + 2); SBAR(); \
    sA = MFMA32(k1, qA1, sA); SBAR(); EX4(sB, 4); PK8(p0, sB, 0); SBAR(); \
    oA = MFMA32(v1, BF(p1), oA); SBAR(); EX4(sB, 8); WOPF((PF) + 3); SBAR(); \
    oB = MFMA32(v0, BF(p0), oB); SBAR(); if (NX) v0 = RDS((u) + 1, 2048); EX4(sB, 12); PK8(p1, sB, 8); SBAR(); } while (0)
#define CSTAGE(LAST, PF) do { \
    CTILE(0, true, PF); CTILE(1, true, (PF) + 4); CTILE(2, true, (PF) + 8); \
    if (!(LAST)) RBAR(); \
    CTILE(3, !(LAST), (PF) + 12); } while (0)

DI void st16c(void* p, u32x4 v) {
  asm volatile("global_store_dwordx4 %0, %1, off sc1\n\ts_nop 1" ::"v"(p), "v"(v) : "memory");
}
DI void attn_phase(char* lds, const u16* __restrict__ x, const u16* __restrict__ Qf, const u16* __restrict__ Kf,
                   const u16* __restrict__ Vf, const u16* __restrict__ Wof, const u16* __restrict__ w1,
                   const u16* __restrict__ w2, u16* H, u16* HN) {
  const int tid = threadIdx.x, lane = tid & 63, w = __builtin_amdgcn_readfirstlane(tid >> 6), hi = lane >> 5;
  const int tg = blockIdx.x;
  STAMP(1, 0);
  const int kh = w >> 2, ng = w & 3;
  const char* wob = (const char*)(Wof + (size_t)(7 * ng * 14 + 7 * kh) * 512);
  const unsigned wl = lane * 16;
  bf16x8 wf[7][7];
#define WOFR(i_) (*(const bf16x8*)(wob + (((i_) / 7) * 14 + (i_) % 7) * 1024 + wl))
#define LDWO(i0, i1) do { _Pragma("unroll") for (int i_ = i0; i_ < i1; ++i_) wf[i_ / 7][i_ % 7] = WOFR(i_); } while (0)
#define BOF(k) (*(const bf16x8*)(lds + K2_OT + ((4 * (k) + (lane >> 4)) * 8 + (lane & 7)) * 16))
#define HBST(acc) do { if ((lane & 15) < 8) { \
      _Pragma("unroll") for (int n = 0; n < 7; ++n) *(f32x4*)(lds + K2_HB + ((kh * 8 + (lane & 15)) * HBS + 16 * (7 * ng + n) + 4 * (lane >> 4)) * 4) = acc[n]; \
    } } while (0)
  u32x4 xv;
  if (w < 4) {
    const f32x16 zero = {};
    const int qh = w & 1, kp = w >> 1;
    const lds_cptr rp0 = (lds_cptr)lds + K2_RING + lane * 16;
    const lds_cptr rp = rp0 + kp * 4096;
    lds_cptr B0 = rp, B1 = rp + 32768;
    const u16* qp = Qf + ((size_t)((tg * 4 + 2 * qh) * 2) * 64 + lane) * 8;
    bf16x8 qA0 = *(const bf16x8*)(qp), qA1 = *(const bf16x8*)(qp + 512), qB0 = *(const bf16x8*)(qp + 1024), qB1 = *(const bf16x8*)(qp + 1536);
    RBAR();
    bf16x8 k0 = ldsr16(rp0), k1 = ldsr16(rp0 + 1024), v0 = RDS(0, 2048), v1 = v0;
    (void)set_ref(qA1, 0.f, hi);
    (void)set_ref(qB1, 0.f, hi);
    f32x16 sA = MFMA32(k0, qA0, zero), sB = MFMA32(k0, qB0, zero);
    sA = MFMA32(k1, qA1, sA);
    sB = MFMA32(k1, qB1, sB);
    k0 = RDS(0, 0); k1 = RDS(0, 1024);
    (void)set_ref(qA1, tile_max(sA), hi);
    (void)set_ref(qB1, tile_max(sB), hi);
    sA = MFMA32(k0, qA0, zero);
    sA = MFMA32(k1, qA1, sA);
    f32x16 oA = zero, oB = zero;
    u32x4 p0, p1 = {0u, 0u, 0u, 0u};
    SBAR();
    STAMP(1, 8);
#pragma unroll 1
    for (int st = 0; st < 4; ++st) {
      CSTAGE(false, 64);
      B0 = B1; B1 = rp + ((st + 2) & 3) * 32768;
    }
    CSTAGE(false, 0);
    B0 = B1; B1 = rp + 2 * 32768;
    CSTAGE(false, 16);
    B0 = B1; B1 = rp + 3 * 32768;
    CSTAGE(false, 64);
    B0 = B1;
    CSTAGE(true, 64);
    oB = MFMA32(v1, BF(p1), oB);
    STAMP(1, 10);
    {
      const f32x16 snd = kp ? oA : oB;
#pragma unroll
      for (int r4 = 0; r4 < 4; ++r4)
        *(f32x4*)(lds + K2_X + ((w * 4 + r4) * 64 + lane) * 16) = (f32x4){snd[4 * r4], snd[4 * r4 + 1], snd[4 * r4 + 2], snd[4 * r4 + 3]};
    }
    f32x16 o0 = kp ? oB : oA;
    bf16x8 qk0 = kp ? qB0 : qA0, qk1 = kp ? qB1 : qA1;
    asm volatile("" : "+v"(o0), "+v"(qk0), "+v"(qk1));
    SBAR();
    LBAR();
#pragma unroll
    for (int r4 = 0; r4 < 4; ++r4) {
      const f32x4 t = *(const f32x4*)(lds + K2_X + (((w ^ 2) * 4 + r4) * 64 + lane) * 16);
      o0[4 * r4] += t[0]; o0[4 * r4 + 1] += t[1]; o0[4 * r4 + 2] += t[2]; o0[4 * r4 + 3] += t[3];
    }
    const int qb = 2 * qh + kp;
    {
      float chk = 0.f;
#pragma unroll
      for (int r = 0; r < 16; ++r) chk = __builtin_fmaf(o0[r], 0.f, chk);
      const bool bad = !(chk == 0.f) || !(o0[12] < 1.8e19f);
      if (__any(bad)) {
        attn_safe(Kf, Vf, qk0, qk1, o0, lane, hi);
      }
    }
    {
      auto sw = __builtin_amdgcn_permlane32_swap(__float_as_uint(o0[12]), __float_as_uint(o0[12]), false, false);
      const float il = 1.f / __uint_as_float(sw[1]);
      const int c = lane & 31;
#pragma unroll
      for (int r4 = 0; r4 < 4; ++r4) {
        const int d = 8 * r4 + 4 * hi, k = 28 * (4 * qb + (c >> 3)) + d;
        if (d < 28)
          *(u32x2*)(lds + K2_OT + ((k >> 3) * 8 + (c & 7)) * 16 + (k & 7) * 2) =
              (u32x2){cvtpk(o0[4 * r4] * il, o0[4 * r4 + 1] * il), cvtpk(o0[4 * r4 + 2] * il, o0[4 * r4 + 3] * il)};
      }
    }
    STAMP(1, 1);
    LBAR();
    STAMP(1, 2);
    LDWO(16, 39);
    xv = *(const u32x4*)(x + (size_t)(tg * 8 + w) * D + (lane < 56 ? lane : 55) * 8);
    SBAR();
    {
      bf16x8 bo[7];
#pragma unroll
      for (int k = 0; k < 7; ++k) bo[k] = BOF(k);
#pragma unroll
      for (int i = 0; i < 16; ++i) wf[i / 7][i % 7] = *(const bf16x8*)(lds + K2_RING + (i >> 3) * 32768 + w * 8192 + (i & 7) * 1024 + lane * 16);
      f32x4 acc[7];
#pragma unroll
      for (int n = 0; n < 7; ++n) acc[n] = (f32x4){0.f, 0.f, 0.f, 0.f};
#pragma unroll
      for (int i = 0; i < 39; ++i) acc[i / 7] = MFMA16(wf[i / 7][i % 7], bo[i % 7], acc[i / 7]);
      HBST(acc);
    }
  } else {
    const int hw = w - 4;
    __builtin_amdgcn_s_setprio(3);
    const unsigned ring = (unsigned)(uintptr_t)lds + K2_RING + hw * 8192;
    const u16* kp = Kf + (size_t)hw * 2048;
    const u16* vp = Vf + (size_t)hw * 2048;
    const unsigned l16 = lane * 16;
#define DMAS(s_) do { const unsigned sl_ = ring + ((s_) & 3) * 32768; \
    _Pragma("unroll") for (int u_ = 0; u_ < 2; ++u_) { \
      glds16s(kp + (s_) * 8192 + u_ * 1024, l16, sl_ + u_ * 4096); glds16s(kp + (s_) * 8192 + u_ * 1024 + 512, l16, sl_ + u_ * 4096 + 1024); \
      glds16s(vp + (s_) * 8192 + u_ * 1024, l16, sl_ + u_ * 4096 + 2048); glds16s(vp + (s_) * 8192 + u_ * 1024 + 512, l16, sl_ + u_ * 4096 + 3072); } } while (0)
    const unsigned dum = (unsigned)(uintptr_t)lds + K2_DUM + hw * 1024;
    const int xg = tg & 7, rk = tg >> 3;
    const int ts = xg < 4 ? 25 * xg : 100 + 24 * (xg - 4), te = ts + (xg < 4 ? 25 : 24);
    const int n0 = ts / 7, n1 = (te - 1) / 7;
    const int nb0 = n0 + 3 <= 27 ? n0 : 24;
    const u16* p1 = w1 + (size_t)(64 * rk + 16 * hw) * F + nb0 * 64;
    const u16* p1b = w1 + (size_t)(64 * rk + 16 * hw) * F + n1 * 64;
    const u16* p2 = w2 + (size_t)(56 * rk + 14 * hw) * S + 4 * xg * 64;
    const unsigned o1 = ((lane >> 5) * F + (lane & 31) * 8) * 2, o1b = ((lane >> 3) * F + (lane & 7) * 8) * 2, o2 = ((lane >> 5) * S + (lane & 31) * 8) * 2;
#define PFD(i0, i1) do { _Pragma("unroll") for (int i_ = i0; i_ < i1; ++i_) { \
      if (i_ < 8) glds16s(p1 + (size_t)i_ * 2 * F, o1, dum); \
      else if (i_ < 10) glds16s(p1b + (size_t)(i_ - 8) * 8 * F, o1b, dum); \
      else glds16s(p2 + (size_t)(i_ - 10) * 2 * S, o2, dum); } } while (0)
    DMAS(0); DMAS(1); DMAS(2);
    waitv<16>(); RBAR();
    LDWO(0, 12); SBAR(); PFD(0, 3);
    waitv<23>(); RBAR();
    DMAS(3); LDWO(12, 24); SBAR(); PFD(3, 6);
    waitv<38>(); RBAR();
    DMAS(4); LDWO(24, 36); SBAR(); PFD(6, 9);
    waitv<38>(); RBAR();
    DMAS(5); LDWO(36, 49); SBAR(); PFD(9, 12);
    waitv<39>(); RBAR();
    bf16x8 wx[10];
    DMAS(6);
#pragma unroll
    for (int i = 0; i < 10; ++i) wx[i] = *(const bf16x8*)(wob - 7 * 1024 + (((39 + i) / 7) * 14 + (39 + i) % 7) * 1024 + wl);
    SBAR(); PFD(12, 15);
    waitv<37>(); RBAR();
    DMAS(7); PFD(15, 17);
    waitv<23>(); RBAR();
#pragma unroll
    for (int i = 0; i < 8; ++i) glds16s(wob - 7 * 1024 + ((i / 7) * 14 + i % 7) * 1024, wl, ring + i * 1024);
    waitv<10>(); RBAR();
#pragma unroll
    for (int i = 8; i < 16; ++i) glds16s(wob - 7 * 1024 + ((i / 7) * 14 + i % 7) * 1024, wl, ring + 32768 + (i - 8) * 1024);
    xv = *(const u32x4*)(x + (size_t)(tg * 8 + w) * D + (lane < 56 ? lane : 55) * 8);
    SBAR();
    __builtin_amdgcn_s_setprio(0);
    RBAR();
    waitv<1>();
    STAMP(1, 1);
    LBAR();
    STAMP(1, 2);
    {
      f32x4 acc[7];
      {
        bf16x8 bo[7];
#pragma unroll
        for (int k = 0; k < 7; ++k) bo[k] = BOF(k);
        acc[5] = (f32x4){0.f, 0.f, 0.f, 0.f}; acc[6] = acc[5];
#pragma unroll
        for (int i = 0; i < 10; ++i) acc[(39 + i) / 7] = MFMA16(wx[i], bo[(39 + i) % 7], acc[(39 + i) / 7]);
      }
      SBAR();
      bf16x8 bo[7];
#pragma unroll
      for (int k = 0; k < 7; ++k) bo[k] = BOF(7 + k);
#pragma unroll
      for (int n = 0; n < 7; ++n) {
        if (n < 5) acc[n] = (f32x4){0.f, 0.f, 0.f, 0.f};
#pragma unroll
        for (int k = 0; k < 7; ++k) acc[n] = MFMA16(wf[n][k], bo[k], acc[n]);
      }
      HBST(acc);
    }
  }
  LBAR();
  STAMP(1, 3);
  {
    const int s = tg * 8 + w;
    const bool act = lane < 56;
    const int lc = act ? lane : 55;
    float v[8];
    const f32x4 a0 = *(const f32x4*)(lds + K2_HB + (w * HBS + lc * 8) * 4), a1 = *(const f32x4*)(lds + K2_HB + (w * HBS + lc * 8 + 4) * 4);
    const f32x4 c0 = *(const f32x4*)(lds + K2_HB + ((8 + w) * HBS + lc * 8) * 4), c1 = *(const f32x4*)(lds + K2_HB + ((8 + w) * HBS + lc * 8 + 4) * 4);
    u32x4 hv;
#pragma unroll
    for (int i = 0; i < 2; ++i) {
      hv[i] = cvtpk(bflo(xv[i]) + a0[2 * i] + c0[2 * i], bfhi(xv[i]) + a0[2 * i + 1] + c0[2 * i + 1]);
      hv[2 + i] = cvtpk(bflo(xv[2 + i]) + a1[2 * i] + c1[2 * i], bfhi(xv[2 + i]) + a1[2 * i + 1] + c1[2 * i + 1]);
    }
    if (act) st16c(H + (size_t)s * D + lane * 8, hv);
#pragma unroll
    for (int i = 0; i < 4; ++i) { v[2 * i] = act ? bflo(hv[i]) : 0.f; v[2 * i + 1] = act ? bfhi(hv[i]) : 0.f; }
#pragma unroll
    for (int pass = 0; pass < 2; ++pass) {
      float sm = 0.f;
#pragma unroll
      for (int i = 0; i < 8; ++i) sm += v[i];
      const float mean = wave_sum(sm) * (1.f / D);
      float q = 0.f;
#pragma unroll
      for (int i = 0; i < 8; ++i) { v[i] = act ? v[i] - mean : 0.f; q += v[i] * v[i]; }
      const float rs = __builtin_amdgcn_rsqf(wave_sum(q) * (1.f / D) + LN_EPS);
#pragma unroll
      for (int i = 0; i < 8; ++i) v[i] *= rs;
    }
    if (act) *(u32x4*)(lds + K2_HS + w * 912 + lane * 16) = (u32x4){cvtpk(v[0], v[1]), cvtpk(v[2], v[3]), cvtpk(v[4], v[5]), cvtpk(v[6], v[7])};
  }
  LBAR();
  if (tid < D / 2) {
    unsigned r[8];
#pragma unroll
    for (int i = 0; i < 8; ++i) r[i] = *(const unsigned*)(lds + K2_HS + i * 912 + tid * 4);
    u32x4 lo, hi2;
#pragma unroll
    for (int i = 0; i < 4; ++i) {
      lo[i] = (r[2 * i] & 0xffffu) | (r[2 * i + 1] << 16);
      hi2[i] = (r[2 * i] >> 16) | (r[2 * i + 1] & 0xffff0000u);
    }
    u32x4* dst = (u32x4*)(HN + ((size_t)tg * D + 2 * tid) * 8);
    st16c(dst, lo); st16c(dst + 1, hi2);
  }
  STAMP(1, 4);
}

constexpr int G_RING = 4, G_LDS = 8 * G_RING * 4096;
typedef __attribute__((address_space(1))) unsigned gu32;
typedef __attribute__((address_space(1))) unsigned long long gu64;
#define RLX_AGENT __ATOMIC_RELAXED, __HIP_MEMORY_SCOPE_AGENT
template <int OFF> DI void gload16c(bf16x8& d, const void* p) {
  asm volatile("global_load_dwordx4 %0, %1, off offset:%2 sc1" : "=v"(d) : "v"(p), "i"(OFF) : "memory");
}
template <int PH, int NSH>
DI void mlp_tile(char* lds, int t, const u16* act, const u16* __restrict__ W, const u16* __restrict__ bias, const u16* H, u16* out,
                 unsigned* cnt, unsigned want) {
  constexpr int KT = PH == 0 ? S : F, LDW = PH == 0 ? F : S, NK = KT / 256, NR = NK - G_RING, LA = 5;
  const int tid = threadIdx.x, lane = tid & 63, w = __builtin_amdgcn_readfirstlane(tid >> 6);
  const int nt = t / 7, mt = t - nt * 7;
  const unsigned lds0 = (unsigned)(uintptr_t)lds + w * (G_RING * 4096);
  const int g = lane >> 4, q4 = (lane & 15) >> 2, p = lane & 3, l15 = lane & 15;
  STAMP(2 + PH, 0);
  const int enb = w >> 1, emb = 2 * (w & 1);
  u32x2 eb;
  if (PH == 0) eb = *(const u32x2*)(bias + 64 * nt + 16 * enb + 4 * g);
  else eb[0] = bias[64 * nt + 16 * enb + l15];
  SBAR();
  const u16* asrc = act + ((size_t)(4 * w + g) * D + 64 * mt + l15) * 8;
  const int r8 = lane >> 3, fe = (r8 >> 1) & 1;
  const u16* wsrc0 = W + (size_t)(32 * w + r8) * LDW + 64 * nt + ((lane & 7) ^ (fe << 1)) * 8;
  const u16* wsrc1 = W + (size_t)(32 * w + 8 + r8) * LDW + 64 * nt + ((lane & 7) ^ ((fe | 2) << 1)) * 8;
  const int f = ((q4 >> 1) & 1) | ((g & 1) << 1);
  int woff[4];
#pragma unroll
  for (int nb = 0; nb < 4; ++nb) woff[nb] = w * (G_RING * 4096) + (8 * g + q4) * 128 + (((2 * nb + (p >> 1)) ^ (f << 1)) * 16) + 8 * (p & 1);
  bf16x8 af[NK][4];
  f32x4 acc[4][4] = {};
#define STAGE_W(i) do { const unsigned sl_ = lds0 + ((i) % G_RING) * 4096; const size_t o_ = (size_t)(i) * 256 * LDW; \
    glds16(wsrc0 + o_, sl_); glds16(wsrc1 + o_, sl_ + 1024); glds16(wsrc0 + o_ + 16 * LDW, sl_ + 2048); glds16(wsrc1 + o_ + 16 * LDW, sl_ + 3072); } while (0)
#define LOAD_A(i) do { const u16* ap_ = asrc + (size_t)(i) * 32 * D * 8; \
    gload16c<0>(af[i][0], ap_); gload16c<256>(af[i][1], ap_); gload16c<512>(af[i][2], ap_); gload16c<768>(af[i][3], ap_); } while (0)
#define RDW(WF, i) do { _Pragma("unroll") for (int nb_ = 0; nb_ < 4; ++nb_) { const lds_cptr sp_ = (lds_cptr)lds + woff[nb_] + ((i) % G_RING) * 4096; \
      WF[nb_] = cat8(trread(sp_), trread(sp_ + 512)); } } while (0)
#define MMA(WF, i) do { _Pragma("unroll") for (int nb_ = 0; nb_ < 4; ++nb_) _Pragma("unroll") for (int mb_ = 0; mb_ < 4; ++mb_) \
      acc[nb_][mb_] = PH == 0 ? MFMA16(WF[nb_], af[i][mb_], acc[nb_][mb_]) : MFMA16(af[i][mb_], WF[nb_], acc[nb_][mb_]); } while (0)
  bf16x8 wr[NR][4];
#pragma unroll
  for (int j = 0; j < NR; ++j) STAGE_W(G_RING + j);
  if (NR < G_RING) STAGE_W(G_RING - 1);
#pragma unroll
  for (int j = 0; j < NR; ++j) {
    waitv<12>();
    RDW(wr[j], G_RING + j);
    asm volatile("s_waitcnt lgkmcnt(0)" ::: "memory");
    STAGE_W(j);
  }
  STAMP(2 + PH, 1);
  if (w == 0) {
    gu32* cp = (gu32*)(cnt + (lane < NSH ? lane : 0) * 64);
    for (unsigned spins = 0; spins < (1u << 20); ++spins) {
      if (__all(__hip_atomic_load(cp, RLX_AGENT) >= want)) break;
      __builtin_amdgcn_s_sleep(1);
    }
  }
  asm volatile("s_barrier" ::: "memory");
  STAMP(2 + PH, 2);
  unsigned long long eh0 = 0, eh1 = 0;
  if (PH == 1) {
    const int s = 64 * nt + 16 * enb + l15;
    eh0 = __hip_atomic_load((gu64*)(H + (size_t)s * D + 64 * mt + 16 * emb + 4 * g), RLX_AGENT);
    eh1 = __hip_atomic_load((gu64*)(H + (size_t)s * D + 64 * mt + 16 * emb + 16 + 4 * g), RLX_AGENT);
  }
  SBAR();
#pragma unroll
  for (int j = 0; j < LA; ++j) LOAD_A((j + G_RING) % NK);
#pragma unroll
  for (int j = 0; j < NK; ++j) {
    const int i = (j + G_RING) % NK;
    const int rem = NK - 1 - j < LA - 1 ? NK - 1 - j : LA - 1;
    if (rem == 4) waitv4<16>(af[i][0], af[i][1], af[i][2], af[i][3]);
    else if (rem == 3) waitv4<12>(af[i][0], af[i][1], af[i][2], af[i][3]);
    else if (rem == 2) waitv4<8>(af[i][0], af[i][1], af[i][2], af[i][3]);
    else if (rem == 1) waitv4<4>(af[i][0], af[i][1], af[i][2], af[i][3]);
    else waitv4<0>(af[i][0], af[i][1], af[i][2], af[i][3]);
    if (j < NR) {
      MMA(wr[j], i);
    } else {
      bf16x8 wf[4];
      RDW(wf, i);
      MMA(wf, i);
    }
    SBAR();
    if (j + LA < NK) LOAD_A((j + LA + G_RING) % NK);
  }
#undef STAGE_W
#undef LOAD_A
#undef RDW
#undef MMA
  STAMP(2 + PH, 4);
  {
    f32x4* xb = (f32x4*)lds;
#pragma unroll
    for (int nb = 0; nb < 4; ++nb)
#pragma unroll
      for (int mb = 0; mb < 4; ++mb) xb[(w * 16 + nb * 4 + mb) * 64 + lane] = acc[nb][mb];
  }
  LBAR();
  {
    const f32x4* xb = (const f32x4*)lds;
#pragma unroll
    for (int e = 0; e < 2; ++e) {
      f32x4 a = xb[(2 * w + e) * 64 + lane];
#pragma unroll
      for (int v = 1; v < 8; ++v) a += xb[(v * 16 + 2 * w + e) * 64 + lane];
      const int mb = emb + e;
      if (PH == 0) {
        const int fi = 64 * nt + 16 * enb + 4 * g, d = 64 * mt + 16 * mb + l15;
        float z[4] = {a[0] + bflo(eb[0]), a[1] + bfhi(eb[0]), a[2] + bflo(eb[1]), a[3] + bfhi(eb[1])};
#pragma unroll
        for (int i = 0; i < 4; ++i) {
          const float zz = z[i], a2 = zz * (1.f + 0.044715f * zz * zz) * (-2.f * 0.7978845608028654f * 1.4426950408889634f);
          z[i] = zz * __builtin_amdgcn_rcpf(1.f + __builtin_amdgcn_exp2f(a2));
        }
        const unsigned long long v = (unsigned long long)cvtpk(z[0], z[1]) | ((unsigned long long)cvtpk(z[2], z[3]) << 32);
        __hip_atomic_store((gu64*)(out + ((size_t)(fi >> 3) * D + d) * 8 + (fi & 7)), v, RLX_AGENT);
      } else {
        const int d = 64 * mt + 16 * mb + 4 * g, s = 64 * nt + 16 * enb + l15;
        const float bb = bflo(eb[0]);
        const unsigned long long hv = e ? eh1 : eh0;
        const unsigned h0 = (unsigned)hv, h1 = (unsigned)(hv >> 32);
        *(u32x2*)(out + (size_t)s * D + d) =
            (u32x2){cvtpk(a[0] + bb + bflo(h0), a[1] + bb + bfhi(h0)), cvtpk(a[2] + bb + bflo(h1), a[3] + bb + bfhi(h1))};
      }
    }
  }
  STAMP(2 + PH, 5);
}
#define PUBLISH(c) do { asm volatile("s_waitcnt vmcnt(0) lgkmcnt(0)\n\ts_barrier" ::: "memory"); \
    if (threadIdx.x == 0) __hip_atomic_fetch_add((gu32*)(c), 1u, RLX_AGENT); } while (0)

extern "C" __global__ __launch_bounds__(512) void k_main(const u16* __restrict__ x, const u16* __restrict__ Qf, const u16* __restrict__ Kf,
                                              const u16* __restrict__ Vf, const u16* __restrict__ Wof, const u16* __restrict__ w1,
                                              const u16* __restrict__ b1, const u16* __restrict__ w2, const u16* __restrict__ b2,
                                              u16* H, u16* HN, u16* U, u16* out, unsigned* cnt) {
  __shared__ __attribute__((aligned(16))) char lds[K2_LDS];
  attn_phase(lds, x, Qf, Kf, Vf, Wof, w1, w2, H, HN);
  PUBLISH(cnt + (8 + (blockIdx.x & 7)) * 64);
  if (blockIdx.x < 196) {
    const int t = xcd_remap(blockIdx.x, 196);
    mlp_tile<0, 8>(lds, t, HN, w1, b1, H, U, cnt + 8 * 64, 32u);
    PUBLISH(cnt + (t % 7) * 64);
  }
  if (blockIdx.x < 224) {
    const int t2 = xcd_remap(blockIdx.x, 224);
    mlp_tile<1, 1>(lds, t2, U, w2, b2, H, out, cnt + (t2 % 7) * 64, 28u);
  }
}

extern "C" void kernel_launch(void* const* d_in, const int* in_sizes, int n_in, void* d_out, int out_size, void* d_ws, size_t ws_size,
                              hipStream_t stream) {
  const u16* x = (const u16*)d_in[0];
  const u16* wq = (const u16*)d_in[1];
  const u16* wk = (const u16*)d_in[2];
  const u16* wv = (const u16*)d_in[3];
  const u16* wo = (const u16*)d_in[4];
  const u16* w1 = (const u16*)d_in[6];
  const u16* b1 = (const u16*)d_in[7];
  const u16* w2 = (const u16*)d_in[8];
  const u16* b2 = (const u16*)d_in[9];
  char* ws = (char*)d_ws;
  u16 *Qf = (u16*)(ws + WS_QF), *Kf = (u16*)(ws + WS_KF), *Vf = (u16*)(ws + WS_VF), *Wof = (u16*)(ws + WS_WOF);
  u16 *H = (u16*)(ws + WS_H), *HN = (u16*)(ws + WS_HN), *U = (u16*)(ws + WS_U);
  unsigned* cnt = (unsigned*)(ws + WS_CNT);
  k_qkv<<<256, 512, 0, stream>>>(x, wq, wk, wv, wo, Qf, Kf, Vf, Wof, cnt);
  k_main<<<256, 512, 0, stream>>>(x, Qf, Kf, Vf, Wof, w1, b1, w2, b2, H, HN, U, (u16*)d_out, cnt);
}
```

```cpp
#include <hip/hip_runtime.h>
#include <hip/hip_bf16.h>

typedef unsigned short u16;
using bf16x8 = __attribute__((ext_vector_type(8))) short;
using s16x4 = __attribute__((ext_vector_type(4))) short;
using f32x4 = __attribute__((ext_vector_type(4))) float;
using f32x16 = __attribute__((ext_vector_type(16))) float;
using u32x4 = __attribute__((ext_vector_type(4))) unsigned;
using u32x2 = __attribute__((ext_vector_type(2))) unsigned;
typedef short v4i16_t __attribute__((ext_vector_type(4)));
typedef __attribute__((address_space(3))) const char* lds_cptr;
typedef __bf16 bf16x2_t __attribute__((ext_vector_type(2)));
typedef float f32x2_t __attribute__((ext_vector_type(2)));

#define DI __device__ __forceinline__
#define MFMA32(a, b, c) __builtin_amdgcn_mfma_f32_32x32x16_bf16(a, b, c, 0, 0, 0)
#define MFMA16(a, b, c) __builtin_amdgcn_mfma_f32_16x16x32_bf16(a, b, c, 0, 0, 0)
#define SBAR() __builtin_amdgcn_sched_barrier(0)
#define LBAR() asm volatile("s_waitcnt lgkmcnt(0)\n\ts_barrier" ::: "memory")
#define WAITV_BAR(N) asm volatile("s_waitcnt vmcnt(" #N ") lgkmcnt(0)\n\ts_barrier" ::: "memory")

constexpr int S = 2048, D = 448, DH = 28, F = 1792;
constexpr float QSCALE = 0.18898223650461362f * 1.4426950408889634f;
constexpr float LN_EPS = 1e-5f;

constexpr size_t WS_QF = 0;
constexpr size_t WS_KF = WS_QF + 2097152;
constexpr size_t WS_VF = WS_KF + 131072;
constexpr size_t WS_WOF = WS_VF + 131072;
constexpr size_t WS_H = WS_WOF + 401408;
constexpr size_t WS_HN = WS_H + 1835008;
constexpr size_t WS_U = WS_HN + 1835008;
constexpr size_t WS_CNT = WS_U + 1605632;

#define STAMP(k, i)
#define CSTAMP(k, i)

DI unsigned cvtpk(float lo, float hi) {
  const f32x2_t v = {lo, hi};
  return __builtin_bit_cast(unsigned, __builtin_convertvector(v, bf16x2_t));
}
DI float bflo(unsigned u) { return __uint_as_float(u << 16); }
DI float bfhi(unsigned u) { return __uint_as_float(u & 0xffff0000u); }
template <int CTRL> DI float dppf(float v) {
  return __uint_as_float(__builtin_amdgcn_update_dpp(0u, __float_as_uint(v), CTRL, 0xf, 0xf, true));
}
DI float wave_sum(float v) {
  v += dppf<0xB1>(v);
  v += dppf<0x4E>(v);
  v += dppf<0x141>(v);
  v += dppf<0x140>(v);
  auto a = __builtin_amdgcn_permlane16_swap(__float_as_uint(v), __float_as_uint(v), false, false);
  v = __uint_as_float(a[0]) + __uint_as_float(a[1]);
  auto b = __builtin_amdgcn_permlane32_swap(__float_as_uint(v), __float_as_uint(v), false, false);
  return __uint_as_float(b[0]) + __uint_as_float(b[1]);
}
DI float half_max(float v) {
  auto b = __builtin_amdgcn_permlane32_swap(__float_as_uint(v), __float_as_uint(v), false, false);
  return __builtin_fmaxf(__uint_as_float(b[0]), __uint_as_float(b[1]));
}
DI void glds16(const void* g, unsigned lds_base) {
  unsigned sv;
  asm volatile("s_mov_b32 %0, m0\n\ts_mov_b32 m0, %2\n\ts_nop 0\n\tglobal_load_lds_dwordx4 %1, off\n\ts_mov_b32 m0, %0"
               : "=&s"(sv) : "v"(g), "s"(lds_base) : "memory");
}
DI void glds16s(const void* sbase, unsigned voff, unsigned lds_base) {
  unsigned sv;
  asm volatile("s_mov_b32 %0, m0\n\ts_mov_b32 m0, %3\n\ts_nop 0\n\tglobal_load_lds_dwordx4 %1, %2\n\ts_mov_b32 m0, %0"
               : "=&s"(sv) : "v"(voff), "s"(sbase), "s"(lds_base) : "memory");
}
DI void glds4(const void* g, unsigned lds_base) {
  unsigned sv;
  asm volatile("s_mov_b32 %0, m0\n\ts_mov_b32 m0, %2\n\ts_nop 0\n\tglobal_load_lds_dword %1, off\n\ts_mov_b32 m0, %0"
               : "=&s"(sv) : "v"(g), "s"(lds_base) : "memory");
}
template <int N> DI void waitv4(bf16x8& a, bf16x8& b, bf16x8& c, bf16x8& d) {
  asm volatile("s_waitcnt vmcnt(%4)" : "+v"(a), "+v"(b), "+v"(c), "+v"(d) : "i"(N) : "memory");
}
template <int OFF> DI void gload16(bf16x8& d, const void* p) {
  asm volatile("global_load_dwordx4 %0, %1, off offset:%2" : "=v"(d) : "v"(p), "i"(OFF) : "memory");
}
DI s16x4 trread(lds_cptr p) {
  return __builtin_bit_cast(s16x4, __builtin_amdgcn_ds_read_tr16_b64_v4i16((__attribute__((address_space(3))) v4i16_t*)p));
}
DI bf16x8 ldsr16(lds_cptr p) { return *(const __attribute__((address_space(3))) bf16x8*)p; }
DI bf16x8 cat8(s16x4 a, s16x4 b) { return __builtin_shufflevector(a, b, 0, 1, 2, 3, 4, 5, 6, 7); }
DI int xcd_remap(int bid, int nwg) {
  const int q = nwg >> 3, r = nwg & 7, x = bid & 7, o = bid >> 3;
  return (x < r ? x * (q + 1) : r * (q + 1) + (x - r) * q) + o;
}

constexpr int K1_WI = 448 * 128, K1_X = 2 * K1_WI, K1_XS = 528, K1_DUM = K1_X + 64 * K1_XS, K1_LDS = K1_DUM + 1024;
constexpr int KMAIN_KB = 28;
__global__ __launch_bounds__(512) void k_qkv(const u16* __restrict__ x, const u16* __restrict__ wq, const u16* __restrict__ wk,
                                             const u16* __restrict__ wv, const u16* __restrict__ wo, u16* __restrict__ Qf,
                                             u16* __restrict__ Kf, u16* __restrict__ Vf, u16* __restrict__ Wof, unsigned* __restrict__ cnt) {
  __shared__ __attribute__((aligned(16))) char lds[K1_LDS];
  const int tid = threadIdx.x, lane = tid & 63, w = __builtin_amdgcn_readfirstlane(tid >> 6);
  const int t = xcd_remap(blockIdx.x, 256), rt = t >> 2, ct = t & 3;
  const unsigned lds0 = (unsigned)(uintptr_t)lds;
  STAMP(0, 0);
  bf16x8 xr[4];
  const bool act = lane < 56;
  {
    const u16* xp_ = x + (size_t)(rt * 32 + w * 4) * D + (act ? lane : 55) * 8;
    gload16<0>(xr[0], xp_); gload16<896>(xr[1], xp_); gload16<1792>(xr[2], xp_); gload16<2688>(xr[3], xp_);
  }
#pragma unroll
  for (int im = 0; im < 2; ++im) {
    const int c64 = 2 * ct + im;
    if (c64 < 7) {
#pragma unroll
      for (int i = 0; i < 7; ++i) {
        const int piece = w * 7 + i, row = piece * 8 + (lane >> 3);
        const int f = ((row >> 1) & 1) | (((row >> 3) & 1) << 1);
        const int c = (lane & 7) ^ (f << 1);
        glds16(wq + (size_t)row * D + c64 * 64 + c * 8, lds0 + im * K1_WI + piece * 1024);
      }
    } else {
#pragma unroll
      for (int i = 0; i < 7; ++i) {
        const int pc = w * 7 + i < 50 ? w * 7 + i : 49, mat = pc >= 25, piece = pc - 25 * mat;
        const int e = piece * 512 + lane * 8;
        glds16((mat ? wv : wk) + (e < 448 * 28 ? e : 448 * 28 - 8), lds0 + im * K1_WI + mat * 25600 + piece * 1024);
      }
    }
  }
  u16 wt[8];
  const int frag0 = blockIdx.x + 256 * (w & 1);
  const bool dofrag = w < 2 && frag0 < 392;
  const int frag = dofrag ? frag0 : 0;
  {
    const int nb = frag / 14, ks = frag - nb * 14;
#pragma unroll
    for (int j = 0; j < 8; ++j) wt[j] = wo[(size_t)(32 * ks + 8 * (lane >> 4) + j) * D + 16 * nb + (lane & 15)];
  }
  SBAR();
  waitv4<22>(xr[0], xr[1], xr[2], xr[3]);
  {
    u32x4 xv[4];
#pragma unroll
    for (int r = 0; r < 4; ++r) xv[r] = __builtin_bit_cast(u32x4, xr[r]);
    float v[4][8], mean[4], rs[4];
#pragma unroll
    for (int r = 0; r < 4; ++r) {
      float s = 0.f;
#pragma unroll
      for (int i = 0; i < 4; ++i) {
        v[r][2 * i] = act ? bflo(xv[r][i]) : 0.f; v[r][2 * i + 1] = act ? bfhi(xv[r][i]) : 0.f;
        s += v[r][2 * i] + v[r][2 * i + 1];
      }
      mean[r] = s;
    }
#pragma unroll
    for (int r = 0; r < 4; ++r) mean[r] = wave_sum(mean[r]) * (1.f / D);
#pragma unroll
    for (int r = 0; r < 4; ++r) {
      float q = 0.f;
#pragma unroll
      for (int i = 0; i < 8; ++i) { v[r][i] = act ? v[r][i] - mean[r] : 0.f; q += v[r][i] * v[r][i]; }
      rs[r] = q;
    }
#pragma unroll
    for (int r = 0; r < 4; ++r) rs[r] = __builtin_amdgcn_rsqf(wave_sum(rs[r]) * (1.f / D) + LN_EPS);
    char* xdst = lds + K1_X + lane * K1_XS + w * 64;
#pragma unroll
    for (int r = 0; r < 4; ++r) {
      u32x4 o;
#pragma unroll
      for (int i = 0; i < 4; ++i) o[i] = cvtpk(v[r][2 * i] * rs[r], v[r][2 * i + 1] * rs[r]);
      *(u32x4*)(xdst + r * 16) = o;
    }
  }
  STAMP(0, 1);
  WAITV_BAR(0);
  STAMP(0, 2);
  if (w == 7 && (blockIdx.x >> 3) < KMAIN_KB) {
    unsigned long long pc;
    int off;
    asm volatile("s_getpc_b64 %0\n1:\n\ts_mov_b32 %1, k_main-1b" : "=s"(pc), "=s"(off));
    glds16s((const char*)(pc + (long long)off) + (blockIdx.x >> 3) * 1024, lane * 16, lds0 + K1_DUM);
  }
  const int nb = w & 3, im = w >> 2, c64 = 2 * ct + im, g = lane >> 4, q4 = (lane & 15) >> 2, p = lane & 3, l15 = lane & 15;
  const int f = ((q4 >> 1) & 1) | ((g & 1) << 1);
  const bool kv = c64 == 7;
  const int rs_ = kv ? 56 : 128;
  const lds_cptr wp = (lds_cptr)lds + im * K1_WI + (8 * g + q4) * rs_ +
                      (kv ? (nb >> 1) * 25600 + (nb & 1) * 32 + 8 * p : (((2 * nb + (p >> 1)) ^ (f << 1)) * 16) + 8 * (p & 1));
  const lds_cptr xp = (lds_cptr)lds + K1_X + g * K1_XS + l15 * 16;
  f32x4 acc0 = {0.f, 0.f, 0.f, 0.f}, acc1 = {0.f, 0.f, 0.f, 0.f};
  {
    bf16x8 a[14], b0[14], b1[14];
#pragma unroll
    for (int ks = 0; ks < 14; ++ks) {
      a[ks] = cat8(trread(wp + ks * 32 * rs_), trread(wp + ks * 32 * rs_ + 4 * rs_));
      b0[ks] = ldsr16(xp + ks * 4 * K1_XS);
      b1[ks] = ldsr16(xp + ks * 4 * K1_XS + 256);
    }
    SBAR();
#pragma unroll
    for (int ks = 0; ks < 14; ++ks) {
      acc0 = MFMA16(a[ks], b0[ks], acc0);
      acc1 = MFMA16(a[ks], b1[ks], acc1);
    }
  }
  STAMP(0, 3);
#pragma unroll
  for (int tb = 0; tb < 2; ++tb) {
    const f32x4 a = tb ? acc1 : acc0;
    const int s = rt * 32 + 16 * tb + l15;
    const int nl = 16 * nb + 4 * g;
    if (c64 < 7) {
      const int n0 = 64 * c64 + nl, h = n0 / 28, d0 = n0 - 28 * h;
      const int tg = s >> 3, c = (h & 3) * 8 + (s & 7), b = h >> 2;
      const size_t off = ((((size_t)(tg * 4 + b) * 2 + (d0 >> 4)) * 64) + ((d0 >> 3) & 1) * 32 + c) * 8 + (d0 & 7);
      *(u32x2*)(Qf + off) = (u32x2){cvtpk(a[0] * QSCALE, a[1] * QSCALE), cvtpk(a[2] * QSCALE, a[3] * QSCALE)};
    } else {
      const int kt = s >> 5, kk = s & 31;
      const int d0 = 16 * (nb & 1) + 4 * g;
      if (nb < 2) {
        const size_t off = ((((size_t)kt * 2 + (d0 >> 4)) * 64) + ((d0 >> 3) & 1) * 32 + kk) * 8 + (d0 & 7);
        u32x2 v = {cvtpk(a[0], a[1]), cvtpk(a[2], a[3])};
        if (d0 == 28) v = (u32x2){0x3f803f80u, 0x00003f80u};
        *(u32x2*)(Kf + off) = v;
      } else {
        const int sp = kk >> 4, j = ((kk >> 3) & 1) * 4 + (kk & 3), hi = (kk >> 2) & 1;
        const size_t off = ((((size_t)kt * 2 + sp) * 64) + hi * 32 + d0) * 8 + j;
        unsigned v0 = cvtpk(a[0], a[1]), v1 = cvtpk(a[2], a[3]);
        if (d0 == 28) { v0 = 0x00003f80u; v1 = 0u; }
        Vf[off] = (u16)v0; Vf[off + 8] = (u16)(v0 >> 16); Vf[off + 16] = (u16)v1; Vf[off + 24] = (u16)(v1 >> 16);
      }
    }
  }
  if (dofrag) {
    u32x4 o;
#pragma unroll
    for (int i = 0; i < 4; ++i) o[i] = (unsigned)wt[2 * i] | ((unsigned)wt[2 * i + 1] << 16);
    *(u32x4*)(Wof + ((size_t)frag * 64 + lane) * 8) = o;
  }
  if (blockIdx.x == 0) { cnt[tid] = 0u; cnt[512 + tid] = 0u; }
  if (w == 7) asm volatile("s_waitcnt vmcnt(0)" ::: "memory");
  STAMP(0, 4);
}

constexpr int K2_RING = 0, K2_OT = 131072, K2_DUM = K2_OT + 7168, K2_X = K2_DUM + 4096, K2_LDS = K2_X + 16384;
constexpr int K2_HB = 65536, HBS = 452, K2_HS = K2_X;

DI float set_ref(bf16x8& q1, float m, int hi) {
  const float a = bflo(cvtpk(-m, 0.f)), r1 = -m - a, b = bflo(cvtpk(r1, 0.f)), c = bflo(cvtpk(r1 - b, 0.f));
  u32x4 v = __builtin_bit_cast(u32x4, q1);
  v[2] = hi ? cvtpk(a, b) : v[2];
  v[3] = hi ? cvtpk(c, 0.f) : v[3];
  q1 = __builtin_bit_cast(bf16x8, v);
  return -((a + b) + c);
}
DI float tile_max(const f32x16& s) {
  float a = __builtin_fmaxf(__builtin_fmaxf(s[0], s[1]), s[2]), b = __builtin_fmaxf(__builtin_fmaxf(s[3], s[4]), s[5]);
  a = __builtin_fmaxf(__builtin_fmaxf(a, s[6]), s[7]); b = __builtin_fmaxf(__builtin_fmaxf(b, s[8]), s[9]);
  a = __builtin_fmaxf(__builtin_fmaxf(a, s[10]), s[11]); b = __builtin_fmaxf(__builtin_fmaxf(b, s[12]), s[13]);
  a = __builtin_fmaxf(__builtin_fmaxf(a, s[14]), s[15]);
  return half_max(__builtin_fmaxf(a, b));
}
template <int N> DI void waitv() { asm volatile("s_waitcnt vmcnt(%0)" ::"i"(N) : "memory"); }
#define RBAR() asm volatile("s_barrier" ::: "memory")

DI void attn_safe(const u16* __restrict__ Kf, const u16* __restrict__ Vf, bf16x8 q0, bf16x8 q1, f32x16& o, int lane, int hi) {
  const f32x16 zero = {};
  float mref = 0.f;
  o = zero;
#pragma unroll 1
  for (int t = 0; t < 64; ++t) {
    const bf16x8 k0 = *(const bf16x8*)(Kf + t * 1024 + lane * 8), k1 = *(const bf16x8*)(Kf + t * 1024 + 512 + lane * 8);
    const bf16x8 v0 = *(const bf16x8*)(Vf + t * 1024 + lane * 8), v1 = *(const bf16x8*)(Vf + t * 1024 + 512 + lane * 8);
    (void)set_ref(q1, 0.f, hi);
    f32x16 s = MFMA32(k0, q0, zero);
    s = MFMA32(k1, q1, s);
    const float tm = tile_max(s);
    const float mn = set_ref(q1, t == 0 ? tm : __builtin_fmaxf(tm, mref), hi);
    const float al = t == 0 ? 0.f : __builtin_amdgcn_exp2f(mref - mn);
#pragma unroll
    for (int r = 0; r < 16; ++r) o[r] *= al;
    mref = mn;
    s = MFMA32(k0, q0, zero);
    s = MFMA32(k1, q1, s);
    u32x4 p0, p1;
#pragma unroll
    for (int i = 0; i < 4; ++i) {
      p0[i] = cvtpk(__builtin_amdgcn_exp2f(s[2 * i]), __builtin_amdgcn_exp2f(s[2 * i + 1]));
      p1[i] = cvtpk(__builtin_amdgcn_exp2f(s[8 + 2 * i]), __builtin_amdgcn_exp2f(s[8 + 2 * i + 1]));
    }
    o = MFMA32(v0, __builtin_bit_cast(bf16x8, p0), o);
    o = MFMA32(v1, __builtin_bit_cast(bf16x8, p1), o);
  }
}

#define EX4(S, i) do { S[i] = __builtin_amdgcn_exp2f(S[i]); S[i + 1] = __builtin_amdgcn_exp2f(S[i + 1]); \
    S[i + 2] = __builtin_amdgcn_exp2f(S[i + 2]); S[i + 3] = __builtin_amdgcn_exp2f(S[i + 3]); } while (0)
#define PK8(P, S, i) do { P[0] = cvtpk(S[i], S[i + 1]); P[1] = cvtpk(S[i + 2], S[i + 3]); P[2] = cvtpk(S[i + 4], S[i + 5]); P[3] = cvtpk(S[i + 6], S[i + 7]); } while (0)
#define BF(P) __builtin_bit_cast(bf16x8, P)
constexpr int CPRE = 0;
#define WOPF(j) do { if ((j) < CPRE) wf[(j) / 7][(j) % 7] = WOFR(j); } while (0)
#define RDS(u, off) ldsr16(((u) < 4 ? B0 : B1) + ((u) & 3) * 8192 + (off))
#define CTILE(u, NX, PF) do { \
      \
    sB = MFMA32(k0, qB0, zero); SBAR(); if (NX) k0 = RDS((u) + 1, 0); EX4(sA, 0); WOPF(PF); SBAR(); \
    sB = MFMA32(k1, qB1, sB); SBAR(); if (NX) k1 = RDS((u) + 1, 1024); EX4(sA, 4); PK8(p0, sA, 0); SBAR(); \
    oB = MFMA32(v1, BF(p1), oB); SBAR(); v1 = RDS(u, 3072); EX4(sA, 8); WOPF((PF) + 1); SBAR(); \
    oA = MFMA32(v0, BF(p0), oA); SBAR(); EX4(sA, 12); PK8(p1, sA, 8); SBAR(); \
      \
    sA = MFMA32(k0, qA0, zero); SBAR(); EX4(sB, 0); WOPF((PF) + 2); SBAR(); \
    sA = MFMA32(k1, qA1, sA); SBAR(); EX4(sB, 4); PK8(p0, sB, 0); SBAR(); \
    oA = MFMA32(v1, BF(p1), oA); SBAR(); EX4(sB, 8); WOPF((PF) + 3); SBAR(); \
    oB = MFMA32(v0, BF(p0), oB); SBAR(); if (NX) v0 = RDS((u) + 1, 2048); EX4(sB, 12); PK8(p1, sB, 8); SBAR(); } while (0)
#define CSTAGE(LAST, PF) do { \
    CTILE(0, true, PF); CTILE(1, true, (PF) + 4); CTILE(2, true, (PF) + 8); \
    if (!(LAST)) RBAR(); \
    CTILE(3, !(LAST), (PF) + 12); } while (0)

DI void st16c(void* p, u32x4 v) {
  asm volatile("global_store_dwordx4 %0, %1, off sc1\n\ts_nop 1" ::"v"(p), "v"(v) : "memory");
}
DI void attn_phase(char* lds, const u16* __restrict__ x, const u16* __restrict__ Qf, const u16* __restrict__ Kf,
                   const u16* __restrict__ Vf, const u16* __restrict__ Wof, const u16* __restrict__ w1,
                   const u16* __restrict__ w2, u16* H, u16* HN, const u16* outp) {
  const int tid = threadIdx.x, lane = tid & 63, w = __builtin_amdgcn_readfirstlane(tid >> 6), hi = lane >> 5;
  const int tg = blockIdx.x;
  STAMP(1, 0);
  const int kh = w >> 2, ng = w & 3;
  const char* wob = (const char*)(Wof + (size_t)(7 * ng * 14 + 7 * kh) * 512);
  const unsigned wl = lane * 16;
  bf16x8 wf[7][7];
#define WOFR(i_) (*(const bf16x8*)(wob + (((i_) / 7) * 14 + (i_) % 7) * 1024 + wl))
#define LDWO(i0, i1) do { _Pragma("unroll") for (int i_ = i0; i_ < i1; ++i_) wf[i_ / 7][i_ % 7] = WOFR(i_); } while (0)
#define BOF(k) (*(const bf16x8*)(lds + K2_OT + ((4 * (k) + (lane >> 4)) * 8 + (lane & 7)) * 16))
#define HBST(acc) do { if ((lane & 15) < 8) { \
      _Pragma("unroll") for (int n = 0; n < 7; ++n) *(f32x4*)(lds + K2_HB + ((kh * 8 + (lane & 15)) * HBS + 16 * (7 * ng + n) + 4 * (lane >> 4)) * 4) = acc[n]; \
    } } while (0)
  u32x4 xv;
  if (w < 4) {
    const f32x16 zero = {};
    const int qh = w & 1, kp = w >> 1;
    const lds_cptr rp0 = (lds_cptr)lds + K2_RING + lane * 16;
    const lds_cptr rp = rp0 + kp * 4096;
    lds_cptr B0 = rp, B1 = rp + 32768;
    const u16* qp = Qf + ((size_t)((tg * 4 + 2 * qh) * 2) * 64 + lane) * 8;
    bf16x8 qA0 = *(const bf16x8*)(qp), qA1 = *(const bf16x8*)(qp + 512), qB0 = *(const bf16x8*)(qp + 1024), qB1 = *(const bf16x8*)(qp + 1536);
    RBAR();
    bf16x8 k0 = ldsr16(rp0), k1 = ldsr16(rp0 + 1024), v0 = RDS(0, 2048), v1 = v0;
    (void)set_ref(qA1, 0.f, hi);
    (void)set_ref(qB1, 0.f, hi);
    f32x16 sA = MFMA32(k0, qA0, zero), sB = MFMA32(k0, qB0, zero);
    sA = MFMA32(k1, qA1, sA);
    sB = MFMA32(k1, qB1, sB);
    k0 = RDS(0, 0); k1 = RDS(0, 1024);
    (void)set_ref(qA1, tile_max(sA), hi);
    (void)set_ref(qB1, tile_max(sB), hi);
    sA = MFMA32(k0, qA0, zero);
    sA = MFMA32(k1, qA1, sA);
    f32x16 oA = zero, oB = zero;
    u32x4 p0, p1 = {0u, 0u, 0u, 0u};
    SBAR();
    STAMP(1, 8);
#pragma unroll 1
    for (int st = 0; st < 4; ++st) {
      CSTAGE(false, 64);
      B0 = B1; B1 = rp + ((st + 2) & 3) * 32768;
    }
    CSTAGE(false, 0);
    B0 = B1; B1 = rp + 2 * 32768;
    CSTAGE(false, 16);
    B0 = B1; B1 = rp + 3 * 32768;
    CSTAGE(false, 64);
    B0 = B1;
    CSTAGE(true, 64);
    oB = MFMA32(v1, BF(p1), oB);
    STAMP(1, 10);
    {
      const f32x16 snd = kp ? oA : oB;
#pragma unroll
      for (int r4 = 0; r4 < 4; ++r4)
        *(f32x4*)(lds + K2_X + ((w * 4 + r4) * 64 + lane) * 16) = (f32x4){snd[4 * r4], snd[4 * r4 + 1], snd[4 * r4 + 2], snd[4 * r4 + 3]};
    }
    f32x16 o0 = kp ? oB : oA;
    bf16x8 qk0 = kp ? qB0 : qA0, qk1 = kp ? qB1 : qA1;
    asm volatile("" : "+v"(o0), "+v"(qk0), "+v"(qk1));
    SBAR();
    LBAR();
#pragma unroll
    for (int r4 = 0; r4 < 4; ++r4) {
      const f32x4 t = *(const f32x4*)(lds + K2_X + (((w ^ 2) * 4 + r4) * 64 + lane) * 16);
      o0[4 * r4] += t[0]; o0[4 * r4 + 1] += t[1]; o0[4 * r4 + 2] += t[2]; o0[4 * r4 + 3] += t[3];
    }
    const int qb = 2 * qh + kp;
    {
      float chk = 0.f;
#pragma unroll
      for (int r = 0; r < 16; ++r) chk = __builtin_fmaf(o0[r], 0.f, chk);
      const bool bad = !(chk == 0.f) || !(o0[12] < 1.8e19f);
      if (__any(bad)) {
        attn_safe(Kf, Vf, qk0, qk1, o0, lane, hi);
      }
    }
    {
      auto sw = __builtin_amdgcn_permlane32_swap(__float_as_uint(o0[12]), __float_as_uint(o0[12]), false, false);
      const float il = 1.f / __uint_as_float(sw[1]);
      const int c = lane & 31;
#pragma unroll
      for (int r4 = 0; r4 < 4; ++r4) {
        const int d = 8 * r4 + 4 * hi, k = 28 * (4 * qb + (c >> 3)) + d;
        if (d < 28)
          *(u32x2*)(lds + K2_OT + ((k >> 3) * 8 + (c & 7)) * 16 + (k & 7) * 2) =
              (u32x2){cvtpk(o0[4 * r4] * il, o0[4 * r4 + 1] * il), cvtpk(o0[4 * r4 + 2] * il, o0[4 * r4 + 3] * il)};
      }
    }
    STAMP(1, 1);
    LBAR();
    STAMP(1, 2);
    LDWO(16, 39);
    xv = *(const u32x4*)(x + (size_t)(tg * 8 + w) * D + (lane < 56 ? lane : 55) * 8);
    SBAR();
    {
      bf16x8 bo[7];
#pragma unroll
      for (int k = 0; k < 7; ++k) bo[k] = BOF(k);
#pragma unroll
      for (int i = 0; i < 16; ++i) wf[i / 7][i % 7] = *(const bf16x8*)(lds + K2_RING + (i >> 3) * 32768 + w * 8192 + (i & 7) * 1024 + lane * 16);
      f32x4 acc[7];
#pragma unroll
      for (int n = 0; n < 7; ++n) acc[n] = (f32x4){0.f, 0.f, 0.f, 0.f};
#pragma unroll
      for (int i = 0; i < 39; ++i) acc[i / 7] = MFMA16(wf[i / 7][i % 7], bo[i % 7], acc[i / 7]);
      HBST(acc);
    }
  } else {
    const int hw = w - 4;
    __builtin_amdgcn_s_setprio(3);
    const unsigned ring = (unsigned)(uintptr_t)lds + K2_RING + hw * 8192;
    const u16* kp = Kf + (size_t)hw * 2048;
    const u16* vp = Vf + (size_t)hw * 2048;
    const unsigned l16 = lane * 16;
#define DMAS(s_) do { const unsigned sl_ = ring + ((s_) & 3) * 32768; \
    _Pragma("unroll") for (int u_ = 0; u_ < 2; ++u_) { \
      glds16s(kp + (s_) * 8192 + u_ * 1024, l16, sl_ + u_ * 4096); glds16s(kp + (s_) * 8192 + u_ * 1024 + 512, l16, sl_ + u_ * 4096 + 1024); \
      glds16s(vp + (s_) * 8192 + u_ * 1024, l16, sl_ + u_ * 4096 + 2048); glds16s(vp + (s_) * 8192 + u_ * 1024 + 512, l16, sl_ + u_ * 4096 + 3072); } } while (0)
    const unsigned dum = (unsigned)(uintptr_t)lds + K2_DUM + hw * 1024;
    const int xg = tg & 7, rk = tg >> 3;
    const int ts = xg < 4 ? 25 * xg : 100 + 24 * (xg - 4), te = ts + (xg < 4 ? 25 : 24);
    const int n0 = ts / 7, n1 = (te - 1) / 7;
    const int nb0 = n0 + 3 <= 27 ? n0 : 24;
    const u16* p1 = w1 + (size_t)(64 * rk + 16 * hw) * F + nb0 * 64;
    const u16* p1b = w1 + (size_t)(64 * rk + 16 * hw) * F + n1 * 64;
    const u16* p2 = w2 + (size_t)(56 * rk + 14 * hw) * S + 4 * xg * 64;
    const unsigned o1 = ((lane >> 5) * F + (lane & 31) * 8) * 2, o1b = ((lane >> 3) * F + (lane & 7) * 8) * 2, o2 = ((lane >> 5) * S + (lane & 31) * 8) * 2;
#define PFD(i0, i1) do { _Pragma("unroll") for (int i_ = i0; i_ < i1; ++i_) { \
      if (i_ < 8) glds16s(p1 + (size_t)i_ * 2 * F, o1, dum); \
      else if (i_ < 10) glds16s(p1b + (size_t)(i_ - 8) * 8 * F, o1b, dum); \
      else glds16s(p2 + (size_t)(i_ - 10) * 2 * S, o2, dum); } } while (0)
    DMAS(0); DMAS(1); DMAS(2);
    waitv<16>(); RBAR();
    LDWO(0, 12); SBAR(); PFD(0, 3);
    waitv<23>(); RBAR();
    DMAS(3); LDWO(12, 24); SBAR(); PFD(3, 6);
    waitv<38>(); RBAR();
    DMAS(4); LDWO(24, 36); SBAR(); PFD(6, 9);
    waitv<38>(); RBAR();
    DMAS(5); LDWO(36, 49); SBAR(); PFD(9, 12);
    waitv<39>(); RBAR();
    bf16x8 wx[10];
    DMAS(6);
#pragma unroll
    for (int i = 0; i < 10; ++i) wx[i] = *(const bf16x8*)(wob - 7 * 1024 + (((39 + i) / 7) * 14 + (39 + i) % 7) * 1024 + wl);
    SBAR(); PFD(12, 15);
    waitv<37>(); RBAR();
    DMAS(7); PFD(15, 17);
    glds4(outp + (size_t)(lane < 56 ? lane : 55) * 16384 + (tg & 31) * 512, dum);
    waitv<24>(); RBAR();
#pragma unroll
    for (int i = 0; i < 8; ++i) glds16s(wob - 7 * 1024 + ((i / 7) * 14 + i % 7) * 1024, wl, ring + i * 1024);
    waitv<11>(); RBAR();
#pragma unroll
    for (int i = 8; i < 16; ++i) glds16s(wob - 7 * 1024 + ((i / 7) * 14 + i % 7) * 1024, wl, ring + 32768 + (i - 8) * 1024);
    xv = *(const u32x4*)(x + (size_t)(tg * 8 + w) * D + (lane < 56 ? lane : 55) * 8);
    SBAR();
    __builtin_amdgcn_s_setprio(0);
    RBAR();
    waitv<1>();
    STAMP(1, 1);
    LBAR();
    STAMP(1, 2);
    {
      f32x4 acc[7];
      {
        bf16x8 bo[7];
#pragma unroll
        for (int k = 0; k < 7; ++k) bo[k] = BOF(k);
        acc[5] = (f32x4){0.f, 0.f, 0.f, 0.f}; acc[6] = acc[5];
#pragma unroll
        for (int i = 0; i < 10; ++i) acc[(39 + i) / 7] = MFMA16(wx[i], bo[(39 + i) % 7], acc[(39 + i) / 7]);
      }
      SBAR();
      bf16x8 bo[7];
#pragma unroll
      for (int k = 0; k < 7; ++k) bo[k] = BOF(7 + k);
#pragma unroll
      for (int n = 0; n < 7; ++n) {
        if (n < 5) acc[n] = (f32x4){0.f, 0.f, 0.f, 0.f};
#pragma unroll
        for (int k = 0; k < 7; ++k) acc[n] = MFMA16(wf[n][k], bo[k], acc[n]);
      }
      HBST(acc);
    }
  }
  LBAR();
  STAMP(1, 3);
  {
    const int s = tg * 8 + w;
    const bool act = lane < 56;
    const int lc = act ? lane : 55;
    float v[8];
    const f32x4 a0 = *(const f32x4*)(lds + K2_HB + (w * HBS + lc * 8) * 4), a1 = *(const f32x4*)(lds + K2_HB + (w * HBS + lc * 8 + 4) * 4);
    const f32x4 c0 = *(const f32x4*)(lds + K2_HB + ((8 + w) * HBS + lc * 8) * 4), c1 = *(const f32x4*)(lds + K2_HB + ((8 + w) * HBS + lc * 8 + 4) * 4);
    u32x4 hv;
#pragma unroll
    for (int i = 0; i < 2; ++i) {
      hv[i] = cvtpk(bflo(xv[i]) + a0[2 * i] + c0[2 * i], bfhi(xv[i]) + a0[2 * i + 1] + c0[2 * i + 1]);
      hv[2 + i] = cvtpk(bflo(xv[2 + i]) + a1[2 * i] + c1[2 * i], bfhi(xv[2 + i]) + a1[2 * i + 1] + c1[2 * i + 1]);
    }
    if (act) st16c(H + (size_t)s * D + lane * 8, hv);
#pragma unroll
    for (int i = 0; i < 4; ++i) { v[2 * i] = act ? bflo(hv[i]) : 0.f; v[2 * i + 1] = act ? bfhi(hv[i]) : 0.f; }
#pragma unroll
    for (int pass = 0; pass < 2; ++pass) {
      float sm = 0.f;
#pragma unroll
      for (int i = 0; i < 8; ++i) sm += v[i];
      const float mean = wave_sum(sm) * (1.f / D);
      float q = 0.f;
#pragma unroll
      for (int i = 0; i < 8; ++i) { v[i] = act ? v[i] - mean : 0.f; q += v[i] * v[i]; }
      const float rs = __builtin_amdgcn_rsqf(wave_sum(q) * (1.f / D) + LN_EPS);
#pragma unroll
      for (int i = 0; i < 8; ++i) v[i] *= rs;
    }
    if (act) *(u32x4*)(lds + K2_HS + w * 912 + lane * 16) = (u32x4){cvtpk(v[0], v[1]), cvtpk(v[2], v[3]), cvtpk(v[4], v[5]), cvtpk(v[6], v[7])};
  }
  LBAR();
  if (tid < D / 2) {
    unsigned r[8];
#pragma unroll
    for (int i = 0; i < 8; ++i) r[i] = *(const unsigned*)(lds + K2_HS + i * 912 + tid * 4);
    u32x4 lo, hi2;
#pragma unroll
    for (int i = 0; i < 4; ++i) {
      lo[i] = (r[2 * i] & 0xffffu) | (r[2 * i + 1] << 16);
      hi2[i] = (r[2 * i] >> 16) | (r[2 * i + 1] & 0xffff0000u);
    }
    u32x4* dst = (u32x4*)(HN + ((size_t)tg * D + 2 * tid) * 8);
    st16c(dst, lo); st16c(dst + 1, hi2);
  }
  STAMP(1, 4);
}

constexpr int G_RING = 4, G_LDS = 8 * G_RING * 4096;
typedef __attribute__((address_space(1))) unsigned gu32;
typedef __attribute__((address_space(1))) unsigned long long gu64;
#define RLX_AGENT __ATOMIC_RELAXED, __HIP_MEMORY_SCOPE_AGENT
template <int OFF> DI void gload16c(bf16x8& d, const void* p) {
  asm volatile("global_load_dwordx4 %0, %1, off offset:%2 sc1" : "=v"(d) : "v"(p), "i"(OFF) : "memory");
}
template <int PH, int NSH>
DI void mlp_tile(char* lds, int t, const u16* act, const u16* __restrict__ W, const u16* __restrict__ bias, const u16* H, u16* out,
                 unsigned* cnt, unsigned want) {
  constexpr int KT = PH == 0 ? S : F, LDW = PH == 0 ? F : S, NK = KT / 256, NR = NK - G_RING, LA = 5;
  const int tid = threadIdx.x, lane = tid & 63, w = __builtin_amdgcn_readfirstlane(tid >> 6);
  const int nt = t / 7, mt = t - nt * 7;
  const unsigned lds0 = (unsigned)(uintptr_t)lds + w * (G_RING * 4096);
  const int g = lane >> 4, q4 = (lane & 15) >> 2, p = lane & 3, l15 = lane & 15;
  STAMP(2 + PH, 0);
  const int enb = w >> 1, emb = 2 * (w & 1);
  u32x2 eb;
  if (PH == 0) eb = *(const u32x2*)(bias + 64 * nt + 16 * enb + 4 * g);
  else eb[0] = bias[64 * nt + 16 * enb + l15];
  SBAR();
  const u16* asrc = act + ((size_t)(4 * w + g) * D + 64 * mt + l15) * 8;
  const int r8 = lane >> 3, fe = (r8 >> 1) & 1;
  const u16* wsrc0 = W + (size_t)(32 * w + r8) * LDW + 64 * nt + ((lane & 7) ^ (fe << 1)) * 8;
  const u16* wsrc1 = W + (size_t)(32 * w + 8 + r8) * LDW + 64 * nt + ((lane & 7) ^ ((fe | 2) << 1)) * 8;
  const int f = ((q4 >> 1) & 1) | ((g & 1) << 1);
  int woff[4];
#pragma unroll
  for (int nb = 0; nb < 4; ++nb) woff[nb] = w * (G_RING * 4096) + (8 * g + q4) * 128 + (((2 * nb + (p >> 1)) ^ (f << 1)) * 16) + 8 * (p & 1);
  bf16x8 af[NK][4];
  f32x4 acc[4][4] = {};
#define STAGE_W(i) do { const unsigned sl_ = lds0 + ((i) % G_RING) * 4096; const size_t o_ = (size_t)(i) * 256 * LDW; \
    glds16(wsrc0 + o_, sl_); glds16(wsrc1 + o_, sl_ + 1024); glds16(wsrc0 + o_ + 16 * LDW, sl_ + 2048); glds16(wsrc1 + o_ + 16 * LDW, sl_ + 3072); } while (0)
#define LOAD_A(i) do { const u16* ap_ = asrc + (size_t)(i) * 32 * D * 8; \
    gload16c<0>(af[i][0], ap_); gload16c<256>(af[i][1], ap_); gload16c<512>(af[i][2], ap_); gload16c<768>(af[i][3], ap_); } while (0)
#define RDW(WF, i) do { _Pragma("unroll") for (int nb_ = 0; nb_ < 4; ++nb_) { const lds_cptr sp_ = (lds_cptr)lds + woff[nb_] + ((i) % G_RING) * 4096; \
      WF[nb_] = cat8(trread(sp_), trread(sp_ + 512)); } } while (0)
#define MMA(WF, i) do { _Pragma("unroll") for (int nb_ = 0; nb_ < 4; ++nb_) _Pragma("unroll") for (int mb_ = 0; mb_ < 4; ++mb_) \
      acc[nb_][mb_] = PH == 0 ? MFMA16(WF[nb_], af[i][mb_], acc[nb_][mb_]) : MFMA16(af[i][mb_], WF[nb_], acc[nb_][mb_]); } while (0)
  bf16x8 wr[NR][4];
#pragma unroll
  for (int j = 0; j < NR; ++j) STAGE_W(G_RING + j);
  if (NR < G_RING) STAGE_W(G_RING - 1);
#pragma unroll
  for (int j = 0; j < NR; ++j) {
    waitv<12>();
    RDW(wr[j], G_RING + j);
    asm volatile("s_waitcnt lgkmcnt(0)" ::: "memory");
    STAGE_W(j);
  }
  STAMP(2 + PH, 1);
  if (w == 0) {
    gu32* cp = (gu32*)(cnt + (lane < NSH ? lane : 0) * 64);
    for (unsigned spins = 0; spins < (1u << 20); ++spins) {
      if (__all(__hip_atomic_load(cp, RLX_AGENT) >= want)) break;
      __builtin_amdgcn_s_sleep(1);
    }
  }
  asm volatile("s_barrier" ::: "memory");
  STAMP(2 + PH, 2);
  unsigned long long eh0 = 0, eh1 = 0;
  if (PH == 1) {
    const int s = 64 * nt + 16 * enb + l15;
    eh0 = __hip_atomic_load((gu64*)(H + (size_t)s * D + 64 * mt + 16 * emb + 4 * g), RLX_AGENT);
    eh1 = __hip_atomic_load((gu64*)(H + (size_t)s * D + 64 * mt + 16 * emb + 16 + 4 * g), RLX_AGENT);
  }
  SBAR();
#pragma unroll
  for (int j = 0; j < LA; ++j) LOAD_A((j + G_RING) % NK);
#pragma unroll
  for (int j = 0; j < NK; ++j) {
    const int i = (j + G_RING) % NK;
    const int rem = NK - 1 - j < LA - 1 ? NK - 1 - j : LA - 1;
    if (rem == 4) waitv4<16>(af[i][0], af[i][1], af[i][2], af[i][3]);
    else if (rem == 3) waitv4<12>(af[i][0], af[i][1], af[i][2], af[i][3]);
    else if (rem == 2) waitv4<8>(af[i][0], af[i][1], af[i][2], af[i][3]);
    else if (rem == 1) waitv4<4>(af[i][0], af[i][1], af[i][2], af[i][3]);
    else waitv4<0>(af[i][0], af[i][1], af[i][2], af[i][3]);
    if (j < NR) {
      MMA(wr[j], i);
    } else {
      bf16x8 wf[4];
      RDW(wf, i);
      MMA(wf, i);
    }
    SBAR();
    if (j + LA < NK) LOAD_A((j + LA + G_RING) % NK);
  }
#undef STAGE_W
#undef LOAD_A
#undef RDW
#undef MMA
  STAMP(2 + PH, 4);
  {
    f32x4* xb = (f32x4*)lds;
#pragma unroll
    for (int nb = 0; nb < 4; ++nb)
#pragma unroll
      for (int mb = 0; mb < 4; ++mb) xb[(w * 16 + nb * 4 + mb) * 64 + lane] = acc[nb][mb];
  }
  LBAR();
  {
    const f32x4* xb = (const f32x4*)lds;
#pragma unroll
    for (int e = 0; e < 2; ++e) {
      f32x4 a = xb[(2 * w + e) * 64 + lane];
#pragma unroll
      for (int v = 1; v < 8; ++v) a += xb[(v * 16 + 2 * w + e) * 64 + lane];
      const int mb = emb + e;
      if (PH == 0) {
        const int fi = 64 * nt + 16 * enb + 4 * g, d = 64 * mt + 16 * mb + l15;
        float z[4] = {a[0] + bflo(eb[0]), a[1] + bfhi(eb[0]), a[2] + bflo(eb[1]), a[3] + bfhi(eb[1])};
#pragma unroll
        for (int i = 0; i < 4; ++i) {
          const float zz = z[i], a2 = zz * (1.f + 0.044715f * zz * zz) * (-2.f * 0.7978845608028654f * 1.4426950408889634f);
          z[i] = zz * __builtin_amdgcn_rcpf(1.f + __builtin_amdgcn_exp2f(a2));
        }
        const unsigned long long v = (unsigned long long)cvtpk(z[0], z[1]) | ((unsigned long long)cvtpk(z[2], z[3]) << 32);
        __hip_atomic_store((gu64*)(out + ((size_t)(fi >> 3) * D + d) * 8 + (fi & 7)), v, RLX_AGENT);
      } else {
        const int d = 64 * mt + 16 * mb + 4 * g, s = 64 * nt + 16 * enb + l15;
        const float bb = bflo(eb[0]);
        const unsigned long long hv = e ? eh1 : eh0;
        const unsigned h0 = (unsigned)hv, h1 = (unsigned)(hv >> 32);
        *(u32x2*)(out + (size_t)s * D + d) =
            (u32x2){cvtpk(a[0] + bb + bflo(h0), a[1] + bb + bfhi(h0)), cvtpk(a[2] + bb + bflo(h1), a[3] + bb + bfhi(h1))};
      }
    }
  }
  STAMP(2 + PH, 5);
}
#define PUBLISH(c) do { asm volatile("s_waitcnt vmcnt(0) lgkmcnt(0)\n\ts_barrier" ::: "memory"); \
    if (threadIdx.x == 0) __hip_atomic_fetch_add((gu32*)(c), 1u, RLX_AGENT); } while (0)

extern "C" __global__ __launch_bounds__(512) void k_main(const u16* __restrict__ x, const u16* __restrict__ Qf, const u16* __restrict__ Kf,
                                              const u16* __restrict__ Vf, const u16* __restrict__ Wof, const u16* __restrict__ w1,
                                              const u16* __restrict__ b1, const u16* __restrict__ w2, const u16* __restrict__ b2,
                                              u16* H, u16* HN, u16* U, u16* out, unsigned* cnt) {
  __shared__ __attribute__((aligned(16))) char lds[K2_LDS];
  attn_phase(lds, x, Qf, Kf, Vf, Wof, w1, w2, H, HN, out);
  PUBLISH(cnt + (8 + (blockIdx.x & 7)) * 64);
  if (blockIdx.x < 196) {
    const int t = xcd_remap(blockIdx.x, 196);
    mlp_tile<0, 8>(lds, t, HN, w1, b1, H, U, cnt + 8 * 64, 32u);
    PUBLISH(cnt + (t % 7) * 64);
  }
  if (blockIdx.x < 224) {
    const int t2 = xcd_remap(blockIdx.x, 224);
    mlp_tile<1, 1>(lds, t2, U, w2, b2, H, out, cnt + (t2 % 7) * 64, 28u);
  }
}

extern "C" void kernel_launch(void* const* d_in, const int* in_sizes, int n_in, void* d_out, int out_size, void* d_ws, size_t ws_size,
                              hipStream_t stream) {
  const u16* x = (const u16*)d_in[0];
  const u16* wq = (const u16*)d_in[1];
  const u16* wk = (const u16*)d_in[2];
  const u16* wv = (const u16*)d_in[3];
  const u16* wo = (const u16*)d_in[4];
  const u16* w1 = (const u16*)d_in[6];
  const u16* b1 = (const u16*)d_in[7];
  const u16* w2 = (const u16*)d_in[8];
  const u16* b2 = (const u16*)d_in[9];
  char* ws = (char*)d_ws;
  u16 *Qf = (u16*)(ws + WS_QF), *Kf = (u16*)(ws + WS_KF), *Vf = (u16*)(ws + WS_VF), *Wof = (u16*)(ws + WS_WOF);
  u16 *H = (u16*)(ws + WS_H), *HN = (u16*)(ws + WS_HN), *U = (u16*)(ws + WS_U);
  unsigned* cnt = (unsigned*)(ws + WS_CNT);
  k_qkv<<<256, 512, 0, stream>>>(x, wq, wk, wv, wo, Qf, Kf, Vf, Wof, cnt);
  k_main<<<256, 512, 0, stream>>>(x, Qf, Kf, Vf, Wof, w1, b1, w2, b2, H, HN, U, (u16*)d_out, cnt);
}
```

```cpp
#include <hip/hip_runtime.h>
#include <hip/hip_bf16.h>

typedef unsigned short u16;
using bf16x8 = __attribute__((ext_vector_type(8))) short;
using s16x4 = __attribute__((ext_vector_type(4))) short;
using f32x4 = __attribute__((ext_vector_type(4))) float;
using f32x16 = __attribute__((ext_vector_type(16))) float;
using u32x4 = __attribute__((ext_vector_type(4))) unsigned;
using u32x2 = __attribute__((ext_vector_type(2))) unsigned;
typedef short v4i16_t __attribute__((ext_vector_type(4)));
typedef __attribute__((address_space(3))) const char* lds_cptr;
typedef __bf16 bf16x2_t __attribute__((ext_vector_type(2)));
typedef float f32x2_t __attribute__((ext_vector_type(2)));

#define DI __device__ __forceinline__
#define MFMA32(a, b, c) __builtin_amdgcn_mfma_f32_32x32x16_bf16(a, b, c, 0, 0, 0)
#define MFMA16(a, b, c) __builtin_amdgcn_mfma_f32_16x16x32_bf16(a, b, c, 0, 0, 0)
#define SBAR() __builtin_amdgcn_sched_barrier(0)
#define LBAR() asm volatile("s_waitcnt lgkmcnt(0)\n\ts_barrier" ::: "memory")
#define WAITV_BAR(N) asm volatile("s_waitcnt vmcnt(" #N ") lgkmcnt(0)\n\ts_barrier" ::: "memory")

constexpr int S = 2048, D = 448, DH = 28, F = 1792;
constexpr float QSCALE = 0.18898223650461362f * 1.4426950408889634f;
constexpr float LN_EPS = 1e-5f;

constexpr size_t WS_QF = 0;
constexpr size_t WS_KF = WS_QF + 2097152;
constexpr size_t WS_VF = WS_KF + 131072;
constexpr size_t WS_WOF = WS_VF + 131072;
constexpr size_t WS_H = WS_WOF + 401408;
constexpr size_t WS_HN = WS_H + 1835008;
constexpr size_t WS_U = WS_HN + 1835008;
constexpr size_t WS_CNT = WS_U + 1605632;

#define STAMP(k, i)
#define CSTAMP(k, i)

DI unsigned cvtpk(float lo, float hi) {
  const f32x2_t v = {lo, hi};
  return __builtin_bit_cast(unsigned, __builtin_convertvector(v, bf16x2_t));
}
DI float bflo(unsigned u) { return __uint_as_float(u << 16); }
DI float bfhi(unsigned u) { return __uint_as_float(u & 0xffff0000u); }
template <int CTRL> DI float dppf(float v) {
  return __uint_as_float(__builtin_amdgcn_update_dpp(0u, __float_as_uint(v), CTRL, 0xf, 0xf, true));
}
DI float wave_sum(float v) {
  v += dppf<0xB1>(v);
  v += dppf<0x4E>(v);
  v += dppf<0x141>(v);
  v += dppf<0x140>(v);
  auto a = __builtin_amdgcn_permlane16_swap(__float_as_uint(v), __float_as_uint(v), false, false);
  v = __uint_as_float(a[0]) + __uint_as_float(a[1]);
  auto b = __builtin_amdgcn_permlane32_swap(__float_as_uint(v), __float_as_uint(v), false, false);
  return __uint_as_float(b[0]) + __uint_as_float(b[1]);
}
DI float half_max(float v) {
  auto b = __builtin_amdgcn_permlane32_swap(__float_as_uint(v), __float_as_uint(v), false, false);
  return __builtin_fmaxf(__uint_as_float(b[0]), __uint_as_float(b[1]));
}
DI void glds16(const void* g, unsigned lds_base) {
  unsigned sv;
  asm volatile("s_mov_b32 %0, m0\n\ts_mov_b32 m0, %2\n\ts_nop 0\n\tglobal_load_lds_dwordx4 %1, off\n\ts_mov_b32 m0, %0"
               : "=&s"(sv) : "v"(g), "s"(lds_base) : "memory");
}
DI void glds16s(const void* sbase, unsigned voff, unsigned lds_base) {
  unsigned sv;
  asm volatile("s_mov_b32 %0, m0\n\ts_mov_b32 m0, %3\n\ts_nop 0\n\tglobal_load_lds_dwordx4 %1, %2\n\ts_mov_b32 m0, %0"
               : "=&s"(sv) : "v"(voff), "s"(sbase), "s"(lds_base) : "memory");
}
DI void glds4(const void* g, unsigned lds_base) {
  unsigned sv;
  asm volatile("s_mov_b32 %0, m0\n\ts_mov_b32 m0, %2\n\ts_nop 0\n\tglobal_load_lds_dword %1, off\n\ts_mov_b32 m0, %0"
               : "=&s"(sv) : "v"(g), "s"(lds_base) : "memory");
}
template <int N> DI void waitv4(bf16x8& a, bf16x8& b, bf16x8& c, bf16x8& d) {
  asm volatile("s_waitcnt vmcnt(%4)" : "+v"(a), "+v"(b), "+v"(c), "+v"(d) : "i"(N) : "memory");
}
template <int OFF> DI void gload16(bf16x8& d, const void* p) {
  asm volatile("global_load_dwordx4 %0, %1, off offset:%2" : "=v"(d) : "v"(p), "i"(OFF) : "memory");
}
DI s16x4 trread(lds_cptr p) {
  return __builtin_bit_cast(s16x4, __builtin_amdgcn_ds_read_tr16_b64_v4i16((__attribute__((address_space(3))) v4i16_t*)p));
}
DI bf16x8 ldsr16(lds_cptr p) { return *(const __attribute__((address_space(3))) bf16x8*)p; }
DI bf16x8 cat8(s16x4 a, s16x4 b) { return __builtin_shufflevector(a, b, 0, 1, 2, 3, 4, 5, 6, 7); }
DI int xcd_remap(int bid, int nwg) {
  const int q = nwg >> 3, r = nwg & 7, x = bid & 7, o = bid >> 3;
  return (x < r ? x * (q + 1) : r * (q + 1) + (x - r) * q) + o;
}

constexpr int K1_WI = 448 * 128, K1_X = 2 * K1_WI, K1_XS = 528, K1_DUM = K1_X + 64 * K1_XS, K1_LDS = K1_DUM + 1024;
constexpr int KMAIN_KB = 28;
__global__ __launch_bounds__(512) void k_qkv(const u16* __restrict__ x, const u16* __restrict__ wq, const u16* __restrict__ wk,
                                             const u16* __restrict__ wv, const u16* __restrict__ wo, u16* __restrict__ Qf,
                                             u16* __restrict__ Kf, u16* __restrict__ Vf, u16* __restrict__ Wof, unsigned* __restrict__ cnt) {
  __shared__ __attribute__((aligned(16))) char lds[K1_LDS];
  const int tid = threadIdx.x, lane = tid & 63, w = __builtin_amdgcn_readfirstlane(tid >> 6);
  const int t = xcd_remap(blockIdx.x, 256), rt = t >> 2, ct = t & 3;
  const unsigned lds0 = (unsigned)(uintptr_t)lds;
  STAMP(0, 0);
  bf16x8 xr[4];
  const bool act = lane < 56;
  {
    const u16* xp_ = x + (size_t)(rt * 32 + w * 4) * D + (act ? lane : 55) * 8;
    gload16<0>(xr[0], xp_); gload16<896>(xr[1], xp_); gload16<1792>(xr[2], xp_); gload16<2688>(xr[3], xp_);
  }
#pragma unroll
  for (int im = 0; im < 2; ++im) {
    const int c64 = 2 * ct + im;
    if (c64 < 7) {
#pragma unroll
      for (int i = 0; i < 7; ++i) {
        const int piece = w * 7 + i, row = piece * 8 + (lane >> 3);
        const int f = ((row >> 1) & 1) | (((row >> 3) & 1) << 1);
        const int c = (lane & 7) ^ (f << 1);
        glds16(wq + (size_t)row * D + c64 * 64 + c * 8, lds0 + im * K1_WI + piece * 1024);
      }
    } else {
#pragma unroll
      for (int i = 0; i < 7; ++i) {
        const int pc = w * 7 + i < 50 ? w * 7 + i : 49, mat = pc >= 25, piece = pc - 25 * mat;
        const int e = piece * 512 + lane * 8;
        glds16((mat ? wv : wk) + (e < 448 * 28 ? e : 448 * 28 - 8), lds0 + im * K1_WI + mat * 25600 + piece * 1024);
      }
    }
  }
  u16 wt[8];
  const int frag0 = blockIdx.x + 256 * (w & 1);
  const bool dofrag = w < 2 && frag0 < 392;
  const int frag = dofrag ? frag0 : 0;
  {
    const int nb = frag / 14, ks = frag - nb * 14;
#pragma unroll
    for (int j = 0; j < 8; ++j) wt[j] = wo[(size_t)(32 * ks + 8 * (lane >> 4) + j) * D + 16 * nb + (lane & 15)];
  }
  SBAR();
  waitv4<22>(xr[0], xr[1], xr[2], xr[3]);
  {
    u32x4 xv[4];
#pragma unroll
    for (int r = 0; r < 4; ++r) xv[r] = __builtin_bit_cast(u32x4, xr[r]);
    float v[4][8], mean[4], rs[4];
#pragma unroll
    for (int r = 0; r < 4; ++r) {
      float s = 0.f;
#pragma unroll
      for (int i = 0; i < 4; ++i) {
        v[r][2 * i] = act ? bflo(xv[r][i]) : 0.f; v[r][2 * i + 1] = act ? bfhi(xv[r][i]) : 0.f;
        s += v[r][2 * i] + v[r][2 * i + 1];
      }
      mean[r] = s;
    }
#pragma unroll
    for (int r = 0; r < 4; ++r) mean[r] = wave_sum(mean[r]) * (1.f / D);
#pragma unroll
    for (int r = 0; r < 4; ++r) {
      float q = 0.f;
#pragma unroll
      for (int i = 0; i < 8; ++i) { v[r][i] = act ? v[r][i] - mean[r] : 0.f; q += v[r][i] * v[r][i]; }
      rs[r] = q;
    }
#pragma unroll
    for (int r = 0; r < 4; ++r) rs[r] = __builtin_amdgcn_rsqf(wave_sum(rs[r]) * (1.f / D) + LN_EPS);
    char* xdst = lds + K1_X + lane * K1_XS + w * 64;
#pragma unroll
    for (int r = 0; r < 4; ++r) {
      u32x4 o;
#pragma unroll
      for (int i = 0; i < 4; ++i) o[i] = cvtpk(v[r][2 * i] * rs[r], v[r][2 * i + 1] * rs[r]);
      *(u32x4*)(xdst + r * 16) = o;
    }
  }
  STAMP(0, 1);
  WAITV_BAR(0);
  STAMP(0, 2);
  if (w == 7 && (blockIdx.x >> 3) < KMAIN_KB) {
    unsigned long long pc;
    int off;
    asm volatile("s_getpc_b64 %0\n1:\n\ts_mov_b32 %1, k_main-1b" : "=s"(pc), "=s"(off));
    glds16s((const char*)(pc + (long long)off) + (blockIdx.x >> 3) * 1024, lane * 16, lds0 + K1_DUM);
  }
  const int nb = w & 3, im = w >> 2, c64 = 2 * ct + im, g = lane >> 4, q4 = (lane & 15) >> 2, p = lane & 3, l15 = lane & 15;
  const int f = ((q4 >> 1) & 1) | ((g & 1) << 1);
  const bool kv = c64 == 7;
  const int rs_ = kv ? 56 : 128;
  const lds_cptr wp = (lds_cptr)lds + im * K1_WI + (8 * g + q4) * rs_ +
                      (kv ? (nb >> 1) * 25600 + (nb & 1) * 32 + 8 * p : (((2 * nb + (p >> 1)) ^ (f << 1)) * 16) + 8 * (p & 1));
  const lds_cptr xp = (lds_cptr)lds + K1_X + g * K1_XS + l15 * 16;
  f32x4 acc0 = {0.f, 0.f, 0.f, 0.f}, acc1 = {0.f, 0.f, 0.f, 0.f};
  {
    bf16x8 a[14], b0[14], b1[14];
#pragma unroll
    for (int ks = 0; ks < 14; ++ks) {
      a[ks] = cat8(trread(wp + ks * 32 * rs_), trread(wp + ks * 32 * rs_ + 4 * rs_));
      b0[ks] = ldsr16(xp + ks * 4 * K1_XS);
      b1[ks] = ldsr16(xp + ks * 4 * K1_XS + 256);
    }
    SBAR();
#pragma unroll
    for (int ks = 0; ks < 14; ++ks) {
      acc0 = MFMA16(a[ks], b0[ks], acc0);
      acc1 = MFMA16(a[ks], b1[ks], acc1);
    }
  }
  STAMP(0, 3);
#pragma unroll
  for (int tb = 0; tb < 2; ++tb) {
    const f32x4 a = tb ? acc1 : acc0;
    const int s = rt * 32 + 16 * tb + l15;
    const int nl = 16 * nb + 4 * g;
    if (c64 < 7) {
      const int n0 = 64 * c64 + nl, h = n0 / 28, d0 = n0 - 28 * h;
      const int tg = s >> 3, c = (h & 3) * 8 + (s & 7), b = h >> 2;
      const size_t off = ((((size_t)(tg * 4 + b) * 2 + (d0 >> 4)) * 64) + ((d0 >> 3) & 1) * 32 + c) * 8 + (d0 & 7);
      *(u32x2*)(Qf + off) = (u32x2){cvtpk(a[0] * QSCALE, a[1] * QSCALE), cvtpk(a[2] * QSCALE, a[3] * QSCALE)};
    } else {
      const int kt = s >> 5, kk = s & 31;
      const int d0 = 16 * (nb & 1) + 4 * g;
      if (nb < 2) {
        const size_t off = ((((size_t)kt * 2 + (d0 >> 4)) * 64) + ((d0 >> 3) & 1) * 32 + kk) * 8 + (d0 & 7);
        u32x2 v = {cvtpk(a[0], a[1]), cvtpk(a[2], a[3])};
        if (d0 == 28) v = (u32x2){0x3f803f80u, 0x00003f80u};
        *(u32x2*)(Kf + off) = v;
      } else {
        const int sp = kk >> 4, j = ((kk >> 3) & 1) * 4 + (kk & 3), hi = (kk >> 2) & 1;
        const size_t off = ((((size_t)kt * 2 + sp) * 64) + hi * 32 + d0) * 8 + j;
        unsigned v0 = cvtpk(a[0], a[1]), v1 = cvtpk(a[2], a[3]);
        if (d0 == 28) { v0 = 0x00003f80u; v1 = 0u; }
        Vf[off] = (u16)v0; Vf[off + 8] = (u16)(v0 >> 16); Vf[off + 16] = (u16)v1; Vf[off + 24] = (u16)(v1 >> 16);
      }
    }
  }
  if (dofrag) {
    u32x4 o;
#pragma unroll
    for (int i = 0; i < 4; ++i) o[i] = (unsigned)wt[2 * i] | ((unsigned)wt[2 * i + 1] << 16);
    *(u32x4*)(Wof + ((size_t)frag * 64 + lane) * 8) = o;
  }
  if (blockIdx.x == 0) { cnt[tid] = 0u; cnt[512 + tid] = 0u; }
  if (w == 7) asm volatile("s_waitcnt vmcnt(0)" ::: "memory");
  STAMP(0, 4);
}

constexpr int K2_RING = 0, K2_OT = 131072, K2_DUM = K2_OT + 7168, K2_X = K2_DUM + 4096, K2_LDS = K2_X + 16384;
constexpr int K2_HB = 65536, HBS = 452, K2_HS = K2_X;

DI float set_ref(bf16x8& q1, float m, int hi) {
  const float a = bflo(cvtpk(-m, 0.f)), r1 = -m - a, b = bflo(cvtpk(r1, 0.f)), c = bflo(cvtpk(r1 - b, 0.f));
  u32x4 v = __builtin_bit_cast(u32x4, q1);
  v[2] = hi ? cvtpk(a, b) : v[2];
  v[3] = hi ? cvtpk(c, 0.f) : v[3];
  q1 = __builtin_bit_cast(bf16x8, v);
  return -((a + b) + c);
}
DI float tile_max(const f32x16& s) {
  float a = __builtin_fmaxf(__builtin_fmaxf(s[0], s[1]), s[2]), b = __builtin_fmaxf(__builtin_fmaxf(s[3], s[4]), s[5]);
  a = __builtin_fmaxf(__builtin_fmaxf(a, s[6]), s[7]); b = __builtin_fmaxf(__builtin_fmaxf(b, s[8]), s[9]);
  a = __builtin_fmaxf(__builtin_fmaxf(a, s[10]), s[11]); b = __builtin_fmaxf(__builtin_fmaxf(b, s[12]), s[13]);
  a = __builtin_fmaxf(__builtin_fmaxf(a, s[14]), s[15]);
  return half_max(__builtin_fmaxf(a, b));
}
template <int N> DI void waitv() { asm volatile("s_waitcnt vmcnt(%0)" ::"i"(N) : "memory"); }
#define RBAR() asm volatile("s_barrier" ::: "memory")

DI void attn_safe(const u16* __restrict__ Kf, const u16* __restrict__ Vf, bf16x8 q0, bf16x8 q1, f32x16& o, int lane, int hi) {
  const f32x16 zero = {};
  float mref = 0.f;
  o = zero;
#pragma unroll 1
  for (int t = 0; t < 64; ++t) {
    const bf16x8 k0 = *(const bf16x8*)(Kf + t * 1024 + lane * 8), k1 = *(const bf16x8*)(Kf + t * 1024 + 512 + lane * 8);
    const bf16x8 v0 = *(const bf16x8*)(Vf + t * 1024 + lane * 8), v1 = *(const bf16x8*)(Vf + t * 1024 + 512 + lane * 8);
    (void)set_ref(q1, 0.f, hi);
    f32x16 s = MFMA32(k0, q0, zero);
    s = MFMA32(k1, q1, s);
    const float tm = tile_max(s);
    const float mn = set_ref(q1, t == 0 ? tm : __builtin_fmaxf(tm, mref), hi);
    const float al = t == 0 ? 0.f : __builtin_amdgcn_exp2f(mref - mn);
#pragma unroll
    for (int r = 0; r < 16; ++r) o[r] *= al;
    mref = mn;
    s = MFMA32(k0, q0, zero);
    s = MFMA32(k1, q1, s);
    u32x4 p0, p1;
#pragma unroll
    for (int i = 0; i < 4; ++i) {
      p0[i] = cvtpk(__builtin_amdgcn_exp2f(s[2 * i]), __builtin_amdgcn_exp2f(s[2 * i + 1]));
      p1[i] = cvtpk(__builtin_amdgcn_exp2f(s[8 + 2 * i]), __builtin_amdgcn_exp2f(s[8 + 2 * i + 1]));
    }
    o = MFMA32(v0, __builtin_bit_cast(bf16x8, p0), o);
    o = MFMA32(v1, __builtin_bit_cast(bf16x8, p1), o);
  }
}

#define EX4(S, i) do { S[i] = __builtin_amdgcn_exp2f(S[i]); S[i + 1] = __builtin_amdgcn_exp2f(S[i + 1]); \
    S[i + 2] = __builtin_amdgcn_exp2f(S[i + 2]); S[i + 3] = __builtin_amdgcn_exp2f(S[i + 3]); } while (0)
#define PK8(P, S, i) do { P[0] = cvtpk(S[i], S[i + 1]); P[1] = cvtpk(S[i + 2], S[i + 3]); P[2] = cvtpk(S[i + 4], S[i + 5]); P[3] = cvtpk(S[i + 6], S[i + 7]); } while (0)
#define BF(P) __builtin_bit_cast(bf16x8, P)
constexpr int CPRE = 0;
#define WOPF(j) do { if ((j) < CPRE) wf[(j) / 7][(j) % 7] = WOFR(j); } while (0)
#define RDS(u, off) ldsr16(((u) < 4 ? B0 : B1) + ((u) & 3) * 8192 + (off))
#define CTILE(u, NX, PF) do { \
      \
    sB = MFMA32(k0, qB0, zero); SBAR(); if (NX) k0 = RDS((u) + 1, 0); EX4(sA, 0); WOPF(PF); SBAR(); \
    sB = MFMA32(k1, qB1, sB); SBAR(); if (NX) k1 = RDS((u) + 1, 1024); EX4(sA, 4); PK8(p0, sA, 0); SBAR(); \
    oB = MFMA32(v1, BF(p1), oB); SBAR(); v1 = RDS(u, 3072); EX4(sA, 8); WOPF((PF) + 1); SBAR(); \
    oA = MFMA32(v0, BF(p0), oA); SBAR(); EX4(sA, 12); PK8(p1, sA, 8); SBAR(); \
      \
    sA = MFMA32(k0, qA0, zero); SBAR(); EX4(sB, 0); WOPF((PF) + 2); SBAR(); \
    sA = MFMA32(k1, qA1, sA); SBAR(); EX4(sB, 4); PK8(p0, sB, 0); SBAR(); \
    oA = MFMA32(v1, BF(p1), oA); SBAR(); EX4(sB, 8); WOPF((PF) + 3); SBAR(); \
    oB = MFMA32(v0, BF(p0), oB); SBAR(); if (NX) v0 = RDS((u) + 1, 2048); EX4(sB, 12); PK8(p1, sB, 8); SBAR(); } while (0)
#define CSTAGE(LAST, PF) do { \
    CTILE(0, true, PF); CTILE(1, true, (PF) + 4); CTILE(2, true, (PF) + 8); \
    if (!(LAST)) RBAR(); \
    CTILE(3, !(LAST), (PF) + 12); } while (0)

DI void st16c(void* p, u32x4 v) {
  asm volatile("global_store_dwordx4 %0, %1, off sc1\n\ts_nop 1" ::"v"(p), "v"(v) : "memory");
}
DI void attn_phase(char* lds, const u16* __restrict__ x, const u16* __restrict__ Qf, const u16* __restrict__ Kf,
                   const u16* __restrict__ Vf, const u16* __restrict__ Wof, const u16* __restrict__ w1,
                   const u16* __restrict__ w2, u16* H, u16* HN) {
  const int tid = threadIdx.x, lane = tid & 63, w = __builtin_amdgcn_readfirstlane(tid >> 6), hi = lane >> 5;
  const int tg = blockIdx.x;
  STAMP(1, 0);
  const int kh = w >> 2, ng = w & 3;
  const char* wob = (const char*)(Wof + (size_t)(7 * ng * 14 + 7 * kh) * 512);
  const unsigned wl = lane * 16;
  bf16x8 wf[7][7];
#define WOFR(i_) (*(const bf16x8*)(wob + (((i_) / 7) * 14 + (i_) % 7) * 1024 + wl))
#define LDWO(i0, i1) do { _Pragma("unroll") for (int i_ = i0; i_ < i1; ++i_) wf[i_ / 7][i_ % 7] = WOFR(i_); } while (0)
#define BOF(k) (*(const bf16x8*)(lds + K2_OT + ((4 * (k) + (lane >> 4)) * 8 + (lane & 7)) * 16))
#define HBST(acc) do { if ((lane & 15) < 8) { \
      _Pragma("unroll") for (int n = 0; n < 7; ++n) *(f32x4*)(lds + K2_HB + ((kh * 8 + (lane & 15)) * HBS + 16 * (7 * ng + n) + 4 * (lane >> 4)) * 4) = acc[n]; \
    } } while (0)
  u32x4 xv;
  if (w < 4) {
    const f32x16 zero = {};
    const int qh = w & 1, kp = w >> 1;
    const lds_cptr rp0 = (lds_cptr)lds + K2_RING + lane * 16;
    const lds_cptr rp = rp0 + kp * 4096;
    lds_cptr B0 = rp, B1 = rp + 32768;
    const u16* qp = Qf + ((size_t)((tg * 4 + 2 * qh) * 2) * 64 + lane) * 8;
    bf16x8 qA0 = *(const bf16x8*)(qp), qA1 = *(const bf16x8*)(qp + 512), qB0 = *(const bf16x8*)(qp + 1024), qB1 = *(const bf16x8*)(qp + 1536);
    RBAR();
    bf16x8 k0 = ldsr16(rp0), k1 = ldsr16(rp0 + 1024), v0 = RDS(0, 2048), v1 = v0;
    (void)set_ref(qA1, 0.f, hi);
    (void)set_ref(qB1, 0.f, hi);
    f32x16 sA = MFMA32(k0, qA0, zero), sB = MFMA32(k0, qB0, zero);
    sA = MFMA32(k1, qA1, sA);
    sB = MFMA32(k1, qB1, sB);
    k0 = RDS(0, 0); k1 = RDS(0, 1024);
    (void)set_ref(qA1, tile_max(sA), hi);
    (void)set_ref(qB1, tile_max(sB), hi);
    sA = MFMA32(k0, qA0, zero);
    sA = MFMA32(k1, qA1, sA);
    f32x16 oA = zero, oB = zero;
    u32x4 p0, p1 = {0u, 0u, 0u, 0u};
    SBAR();
    STAMP(1, 8);
#pragma unroll 1
    for (int st = 0; st < 4; ++st) {
      CSTAGE(false, 64);
      B0 = B1; B1 = rp + ((st + 2) & 3) * 32768;
    }
    CSTAGE(false, 0);
    B0 = B1; B1 = rp + 2 * 32768;
    CSTAGE(false, 16);
    B0 = B1; B1 = rp + 3 * 32768;
    CSTAGE(false, 64);
    B0 = B1;
    CSTAGE(true, 64);
    oB = MFMA32(v1, BF(p1), oB);
    STAMP(1, 10);
    {
      const f32x16 snd = kp ? oA : oB;
#pragma unroll
      for (int r4 = 0; r4 < 4; ++r4)
        *(f32x4*)(lds + K2_X + ((w * 4 + r4) * 64 + lane) * 16) = (f32x4){snd[4 * r4], snd[4 * r4 + 1], snd[4 * r4 + 2], snd[4 * r4 + 3]};
    }
    f32x16 o0 = kp ? oB : oA;
    bf16x8 qk0 = kp ? qB0 : qA0, qk1 = kp ? qB1 : qA1;
    asm volatile("" : "+v"(o0), "+v"(qk0), "+v"(qk1));
    SBAR();
    LBAR();
#pragma unroll
    for (int r4 = 0; r4 < 4; ++r4) {
      const f32x4 t = *(const f32x4*)(lds + K2_X + (((w ^ 2) * 4 + r4) * 64 + lane) * 16);
      o0[4 * r4] += t[0]; o0[4 * r4 + 1] += t[1]; o0[4 * r4 + 2] += t[2]; o0[4 * r4 + 3] += t[3];
    }
    const int qb = 2 * qh + kp;
    {
      float chk = 0.f;
#pragma unroll
      for (int r = 0; r < 16; ++r) chk = __builtin_fmaf(o0[r], 0.f, chk);
      const bool bad = !(chk == 0.f) || !(o0[12] < 1.8e19f);
      if (__any(bad)) {
        attn_safe(Kf, Vf, qk0, qk1, o0, lane, hi);
      }
    }
    {
      auto sw = __builtin_amdgcn_permlane32_swap(__float_as_uint(o0[12]), __float_as_uint(o0[12]), false, false);
      const float il = 1.f / __uint_as_float(sw[1]);
      const int c = lane & 31;
#pragma unroll
      for (int r4 = 0; r4 < 4; ++r4) {
        const int d = 8 * r4 + 4 * hi, k = 28 * (4 * qb + (c >> 3)) + d;
        if (d < 28)
          *(u32x2*)(lds + K2_OT + ((k >> 3) * 8 + (c & 7)) * 16 + (k & 7) * 2) =
              (u32x2){cvtpk(o0[4 * r4] * il, o0[4 * r4 + 1] * il), cvtpk(o0[4 * r4 + 2] * il, o0[4 * r4 + 3] * il)};
      }
    }
    STAMP(1, 1);
    LBAR();
    STAMP(1, 2);
    LDWO(16, 39);
    xv = *(const u32x4*)(x + (size_t)(tg * 8 + w) * D + (lane < 56 ? lane : 55) * 8);
    SBAR();
    {
      bf16x8 bo[7];
#pragma unroll
      for (int k = 0; k < 7; ++k) bo[k] = BOF(k);
#pragma unroll
      for (int i = 0; i < 16; ++i) wf[i / 7][i % 7] = *(const bf16x8*)(lds + K2_RING + (i >> 3) * 32768 + w * 8192 + (i & 7) * 1024 + lane * 16);
      f32x4 acc[7];
#pragma unroll
      for (int n = 0; n < 7; ++n) acc[n] = (f32x4){0.f, 0.f, 0.f, 0.f};
#pragma unroll
      for (int i = 0; i < 39; ++i) acc[i / 7] = MFMA16(wf[i / 7][i % 7], bo[i % 7], acc[i / 7]);
      HBST(acc);
    }
  } else {
    const int hw = w - 4;
    __builtin_amdgcn_s_setprio(3);
    const unsigned ring = (unsigned)(uintptr_t)lds + K2_RING + hw * 8192;
    const u16* kp = Kf + (size_t)hw * 2048;
    const u16* vp = Vf + (size_t)hw * 2048;
    const unsigned l16 = lane * 16;
#define DMAS(s_) do { const unsigned sl_ = ring + ((s_) & 3) * 32768; \
    _Pragma("unroll") for (int u_ = 0; u_ < 2; ++u_) { \
      glds16s(kp + (s_) * 8192 + u_ * 1024, l16, sl_ + u_ * 4096); glds16s(kp + (s_) * 8192 + u_ * 1024 + 512, l16, sl_ + u_ * 4096 + 1024); \
      glds16s(vp + (s_) * 8192 + u_ * 1024, l16, sl_ + u_ * 4096 + 2048); glds16s(vp + (s_) * 8192 + u_ * 1024 + 512, l16, sl_ + u_ * 4096 + 3072); } } while (0)
    const unsigned dum = (unsigned)(uintptr_t)lds + K2_DUM + hw * 1024;
    const int xg = tg & 7, rk = tg >> 3;
    const int ts = xg < 4 ? 25 * xg : 100 + 24 * (xg - 4), te = ts + (xg < 4 ? 25 : 24);
    const int n0 = ts / 7, n1 = (te - 1) / 7;
    const int nb0 = n0 + 3 <= 27 ? n0 : 24;
    const u16* p1 = w1 + (size_t)(64 * rk + 16 * hw) * F + nb0 * 64;
    const u16* p1b = w1 + (size_t)(64 * rk + 16 * hw) * F + n1 * 64;
    const u16* p2 = w2 + (size_t)(56 * rk + 14 * hw) * S + 4 * xg * 64;
    const unsigned o1 = ((lane >> 5) * F + (lane & 31) * 8) * 2, o1b = ((lane >> 3) * F + (lane & 7) * 8) * 2, o2 = ((lane >> 5) * S + (lane & 31) * 8) * 2;
#define PFD(i0, i1) do { _Pragma("unroll") for (int i_ = i0; i_ < i1; ++i_) { \
      if (i_ < 8) glds16s(p1 + (size_t)i_ * 2 * F, o1, dum); \
      else if (i_ < 10) glds16s(p1b + (size_t)(i_ - 8) * 8 * F, o1b, dum); \
      else glds16s(p2 + (size_t)(i_ - 10) * 2 * S, o2, dum); } } while (0)
    DMAS(0); DMAS(1); DMAS(2);
    waitv<16>(); RBAR();
    LDWO(0, 12); SBAR(); PFD(0, 3);
    waitv<23>(); RBAR();
    DMAS(3); LDWO(12, 24); SBAR(); PFD(3, 6);
    waitv<38>(); RBAR();
    DMAS(4); LDWO(24, 36); SBAR(); PFD(6, 9);
    waitv<38>(); RBAR();
    DMAS(5); LDWO(36, 49); SBAR(); PFD(9, 12);
    waitv<39>(); RBAR();
    bf16x8 wx[10];
    DMAS(6);
#pragma unroll
    for (int i = 0; i < 10; ++i) wx[i] = *(const bf16x8*)(wob - 7 * 1024 + (((39 + i) / 7) * 14 + (39 + i) % 7) * 1024 + wl);
    SBAR(); PFD(12, 15);
    waitv<37>(); RBAR();
    DMAS(7); PFD(15, 17);
    waitv<23>(); RBAR();
#pragma unroll
    for (int i = 0; i < 8; ++i) glds16s(wob - 7 * 1024 + ((i / 7) * 14 + i % 7) * 1024, wl, ring + i * 1024);
    waitv<10>(); RBAR();
#pragma unroll
    for (int i = 8; i < 16; ++i) glds16s(wob - 7 * 1024 + ((i / 7) * 14 + i % 7) * 1024, wl, ring + 32768 + (i - 8) * 1024);
    xv = *(const u32x4*)(x + (size_t)(tg * 8 + w) * D + (lane < 56 ? lane : 55) * 8);
    SBAR();
    __builtin_amdgcn_s_setprio(0);
    RBAR();
    waitv<1>();
    STAMP(1, 1);
    LBAR();
    STAMP(1, 2);
    {
      f32x4 acc[7];
      {
        bf16x8 bo[7];
#pragma unroll
        for (int k = 0; k < 7; ++k) bo[k] = BOF(k);
        acc[5] = (f32x4){0.f, 0.f, 0.f, 0.f}; acc[6] = acc[5];
#pragma unroll
        for (int i = 0; i < 10; ++i) acc[(39 + i) / 7] = MFMA16(wx[i], bo[(39 + i) % 7], acc[(39 + i) / 7]);
      }
      SBAR();
      bf16x8 bo[7];
#pragma unroll
      for (int k = 0; k < 7; ++k) bo[k] = BOF(7 + k);
#pragma unroll
      for (int n = 0; n < 7; ++n) {
        if (n < 5) acc[n] = (f32x4){0.f, 0.f, 0.f, 0.f};
#pragma unroll
        for (int k = 0; k < 7; ++k) acc[n] = MFMA16(wf[n][k], bo[k], acc[n]);
      }
      HBST(acc);
    }
  }
  LBAR();
  STAMP(1, 3);
  {
    const int s = tg * 8 + w;
    const bool act = lane < 56;
    const int lc = act ? lane : 55;
    float v[8];
    const f32x4 a0 = *(const f32x4*)(lds + K2_HB + (w * HBS + lc * 8) * 4), a1 = *(const f32x4*)(lds + K2_HB + (w * HBS + lc * 8 + 4) * 4);
    const f32x4 c0 = *(const f32x4*)(lds + K2_HB + ((8 + w) * HBS + lc * 8) * 4), c1 = *(const f32x4*)(lds + K2_HB + ((8 + w) * HBS + lc * 8 + 4) * 4);
    u32x4 hv;
#pragma unroll
    for (int i = 0; i < 2; ++i) {
      hv[i] = cvtpk(bflo(xv[i]) + a0[2 * i] + c0[2 * i], bfhi(xv[i]) + a0[2 * i + 1] + c0[2 * i + 1]);
      hv[2 + i] = cvtpk(bflo(xv[2 + i]) + a1[2 * i] + c1[2 * i], bfhi(xv[2 + i]) + a1[2 * i + 1] + c1[2 * i + 1]);
    }
    if (act) st16c(H + (size_t)s * D + lane * 8, hv);
#pragma unroll
    for (int i = 0; i < 4; ++i) { v[2 * i] = act ? bflo(hv[i]) : 0.f; v[2 * i + 1] = act ? bfhi(hv[i]) : 0.f; }
#pragma unroll
    for (int pass = 0; pass < 2; ++pass) {
      float sm = 0.f;
#pragma unroll
      for (int i = 0; i < 8; ++i) sm += v[i];
      const float mean = wave_sum(sm) * (1.f / D);
      float q = 0.f;
#pragma unroll
      for (int i = 0; i < 8; ++i) { v[i] = act ? v[i] - mean : 0.f; q += v[i] * v[i]; }
      const float rs = __builtin_amdgcn_rsqf(wave_sum(q) * (1.f / D) + LN_EPS);
#pragma unroll
      for (int i = 0; i < 8; ++i) v[i] *= rs;
    }
    if (act) *(u32x4*)(lds + K2_HS + w * 912 + lane * 16) = (u32x4){cvtpk(v[0], v[1]), cvtpk(v[2], v[3]), cvtpk(v[4], v[5]), cvtpk(v[6], v[7])};
  }
  LBAR();
  if (tid < D / 2) {
    unsigned r[8];
#pragma unroll
    for (int i = 0; i < 8; ++i) r[i] = *(const unsigned*)(lds + K2_HS + i * 912 + tid * 4);
    u32x4 lo, hi2;
#pragma unroll
    for (int i = 0; i < 4; ++i) {
      lo[i] = (r[2 * i] & 0xffffu) | (r[2 * i + 1] << 16);
      hi2[i] = (r[2 * i] >> 16) | (r[2 * i + 1] & 0xffff0000u);
    }
    u32x4* dst = (u32x4*)(HN + ((size_t)tg * D + 2 * tid) * 8);
    st16c(dst, lo); st16c(dst + 1, hi2);
  }
  STAMP(1, 4);
}

constexpr int G_RING = 4, G_LDS = 8 * G_RING * 4096;
typedef __attribute__((address_space(1))) unsigned gu32;
typedef __attribute__((address_space(1))) unsigned long long gu64;
#define RLX_AGENT __ATOMIC_RELAXED, __HIP_MEMORY_SCOPE_AGENT
template <int OFF> DI void gload16c(bf16x8& d, const void* p) {
  asm volatile("global_load_dwordx4 %0, %1, off offset:%2 sc1" : "=v"(d) : "v"(p), "i"(OFF) : "memory");
}
template <int PH, int NSH>
DI void mlp_tile(char* lds, int t, const u16* act, const u16* __restrict__ W, const u16* __restrict__ bias, const u16* H, u16* out,
                 unsigned* cnt, unsigned want, unsigned* pub) {
  constexpr int KT = PH == 0 ? S : F, LDW = PH == 0 ? F : S, NK = KT / 256, NR = NK - G_RING, LA = 5;
  const int tid = threadIdx.x, lane = tid & 63, w = __builtin_amdgcn_readfirstlane(tid >> 6);
  const int nt = t / 7, mt = t - nt * 7;
  const unsigned lds0 = (unsigned)(uintptr_t)lds + w * (G_RING * 4096);
  const int g = lane >> 4, q4 = (lane & 15) >> 2, p = lane & 3, l15 = lane & 15;
  STAMP(2 + PH, 0);
  const int enb = w >> 1, emb = 2 * (w & 1);
  const u16* asrc = act + ((size_t)(4 * w + g) * D + 64 * mt + l15) * 8;
  const int r8 = lane >> 3, fe = (r8 >> 1) & 1;
  const u16* wsrc0 = W + (size_t)(32 * w + r8) * LDW + 64 * nt + ((lane & 7) ^ (fe << 1)) * 8;
  const u16* wsrc1 = W + (size_t)(32 * w + 8 + r8) * LDW + 64 * nt + ((lane & 7) ^ ((fe | 2) << 1)) * 8;
  const int f = ((q4 >> 1) & 1) | ((g & 1) << 1);
  int woff[4];
#pragma unroll
  for (int nb = 0; nb < 4; ++nb) woff[nb] = w * (G_RING * 4096) + (8 * g + q4) * 128 + (((2 * nb + (p >> 1)) ^ (f << 1)) * 16) + 8 * (p & 1);
  bf16x8 af[NK][4];
  f32x4 acc[4][4] = {};
#define STAGE_W(i) do { const unsigned sl_ = lds0 + ((i) % G_RING) * 4096; const size_t o_ = (size_t)(i) * 256 * LDW; \
    glds16(wsrc0 + o_, sl_); glds16(wsrc1 + o_, sl_ + 1024); glds16(wsrc0 + o_ + 16 * LDW, sl_ + 2048); glds16(wsrc1 + o_ + 16 * LDW, sl_ + 3072); } while (0)
#define LOAD_A(i) do { const u16* ap_ = asrc + (size_t)(i) * 32 * D * 8; \
    gload16c<0>(af[i][0], ap_); gload16c<256>(af[i][1], ap_); gload16c<512>(af[i][2], ap_); gload16c<768>(af[i][3], ap_); } while (0)
#define RDW(WF, i) do { _Pragma("unroll") for (int nb_ = 0; nb_ < 4; ++nb_) { const lds_cptr sp_ = (lds_cptr)lds + woff[nb_] + ((i) % G_RING) * 4096; \
      WF[nb_] = cat8(trread(sp_), trread(sp_ + 512)); } } while (0)
#define MMA(WF, i) do { _Pragma("unroll") for (int nb_ = 0; nb_ < 4; ++nb_) _Pragma("unroll") for (int mb_ = 0; mb_ < 4; ++mb_) \
      acc[nb_][mb_] = PH == 0 ? MFMA16(WF[nb_], af[i][mb_], acc[nb_][mb_]) : MFMA16(af[i][mb_], WF[nb_], acc[nb_][mb_]); } while (0)
  bf16x8 wr[NR][4];
#pragma unroll
  for (int j = 0; j < NR; ++j) STAGE_W(G_RING + j);
  if (NR < G_RING) STAGE_W(G_RING - 1);
  if (pub) {
    asm volatile("s_waitcnt vmcnt(16)\n\ts_barrier" ::: "memory");
    if (threadIdx.x == 0) __hip_atomic_fetch_add((gu32*)pub, 1u, RLX_AGENT);
  }
#pragma unroll
  for (int j = 0; j < NR; ++j) {
    waitv<12>();
    RDW(wr[j], G_RING + j);
    asm volatile("s_waitcnt lgkmcnt(0)" ::: "memory");
    STAGE_W(j);
  }
  STAMP(2 + PH, 1);
  if (w == 0) {
    gu32* cp = (gu32*)(cnt + (lane < NSH ? lane : 0) * 64);
    for (unsigned spins = 0; spins < (1u << 20); ++spins) {
      if (__all(__hip_atomic_load(cp, RLX_AGENT) >= want)) break;
      __builtin_amdgcn_s_sleep(1);
    }
  }
  asm volatile("s_barrier" ::: "memory");
  STAMP(2 + PH, 2);
  u32x2 eb;
  if (PH == 0) eb = *(const u32x2*)(bias + 64 * nt + 16 * enb + 4 * g);
  else eb[0] = bias[64 * nt + 16 * enb + l15];
  unsigned long long eh0 = 0, eh1 = 0;
  if (PH == 1) {
    const int s = 64 * nt + 16 * enb + l15;
    eh0 = __hip_atomic_load((gu64*)(H + (size_t)s * D + 64 * mt + 16 * emb + 4 * g), RLX_AGENT);
    eh1 = __hip_atomic_load((gu64*)(H + (size_t)s * D + 64 * mt + 16 * emb + 16 + 4 * g), RLX_AGENT);
  }
  SBAR();
  __builtin_amdgcn_s_setprio(3);
#pragma unroll
  for (int j = 0; j < LA; ++j) LOAD_A((j + G_RING) % NK);
#pragma unroll
  for (int j = 0; j < NK; ++j) {
    const int i = (j + G_RING) % NK;
    const int rem = NK - 1 - j < LA - 1 ? NK - 1 - j : LA - 1;
    if (j == 1) __builtin_amdgcn_s_setprio(2);
    else if (j == 2) __builtin_amdgcn_s_setprio(1);
    else if (j == 3) __builtin_amdgcn_s_setprio(0);
    if (rem == 4) waitv4<16>(af[i][0], af[i][1], af[i][2], af[i][3]);
    else if (rem == 3) waitv4<12>(af[i][0], af[i][1], af[i][2], af[i][3]);
    else if (rem == 2) waitv4<8>(af[i][0], af[i][1], af[i][2], af[i][3]);
    else if (rem == 1) waitv4<4>(af[i][0], af[i][1], af[i][2], af[i][3]);
    else waitv4<0>(af[i][0], af[i][1], af[i][2], af[i][3]);
    if (j < NR) {
      MMA(wr[j], i);
    } else {
      bf16x8 wf[4];
      RDW(wf, i);
      MMA(wf, i);
    }
    SBAR();
    if (j + LA < NK) LOAD_A((j + LA + G_RING) % NK);
  }
#undef STAGE_W
#undef LOAD_A
#undef RDW
#undef MMA
  STAMP(2 + PH, 4);
  {
    f32x4* xb = (f32x4*)lds;
#pragma unroll
    for (int nb = 0; nb < 4; ++nb)
#pragma unroll
      for (int mb = 0; mb < 4; ++mb) xb[(w * 16 + nb * 4 + mb) * 64 + lane] = acc[nb][mb];
  }
  LBAR();
  {
    const f32x4* xb = (const f32x4*)lds;
#pragma unroll
    for (int e = 0; e < 2; ++e) {
      f32x4 a = xb[(2 * w + e) * 64 + lane];
#pragma unroll
      for (int v = 1; v < 8; ++v) a += xb[(v * 16 + 2 * w + e) * 64 + lane];
      const int mb = emb + e;
      if (PH == 0) {
        const int fi = 64 * nt + 16 * enb + 4 * g, d = 64 * mt + 16 * mb + l15;
        float z[4] = {a[0] + bflo(eb[0]), a[1] + bfhi(eb[0]), a[2] + bflo(eb[1]), a[3] + bfhi(eb[1])};
#pragma unroll
        for (int i = 0; i < 4; ++i) {
          const float zz = z[i], a2 = zz * (1.f + 0.044715f * zz * zz) * (-2.f * 0.7978845608028654f * 1.4426950408889634f);
          z[i] = zz * __builtin_amdgcn_rcpf(1.f + __builtin_amdgcn_exp2f(a2));
        }
        const unsigned long long v = (unsigned long long)cvtpk(z[0], z[1]) | ((unsigned long long)cvtpk(z[2], z[3]) << 32);
        __hip_atomic_store((gu64*)(out + ((size_t)(fi >> 3) * D + d) * 8 + (fi & 7)), v, RLX_AGENT);
      } else {
        const int d = 64 * mt + 16 * mb + 4 * g, s = 64 * nt + 16 * enb + l15;
        const float bb = bflo(eb[0]);
        const unsigned long long hv = e ? eh1 : eh0;
        const unsigned h0 = (unsigned)hv, h1 = (unsigned)(hv >> 32);
        *(u32x2*)(out + (size_t)s * D + d) =
            (u32x2){cvtpk(a[0] + bb + bflo(h0), a[1] + bb + bfhi(h0)), cvtpk(a[2] + bb + bflo(h1), a[3] + bb + bfhi(h1))};
      }
    }
  }
  STAMP(2 + PH, 5);
}
#define PUBLISH(c) do { asm volatile("s_waitcnt vmcnt(0) lgkmcnt(0)\n\ts_barrier" ::: "memory"); \
    if (threadIdx.x == 0) __hip_atomic_fetch_add((gu32*)(c), 1u, RLX_AGENT); } while (0)

extern "C" __global__ __launch_bounds__(512) void k_main(const u16* __restrict__ x, const u16* __restrict__ Qf, const u16* __restrict__ Kf,
                                              const u16* __restrict__ Vf, const u16* __restrict__ Wof, const u16* __restrict__ w1,
                                              const u16* __restrict__ b1, const u16* __restrict__ w2, const u16* __restrict__ b2,
                                              u16* H, u16* HN, u16* U, u16* out, unsigned* cnt) {
  __shared__ __attribute__((aligned(16))) char lds[K2_LDS];
  attn_phase(lds, x, Qf, Kf, Vf, Wof, w1, w2, H, HN);
  unsigned* pub = cnt + (8 + (blockIdx.x & 7)) * 64;
  if (blockIdx.x < 196) {
    const int t = xcd_remap(blockIdx.x, 196);
    mlp_tile<0, 8>(lds, t, HN, w1, b1, H, U, cnt + 8 * 64, 32u, pub);
    pub = cnt + (t % 7) * 64;
    LBAR();
  }
  if (blockIdx.x < 224) {
    const int t2 = xcd_remap(blockIdx.x, 224);
    mlp_tile<1, 1>(lds, t2, U, w2, b2, H, out, cnt + (t2 % 7) * 64, 28u, pub);
  } else {
    PUBLISH(pub);
  }
}

extern "C" void kernel_launch(void* const* d_in, const int* in_sizes, int n_in, void* d_out, int out_size, void* d_ws, size_t ws_size,
                              hipStream_t stream) {
  const u16* x = (const u16*)d_in[0];
  const u16* wq = (const u16*)d_in[1];
  const u16* wk = (const u16*)d_in[2];
  const u16* wv = (const u16*)d_in[3];
  const u16* wo = (const u16*)d_in[4];
  const u16* w1 = (const u16*)d_in[6];
  const u16* b1 = (const u16*)d_in[7];
  const u16* w2 = (const u16*)d_in[8];
  const u16* b2 = (const u16*)d_in[9];
  char* ws = (char*)d_ws;
  u16 *Qf = (u16*)(ws + WS_QF), *Kf = (u16*)(ws + WS_KF), *Vf = (u16*)(ws + WS_VF), *Wof = (u16*)(ws + WS_WOF);
  u16 *H = (u16*)(ws + WS_H), *HN = (u16*)(ws + WS_HN), *U = (u16*)(ws + WS_U);
  unsigned* cnt = (unsigned*)(ws + WS_CNT);
  k_qkv<<<256, 512, 0, stream>>>(x, wq, wk, wv, wo, Qf, Kf, Vf, Wof, cnt);
  k_main<<<256, 512, 0, stream>>>(x, Qf, Kf, Vf, Wof, w1, b1, w2, b2, H, HN, U, (u16*)d_out, cnt);
}
```

```cpp
#include <hip/hip_runtime.h>
#include <hip/hip_bf16.h>

typedef unsigned short u16;
using bf16x8 = __attribute__((ext_vector_type(8))) short;
using s16x4 = __attribute__((ext_vector_type(4))) short;
using f32x4 = __attribute__((ext_vector_type(4))) float;
using f32x16 = __attribute__((ext_vector_type(16))) float;
using u32x4 = __attribute__((ext_vector_type(4))) unsigned;
using u32x2 = __attribute__((ext_vector_type(2))) unsigned;
typedef short v4i16_t __attribute__((ext_vector_type(4)));
typedef __attribute__((address_space(3))) const char* lds_cptr;
typedef __bf16 bf16x2_t __attribute__((ext_vector_type(2)));
typedef float f32x2_t __attribute__((ext_vector_type(2)));

#define DI __device__ __forceinline__
#define MFMA32(a, b, c) __builtin_amdgcn_mfma_f32_32x32x16_bf16(a, b, c, 0, 0, 0)
#define MFMA16(a, b, c) __builtin_amdgcn_mfma_f32_16x16x32_bf16(a, b, c, 0, 0, 0)
#define SBAR() __builtin_amdgcn_sched_barrier(0)
#define LBAR() asm volatile("s_waitcnt lgkmcnt(0)\n\ts_barrier" ::: "memory")
#define WAITV_BAR(N) asm volatile("s_waitcnt vmcnt(" #N ") lgkmcnt(0)\n\ts_barrier" ::: "memory")

constexpr int S = 2048, D = 448, DH = 28, F = 1792;
constexpr float QSCALE = 0.18898223650461362f * 1.4426950408889634f;
constexpr float LN_EPS = 1e-5f;

constexpr size_t WS_QF = 0;
constexpr size_t WS_KF = WS_QF + 2097152;
constexpr size_t WS_VF = WS_KF + 131072;
constexpr size_t WS_WOF = WS_VF + 131072;
constexpr size_t WS_H = WS_WOF + 401408;
constexpr size_t WS_HN = WS_H + 1835008;
constexpr size_t WS_U = WS_HN + 1835008;
constexpr size_t WS_CNT = WS_U + 1605632;

#define STAMP(k, i)
#define CSTAMP(k, i)

DI unsigned cvtpk(float lo, float hi) {
  const f32x2_t v = {lo, hi};
  return __builtin_bit_cast(unsigned, __builtin_convertvector(v, bf16x2_t));
}
DI float bflo(unsigned u) { return __uint_as_float(u << 16); }
DI float bfhi(unsigned u) { return __uint_as_float(u & 0xffff0000u); }
template <int CTRL> DI float dppf(float v) {
  return __uint_as_float(__builtin_amdgcn_update_dpp(0u, __float_as_uint(v), CTRL, 0xf, 0xf, true));
}
DI float wave_sum(float v) {
  v += dppf<0xB1>(v);
  v += dppf<0x4E>(v);
  v += dppf<0x141>(v);
  v += dppf<0x140>(v);
  auto a = __builtin_amdgcn_permlane16_swap(__float_as_uint(v), __float_as_uint(v), false, false);
  v = __uint_as_float(a[0]) + __uint_as_float(a[1]);
  auto b = __builtin_amdgcn_permlane32_swap(__float_as_uint(v), __float_as_uint(v), false, false);
  return __uint_as_float(b[0]) + __uint_as_float(b[1]);
}
DI float half_max(float v) {
  auto b = __builtin_amdgcn_permlane32_swap(__float_as_uint(v), __float_as_uint(v), false, false);
  return __builtin_fmaxf(__uint_as_float(b[0]), __uint_as_float(b[1]));
}
DI void glds16(const void* g, unsigned lds_base) {
  unsigned sv;
  asm volatile("s_mov_b32 %0, m0\n\ts_mov_b32 m0, %2\n\ts_nop 0\n\tglobal_load_lds_dwordx4 %1, off\n\ts_mov_b32 m0, %0"
               : "=&s"(sv) : "v"(g), "s"(lds_base) : "memory");
}
DI void glds16s(const void* sbase, unsigned voff, unsigned lds_base) {
  unsigned sv;
  asm volatile("s_mov_b32 %0, m0\n\ts_mov_b32 m0, %3\n\ts_nop 0\n\tglobal_load_lds_dwordx4 %1, %2\n\ts_mov_b32 m0, %0"
               : "=&s"(sv) : "v"(voff), "s"(sbase), "s"(lds_base) : "memory");
}
DI void glds4(const void* g, unsigned lds_base) {
  unsigned sv;
  asm volatile("s_mov_b32 %0, m0\n\ts_mov_b32 m0, %2\n\ts_nop 0\n\tglobal_load_lds_dword %1, off\n\ts_mov_b32 m0, %0"
               : "=&s"(sv) : "v"(g), "s"(lds_base) : "memory");
}
template <int N> DI void waitv4(bf16x8& a, bf16x8& b, bf16x8& c, bf16x8& d) {
  asm volatile("s_waitcnt vmcnt(%4)" : "+v"(a), "+v"(b), "+v"(c), "+v"(d) : "i"(N) : "memory");
}
template <int OFF> DI void gload16(bf16x8& d, const void* p) {
  asm volatile("global_load_dwordx4 %0, %1, off offset:%2" : "=v"(d) : "v"(p), "i"(OFF) : "memory");
}
DI s16x4 trread(lds_cptr p) {
  return __builtin_bit_cast(s16x4, __builtin_amdgcn_ds_read_tr16_b64_v4i16((__attribute__((address_space(3))) v4i16_t*)p));
}
DI bf16x8 ldsr16(lds_cptr p) { return *(const __attribute__((address_space(3))) bf16x8*)p; }
DI bf16x8 cat8(s16x4 a, s16x4 b) { return __builtin_shufflevector(a, b, 0, 1, 2, 3, 4, 5, 6, 7); }
DI int xcd_remap(int bid, int nwg) {
  const int q = nwg >> 3, r = nwg & 7, x = bid & 7, o = bid >> 3;
  return (x < r ? x * (q + 1) : r * (q + 1) + (x - r) * q) + o;
}

constexpr int K1_WI = 448 * 128, K1_X = 2 * K1_WI, K1_XS = 528, K1_DUM = K1_X + 64 * K1_XS, K1_LDS = K1_DUM + 1024;
constexpr int KMAIN_KB = 28;
__global__ __launch_bounds__(512) void k_qkv(const u16* __restrict__ x, const u16* __restrict__ wq, const u16* __restrict__ wk,
                                             const u16* __restrict__ wv, const u16* __restrict__ wo, u16* __restrict__ Qf,
                                             u16* __restrict__ Kf, u16* __restrict__ Vf, u16* __restrict__ Wof, unsigned* __restrict__ cnt) {
  __shared__ __attribute__((aligned(16))) char lds[K1_LDS];
  const int tid = threadIdx.x, lane = tid & 63, w = __builtin_amdgcn_readfirstlane(tid >> 6);
  const int t = xcd_remap(blockIdx.x, 256), rt = t >> 2, ct = t & 3;
  const unsigned lds0 = (unsigned)(uintptr_t)lds;
  STAMP(0, 0);
  bf16x8 xr[4];
  const bool act = lane < 56;
  {
    const u16* xp_ = x + (size_t)(rt * 32 + w * 4) * D + (act ? lane : 55) * 8;
    gload16<0>(xr[0], xp_); gload16<896>(xr[1], xp_); gload16<1792>(xr[2], xp_); gload16<2688>(xr[3], xp_);
  }
#pragma unroll
  for (int im = 0; im < 2; ++im) {
    const int c64 = 2 * ct + im;
    if (c64 < 7) {
#pragma unroll
      for (int i = 0; i < 7; ++i) {
        const int piece = w * 7 + i, row = piece * 8 + (lane >> 3);
        const int f = ((row >> 1) & 1) | (((row >> 3) & 1) << 1);
        const int c = (lane & 7) ^ (f << 1);
        glds16(wq + (size_t)row * D + c64 * 64 + c * 8, lds0 + im * K1_WI + piece * 1024);
      }
    } else {
#pragma unroll
      for (int i = 0; i < 7; ++i) {
        const int pc = w * 7 + i < 50 ? w * 7 + i : 49, mat = pc >= 25, piece = pc - 25 * mat;
        const int e = piece * 512 + lane * 8;
        glds16((mat ? wv : wk) + (e < 448 * 28 ? e : 448 * 28 - 8), lds0 + im * K1_WI + mat * 25600 + piece * 1024);
      }
    }
  }
  u16 wt[8];
  const int frag0 = blockIdx.x + 256 * (w & 1);
  const bool dofrag = w < 2 && frag0 < 392;
  const int frag = dofrag ? frag0 : 0;
  {
    const int nb = frag / 14, ks = frag - nb * 14;
#pragma unroll
    for (int j = 0; j < 8; ++j) wt[j] = wo[(size_t)(32 * ks + 8 * (lane >> 4) + j) * D + 16 * nb + (lane & 15)];
  }
  SBAR();
  waitv4<22>(xr[0], xr[1], xr[2], xr[3]);
  {
    u32x4 xv[4];
#pragma unroll
    for (int r = 0; r < 4; ++r) xv[r] = __builtin_bit_cast(u32x4, xr[r]);
    float v[4][8], mean[4], rs[4];
#pragma unroll
    for (int r = 0; r < 4; ++r) {
      float s = 0.f;
#pragma unroll
      for (int i = 0; i < 4; ++i) {
        v[r][2 * i] = act ? bflo(xv[r][i]) : 0.f; v[r][2 * i + 1] = act ? bfhi(xv[r][i]) : 0.f;
        s += v[r][2 * i] + v[r][2 * i + 1];
      }
      mean[r] = s;
    }
#pragma unroll
    for (int r = 0; r < 4; ++r) mean[r] = wave_sum(mean[r]) * (1.f / D);
#pragma unroll
    for (int r = 0; r < 4; ++r) {
      float q = 0.f;
#pragma unroll
      for (int i = 0; i < 8; ++i) { v[r][i] = act ? v[r][i] - mean[r] : 0.f; q += v[r][i] * v[r][i]; }
      rs[r] = q;
    }
#pragma unroll
    for (int r = 0; r < 4; ++r) rs[r] = __builtin_amdgcn_rsqf(wave_sum(rs[r]) * (1.f / D) + LN_EPS);
    char* xdst = lds + K1_X + lane * K1_XS + w * 64;
#pragma unroll
    for (int r = 0; r < 4; ++r) {
      u32x4 o;
#pragma unroll
      for (int i = 0; i < 4; ++i) o[i] = cvtpk(v[r][2 * i] * rs[r], v[r][2 * i + 1] * rs[r]);
      *(u32x4*)(xdst + r * 16) = o;
    }
  }
  STAMP(0, 1);
  WAITV_BAR(0);
  STAMP(0, 2);
  if (w == 7 && (blockIdx.x >> 3) < KMAIN_KB) {
    unsigned long long pc;
    int off;
    asm volatile("s_getpc_b64 %0\n1:\n\ts_mov_b32 %1, k_main-1b" : "=s"(pc), "=s"(off));
    glds16s((const char*)(pc + (long long)off) + (blockIdx.x >> 3) * 1024, lane * 16, lds0 + K1_DUM);
  }
  const int nb = w & 3, im = w >> 2, c64 = 2 * ct + im, g = lane >> 4, q4 = (lane & 15) >> 2, p = lane & 3, l15 = lane & 15;
  const int f = ((q4 >> 1) & 1) | ((g & 1) << 1);
  const bool kv = c64 == 7;
  const int rs_ = kv ? 56 : 128;
  const lds_cptr wp = (lds_cptr)lds + im * K1_WI + (8 * g + q4) * rs_ +
                      (kv ? (nb >> 1) * 25600 + (nb & 1) * 32 + 8 * p : (((2 * nb + (p >> 1)) ^ (f << 1)) * 16) + 8 * (p & 1));
  const lds_cptr xp = (lds_cptr)lds + K1_X + g * K1_XS + l15 * 16;
  f32x4 acc0 = {0.f, 0.f, 0.f, 0.f}, acc1 = {0.f, 0.f, 0.f, 0.f};
  {
    bf16x8 a[14], b0[14], b1[14];
#pragma unroll
    for (int ks = 0; ks < 14; ++ks) {
      a[ks] = cat8(trread(wp + ks * 32 * rs_), trread(wp + ks * 32 * rs_ + 4 * rs_));
      b0[ks] = ldsr16(xp + ks * 4 * K1_XS);
      b1[ks] = ldsr16(xp + ks * 4 * K1_XS + 256);
    }
    SBAR();
#pragma unroll
    for (int ks = 0; ks < 14; ++ks) {
      acc0 = MFMA16(a[ks], b0[ks], acc0);
      acc1 = MFMA16(a[ks], b1[ks], acc1);
    }
  }
  STAMP(0, 3);
#pragma unroll
  for (int tb = 0; tb < 2; ++tb) {
    const f32x4 a = tb ? acc1 : acc0;
    const int s = rt * 32 + 16 * tb + l15;
    const int nl = 16 * nb + 4 * g;
    if (c64 < 7) {
      const int n0 = 64 * c64 + nl, h = n0 / 28, d0 = n0 - 28 * h;
      const int tg = s >> 3, c = (h & 3) * 8 + (s & 7), b = h >> 2;
      const size_t off = ((((size_t)(tg * 4 + b) * 2 + (d0 >> 4)) * 64) + ((d0 >> 3) & 1) * 32 + c) * 8 + (d0 & 7);
      *(u32x2*)(Qf + off) = (u32x2){cvtpk(a[0] * QSCALE, a[1] * QSCALE), cvtpk(a[2] * QSCALE, a[3] * QSCALE)};
    } else {
      const int kt = s >> 5, kk = s & 31;
      const int d0 = 16 * (nb & 1) + 4 * g;
      if (nb < 2) {
        const size_t off = ((((size_t)kt * 2 + (d0 >> 4)) * 64) + ((d0 >> 3) & 1) * 32 + kk) * 8 + (d0 & 7);
        u32x2 v = {cvtpk(a[0], a[1]), cvtpk(a[2], a[3])};
        if (d0 == 28) v = (u32x2){0x3f803f80u, 0x00003f80u};
        *(u32x2*)(Kf + off) = v;
      } else {
        const int sp = kk >> 4, j = ((kk >> 3) & 1) * 4 + (kk & 3), hi = (kk >> 2) & 1;
        const size_t off = ((((size_t)kt * 2 + sp) * 64) + hi * 32 + d0) * 8 + j;
        unsigned v0 = cvtpk(a[0], a[1]), v1 = cvtpk(a[2], a[3]);
        if (d0 == 28) { v0 = 0x00003f80u; v1 = 0u; }
        Vf[off] = (u16)v0; Vf[off + 8] = (u16)(v0 >> 16); Vf[off + 16] = (u16)v1; Vf[off + 24] = (u16)(v1 >> 16);
      }
    }
  }
  if (dofrag) {
    u32x4 o;
#pragma unroll
    for (int i = 0; i < 4; ++i) o[i] = (unsigned)wt[2 * i] | ((unsigned)wt[2 * i + 1] << 16);
    *(u32x4*)(Wof + ((size_t)frag * 64 + lane) * 8) = o;
  }
  if (blockIdx.x == 0) { cnt[tid] = 0u; cnt[512 + tid] = 0u; }
  if (w == 7) asm volatile("s_waitcnt vmcnt(0)" ::: "memory");
  STAMP(0, 4);
}

constexpr int K2_RING = 0, K2_OT = 131072, K2_DUM = K2_OT + 7168, K2_X = K2_DUM + 4096, K2_LDS = K2_X + 16384;
constexpr int K2_HB = 65536, HBS = 452, K2_HS = K2_X;

DI float set_ref(bf16x8& q1, float m, int hi) {
  const float a = bflo(cvtpk(-m, 0.f)), r1 = -m - a, b = bflo(cvtpk(r1, 0.f)), c = bflo(cvtpk(r1 - b, 0.f));
  u32x4 v = __builtin_bit_cast(u32x4, q1);
  v[2] = hi ? cvtpk(a, b) : v[2];
  v[3] = hi ? cvtpk(c, 0.f) : v[3];
  q1 = __builtin_bit_cast(bf16x8, v);
  return -((a + b) + c);
}
DI float tile_max(const f32x16& s) {
  float a = __builtin_fmaxf(__builtin_fmaxf(s[0], s[1]), s[2]), b = __builtin_fmaxf(__builtin_fmaxf(s[3], s[4]), s[5]);
  a = __builtin_fmaxf(__builtin_fmaxf(a, s[6]), s[7]); b = __builtin_fmaxf(__builtin_fmaxf(b, s[8]), s[9]);
  a = __builtin_fmaxf(__builtin_fmaxf(a, s[10]), s[11]); b = __builtin_fmaxf(__builtin_fmaxf(b, s[12]), s[13]);
  a = __builtin_fmaxf(__builtin_fmaxf(a, s[14]), s[15]);
  return half_max(__builtin_fmaxf(a, b));
}
template <int N> DI void waitv() { asm volatile("s_waitcnt vmcnt(%0)" ::"i"(N) : "memory"); }
#define RBAR() asm volatile("s_barrier" ::: "memory")

DI void attn_safe(const u16* __restrict__ Kf, const u16* __restrict__ Vf, bf16x8 q0, bf16x8 q1, f32x16& o, int lane, int hi) {
  const f32x16 zero = {};
  float mref = 0.f;
  o = zero;
#pragma unroll 1
  for (int t = 0; t < 64; ++t) {
    const bf16x8 k0 = *(const bf16x8*)(Kf + t * 1024 + lane * 8), k1 = *(const bf16x8*)(Kf + t * 1024 + 512 + lane * 8);
    const bf16x8 v0 = *(const bf16x8*)(Vf + t * 1024 + lane * 8), v1 = *(const bf16x8*)(Vf + t * 1024 + 512 + lane * 8);
    (void)set_ref(q1, 0.f, hi);
    f32x16 s = MFMA32(k0, q0, zero);
    s = MFMA32(k1, q1, s);
    const float tm = tile_max(s);
    const float mn = set_ref(q1, t == 0 ? tm : __builtin_fmaxf(tm, mref), hi);
    const float al = t == 0 ? 0.f : __builtin_amdgcn_exp2f(mref - mn);
#pragma unroll
    for (int r = 0; r < 16; ++r) o[r] *= al;
    mref = mn;
    s = MFMA32(k0, q0, zero);
    s = MFMA32(k1, q1, s);
    u32x4 p0, p1;
#pragma unroll
    for (int i = 0; i < 4; ++i) {
      p0[i] = cvtpk(__builtin_amdgcn_exp2f(s[2 * i]), __builtin_amdgcn_exp2f(s[2 * i + 1]));
      p1[i] = cvtpk(__builtin_amdgcn_exp2f(s[8 + 2 * i]), __builtin_amdgcn_exp2f(s[8 + 2 * i + 1]));
    }
    o = MFMA32(v0, __builtin_bit_cast(bf16x8, p0), o);
    o = MFMA32(v1, __builtin_bit_cast(bf16x8, p1), o);
  }
}

#define EX4(S, i) do { S[i] = __builtin_amdgcn_exp2f(S[i]); S[i + 1] = __builtin_amdgcn_exp2f(S[i + 1]); \
    S[i + 2] = __builtin_amdgcn_exp2f(S[i + 2]); S[i + 3] = __builtin_amdgcn_exp2f(S[i + 3]); } while (0)
#define PK8(P, S, i) do { P[0] = cvtpk(S[i], S[i + 1]); P[1] = cvtpk(S[i + 2], S[i + 3]); P[2] = cvtpk(S[i + 4], S[i + 5]); P[3] = cvtpk(S[i + 6], S[i + 7]); } while (0)
#define BF(P) __builtin_bit_cast(bf16x8, P)
constexpr int CPRE = 0;
#define WOPF(j) do { if ((j) < CPRE) wf[(j) / 7][(j) % 7] = WOFR(j); } while (0)
#define RDS(u, off) ldsr16(((u) < 4 ? B0 : B1) + ((u) & 3) * 8192 + (off))
#define CTILE(u, NX, PF) do { \
      \
    sB = MFMA32(k0, qB0, zero); SBAR(); if (NX) k0 = RDS((u) + 1, 0); EX4(sA, 0); WOPF(PF); SBAR(); \
    sB = MFMA32(k1, qB1, sB); SBAR(); if (NX) k1 = RDS((u) + 1, 1024); EX4(sA, 4); PK8(p0, sA, 0); SBAR(); \
    oB = MFMA32(v1, BF(p1), oB); SBAR(); v1 = RDS(u, 3072); EX4(sA, 8); WOPF((PF) + 1); SBAR(); \
    oA = MFMA32(v0, BF(p0), oA); SBAR(); EX4(sA, 12); PK8(p1, sA, 8); SBAR(); \
      \
    sA = MFMA32(k0, qA0, zero); SBAR(); EX4(sB, 0); WOPF((PF) + 2); SBAR(); \
    sA = MFMA32(k1, qA1, sA); SBAR(); EX4(sB, 4); PK8(p0, sB, 0); SBAR(); \
    oA = MFMA32(v1, BF(p1), oA); SBAR(); EX4(sB, 8); WOPF((PF) + 3); SBAR(); \
    oB = MFMA32(v0, BF(p0), oB); SBAR(); if (NX) v0 = RDS((u) + 1, 2048); EX4(sB, 12); PK8(p1, sB, 8); SBAR(); } while (0)
#define CSTAGE(LAST, PF) do { \
    CTILE(0, true, PF); CTILE(1, true, (PF) + 4); CTILE(2, true, (PF) + 8); \
    if (!(LAST)) RBAR(); \
    CTILE(3, !(LAST), (PF) + 12); } while (0)

DI void st16c(void* p, u32x4 v) {
  asm volatile("global_store_dwordx4 %0, %1, off sc1\n\ts_nop 1" ::"v"(p), "v"(v) : "memory");
}
DI void attn_phase(char* lds, const u16* __restrict__ x, const u16* __restrict__ Qf, const u16* __restrict__ Kf,
                   const u16* __restrict__ Vf, const u16* __restrict__ Wof, const u16* __restrict__ w1,
                   const u16* __restrict__ w2, u16* H, u16* HN) {
  const int tid = threadIdx.x, lane = tid & 63, w = __builtin_amdgcn_readfirstlane(tid >> 6), hi = lane >> 5;
  const int tg = blockIdx.x;
  STAMP(1, 0);
  const int kh = w >> 2, ng = w & 3;
  const char* wob = (const char*)(Wof + (size_t)(7 * ng * 14 + 7 * kh) * 512);
  const unsigned wl = lane * 16;
  bf16x8 wf[7][7];
#define WOFR(i_) (*(const bf16x8*)(wob + (((i_) / 7) * 14 + (i_) % 7) * 1024 + wl))
#define LDWO(i0, i1) do { _Pragma("unroll") for (int i_ = i0; i_ < i1; ++i_) wf[i_ / 7][i_ % 7] = WOFR(i_); } while (0)
#define BOF(k) (*(const bf16x8*)(lds + K2_OT + ((4 * (k) + (lane >> 4)) * 8 + (lane & 7)) * 16))
#define HBST(acc) do { if ((lane & 15) < 8) { \
      _Pragma("unroll") for (int n = 0; n < 7; ++n) *(f32x4*)(lds + K2_HB + ((kh * 8 + (lane & 15)) * HBS + 16 * (7 * ng + n) + 4 * (lane >> 4)) * 4) = acc[n]; \
    } } while (0)
  u32x4 xv;
  if (w < 4) {
    const f32x16 zero = {};
    const int qh = w & 1, kp = w >> 1;
    const lds_cptr rp0 = (lds_cptr)lds + K2_RING + lane * 16;
    const lds_cptr rp = rp0 + kp * 4096;
    lds_cptr B0 = rp, B1 = rp + 32768;
    const u16* qp = Qf + ((size_t)((tg * 4 + 2 * qh) * 2) * 64 + lane) * 8;
    bf16x8 qA0 = *(const bf16x8*)(qp), qA1 = *(const bf16x8*)(qp + 512), qB0 = *(const bf16x8*)(qp + 1024), qB1 = *(const bf16x8*)(qp + 1536);
    RBAR();
    bf16x8 k0 = ldsr16(rp0), k1 = ldsr16(rp0 + 1024), v0 = RDS(0, 2048), v1 = v0;
    (void)set_ref(qA1, 0.f, hi);
    (void)set_ref(qB1, 0.f, hi);
    f32x16 sA = MFMA32(k0, qA0, zero), sB = MFMA32(k0, qB0, zero);
    sA = MFMA32(k1, qA1, sA);
    sB = MFMA32(k1, qB1, sB);
    k0 = RDS(0, 0); k1 = RDS(0, 1024);
    (void)set_ref(qA1, tile_max(sA), hi);
    (void)set_ref(qB1, tile_max(sB), hi);
    sA = MFMA32(k0, qA0, zero);
    sA = MFMA32(k1, qA1, sA);
    f32x16 oA = zero, oB = zero;
    u32x4 p0, p1 = {0u, 0u, 0u, 0u};
    SBAR();
    STAMP(1, 8);
#pragma unroll 1
    for (int st = 0; st < 4; ++st) {
      CSTAGE(false, 64);
      B0 = B1; B1 = rp + ((st + 2) & 3) * 32768;
    }
    CSTAGE(false, 0);
    B0 = B1; B1 = rp + 2 * 32768;
    CSTAGE(false, 16);
    B0 = B1; B1 = rp + 3 * 32768;
    CSTAGE(false, 64);
    B0 = B1;
    CSTAGE(true, 64);
    oB = MFMA32(v1, BF(p1), oB);
    STAMP(1, 10);
    {
      const f32x16 snd = kp ? oA : oB;
#pragma unroll
      for (int r4 = 0; r4 < 4; ++r4)
        *(f32x4*)(lds + K2_X + ((w * 4 + r4) * 64 + lane) * 16) = (f32x4){snd[4 * r4], snd[4 * r4 + 1], snd[4 * r4 + 2], snd[4 * r4 + 3]};
    }
    f32x16 o0 = kp ? oB : oA;
    bf16x8 qk0 = kp ? qB0 : qA0, qk1 = kp ? qB1 : qA1;
    asm volatile("" : "+v"(o0), "+v"(qk0), "+v"(qk1));
    SBAR();
    LBAR();
#pragma unroll
    for (int r4 = 0; r4 < 4; ++r4) {
      const f32x4 t = *(const f32x4*)(lds + K2_X + (((w ^ 2) * 4 + r4) * 64 + lane) * 16);
      o0[4 * r4] += t[0]; o0[4 * r4 + 1] += t[1]; o0[4 * r4 + 2] += t[2]; o0[4 * r4 + 3] += t[3];
    }
    const int qb = 2 * qh + kp;
    {
      float chk = 0.f;
#pragma unroll
      for (int r = 0; r < 16; ++r) chk = __builtin_fmaf(o0[r], 0.f, chk);
      const bool bad = !(chk == 0.f) || !(o0[12] < 1.8e19f);
      if (__any(bad)) {
        attn_safe(Kf, Vf, qk0, qk1, o0, lane, hi);
      }
    }
    {
      auto sw = __builtin_amdgcn_permlane32_swap(__float_as_uint(o0[12]), __float_as_uint(o0[12]), false, false);
      const float il = 1.f / __uint_as_float(sw[1]);
      const int c = lane & 31;
#pragma unroll
      for (int r4 = 0; r4 < 4; ++r4) {
        const int d = 8 * r4 + 4 * hi, k = 28 * (4 * qb + (c >> 3)) + d;
        if (d < 28)
          *(u32x2*)(lds + K2_OT + ((k >> 3) * 8 + (c & 7)) * 16 + (k & 7) * 2) =
              (u32x2){cvtpk(o0[4 * r4] * il, o0[4 * r4 + 1] * il), cvtpk(o0[4 * r4 + 2] * il, o0[4 * r4 + 3] * il)};
      }
    }
    STAMP(1, 1);
    LBAR();
    STAMP(1, 2);
    LDWO(16, 39);
    xv = *(const u32x4*)(x + (size_t)(tg * 8 + w) * D + (lane < 56 ? lane : 55) * 8);
    SBAR();
    {
      bf16x8 bo[7];
#pragma unroll
      for (int k = 0; k < 7; ++k) bo[k] = BOF(k);
#pragma unroll
      for (int i = 0; i < 16; ++i) wf[i / 7][i % 7] = *(const bf16x8*)(lds + K2_RING + (i >> 3) * 32768 + w * 8192 + (i & 7) * 1024 + lane * 16);
      f32x4 acc[7];
#pragma unroll
      for (int n = 0; n < 7; ++n) acc[n] = (f32x4){0.f, 0.f, 0.f, 0.f};
#pragma unroll
      for (int i = 0; i < 39; ++i) acc[i / 7] = MFMA16(wf[i / 7][i % 7], bo[i % 7], acc[i / 7]);
      HBST(acc);
    }
  } else {
    const int hw = w - 4;
    __builtin_amdgcn_s_setprio(3);
    const unsigned ring = (unsigned)(uintptr_t)lds + K2_RING + hw * 8192;
    const u16* kp = Kf + (size_t)hw * 2048;
    const u16* vp = Vf + (size_t)hw * 2048;
    const unsigned l16 = lane * 16;
#define DMAS(s_) do { const unsigned sl_ = ring + ((s_) & 3) * 32768; \
    _Pragma("unroll") for (int u_ = 0; u_ < 2; ++u_) { \
      glds16s(kp + (s_) * 8192 + u_ * 1024, l16, sl_ + u_ * 4096); glds16s(kp + (s_) * 8192 + u_ * 1024 + 512, l16, sl_ + u_ * 4096 + 1024); \
      glds16s(vp + (s_) * 8192 + u_ * 1024, l16, sl_ + u_ * 4096 + 2048); glds16s(vp + (s_) * 8192 + u_ * 1024 + 512, l16, sl_ + u_ * 4096 + 3072); } } while (0)
    const unsigned dum = (unsigned)(uintptr_t)lds + K2_DUM + hw * 1024;
    const int xg = tg & 7, rk = tg >> 3;
    const int ts = xg < 4 ? 25 * xg : 100 + 24 * (xg - 4), te = ts + (xg < 4 ? 25 : 24);
    const int n0 = ts / 7, n1 = (te - 1) / 7;
    const int nb0 = n0 + 3 <= 27 ? n0 : 24;
    const u16* p1 = w1 + (size_t)(64 * rk + 16 * hw) * F + nb0 * 64;
    const u16* p1b = w1 + (size_t)(64 * rk + 16 * hw) * F + n1 * 64;
    const u16* p2 = w2 + (size_t)(56 * rk + 14 * hw) * S + 4 * xg * 64;
    const unsigned o1 = ((lane >> 5) * F + (lane & 31) * 8) * 2, o1b = ((lane >> 3) * F + (lane & 7) * 8) * 2, o2 = ((lane >> 5) * S + (lane & 31) * 8) * 2;
#define PFD(i0, i1) do { _Pragma("unroll") for (int i_ = i0; i_ < i1; ++i_) { \
      if (i_ < 8) glds16s(p1 + (size_t)i_ * 2 * F, o1, dum); \
      else if (i_ < 10) glds16s(p1b + (size_t)(i_ - 8) * 8 * F, o1b, dum); \
      else glds16s(p2 + (size_t)(i_ - 10) * 2 * S, o2, dum); } } while (0)
    DMAS(0); DMAS(1); DMAS(2);
    waitv<16>(); RBAR();
    LDWO(0, 12); SBAR(); PFD(0, 3);
    waitv<23>(); RBAR();
    DMAS(3); LDWO(12, 24); SBAR(); PFD(3, 6);
    waitv<38>(); RBAR();
    DMAS(4); LDWO(24, 36); SBAR(); PFD(6, 9);
    waitv<38>(); RBAR();
    DMAS(5); LDWO(36, 49); SBAR(); PFD(9, 12);
    waitv<39>(); RBAR();
    bf16x8 wx[10];
    DMAS(6);
#pragma unroll
    for (int i = 0; i < 10; ++i) wx[i] = *(const bf16x8*)(wob - 7 * 1024 + (((39 + i) / 7) * 14 + (39 + i) % 7) * 1024 + wl);
    SBAR(); PFD(12, 15);
    waitv<37>(); RBAR();
    DMAS(7); PFD(15, 17);
    waitv<23>(); RBAR();
#pragma unroll
    for (int i = 0; i < 8; ++i) glds16s(wob - 7 * 1024 + ((i / 7) * 14 + i % 7) * 1024, wl, ring + i * 1024);
    waitv<10>(); RBAR();
#pragma unroll
    for (int i = 8; i < 16; ++i) glds16s(wob - 7 * 1024 + ((i / 7) * 14 + i % 7) * 1024, wl, ring + 32768 + (i - 8) * 1024);
    xv = *(const u32x4*)(x + (size_t)(tg * 8 + w) * D + (lane < 56 ? lane : 55) * 8);
    SBAR();
    __builtin_amdgcn_s_setprio(0);
    RBAR();
    waitv<1>();
    STAMP(1, 1);
    LBAR();
    STAMP(1, 2);
    {
      f32x4 acc[7];
      {
        bf16x8 bo[7];
#pragma unroll
        for (int k = 0; k < 7; ++k) bo[k] = BOF(k);
        acc[5] = (f32x4){0.f, 0.f, 0.f, 0.f}; acc[6] = acc[5];
#pragma unroll
        for (int i = 0; i < 10; ++i) acc[(39 + i) / 7] = MFMA16(wx[i], bo[(39 + i) % 7], acc[(39 + i) / 7]);
      }
      SBAR();
      bf16x8 bo[7];
#pragma unroll
      for (int k = 0; k < 7; ++k) bo[k] = BOF(7 + k);
#pragma unroll
      for (int n = 0; n < 7; ++n) {
        if (n < 5) acc[n] = (f32x4){0.f, 0.f, 0.f, 0.f};
#pragma unroll
        for (int k = 0; k < 7; ++k) acc[n] = MFMA16(wf[n][k], bo[k], acc[n]);
      }
      HBST(acc);
    }
  }
  LBAR();
  STAMP(1, 3);
  {
    const int s = tg * 8 + w;
    const bool act = lane < 56;
    const int lc = act ? lane : 55;
    float v[8];
    const f32x4 a0 = *(const f32x4*)(lds + K2_HB + (w * HBS + lc * 8) * 4), a1 = *(const f32x4*)(lds + K2_HB + (w * HBS + lc * 8 + 4) * 4);
    const f32x4 c0 = *(const f32x4*)(lds + K2_HB + ((8 + w) * HBS + lc * 8) * 4), c1 = *(const f32x4*)(lds + K2_HB + ((8 + w) * HBS + lc * 8 + 4) * 4);
    u32x4 hv;
#pragma unroll
    for (int i = 0; i < 2; ++i) {
      hv[i] = cvtpk(bflo(xv[i]) + a0[2 * i] + c0[2 * i], bfhi(xv[i]) + a0[2 * i + 1] + c0[2 * i + 1]);
      hv[2 + i] = cvtpk(bflo(xv[2 + i]) + a1[2 * i] + c1[2 * i], bfhi(xv[2 + i]) + a1[2 * i + 1] + c1[2 * i + 1]);
    }
    if (act) st16c(H + (size_t)s * D + lane * 8, hv);
#pragma unroll
    for (int i = 0; i < 4; ++i) { v[2 * i] = act ? bflo(hv[i]) : 0.f; v[2 * i + 1] = act ? bfhi(hv[i]) : 0.f; }
#pragma unroll
    for (int pass = 0; pass < 2; ++pass) {
      float sm = 0.f;
#pragma unroll
      for (int i = 0; i < 8; ++i) sm += v[i];
      const float mean = wave_sum(sm) * (1.f / D);
      float q = 0.f;
#pragma unroll
      for (int i = 0; i < 8; ++i) { v[i] = act ? v[i] - mean : 0.f; q += v[i] * v[i]; }
      const float rs = __builtin_amdgcn_rsqf(wave_sum(q) * (1.f / D) + LN_EPS);
#pragma unroll
      for (int i = 0; i < 8; ++i) v[i] *= rs;
    }
    if (act) *(u32x4*)(lds + K2_HS + w * 912 + lane * 16) = (u32x4){cvtpk(v[0], v[1]), cvtpk(v[2], v[3]), cvtpk(v[4], v[5]), cvtpk(v[6], v[7])};
  }
  LBAR();
  if (tid < D / 2) {
    unsigned r[8];
#pragma unroll
    for (int i = 0; i < 8; ++i) r[i] = *(const unsigned*)(lds + K2_HS + i * 912 + tid * 4);
    u32x4 lo, hi2;
#pragma unroll
    for (int i = 0; i < 4; ++i) {
      lo[i] = (r[2 * i] & 0xffffu) | (r[2 * i + 1] << 16);
      hi2[i] = (r[2 * i] >> 16) | (r[2 * i + 1] & 0xffff0000u);
    }
    u32x4* dst = (u32x4*)(HN + ((size_t)tg * D + 2 * tid) * 8);
    st16c(dst, lo); st16c(dst + 1, hi2);
  }
  STAMP(1, 4);
}

constexpr int G_RING = 4, G_LDS = 8 * G_RING * 4096;
typedef __attribute__((address_space(1))) unsigned gu32;
typedef __attribute__((address_space(1))) unsigned long long gu64;
#define RLX_AGENT __ATOMIC_RELAXED, __HIP_MEMORY_SCOPE_AGENT
template <int OFF> DI void gload16c(bf16x8& d, const void* p) {
  asm volatile("global_load_dwordx4 %0, %1, off offset:%2 sc1" : "=v"(d) : "v"(p), "i"(OFF) : "memory");
}
template <int PH, int NSH>
DI void mlp_tile(char* lds, int t, const u16* act, const u16* __restrict__ W, const u16* __restrict__ bias, const u16* H, u16* out,
                 unsigned* cnt, unsigned want) {
  constexpr int KT = PH == 0 ? S : F, LDW = PH == 0 ? F : S, NK = KT / 256, NR = NK - G_RING, LA = 3;
  const int tid = threadIdx.x, lane = tid & 63, w = __builtin_amdgcn_readfirstlane(tid >> 6);
  const int nt = t / 7, mt = t - nt * 7;
  const unsigned lds0 = (unsigned)(uintptr_t)lds + w * (G_RING * 4096);
  const int g = lane >> 4, q4 = (lane & 15) >> 2, p = lane & 3, l15 = lane & 15;
  STAMP(2 + PH, 0);
  const int enb = w >> 1, emb = 2 * (w & 1);
  u32x2 eb;
  if (PH == 0) eb = *(const u32x2*)(bias + 64 * nt + 16 * enb + 4 * g);
  else eb[0] = bias[64 * nt + 16 * enb + l15];
  SBAR();
  const u16* asrc = act + ((size_t)(4 * w + g) * D + 64 * mt + l15) * 8;
  const int r8 = lane >> 3, fe = (r8 >> 1) & 1;
  const u16* wsrc0 = W + (size_t)(32 * w + r8) * LDW + 64 * nt + ((lane & 7) ^ (fe << 1)) * 8;
  const u16* wsrc1 = W + (size_t)(32 * w + 8 + r8) * LDW + 64 * nt + ((lane & 7) ^ ((fe | 2) << 1)) * 8;
  const int f = ((q4 >> 1) & 1) | ((g & 1) << 1);
  int woff[4];
#pragma unroll
  for (int nb = 0; nb < 4; ++nb) woff[nb] = w * (G_RING * 4096) + (8 * g + q4) * 128 + (((2 * nb + (p >> 1)) ^ (f << 1)) * 16) + 8 * (p & 1);
  bf16x8 af[NK][4];
  f32x4 acc[4][4] = {};
#define STAGE_W(i) do { const unsigned sl_ = lds0 + ((i) % G_RING) * 4096; const size_t o_ = (size_t)(i) * 256 * LDW; \
    glds16(wsrc0 + o_, sl_); glds16(wsrc1 + o_, sl_ + 1024); glds16(wsrc0 + o_ + 16 * LDW, sl_ + 2048); glds16(wsrc1 + o_ + 16 * LDW, sl_ + 3072); } while (0)
#define LOAD_A(i) do { const u16* ap_ = asrc + (size_t)(i) * 32 * D * 8; \
    gload16c<0>(af[i][0], ap_); gload16c<256>(af[i][1], ap_); gload16c<512>(af[i][2], ap_); gload16c<768>(af[i][3], ap_); } while (0)
#define RDW(WF, i) do { _Pragma("unroll") for (int nb_ = 0; nb_ < 4; ++nb_) { const lds_cptr sp_ = (lds_cptr)lds + woff[nb_] + ((i) % G_RING) * 4096; \
      WF[nb_] = cat8(trread(sp_), trread(sp_ + 512)); } } while (0)
#define MMA(WF, i) do { _Pragma("unroll") for (int nb_ = 0; nb_ < 4; ++nb_) _Pragma("unroll") for (int mb_ = 0; mb_ < 4; ++mb_) \
      acc[nb_][mb_] = PH == 0 ? MFMA16(WF[nb_], af[i][mb_], acc[nb_][mb_]) : MFMA16(af[i][mb_], WF[nb_], acc[nb_][mb_]); } while (0)
  bf16x8 wr[NR][4];
#pragma unroll
  for (int j = 0; j < NR; ++j) STAGE_W(G_RING + j);
  if (NR < G_RING) STAGE_W(G_RING - 1);
#pragma unroll
  for (int j = 0; j < NR; ++j) {
    waitv<12>();
    RDW(wr[j], G_RING + j);
    asm volatile("s_waitcnt lgkmcnt(0)" ::: "memory");
    STAGE_W(j);
  }
  STAMP(2 + PH, 1);
  if (w == 0) {
    gu32* cp = (gu32*)(cnt + (lane < NSH ? lane : 0) * 64);
    for (unsigned spins = 0; spins < (1u << 20); ++spins) {
      if (__all(__hip_atomic_load(cp, RLX_AGENT) >= want)) break;
      __builtin_amdgcn_s_sleep(1);
    }
  }
  asm volatile("s_barrier" ::: "memory");
  STAMP(2 + PH, 2);
  unsigned long long eh0 = 0, eh1 = 0;
  if (PH == 1) {
    const int s = 64 * nt + 16 * enb + l15;
    eh0 = __hip_atomic_load((gu64*)(H + (size_t)s * D + 64 * mt + 16 * emb + 4 * g), RLX_AGENT);
    eh1 = __hip_atomic_load((gu64*)(H + (size_t)s * D + 64 * mt + 16 * emb + 16 + 4 * g), RLX_AGENT);
  }
  SBAR();
#pragma unroll
  for (int j = 0; j < LA; ++j) LOAD_A((j + G_RING) % NK);
#pragma unroll
  for (int j = 0; j < NK; ++j) {
    const int i = (j + G_RING) % NK;
    const int rem = NK - 1 - j < LA - 1 ? NK - 1 - j : LA - 1;
    if (rem == 6) waitv4<24>(af[i][0], af[i][1], af[i][2], af[i][3]);
    else if (rem == 5) waitv4<20>(af[i][0], af[i][1], af[i][2], af[i][3]);
    else if (rem == 4) waitv4<16>(af[i][0], af[i][1], af[i][2], af[i][3]);
    else if (rem == 3) waitv4<12>(af[i][0], af[i][1], af[i][2], af[i][3]);
    else if (rem == 2) waitv4<8>(af[i][0], af[i][1], af[i][2], af[i][3]);
    else if (rem == 1) waitv4<4>(af[i][0], af[i][1], af[i][2], af[i][3]);
    else waitv4<0>(af[i][0], af[i][1], af[i][2], af[i][3]);
    if (j < NR) {
      MMA(wr[j], i);
    } else {
      bf16x8 wf[4];
      RDW(wf, i);
      MMA(wf, i);
    }
    SBAR();
    if (j + LA < NK) LOAD_A((j + LA + G_RING) % NK);
  }
#undef STAGE_W
#undef LOAD_A
#undef RDW
#undef MMA
  STAMP(2 + PH, 4);
  {
    f32x4* xb = (f32x4*)lds;
#pragma unroll
    for (int nb = 0; nb < 4; ++nb)
#pragma unroll
      for (int mb = 0; mb < 4; ++mb) xb[(w * 16 + nb * 4 + mb) * 64 + lane] = acc[nb][mb];
  }
  LBAR();
  {
    const f32x4* xb = (const f32x4*)lds;
#pragma unroll
    for (int e = 0; e < 2; ++e) {
      f32x4 a = xb[(2 * w + e) * 64 + lane];
#pragma unroll
      for (int v = 1; v < 8; ++v) a += xb[(v * 16 + 2 * w + e) * 64 + lane];
      const int mb = emb + e;
      if (PH == 0) {
        const int fi = 64 * nt + 16 * enb + 4 * g, d = 64 * mt + 16 * mb + l15;
        float z[4] = {a[0] + bflo(eb[0]), a[1] + bfhi(eb[0]), a[2] + bflo(eb[1]), a[3] + bfhi(eb[1])};
#pragma unroll
        for (int i = 0; i < 4; ++i) {
          const float zz = z[i], a2 = zz * (1.f + 0.044715f * zz * zz) * (-2.f * 0.7978845608028654f * 1.4426950408889634f);
          z[i] = zz * __builtin_amdgcn_rcpf(1.f + __builtin_amdgcn_exp2f(a2));
        }
        const unsigned long long v = (unsigned long long)cvtpk(z[0], z[1]) | ((unsigned long long)cvtpk(z[2], z[3]) << 32);
        __hip_atomic_store((gu64*)(out + ((size_t)(fi >> 3) * D + d) * 8 + (fi & 7)), v, RLX_AGENT);
      } else {
        const int d = 64 * mt + 16 * mb + 4 * g, s = 64 * nt + 16 * enb + l15;
        const float bb = bflo(eb[0]);
        const unsigned long long hv = e ? eh1 : eh0;
        const unsigned h0 = (unsigned)hv, h1 = (unsigned)(hv >> 32);
        *(u32x2*)(out + (size_t)s * D + d) =
            (u32x2){cvtpk(a[0] + bb + bflo(h0), a[1] + bb + bfhi(h0)), cvtpk(a[2] + bb + bflo(h1), a[3] + bb + bfhi(h1))};
      }
    }
  }
  STAMP(2 + PH, 5);
}
#define PUBLISH(c) do { asm volatile("s_waitcnt vmcnt(0) lgkmcnt(0)\n\ts_barrier" ::: "memory"); \
    if (threadIdx.x == 0) __hip_atomic_fetch_add((gu32*)(c), 1u, RLX_AGENT); } while (0)

extern "C" __global__ __launch_bounds__(512) void k_main(const u16* __restrict__ x, const u16* __restrict__ Qf, const u16* __restrict__ Kf,
                                              const u16* __restrict__ Vf, const u16* __restrict__ Wof, const u16* __restrict__ w1,
                                              const u16* __restrict__ b1, const u16* __restrict__ w2, const u16* __restrict__ b2,
                                              u16* H, u16* HN, u16* U, u16* out, unsigned* cnt) {
  __shared__ __attribute__((aligned(16))) char lds[K2_LDS];
  attn_phase(lds, x, Qf, Kf, Vf, Wof, w1, w2, H, HN);
  PUBLISH(cnt + (8 + (blockIdx.x & 7)) * 64);
  if (blockIdx.x < 196) {
    const int t = xcd_remap(blockIdx.x, 196);
    mlp_tile<0, 8>(lds, t, HN, w1, b1, H, U, cnt + 8 * 64, 32u);
    PUBLISH(cnt + (t % 7) * 64);
  }
  if (blockIdx.x < 224) {
    const int t2 = xcd_remap(blockIdx.x, 224);
    mlp_tile<1, 1>(lds, t2, U, w2, b2, H, out, cnt + (t2 % 7) * 64, 28u);
  }
}

extern "C" void kernel_launch(void* const* d_in, const int* in_sizes, int n_in, void* d_out, int out_size, void* d_ws, size_t ws_size,
                              hipStream_t stream) {
  const u16* x = (const u16*)d_in[0];
  const u16* wq = (const u16*)d_in[1];
  const u16* wk = (const u16*)d_in[2];
  const u16* wv = (const u16*)d_in[3];
  const u16* wo = (const u16*)d_in[4];
  const u16* w1 = (const u16*)d_in[6];
  const u16* b1 = (const u16*)d_in[7];
  const u16* w2 = (const u16*)d_in[8];
  const u16* b2 = (const u16*)d_in[9];
  char* ws = (char*)d_ws;
  u16 *Qf = (u16*)(ws + WS_QF), *Kf = (u16*)(ws + WS_KF), *Vf = (u16*)(ws + WS_VF), *Wof = (u16*)(ws + WS_WOF);
  u16 *H = (u16*)(ws + WS_H), *HN = (u16*)(ws + WS_HN), *U = (u16*)(ws + WS_U);
  unsigned* cnt = (unsigned*)(ws + WS_CNT);
  k_qkv<<<256, 512, 0, stream>>>(x, wq, wk, wv, wo, Qf, Kf, Vf, Wof, cnt);
  k_main<<<256, 512, 0, stream>>>(x, Qf, Kf, Vf, Wof, w1, b1, w2, b2, H, HN, U, (u16*)d_out, cnt);
}
```

```cpp
#include <hip/hip_runtime.h>
#include <hip/hip_bf16.h>

typedef unsigned short u16;
using bf16x8 = __attribute__((ext_vector_type(8))) short;
using s16x4 = __attribute__((ext_vector_type(4))) short;
using f32x4 = __attribute__((ext_vector_type(4))) float;
using f32x16 = __attribute__((ext_vector_type(16))) float;
using u32x4 = __attribute__((ext_vector_type(4))) unsigned;
using u32x2 = __attribute__((ext_vector_type(2))) unsigned;
typedef short v4i16_t __attribute__((ext_vector_type(4)));
typedef __attribute__((address_space(3))) const char* lds_cptr;
typedef __bf16 bf16x2_t __attribute__((ext_vector_type(2)));
typedef float f32x2_t __attribute__((ext_vector_type(2)));

#define DI __device__ __forceinline__
#define MFMA32(a, b, c) __builtin_amdgcn_mfma_f32_32x32x16_bf16(a, b, c, 0, 0, 0)
#define MFMA16(a, b, c) __builtin_amdgcn_mfma_f32_16x16x32_bf16(a, b, c, 0, 0, 0)
#define SBAR() __builtin_amdgcn_sched_barrier(0)
#define LBAR() asm volatile("s_waitcnt lgkmcnt(0)\n\ts_barrier" ::: "memory")
#define WAITV_BAR(N) asm volatile("s_waitcnt vmcnt(" #N ") lgkmcnt(0)\n\ts_barrier" ::: "memory")

constexpr int S = 2048, D = 448, DH = 28, F = 1792;
constexpr float QSCALE = 0.18898223650461362f * 1.4426950408889634f;
constexpr float LN_EPS = 1e-5f;

constexpr size_t WS_QF = 0;
constexpr size_t WS_KF = WS_QF + 2097152;
constexpr size_t WS_VF = WS_KF + 131072;
constexpr size_t WS_WOF = WS_VF + 131072;
constexpr size_t WS_H = WS_WOF + 401408;
constexpr size_t WS_HN = WS_H + 1835008;
constexpr size_t WS_U = WS_HN + 1835008;
constexpr size_t WS_CNT = WS_U + 1605632;

#define STAMP(k, i)
#define CSTAMP(k, i)

DI unsigned cvtpk(float lo, float hi) {
  const f32x2_t v = {lo, hi};
  return __builtin_bit_cast(unsigned, __builtin_convertvector(v, bf16x2_t));
}
DI float bflo(unsigned u) { return __uint_as_float(u << 16); }
DI float bfhi(unsigned u) { return __uint_as_float(u & 0xffff0000u); }
template <int CTRL> DI float dppf(float v) {
  return __uint_as_float(__builtin_amdgcn_update_dpp(0u, __float_as_uint(v), CTRL, 0xf, 0xf, true));
}
DI float wave_sum(float v) {
  v += dppf<0xB1>(v);
  v += dppf<0x4E>(v);
  v += dppf<0x141>(v);
  v += dppf<0x140>(v);
  auto a = __builtin_amdgcn_permlane16_swap(__float_as_uint(v), __float_as_uint(v), false, false);
  v = __uint_as_float(a[0]) + __uint_as_float(a[1]);
  auto b = __builtin_amdgcn_permlane32_swap(__float_as_uint(v), __float_as_uint(v), false, false);
  return __uint_as_float(b[0]) + __uint_as_float(b[1]);
}
DI float half_max(float v) {
  auto b = __builtin_amdgcn_permlane32_swap(__float_as_uint(v), __float_as_uint(v), false, false);
  return __builtin_fmaxf(__uint_as_float(b[0]), __uint_as_float(b[1]));
}
DI void glds16(const void* g, unsigned lds_base) {
  unsigned sv;
  asm volatile("s_mov_b32 %0, m0\n\ts_mov_b32 m0, %2\n\ts_nop 0\n\tglobal_load_lds_dwordx4 %1, off\n\ts_mov_b32 m0, %0"
               : "=&s"(sv) : "v"(g), "s"(lds_base) : "memory");
}
DI void glds16s(const void* sbase, unsigned voff, unsigned lds_base) {
  unsigned sv;
  asm volatile("s_mov_b32 %0, m0\n\ts_mov_b32 m0, %3\n\ts_nop 0\n\tglobal_load_lds_dwordx4 %1, %2\n\ts_mov_b32 m0, %0"
               : "=&s"(sv) : "v"(voff), "s"(sbase), "s"(lds_base) : "memory");
}
DI void glds4(const void* g, unsigned lds_base) {
  unsigned sv;
  asm volatile("s_mov_b32 %0, m0\n\ts_mov_b32 m0, %2\n\ts_nop 0\n\tglobal_load_lds_dword %1, off\n\ts_mov_b32 m0, %0"
               : "=&s"(sv) : "v"(g), "s"(lds_base) : "memory");
}
template <int N> DI void waitv4(bf16x8& a, bf16x8& b, bf16x8& c, bf16x8& d) {
  asm volatile("s_waitcnt vmcnt(%4)" : "+v"(a), "+v"(b), "+v"(c), "+v"(d) : "i"(N) : "memory");
}
template <int OFF> DI void gload16(bf16x8& d, const void* p) {
  asm volatile("global_load_dwordx4 %0, %1, off offset:%2" : "=v"(d) : "v"(p), "i"(OFF) : "memory");
}
DI s16x4 trread(lds_cptr p) {
  return __builtin_bit_cast(s16x4, __builtin_amdgcn_ds_read_tr16_b64_v4i16((__attribute__((address_space(3))) v4i16_t*)p));
}
DI bf16x8 ldsr16(lds_cptr p) { return *(const __attribute__((address_space(3))) bf16x8*)p; }
DI bf16x8 cat8(s16x4 a, s16x4 b) { return __builtin_shufflevector(a, b, 0, 1, 2, 3, 4, 5, 6, 7); }
DI int xcd_remap(int bid, int nwg) {
  const int q = nwg >> 3, r = nwg & 7, x = bid & 7, o = bid >> 3;
  return (x < r ? x * (q + 1) : r * (q + 1) + (x - r) * q) + o;
}

constexpr int K1_WI = 448 * 128, K1_X = 2 * K1_WI, K1_XS = 528, K1_DUM = K1_X + 64 * K1_XS, K1_LDS = K1_DUM + 1024;
constexpr int KMAIN_KB = 28;
__global__ __launch_bounds__(512) void k_qkv(const u16* __restrict__ x, const u16* __restrict__ wq, const u16* __restrict__ wk,
                                             const u16* __restrict__ wv, const u16* __restrict__ wo, u16* __restrict__ Qf,
                                             u16* __restrict__ Kf, u16* __restrict__ Vf, u16* __restrict__ Wof, unsigned* __restrict__ cnt) {
  __shared__ __attribute__((aligned(16))) char lds[K1_LDS];
  const int tid = threadIdx.x, lane = tid & 63, w = __builtin_amdgcn_readfirstlane(tid >> 6);
  const int t = xcd_remap(blockIdx.x, 256), rt = t >> 2, ct = t & 3;
  const unsigned lds0 = (unsigned)(uintptr_t)lds;
  STAMP(0, 0);
  if (w < 5) {
    unsigned long long pc;
    asm volatile("s_getpc_b64 %0" : "=s"(pc));
    glds16s((const char*)(pc & ~63ull) + w * 1024, lane * 16, lds0 + K1_DUM);
  }
  bf16x8 xr[4];
  const bool act = lane < 56;
  {
    const u16* xp_ = x + (size_t)(rt * 32 + w * 4) * D + (act ? lane : 55) * 8;
    gload16<0>(xr[0], xp_); gload16<896>(xr[1], xp_); gload16<1792>(xr[2], xp_); gload16<2688>(xr[3], xp_);
  }
#pragma unroll
  for (int im = 0; im < 2; ++im) {
    const int c64 = 2 * ct + im;
    if (c64 < 7) {
#pragma unroll
      for (int i = 0; i < 7; ++i) {
        const int piece = w * 7 + i, row = piece * 8 + (lane >> 3);
        const int f = ((row >> 1) & 1) | (((row >> 3) & 1) << 1);
        const int c = (lane & 7) ^ (f << 1);
        glds16(wq + (size_t)row * D + c64 * 64 + c * 8, lds0 + im * K1_WI + piece * 1024);
      }
    } else {
#pragma unroll
      for (int i = 0; i < 7; ++i) {
        const int pc = w * 7 + i < 50 ? w * 7 + i : 49, mat = pc >= 25, piece = pc - 25 * mat;
        const int e = piece * 512 + lane * 8;
        glds16((mat ? wv : wk) + (e < 448 * 28 ? e : 448 * 28 - 8), lds0 + im * K1_WI + mat * 25600 + piece * 1024);
      }
    }
  }
  u16 wt[8];
  const int frag0 = blockIdx.x + 256 * (w & 1);
  const bool dofrag = w < 2 && frag0 < 392;
  const int frag = dofrag ? frag0 : 0;
  {
    const int nb = frag / 14, ks = frag - nb * 14;
#pragma unroll
    for (int j = 0; j < 8; ++j) wt[j] = wo[(size_t)(32 * ks + 8 * (lane >> 4) + j) * D + 16 * nb + (lane & 15)];
  }
  SBAR();
  waitv4<22>(xr[0], xr[1], xr[2], xr[3]);
  {
    u32x4 xv[4];
#pragma unroll
    for (int r = 0; r < 4; ++r) xv[r] = __builtin_bit_cast(u32x4, xr[r]);
    float v[4][8], mean[4], rs[4];
#pragma unroll
    for (int r = 0; r < 4; ++r) {
      float s = 0.f;
#pragma unroll
      for (int i = 0; i < 4; ++i) {
        v[r][2 * i] = act ? bflo(xv[r][i]) : 0.f; v[r][2 * i + 1] = act ? bfhi(xv[r][i]) : 0.f;
        s += v[r][2 * i] + v[r][2 * i + 1];
      }
      mean[r] = s;
    }
#pragma unroll
    for (int r = 0; r < 4; ++r) mean[r] = wave_sum(mean[r]) * (1.f / D);
#pragma unroll
    for (int r = 0; r < 4; ++r) {
      float q = 0.f;
#pragma unroll
      for (int i = 0; i < 8; ++i) { v[r][i] = act ? v[r][i] - mean[r] : 0.f; q += v[r][i] * v[r][i]; }
      rs[r] = q;
    }
#pragma unroll
    for (int r = 0; r < 4; ++r) rs[r] = __builtin_amdgcn_rsqf(wave_sum(rs[r]) * (1.f / D) + LN_EPS);
    char* xdst = lds + K1_X + lane * K1_XS + w * 64;
#pragma unroll
    for (int r = 0; r < 4; ++r) {
      u32x4 o;
#pragma unroll
      for (int i = 0; i < 4; ++i) o[i] = cvtpk(v[r][2 * i] * rs[r], v[r][2 * i + 1] * rs[r]);
      *(u32x4*)(xdst + r * 16) = o;
    }
  }
  STAMP(0, 1);
  WAITV_BAR(0);
  STAMP(0, 2);
  if (w == 7 && (blockIdx.x >> 3) < KMAIN_KB) {
    unsigned long long pc;
    int off;
    asm volatile("s_getpc_b64 %0\n1:\n\ts_mov_b32 %1, k_main-1b" : "=s"(pc), "=s"(off));
    glds16s((const char*)(pc + (long long)off) + (blockIdx.x >> 3) * 1024, lane * 16, lds0 + K1_DUM);
  }
  const int nb = w & 3, im = w >> 2, c64 = 2 * ct + im, g = lane >> 4, q4 = (lane & 15) >> 2, p = lane & 3, l15 = lane & 15;
  const int f = ((q4 >> 1) & 1) | ((g & 1) << 1);
  const bool kv = c64 == 7;
  const int rs_ = kv ? 56 : 128;
  const lds_cptr wp = (lds_cptr)lds + im * K1_WI + (8 * g + q4) * rs_ +
                      (kv ? (nb >> 1) * 25600 + (nb & 1) * 32 + 8 * p : (((2 * nb + (p >> 1)) ^ (f << 1)) * 16) + 8 * (p & 1));
  const lds_cptr xp = (lds_cptr)lds + K1_X + g * K1_XS + l15 * 16;
  f32x4 acc0 = {0.f, 0.f, 0.f, 0.f}, acc1 = {0.f, 0.f, 0.f, 0.f};
  {
    bf16x8 a[14], b0[14], b1[14];
#pragma unroll
    for (int ks = 0; ks < 14; ++ks) {
      a[ks] = cat8(trread(wp + ks * 32 * rs_), trread(wp + ks * 32 * rs_ + 4 * rs_));
      b0[ks] = ldsr16(xp + ks * 4 * K1_XS);
      b1[ks] = ldsr16(xp + ks * 4 * K1_XS + 256);
    }
    SBAR();
#pragma unroll
    for (int ks = 0; ks < 14; ++ks) {
      acc0 = MFMA16(a[ks], b0[ks], acc0);
      acc1 = MFMA16(a[ks], b1[ks], acc1);
    }
  }
  STAMP(0, 3);
#pragma unroll
  for (int tb = 0; tb < 2; ++tb) {
    const f32x4 a = tb ? acc1 : acc0;
    const int s = rt * 32 + 16 * tb + l15;
    const int nl = 16 * nb + 4 * g;
    if (c64 < 7) {
      const int n0 = 64 * c64 + nl, h = n0 / 28, d0 = n0 - 28 * h;
      const int tg = s >> 3, c = (h & 3) * 8 + (s & 7), b = h >> 2;
      const size_t off = ((((size_t)(tg * 4 + b) * 2 + (d0 >> 4)) * 64) + ((d0 >> 3) & 1) * 32 + c) * 8 + (d0 & 7);
      *(u32x2*)(Qf + off) = (u32x2){cvtpk(a[0] * QSCALE, a[1] * QSCALE), cvtpk(a[2] * QSCALE, a[3] * QSCALE)};
    } else {
      const int kt = s >> 5, kk = s & 31;
      const int d0 = 16 * (nb & 1) + 4 * g;
      if (nb < 2) {
        const size_t off = ((((size_t)kt * 2 + (d0 >> 4)) * 64) + ((d0 >> 3) & 1) * 32 + kk) * 8 + (d0 & 7);
        u32x2 v = {cvtpk(a[0], a[1]), cvtpk(a[2], a[3])};
        if (d0 == 28) v = (u32x2){0x3f803f80u, 0x00003f80u};
        *(u32x2*)(Kf + off) = v;
      } else {
        const int sp = kk >> 4, j = ((kk >> 3) & 1) * 4 + (kk & 3), hi = (kk >> 2) & 1;
        const size_t off = ((((size_t)kt * 2 + sp) * 64) + hi * 32 + d0) * 8 + j;
        unsigned v0 = cvtpk(a[0], a[1]), v1 = cvtpk(a[2], a[3]);
        if (d0 == 28) { v0 = 0x00003f80u; v1 = 0u; }
        Vf[off] = (u16)v0; Vf[off + 8] = (u16)(v0 >> 16); Vf[off + 16] = (u16)v1; Vf[off + 24] = (u16)(v1 >> 16);
      }
    }
  }
  if (dofrag) {
    u32x4 o;
#pragma unroll
    for (int i = 0; i < 4; ++i) o[i] = (unsigned)wt[2 * i] | ((unsigned)wt[2 * i + 1] << 16);
    *(u32x4*)(Wof + ((size_t)frag * 64 + lane) * 8) = o;
  }
  if (blockIdx.x == 0) { cnt[tid] = 0u; cnt[512 + tid] = 0u; }
  if (w == 7) asm volatile("s_waitcnt vmcnt(0)" ::: "memory");
  STAMP(0, 4);
}

constexpr int K2_RING = 0, K2_OT = 131072, K2_DUM = K2_OT + 7168, K2_X = K2_DUM + 4096, K2_LDS = K2_X + 16384;
constexpr int K2_HB = 65536, HBS = 452, K2_HS = K2_X;

DI float set_ref(bf16x8& q1, float m, int hi) {
  const float a = bflo(cvtpk(-m, 0.f)), r1 = -m - a, b = bflo(cvtpk(r1, 0.f)), c = bflo(cvtpk(r1 - b, 0.f));
  u32x4 v = __builtin_bit_cast(u32x4, q1);
  v[2] = hi ? cvtpk(a, b) : v[2];
  v[3] = hi ? cvtpk(c, 0.f) : v[3];
  q1 = __builtin_bit_cast(bf16x8, v);
  return -((a + b) + c);
}
DI float tile_max(const f32x16& s) {
  float a = __builtin_fmaxf(__builtin_fmaxf(s[0], s[1]), s[2]), b = __builtin_fmaxf(__builtin_fmaxf(s[3], s[4]), s[5]);
  a = __builtin_fmaxf(__builtin_fmaxf(a, s[6]), s[7]); b = __builtin_fmaxf(__builtin_fmaxf(b, s[8]), s[9]);
  a = __builtin_fmaxf(__builtin_fmaxf(a, s[10]), s[11]); b = __builtin_fmaxf(__builtin_fmaxf(b, s[12]), s[13]);
  a = __builtin_fmaxf(__builtin_fmaxf(a, s[14]), s[15]);
  return half_max(__builtin_fmaxf(a, b));
}
template <int N> DI void waitv() { asm volatile("s_waitcnt vmcnt(%0)" ::"i"(N) : "memory"); }
#define RBAR() asm volatile("s_barrier" ::: "memory")

DI void attn_safe(const u16* __restrict__ Kf, const u16* __restrict__ Vf, bf16x8 q0, bf16x8 q1, f32x16& o, int lane, int hi) {
  const f32x16 zero = {};
  float mref = 0.f;
  o = zero;
#pragma unroll 1
  for (int t = 0; t < 64; ++t) {
    const bf16x8 k0 = *(const bf16x8*)(Kf + t * 1024 + lane * 8), k1 = *(const bf16x8*)(Kf + t * 1024 + 512 + lane * 8);
    const bf16x8 v0 = *(const bf16x8*)(Vf + t * 1024 + lane * 8), v1 = *(const bf16x8*)(Vf + t * 1024 + 512 + lane * 8);
    (void)set_ref(q1, 0.f, hi);
    f32x16 s = MFMA32(k0, q0, zero);
    s = MFMA32(k1, q1, s);
    const float tm = tile_max(s);
    const float mn = set_ref(q1, t == 0 ? tm : __builtin_fmaxf(tm, mref), hi);
    const float al = t == 0 ? 0.f : __builtin_amdgcn_exp2f(mref - mn);
#pragma unroll
    for (int r = 0; r < 16; ++r) o[r] *= al;
    mref = mn;
    s = MFMA32(k0, q0, zero);
    s = MFMA32(k1, q1, s);
    u32x4 p0, p1;
#pragma unroll
    for (int i = 0; i < 4; ++i) {
      p0[i] = cvtpk(__builtin_amdgcn_exp2f(s[2 * i]), __builtin_amdgcn_exp2f(s[2 * i + 1]));
      p1[i] = cvtpk(__builtin_amdgcn_exp2f(s[8 + 2 * i]), __builtin_amdgcn_exp2f(s[8 + 2 * i + 1]));
    }
    o = MFMA32(v0, __builtin_bit_cast(bf16x8, p0), o);
    o = MFMA32(v1, __builtin_bit_cast(bf16x8, p1), o);
  }
}

#define EX4(S, i) do { S[i] = __builtin_amdgcn_exp2f(S[i]); S[i + 1] = __builtin_amdgcn_exp2f(S[i + 1]); \
    S[i + 2] = __builtin_amdgcn_exp2f(S[i + 2]); S[i + 3] = __builtin_amdgcn_exp2f(S[i + 3]); } while (0)
#define PK8(P, S, i) do { P[0] = cvtpk(S[i], S[i + 1]); P[1] = cvtpk(S[i + 2], S[i + 3]); P[2] = cvtpk(S[i + 4], S[i + 5]); P[3] = cvtpk(S[i + 6], S[i + 7]); } while (0)
#define BF(P) __builtin_bit_cast(bf16x8, P)
constexpr int CPRE = 0;
#define WOPF(j) do { if ((j) < CPRE) wf[(j) / 7][(j) % 7] = WOFR(j); } while (0)
#define RDS(u, off) ldsr16(((u) < 4 ? B0 : B1) + ((u) & 3) * 8192 + (off))
#define CTILE(u, NX, PF) do { \
      \
    sB = MFMA32(k0, qB0, zero); SBAR(); if (NX) k0 = RDS((u) + 1, 0); EX4(sA, 0); WOPF(PF); SBAR(); \
    sB = MFMA32(k1, qB1, sB); SBAR(); if (NX) k1 = RDS((u) + 1, 1024); EX4(sA, 4); PK8(p0, sA, 0); SBAR(); \
    oB = MFMA32(v1, BF(p1), oB); SBAR(); v1 = RDS(u, 3072); EX4(sA, 8); WOPF((PF) + 1); SBAR(); \
    oA = MFMA32(v0, BF(p0), oA); SBAR(); EX4(sA, 12); PK8(p1, sA, 8); SBAR(); \
      \
    sA = MFMA32(k0, qA0, zero); SBAR(); EX4(sB, 0); WOPF((PF) + 2); SBAR(); \
    sA = MFMA32(k1, qA1, sA); SBAR(); EX4(sB, 4); PK8(p0, sB, 0); SBAR(); \
    oA = MFMA32(v1, BF(p1), oA); SBAR(); EX4(sB, 8); WOPF((PF) + 3); SBAR(); \
    oB = MFMA32(v0, BF(p0), oB); SBAR(); if (NX) v0 = RDS((u) + 1, 2048); EX4(sB, 12); PK8(p1, sB, 8); SBAR(); } while (0)
#define CSTAGE(LAST, PF) do { \
    CTILE(0, true, PF); CTILE(1, true, (PF) + 4); CTILE(2, true, (PF) + 8); \
    if (!(LAST)) RBAR(); \
    CTILE(3, !(LAST), (PF) + 12); } while (0)

DI void st16c(void* p, u32x4 v) {
  asm volatile("global_store_dwordx4 %0, %1, off sc1\n\ts_nop 1" ::"v"(p), "v"(v) : "memory");
}
DI void attn_phase(char* lds, const u16* __restrict__ x, const u16* __restrict__ Qf, const u16* __restrict__ Kf,
                   const u16* __restrict__ Vf, const u16* __restrict__ Wof, const u16* __restrict__ w1,
                   const u16* __restrict__ w2, u16* H, u16* HN) {
  const int tid = threadIdx.x, lane = tid & 63, w = __builtin_amdgcn_readfirstlane(tid >> 6), hi = lane >> 5;
  const int tg = blockIdx.x;
  STAMP(1, 0);
  const int kh = w >> 2, ng = w & 3;
  const char* wob = (const char*)(Wof + (size_t)(7 * ng * 14 + 7 * kh) * 512);
  const unsigned wl = lane * 16;
  bf16x8 wf[7][7];
#define WOFR(i_) (*(const bf16x8*)(wob + (((i_) / 7) * 14 + (i_) % 7) * 1024 + wl))
#define LDWO(i0, i1) do { _Pragma("unroll") for (int i_ = i0; i_ < i1; ++i_) wf[i_ / 7][i_ % 7] = WOFR(i_); } while (0)
#define BOF(k) (*(const bf16x8*)(lds + K2_OT + ((4 * (k) + (lane >> 4)) * 8 + (lane & 7)) * 16))
#define HBST(acc) do { if ((lane & 15) < 8) { \
      _Pragma("unroll") for (int n = 0; n < 7; ++n) *(f32x4*)(lds + K2_HB + ((kh * 8 + (lane & 15)) * HBS + 16 * (7 * ng + n) + 4 * (lane >> 4)) * 4) = acc[n]; \
    } } while (0)
  u32x4 xv;
  if (w < 4) {
    const f32x16 zero = {};
    const int qh = w & 1, kp = w >> 1;
    const lds_cptr rp0 = (lds_cptr)lds + K2_RING + lane * 16;
    const lds_cptr rp = rp0 + kp * 4096;
    lds_cptr B0 = rp, B1 = rp + 32768;
    const u16* qp = Qf + ((size_t)((tg * 4 + 2 * qh) * 2) * 64 + lane) * 8;
    bf16x8 qA0 = *(const bf16x8*)(qp), qA1 = *(const bf16x8*)(qp + 512), qB0 = *(const bf16x8*)(qp + 1024), qB1 = *(const bf16x8*)(qp + 1536);
    RBAR();
    bf16x8 k0 = ldsr16(rp0), k1 = ldsr16(rp0 + 1024), v0 = RDS(0, 2048), v1 = v0;
    (void)set_ref(qA1, 0.f, hi);
    (void)set_ref(qB1, 0.f, hi);
    f32x16 sA = MFMA32(k0, qA0, zero), sB = MFMA32(k0, qB0, zero);
    sA = MFMA32(k1, qA1, sA);
    sB = MFMA32(k1, qB1, sB);
    k0 = RDS(0, 0); k1 = RDS(0, 1024);
    (void)set_ref(qA1, tile_max(sA), hi);
    (void)set_ref(qB1, tile_max(sB), hi);
    sA = MFMA32(k0, qA0, zero);
    sA = MFMA32(k1, qA1, sA);
    f32x16 oA = zero, oB = zero;
    u32x4 p0, p1 = {0u, 0u, 0u, 0u};
    SBAR();
    STAMP(1, 8);
#pragma unroll 1
    for (int st = 0; st < 4; ++st) {
      CSTAGE(false, 64);
      B0 = B1; B1 = rp + ((st + 2) & 3) * 32768;
    }
    CSTAGE(false, 0);
    B0 = B1; B1 = rp + 2 * 32768;
    CSTAGE(false, 16);
    B0 = B1; B1 = rp + 3 * 32768;
    CSTAGE(false, 64);
    B0 = B1;
    CSTAGE(true, 64);
    oB = MFMA32(v1, BF(p1), oB);
    STAMP(1, 10);
    {
      const f32x16 snd = kp ? oA : oB;
#pragma unroll
      for (int r4 = 0; r4 < 4; ++r4)
        *(f32x4*)(lds + K2_X + ((w * 4 + r4) * 64 + lane) * 16) = (f32x4){snd[4 * r4], snd[4 * r4 + 1], snd[4 * r4 + 2], snd[4 * r4 + 3]};
    }
    f32x16 o0 = kp ? oB : oA;
    bf16x8 qk0 = kp ? qB0 : qA0, qk1 = kp ? qB1 : qA1;
    asm volatile("" : "+v"(o0), "+v"(qk0), "+v"(qk1));
    SBAR();
    LBAR();
#pragma unroll
    for (int r4 = 0; r4 < 4; ++r4) {
      const f32x4 t = *(const f32x4*)(lds + K2_X + (((w ^ 2) * 4 + r4) * 64 + lane) * 16);
      o0[4 * r4] += t[0]; o0[4 * r4 + 1] += t[1]; o0[4 * r4 + 2] += t[2]; o0[4 * r4 + 3] += t[3];
    }
    const int qb = 2 * qh + kp;
    {
      float chk = 0.f;
#pragma unroll
      for (int r = 0; r < 16; ++r) chk = __builtin_fmaf(o0[r], 0.f, chk);
      const bool bad = !(chk == 0.f) || !(o0[12] < 1.8e19f);
      if (__any(bad)) {
        attn_safe(Kf, Vf, qk0, qk1, o0, lane, hi);
      }
    }
    {
      auto sw = __builtin_amdgcn_permlane32_swap(__float_as_uint(o0[12]), __float_as_uint(o0[12]), false, false);
      const float il = 1.f / __uint_as_float(sw[1]);
      const int c = lane & 31;
#pragma unroll
      for (int r4 = 0; r4 < 4; ++r4) {
        const int d = 8 * r4 + 4 * hi, k = 28 * (4 * qb + (c >> 3)) + d;
        if (d < 28)
          *(u32x2*)(lds + K2_OT + ((k >> 3) * 8 + (c & 7)) * 16 + (k & 7) * 2) =
              (u32x2){cvtpk(o0[4 * r4] * il, o0[4 * r4 + 1] * il), cvtpk(o0[4 * r4 + 2] * il, o0[4 * r4 + 3] * il)};
      }
    }
    STAMP(1, 1);
    LBAR();
    STAMP(1, 2);
    LDWO(16, 39);
    xv = *(const u32x4*)(x + (size_t)(tg * 8 + w) * D + (lane < 56 ? lane : 55) * 8);
    SBAR();
    {
      bf16x8 bo[7];
#pragma unroll
      for (int k = 0; k < 7; ++k) bo[k] = BOF(k);
#pragma unroll
      for (int i = 0; i < 16; ++i) wf[i / 7][i % 7] = *(const bf16x8*)(lds + K2_RING + (i >> 3) * 32768 + w * 8192 + (i & 7) * 1024 + lane * 16);
      f32x4 acc[7];
#pragma unroll
      for (int n = 0; n < 7; ++n) acc[n] = (f32x4){0.f, 0.f, 0.f, 0.f};
#pragma unroll
      for (int i = 0; i < 39; ++i) acc[i / 7] = MFMA16(wf[i / 7][i % 7], bo[i % 7], acc[i / 7]);
      HBST(acc);
    }
  } else {
    const int hw = w - 4;
    __builtin_amdgcn_s_setprio(3);
    const unsigned ring = (unsigned)(uintptr_t)lds + K2_RING + hw * 8192;
    const u16* kp = Kf + (size_t)hw * 2048;
    const u16* vp = Vf + (size_t)hw * 2048;
    const unsigned l16 = lane * 16;
#define DMAS(s_) do { const unsigned sl_ = ring + ((s_) & 3) * 32768; \
    _Pragma("unroll") for (int u_ = 0; u_ < 2; ++u_) { \
      glds16s(kp + (s_) * 8192 + u_ * 1024, l16, sl_ + u_ * 4096); glds16s(kp + (s_) * 8192 + u_ * 1024 + 512, l16, sl_ + u_ * 4096 + 1024); \
      glds16s(vp + (s_) * 8192 + u_ * 1024, l16, sl_ + u_ * 4096 + 2048); glds16s(vp + (s_) * 8192 + u_ * 1024 + 512, l16, sl_ + u_ * 4096 + 3072); } } while (0)
    const unsigned dum = (unsigned)(uintptr_t)lds + K2_DUM + hw * 1024;
    const int xg = tg & 7, rk = tg >> 3;
    const int ts = xg < 4 ? 25 * xg : 100 + 24 * (xg - 4), te = ts + (xg < 4 ? 25 : 24);
    const int n0 = ts / 7, n1 = (te - 1) / 7;
    const int nb0 = n0 + 3 <= 27 ? n0 : 24;
    const u16* p1 = w1 + (size_t)(64 * rk + 16 * hw) * F + nb0 * 64;
    const u16* p1b = w1 + (size_t)(64 * rk + 16 * hw) * F + n1 * 64;
    const u16* p2 = w2 + (size_t)(56 * rk + 14 * hw) * S + 4 * xg * 64;
    const unsigned o1 = ((lane >> 5) * F + (lane & 31) * 8) * 2, o1b = ((lane >> 3) * F + (lane & 7) * 8) * 2, o2 = ((lane >> 5) * S + (lane & 31) * 8) * 2;
#define PFD(i0, i1) do { _Pragma("unroll") for (int i_ = i0; i_ < i1; ++i_) { \
      if (i_ < 8) glds16s(p1 + (size_t)i_ * 2 * F, o1, dum); \
      else if (i_ < 10) glds16s(p1b + (size_t)(i_ - 8) * 8 * F, o1b, dum); \
      else glds16s(p2 + (size_t)(i_ - 10) * 2 * S, o2, dum); } } while (0)
    DMAS(0); DMAS(1); DMAS(2);
    waitv<16>(); RBAR();
    LDWO(0, 12); SBAR(); PFD(0, 3);
    waitv<23>(); RBAR();
    DMAS(3); LDWO(12, 24); SBAR(); PFD(3, 6);
    waitv<38>(); RBAR();
    DMAS(4); LDWO(24, 36); SBAR(); PFD(6, 9);
    waitv<38>(); RBAR();
    DMAS(5); LDWO(36, 49); SBAR(); PFD(9, 12);
    waitv<39>(); RBAR();
    bf16x8 wx[10];
    DMAS(6);
#pragma unroll
    for (int i = 0; i < 10; ++i) wx[i] = *(const bf16x8*)(wob - 7 * 1024 + (((39 + i) / 7) * 14 + (39 + i) % 7) * 1024 + wl);
    SBAR(); PFD(12, 15);
    waitv<37>(); RBAR();
    DMAS(7); PFD(15, 17);
    waitv<23>(); RBAR();
#pragma unroll
    for (int i = 0; i < 8; ++i) glds16s(wob - 7 * 1024 + ((i / 7) * 14 + i % 7) * 1024, wl, ring + i * 1024);
    waitv<10>(); RBAR();
#pragma unroll
    for (int i = 8; i < 16; ++i) glds16s(wob - 7 * 1024 + ((i / 7) * 14 + i % 7) * 1024, wl, ring + 32768 + (i - 8) * 1024);
    xv = *(const u32x4*)(x + (size_t)(tg * 8 + w) * D + (lane < 56 ? lane : 55) * 8);
    SBAR();
    __builtin_amdgcn_s_setprio(0);
    RBAR();
    waitv<1>();
    STAMP(1, 1);
    LBAR();
    STAMP(1, 2);
    {
      f32x4 acc[7];
      {
        bf16x8 bo[7];
#pragma unroll
        for (int k = 0; k < 7; ++k) bo[k] = BOF(k);
        acc[5] = (f32x4){0.f, 0.f, 0.f, 0.f}; acc[6] = acc[5];
#pragma unroll
        for (int i = 0; i < 10; ++i) acc[(39 + i) / 7] = MFMA16(wx[i], bo[(39 + i) % 7], acc[(39 + i) / 7]);
      }
      SBAR();
      bf16x8 bo[7];
#pragma unroll
      for (int k = 0; k < 7; ++k) bo[k] = BOF(7 + k);
#pragma unroll
      for (int n = 0; n < 7; ++n) {
        if (n < 5) acc[n] = (f32x4){0.f, 0.f, 0.f, 0.f};
#pragma unroll
        for (int k = 0; k < 7; ++k) acc[n] = MFMA16(wf[n][k], bo[k], acc[n]);
      }
      HBST(acc);
    }
  }
  LBAR();
  STAMP(1, 3);
  {
    const int s = tg * 8 + w;
    const bool act = lane < 56;
    const int lc = act ? lane : 55;
    float v[8];
    const f32x4 a0 = *(const f32x4*)(lds + K2_HB + (w * HBS + lc * 8) * 4), a1 = *(const f32x4*)(lds + K2_HB + (w * HBS + lc * 8 + 4) * 4);
    const f32x4 c0 = *(const f32x4*)(lds + K2_HB + ((8 + w) * HBS + lc * 8) * 4), c1 = *(const f32x4*)(lds + K2_HB + ((8 + w) * HBS + lc * 8 + 4) * 4);
    u32x4 hv;
#pragma unroll
    for (int i = 0; i < 2; ++i) {
      hv[i] = cvtpk(bflo(xv[i]) + a0[2 * i] + c0[2 * i], bfhi(xv[i]) + a0[2 * i + 1] + c0[2 * i + 1]);
      hv[2 + i] = cvtpk(bflo(xv[2 + i]) + a1[2 * i] + c1[2 * i], bfhi(xv[2 + i]) + a1[2 * i + 1] + c1[2 * i + 1]);
    }
    if (act) st16c(H + (size_t)s * D + lane * 8, hv);
#pragma unroll
    for (int i = 0; i < 4; ++i) { v[2 * i] = act ? bflo(hv[i]) : 0.f; v[2 * i + 1] = act ? bfhi(hv[i]) : 0.f; }
#pragma unroll
    for (int pass = 0; pass < 2; ++pass) {
      float sm = 0.f;
#pragma unroll
      for (int i = 0; i < 8; ++i) sm += v[i];
      const float mean = wave_sum(sm) * (1.f / D);
      float q = 0.f;
#pragma unroll
      for (int i = 0; i < 8; ++i) { v[i] = act ? v[i] - mean : 0.f; q += v[i] * v[i]; }
      const float rs = __builtin_amdgcn_rsqf(wave_sum(q) * (1.f / D) + LN_EPS);
#pragma unroll
      for (int i = 0; i < 8; ++i) v[i] *= rs;
    }
    if (act) *(u32x4*)(lds + K2_HS + w * 912 + lane * 16) = (u32x4){cvtpk(v[0], v[1]), cvtpk(v[2], v[3]), cvtpk(v[4], v[5]), cvtpk(v[6], v[7])};
  }
  LBAR();
  if (tid < D / 2) {
    unsigned r[8];
#pragma unroll
    for (int i = 0; i < 8; ++i) r[i] = *(const unsigned*)(lds + K2_HS + i * 912 + tid * 4);
    u32x4 lo, hi2;
#pragma unroll
    for (int i = 0; i < 4; ++i) {
      lo[i] = (r[2 * i] & 0xffffu) | (r[2 * i + 1] << 16);
      hi2[i] = (r[2 * i] >> 16) | (r[2 * i + 1] & 0xffff0000u);
    }
    u32x4* dst = (u32x4*)(HN + ((size_t)tg * D + 2 * tid) * 8);
    st16c(dst, lo); st16c(dst + 1, hi2);
  }
  STAMP(1, 4);
}

constexpr int G_RING = 4, G_LDS = 8 * G_RING * 4096;
typedef __attribute__((address_space(1))) unsigned gu32;
typedef __attribute__((address_space(1))) unsigned long long gu64;
#define RLX_AGENT __ATOMIC_RELAXED, __HIP_MEMORY_SCOPE_AGENT
template <int OFF> DI void gload16c(bf16x8& d, const void* p) {
  asm volatile("global_load_dwordx4 %0, %1, off offset:%2 sc1" : "=v"(d) : "v"(p), "i"(OFF) : "memory");
}
template <int PH, int NSH>
DI void mlp_tile(char* lds, int t, const u16* act, const u16* __restrict__ W, const u16* __restrict__ bias, const u16* H, u16* out,
                 unsigned* cnt, unsigned want) {
  constexpr int KT = PH == 0 ? S : F, LDW = PH == 0 ? F : S, NK = KT / 256, NR = NK - G_RING, LA = 5;
  const int tid = threadIdx.x, lane = tid & 63, w = __builtin_amdgcn_readfirstlane(tid >> 6);
  const int nt = t / 7, mt = t - nt * 7;
  const unsigned lds0 = (unsigned)(uintptr_t)lds + w * (G_RING * 4096);
  const int g = lane >> 4, q4 = (lane & 15) >> 2, p = lane & 3, l15 = lane & 15;
  STAMP(2 + PH, 0);
  const int enb = w >> 1, emb = 2 * (w & 1);
  u32x2 eb;
  if (PH == 0) eb = *(const u32x2*)(bias + 64 * nt + 16 * enb + 4 * g);
  else eb[0] = bias[64 * nt + 16 * enb + l15];
  SBAR();
  const u16* asrc = act + ((size_t)(4 * w + g) * D + 64 * mt + l15) * 8;
  const int r8 = lane >> 3, fe = (r8 >> 1) & 1;
  const u16* wsrc0 = W + (size_t)(32 * w + r8) * LDW + 64 * nt + ((lane & 7) ^ (fe << 1)) * 8;
  const u16* wsrc1 = W + (size_t)(32 * w + 8 + r8) * LDW + 64 * nt + ((lane & 7) ^ ((fe | 2) << 1)) * 8;
  const int f = ((q4 >> 1) & 1) | ((g & 1) << 1);
  int woff[4];
#pragma unroll
  for (int nb = 0; nb < 4; ++nb) woff[nb] = w * (G_RING * 4096) + (8 * g + q4) * 128 + (((2 * nb + (p >> 1)) ^ (f << 1)) * 16) + 8 * (p & 1);
  bf16x8 af[NK][4];
  f32x4 acc[4][4] = {};
#define STAGE_W(i) do { const unsigned sl_ = lds0 + ((i) % G_RING) * 4096; const size_t o_ = (size_t)(i) * 256 * LDW; \
    glds16(wsrc0 + o_, sl_); glds16(wsrc1 + o_, sl_ + 1024); glds16(wsrc0 + o_ + 16 * LDW, sl_ + 2048); glds16(wsrc1 + o_ + 16 * LDW, sl_ + 3072); } while (0)
#define LOAD_A(i) do { const u16* ap_ = asrc + (size_t)(i) * 32 * D * 8; \
    gload16c<0>(af[i][0], ap_); gload16c<256>(af[i][1], ap_); gload16c<512>(af[i][2], ap_); gload16c<768>(af[i][3], ap_); } while (0)
#define RDW(WF, i) do { _Pragma("unroll") for (int nb_ = 0; nb_ < 4; ++nb_) { const lds_cptr sp_ = (lds_cptr)lds + woff[nb_] + ((i) % G_RING) * 4096; \
      WF[nb_] = cat8(trread(sp_), trread(sp_ + 512)); } } while (0)
#define MMA(WF, i) do { _Pragma("unroll") for (int nb_ = 0; nb_ < 4; ++nb_) _Pragma("unroll") for (int mb_ = 0; mb_ < 4; ++mb_) \
      acc[nb_][mb_] = PH == 0 ? MFMA16(WF[nb_], af[i][mb_], acc[nb_][mb_]) : MFMA16(af[i][mb_], WF[nb_], acc[nb_][mb_]); } while (0)
  bf16x8 wr[NR][4];
#pragma unroll
  for (int j = 0; j < NR; ++j) STAGE_W(G_RING + j);
  if (NR < G_RING) STAGE_W(G_RING - 1);
#pragma unroll
  for (int j = 0; j < NR; ++j) {
    waitv<12>();
    RDW(wr[j], G_RING + j);
    asm volatile("s_waitcnt lgkmcnt(0)" ::: "memory");
    STAGE_W(j);
  }
  STAMP(2 + PH, 1);
  if (w == 0) {
    gu32* cp = (gu32*)(cnt + (lane < NSH ? lane : 0) * 64);
    for (unsigned spins = 0; spins < (1u << 20); ++spins) {
      if (__all(__hip_atomic_load(cp, RLX_AGENT) >= want)) break;
      __builtin_amdgcn_s_sleep(1);
    }
  }
  asm volatile("s_barrier" ::: "memory");
  STAMP(2 + PH, 2);
  unsigned long long eh0 = 0, eh1 = 0;
  if (PH == 1) {
    const int s = 64 * nt + 16 * enb + l15;
    eh0 = __hip_atomic_load((gu64*)(H + (size_t)s * D + 64 * mt + 16 * emb + 4 * g), RLX_AGENT);
    eh1 = __hip_atomic_load((gu64*)(H + (size_t)s * D + 64 * mt + 16 * emb + 16 + 4 * g), RLX_AGENT);
  }
  SBAR();
#pragma unroll
  for (int j = 0; j < LA; ++j) LOAD_A((j + G_RING) % NK);
#pragma unroll
  for (int j = 0; j < NK; ++j) {
    const int i = (j + G_RING) % NK;
    const int rem = NK - 1 - j < LA - 1 ? NK - 1 - j : LA - 1;
    if (rem == 4) waitv4<16>(af[i][0], af[i][1], af[i][2], af[i][3]);
    else if (rem == 3) waitv4<12>(af[i][0], af[i][1], af[i][2], af[i][3]);
    else if (rem == 2) waitv4<8>(af[i][0], af[i][1], af[i][2], af[i][3]);
    else if (rem == 1) waitv4<4>(af[i][0], af[i][1], af[i][2], af[i][3]);
    else waitv4<0>(af[i][0], af[i][1], af[i][2], af[i][3]);
    if (j < NR) {
      MMA(wr[j], i);
    } else {
      bf16x8 wf[4];
      RDW(wf, i);
      MMA(wf, i);
    }
    SBAR();
    if (j + LA < NK) LOAD_A((j + LA + G_RING) % NK);
  }
#undef STAGE_W
#undef LOAD_A
#undef RDW
#undef MMA
  STAMP(2 + PH, 4);
  {
    f32x4* xb = (f32x4*)lds;
#pragma unroll
    for (int nb = 0; nb < 4; ++nb)
#pragma unroll
      for (int mb = 0; mb < 4; ++mb) xb[(w * 16 + nb * 4 + mb) * 64 + lane] = acc[nb][mb];
  }
  LBAR();
  {
    const f32x4* xb = (const f32x4*)lds;
#pragma unroll
    for (int e = 0; e < 2; ++e) {
      f32x4 a = xb[(2 * w + e) * 64 + lane];
#pragma unroll
      for (int v = 1; v < 8; ++v) a += xb[(v * 16 + 2 * w + e) * 64 + lane];
      const int mb = emb + e;
      if (PH == 0) {
        const int fi = 64 * nt + 16 * enb + 4 * g, d = 64 * mt + 16 * mb + l15;
        float z[4] = {a[0] + bflo(eb[0]), a[1] + bfhi(eb[0]), a[2] + bflo(eb[1]), a[3] + bfhi(eb[1])};
#pragma unroll
        for (int i = 0; i < 4; ++i) {
          const float zz = z[i], a2 = zz * (1.f + 0.044715f * zz * zz) * (-2.f * 0.7978845608028654f * 1.4426950408889634f);
          z[i] = zz * __builtin_amdgcn_rcpf(1.f + __builtin_amdgcn_exp2f(a2));
        }
        const unsigned long long v = (unsigned long long)cvtpk(z[0], z[1]) | ((unsigned long long)cvtpk(z[2], z[3]) << 32);
        __hip_atomic_store((gu64*)(out + ((size_t)(fi >> 3) * D + d) * 8 + (fi & 7)), v, RLX_AGENT);
      } else {
        const int d = 64 * mt + 16 * mb + 4 * g, s = 64 * nt + 16 * enb + l15;
        const float bb = bflo(eb[0]);
        const unsigned long long hv = e ? eh1 : eh0;
        const unsigned h0 = (unsigned)hv, h1 = (unsigned)(hv >> 32);
        *(u32x2*)(out + (size_t)s * D + d) =
            (u32x2){cvtpk(a[0] + bb + bflo(h0), a[1] + bb + bfhi(h0)), cvtpk(a[2] + bb + bflo(h1), a[3] + bb + bfhi(h1))};
      }
    }
  }
  STAMP(2 + PH, 5);
}
#define PUBLISH(c) do { asm volatile("s_waitcnt vmcnt(0) lgkmcnt(0)\n\ts_barrier" ::: "memory"); \
    if (threadIdx.x == 0) __hip_atomic_fetch_add((gu32*)(c), 1u, RLX_AGENT); } while (0)

extern "C" __global__ __launch_bounds__(512) void k_main(const u16* __restrict__ x, const u16* __restrict__ Qf, const u16* __restrict__ Kf,
                                              const u16* __restrict__ Vf, const u16* __restrict__ Wof, const u16* __restrict__ w1,
                                              const u16* __restrict__ b1, const u16* __restrict__ w2, const u16* __restrict__ b2,
                                              u16* H, u16* HN, u16* U, u16* out, unsigned* cnt) {
  __shared__ __attribute__((aligned(16))) char lds[K2_LDS];
  attn_phase(lds, x, Qf, Kf, Vf, Wof, w1, w2, H, HN);
  PUBLISH(cnt + (8 + (blockIdx.x & 7)) * 64);
  if (blockIdx.x < 196) {
    const int t = xcd_remap(blockIdx.x, 196);
    mlp_tile<0, 8>(lds, t, HN, w1, b1, H, U, cnt + 8 * 64, 32u);
    PUBLISH(cnt + (t % 7) * 64);
  }
  if (blockIdx.x < 224) {
    const int t2 = xcd_remap(blockIdx.x, 224);
    mlp_tile<1, 1>(lds, t2, U, w2, b2, H, out, cnt + (t2 % 7) * 64, 28u);
  }
}

extern "C" void kernel_launch(void* const* d_in, const int* in_sizes, int n_in, void* d_out, int out_size, void* d_ws, size_t ws_size,
                              hipStream_t stream) {
  const u16* x = (const u16*)d_in[0];
  const u16* wq = (const u16*)d_in[1];
  const u16* wk = (const u16*)d_in[2];
  const u16* wv = (const u16*)d_in[3];
  const u16* wo = (const u16*)d_in[4];
  const u16* w1 = (const u16*)d_in[6];
  const u16* b1 = (const u16*)d_in[7];
  const u16* w2 = (const u16*)d_in[8];
  const u16* b2 = (const u16*)d_in[9];
  char* ws = (char*)d_ws;
  u16 *Qf = (u16*)(ws + WS_QF), *Kf = (u16*)(ws + WS_KF), *Vf = (u16*)(ws + WS_VF), *Wof = (u16*)(ws + WS_WOF);
  u16 *H = (u16*)(ws + WS_H), *HN = (u16*)(ws + WS_HN), *U = (u16*)(ws + WS_U);
  unsigned* cnt = (unsigned*)(ws + WS_CNT);
  k_qkv<<<256, 512, 0, stream>>>(x, wq, wk, wv, wo, Qf, Kf, Vf, Wof, cnt);
  k_main<<<256, 512, 0, stream>>>(x, Qf, Kf, Vf, Wof, w1, b1, w2, b2, H, HN, U, (u16*)d_out, cnt);
}
```

```cpp
#include <hip/hip_runtime.h>
#include <hip/hip_bf16.h>

typedef unsigned short u16;
using bf16x8 = __attribute__((ext_vector_type(8))) short;
using s16x4 = __attribute__((ext_vector_type(4))) short;
using f32x4 = __attribute__((ext_vector_type(4))) float;
using f32x16 = __attribute__((ext_vector_type(16))) float;
using u32x4 = __attribute__((ext_vector_type(4))) unsigned;
using u32x2 = __attribute__((ext_vector_type(2))) unsigned;
typedef short v4i16_t __attribute__((ext_vector_type(4)));
typedef __attribute__((address_space(3))) const char* lds_cptr;
typedef __bf16 bf16x2_t __attribute__((ext_vector_type(2)));
typedef float f32x2_t __attribute__((ext_vector_type(2)));

#define DI __device__ __forceinline__
#define MFMA32(a, b, c) __builtin_amdgcn_mfma_f32_32x32x16_bf16(a, b, c, 0, 0, 0)
#define MFMA16(a, b, c) __builtin_amdgcn_mfma_f32_16x16x32_bf16(a, b, c, 0, 0, 0)
#define SBAR() __builtin_amdgcn_sched_barrier(0)
#define LBAR() asm volatile("s_waitcnt lgkmcnt(0)\n\ts_barrier" ::: "memory")
#define WAITV_BAR(N) asm volatile("s_waitcnt vmcnt(" #N ") lgkmcnt(0)\n\ts_barrier" ::: "memory")

constexpr int S = 2048, D = 448, DH = 28, F = 1792;
constexpr float QSCALE = 0.18898223650461362f * 1.4426950408889634f;
constexpr float LN_EPS = 1e-5f;

constexpr size_t WS_QF = 0;
constexpr size_t WS_KF = WS_QF + 2097152;
constexpr size_t WS_VF = WS_KF + 131072;
constexpr size_t WS_WOF = WS_VF + 131072;
constexpr size_t WS_H = WS_WOF + 401408;
constexpr size_t WS_HN = WS_H + 1835008;
constexpr size_t WS_U = WS_HN + 1835008;
constexpr size_t WS_CNT = WS_U + 1605632;

#define STAMP(k, i)
#define CSTAMP(k, i)

DI unsigned cvtpk(float lo, float hi) {
  const f32x2_t v = {lo, hi};
  return __builtin_bit_cast(unsigned, __builtin_convertvector(v, bf16x2_t));
}
DI float bflo(unsigned u) { return __uint_as_float(u << 16); }
DI float bfhi(unsigned u) { return __uint_as_float(u & 0xffff0000u); }
template <int CTRL> DI float dppf(float v) {
  return __uint_as_float(__builtin_amdgcn_update_dpp(0u, __float_as_uint(v), CTRL, 0xf, 0xf, true));
}
DI float wave_sum(float v) {
  v += dppf<0xB1>(v);
  v += dppf<0x4E>(v);
  v += dppf<0x141>(v);
  v += dppf<0x140>(v);
  auto a = __builtin_amdgcn_permlane16_swap(__float_as_uint(v), __float_as_uint(v), false, false);
  v = __uint_as_float(a[0]) + __uint_as_float(a[1]);
  auto b = __builtin_amdgcn_permlane32_swap(__float_as_uint(v), __float_as_uint(v), false, false);
  return __uint_as_float(b[0]) + __uint_as_float(b[1]);
}
DI float half_max(float v) {
  auto b = __builtin_amdgcn_permlane32_swap(__float_as_uint(v), __float_as_uint(v), false, false);
  return __builtin_fmaxf(__uint_as_float(b[0]), __uint_as_float(b[1]));
}
DI void glds16(const void* g, unsigned lds_base) {
  unsigned sv;
  asm volatile("s_mov_b32 %0, m0\n\ts_mov_b32 m0, %2\n\ts_nop 0\n\tglobal_load_lds_dwordx4 %1, off\n\ts_mov_b32 m0, %0"
               : "=&s"(sv) : "v"(g), "s"(lds_base) : "memory");
}
DI void glds16s(const void* sbase, unsigned voff, unsigned lds_base) {
  unsigned sv;
  asm volatile("s_mov_b32 %0, m0\n\ts_mov_b32 m0, %3\n\ts_nop 0\n\tglobal_load_lds_dwordx4 %1, %2\n\ts_mov_b32 m0, %0"
               : "=&s"(sv) : "v"(voff), "s"(sbase), "s"(lds_base) : "memory");
}
DI void glds4(const void* g, unsigned lds_base) {
  unsigned sv;
  asm volatile("s_mov_b32 %0, m0\n\ts_mov_b32 m0, %2\n\ts_nop 0\n\tglobal_load_lds_dword %1, off\n\ts_mov_b32 m0, %0"
               : "=&s"(sv) : "v"(g), "s"(lds_base) : "memory");
}
template <int N> DI void waitv4(bf16x8& a, bf16x8& b, bf16x8& c, bf16x8& d) {
  asm volatile("s_waitcnt vmcnt(%4)" : "+v"(a), "+v"(b), "+v"(c), "+v"(d) : "i"(N) : "memory");
}
template <int OFF> DI void gload16(bf16x8& d, const void* p) {
  asm volatile("global_load_dwordx4 %0, %1, off offset:%2" : "=v"(d) : "v"(p), "i"(OFF) : "memory");
}
DI s16x4 trread(lds_cptr p) {
  return __builtin_bit_cast(s16x4, __builtin_amdgcn_ds_read_tr16_b64_v4i16((__attribute__((address_space(3))) v4i16_t*)p));
}
DI bf16x8 ldsr16(lds_cptr p) { return *(const __attribute__((address_space(3))) bf16x8*)p; }
DI bf16x8 cat8(s16x4 a, s16x4 b) { return __builtin_shufflevector(a, b, 0, 1, 2, 3, 4, 5, 6, 7); }
DI int xcd_remap(int bid, int nwg) {
  const int q = nwg >> 3, r = nwg & 7, x = bid & 7, o = bid >> 3;
  return (x < r ? x * (q + 1) : r * (q + 1) + (x - r) * q) + o;
}

constexpr int K1_WI = 448 * 128, K1_X = 2 * K1_WI, K1_XS = 528, K1_DUM = K1_X + 64 * K1_XS, K1_LDS = K1_DUM + 1024;
constexpr int KMAIN_KB = 28;
__global__ __launch_bounds__(512) void k_qkv(const u16* __restrict__ x, const u16* __restrict__ wq, const u16* __restrict__ wk,
                                             const u16* __restrict__ wv, const u16* __restrict__ wo, u16* __restrict__ Qf,
                                             u16* __restrict__ Kf, u16* __restrict__ Vf, u16* __restrict__ Wof, unsigned* __restrict__ cnt) {
  __shared__ __attribute__((aligned(16))) char lds[K1_LDS];
  const int tid = threadIdx.x, lane = tid & 63, w = __builtin_amdgcn_readfirstlane(tid >> 6);
  const int t = xcd_remap(blockIdx.x, 256), rt = t >> 2, ct = t & 3;
  const unsigned lds0 = (unsigned)(uintptr_t)lds;
  STAMP(0, 0);
  bf16x8 xr[4];
  const bool act = lane < 56;
  {
    const u16* xp_ = x + (size_t)(rt * 32 + w * 4) * D + (act ? lane : 55) * 8;
    gload16<0>(xr[0], xp_); gload16<896>(xr[1], xp_); gload16<1792>(xr[2], xp_); gload16<2688>(xr[3], xp_);
  }
#pragma unroll
  for (int im = 0; im < 2; ++im) {
    const int c64 = 2 * ct + im;
    if (c64 < 7) {
#pragma unroll
      for (int i = 0; i < 7; ++i) {
        const int piece = w * 7 + i, row = piece * 8 + (lane >> 3);
        const int f = ((row >> 1) & 1) | (((row >> 3) & 1) << 1);
        const int c = (lane & 7) ^ (f << 1);
        glds16(wq + (size_t)row * D + c64 * 64 + c * 8, lds0 + im * K1_WI + piece * 1024);
      }
    } else {
#pragma unroll
      for (int i = 0; i < 7; ++i) {
        const int pc = w * 7 + i < 50 ? w * 7 + i : 49, mat = pc >= 25, piece = pc - 25 * mat;
        const int e = piece * 512 + lane * 8;
        glds16((mat ? wv : wk) + (e < 448 * 28 ? e : 448 * 28 - 8), lds0 + im * K1_WI + mat * 25600 + piece * 1024);
      }
    }
  }
  u16 wt[8];
  const int frag0 = blockIdx.x + 256 * (w & 1);
  const bool dofrag = w < 2 && frag0 < 392;
  const int frag = dofrag ? frag0 : 0;
  {
    const int nb = frag / 14, ks = frag - nb * 14;
#pragma unroll
    for (int j = 0; j < 8; ++j) wt[j] = wo[(size_t)(32 * ks + 8 * (lane >> 4) + j) * D + 16 * nb + (lane & 15)];
  }
  SBAR();
  waitv4<22>(xr[0], xr[1], xr[2], xr[3]);
  {
    u32x4 xv[4];
#pragma unroll
    for (int r = 0; r < 4; ++r) xv[r] = __builtin_bit_cast(u32x4, xr[r]);
    float v[4][8], mean[4], rs[4];
#pragma unroll
    for (int r = 0; r < 4; ++r) {
      float s = 0.f;
#pragma unroll
      for (int i = 0; i < 4; ++i) {
        v[r][2 * i] = act ? bflo(xv[r][i]) : 0.f; v[r][2 * i + 1] = act ? bfhi(xv[r][i]) : 0.f;
        s += v[r][2 * i] + v[r][2 * i + 1];
      }
      mean[r] = s;
    }
#pragma unroll
    for (int r = 0; r < 4; ++r) mean[r] = wave_sum(mean[r]) * (1.f / D);
#pragma unroll
    for (int r = 0; r < 4; ++r) {
      float q = 0.f;
#pragma unroll
      for (int i = 0; i < 8; ++i) { v[r][i] = act ? v[r][i] - mean[r] : 0.f; q += v[r][i] * v[r][i]; }
      rs[r] = q;
    }
#pragma unroll
    for (int r = 0; r < 4; ++r) rs[r] = __builtin_amdgcn_rsqf(wave_sum(rs[r]) * (1.f / D) + LN_EPS);
    char* xdst = lds + K1_X + lane * K1_XS + w * 64;
#pragma unroll
    for (int r = 0; r < 4; ++r) {
      u32x4 o;
#pragma unroll
      for (int i = 0; i < 4; ++i) o[i] = cvtpk(v[r][2 * i] * rs[r], v[r][2 * i + 1] * rs[r]);
      *(u32x4*)(xdst + r * 16) = o;
    }
  }
  STAMP(0, 1);
  WAITV_BAR(0);
  STAMP(0, 2);
  if (w == 7 && (blockIdx.x >> 3) < KMAIN_KB) {
    unsigned long long pc;
    int off;
    asm volatile("s_getpc_b64 %0\n1:\n\ts_mov_b32 %1, k_main-1b" : "=s"(pc), "=s"(off));
    glds16s((const char*)(pc + (long long)off) + (blockIdx.x >> 3) * 1024, lane * 16, lds0 + K1_DUM);
  }
  const int nb = w & 3, im = w >> 2, c64 = 2 * ct + im, g = lane >> 4, q4 = (lane & 15) >> 2, p = lane & 3, l15 = lane & 15;
  const int f = ((q4 >> 1) & 1) | ((g & 1) << 1);
  const bool kv = c64 == 7;
  const int rs_ = kv ? 56 : 128;
  const lds_cptr wp = (lds_cptr)lds + im * K1_WI + (8 * g + q4) * rs_ +
                      (kv ? (nb >> 1) * 25600 + (nb & 1) * 32 + 8 * p : (((2 * nb + (p >> 1)) ^ (f << 1)) * 16) + 8 * (p & 1));
  const lds_cptr xp = (lds_cptr)lds + K1_X + g * K1_XS + l15 * 16;
  f32x4 acc0 = {0.f, 0.f, 0.f, 0.f}, acc1 = {0.f, 0.f, 0.f, 0.f};
  {
    bf16x8 a[14], b0[14], b1[14];
#pragma unroll
    for (int ks = 0; ks < 14; ++ks) {
      a[ks] = cat8(trread(wp + ks * 32 * rs_), trread(wp + ks * 32 * rs_ + 4 * rs_));
      b0[ks] = ldsr16(xp + ks * 4 * K1_XS);
      b1[ks] = ldsr16(xp + ks * 4 * K1_XS + 256);
    }
    SBAR();
#pragma unroll
    for (int ks = 0; ks < 14; ++ks) {
      acc0 = MFMA16(a[ks], b0[ks], acc0);
      acc1 = MFMA16(a[ks], b1[ks], acc1);
    }
  }
  STAMP(0, 3);
#pragma unroll
  for (int tb = 0; tb < 2; ++tb) {
    const f32x4 a = tb ? acc1 : acc0;
    const int s = rt * 32 + 16 * tb + l15;
    const int nl = 16 * nb + 4 * g;
    if (c64 < 7) {
      const int n0 = 64 * c64 + nl, h = n0 / 28, d0 = n0 - 28 * h;
      const int tg = s >> 3, c = (h & 3) * 8 + (s & 7), b = h >> 2;
      const size_t off = ((((size_t)(tg * 4 + b) * 2 + (d0 >> 4)) * 64) + ((d0 >> 3) & 1) * 32 + c) * 8 + (d0 & 7);
      *(u32x2*)(Qf + off) = (u32x2){cvtpk(a[0] * QSCALE, a[1] * QSCALE), cvtpk(a[2] * QSCALE, a[3] * QSCALE)};
    } else {
      const int kt = s >> 5, kk = s & 31;
      const int d0 = 16 * (nb & 1) + 4 * g;
      if (nb < 2) {
        const size_t off = ((((size_t)kt * 2 + (d0 >> 4)) * 64) + ((d0 >> 3) & 1) * 32 + kk) * 8 + (d0 & 7);
        u32x2 v = {cvtpk(a[0], a[1]), cvtpk(a[2], a[3])};
        if (d0 == 28) v = (u32x2){0x3f803f80u, 0x00003f80u};
        *(u32x2*)(Kf + off) = v;
      } else {
        const int sp = kk >> 4, j = ((kk >> 3) & 1) * 4 + (kk & 3), hi = (kk >> 2) & 1;
        const size_t off = ((((size_t)kt * 2 + sp) * 64) + hi * 32 + d0) * 8 + j;
        unsigned v0 = cvtpk(a[0], a[1]), v1 = cvtpk(a[2], a[3]);
        if (d0 == 28) { v0 = 0x00003f80u; v1 = 0u; }
        Vf[off] = (u16)v0; Vf[off + 8] = (u16)(v0 >> 16); Vf[off + 16] = (u16)v1; Vf[off + 24] = (u16)(v1 >> 16);
      }
    }
  }
  if (dofrag) {
    u32x4 o;
#pragma unroll
    for (int i = 0; i < 4; ++i) o[i] = (unsigned)wt[2 * i] | ((unsigned)wt[2 * i + 1] << 16);
    *(u32x4*)(Wof + ((size_t)frag * 64 + lane) * 8) = o;
  }
  if (blockIdx.x == 0) { cnt[tid] = 0u; cnt[512 + tid] = 0u; }
  if (w == 7) asm volatile("s_waitcnt vmcnt(0)" ::: "memory");
  STAMP(0, 4);
}

constexpr int K2_RING = 0, K2_OT = 131072, K2_DUM = K2_OT + 7168, K2_X = K2_DUM + 4096, K2_LDS = K2_X + 16384;
constexpr int K2_HS = K2_X;
DI int hb_row(int r) { return 65536 + (r >> 2) * 16384 + 8192 + ((r >> 2) & 1) * 64 + (r & 3) * 1808; }

DI float set_ref(bf16x8& q1, float m, int hi) {
  const float a = bflo(cvtpk(-m, 0.f)), r1 = -m - a, b = bflo(cvtpk(r1, 0.f)), c = bflo(cvtpk(r1 - b, 0.f));
  u32x4 v = __builtin_bit_cast(u32x4, q1);
  v[2] = hi ? cvtpk(a, b) : v[2];
  v[3] = hi ? cvtpk(c, 0.f) : v[3];
  q1 = __builtin_bit_cast(bf16x8, v);
  return -((a + b) + c);
}
DI float tile_max(const f32x16& s) {
  float a = __builtin_fmaxf(__builtin_fmaxf(s[0], s[1]), s[2]), b = __builtin_fmaxf(__builtin_fmaxf(s[3], s[4]), s[5]);
  a = __builtin_fmaxf(__builtin_fmaxf(a, s[6]), s[7]); b = __builtin_fmaxf(__builtin_fmaxf(b, s[8]), s[9]);
  a = __builtin_fmaxf(__builtin_fmaxf(a, s[10]), s[11]); b = __builtin_fmaxf(__builtin_fmaxf(b, s[12]), s[13]);
  a = __builtin_fmaxf(__builtin_fmaxf(a, s[14]), s[15]);
  return half_max(__builtin_fmaxf(a, b));
}
template <int N> DI void waitv() { asm volatile("s_waitcnt vmcnt(%0)" ::"i"(N) : "memory"); }
#define RBAR() asm volatile("s_barrier" ::: "memory")

DI void attn_safe(const u16* __restrict__ Kf, const u16* __restrict__ Vf, bf16x8 q0, bf16x8 q1, f32x16& o, int lane, int hi) {
  const f32x16 zero = {};
  float mref = 0.f;
  o = zero;
#pragma unroll 1
  for (int t = 0; t < 64; ++t) {
    const bf16x8 k0 = *(const bf16x8*)(Kf + t * 1024 + lane * 8), k1 = *(const bf16x8*)(Kf + t * 1024 + 512 + lane * 8);
    const bf16x8 v0 = *(const bf16x8*)(Vf + t * 1024 + lane * 8), v1 = *(const bf16x8*)(Vf + t * 1024 + 512 + lane * 8);
    (void)set_ref(q1, 0.f, hi);
    f32x16 s = MFMA32(k0, q0, zero);
    s = MFMA32(k1, q1, s);
    const float tm = tile_max(s);
    const float mn = set_ref(q1, t == 0 ? tm : __builtin_fmaxf(tm, mref), hi);
    const float al = t == 0 ? 0.f : __builtin_amdgcn_exp2f(mref - mn);
#pragma unroll
    for (int r = 0; r < 16; ++r) o[r] *= al;
    mref = mn;
    s = MFMA32(k0, q0, zero);
    s = MFMA32(k1, q1, s);
    u32x4 p0, p1;
#pragma unroll
    for (int i = 0; i < 4; ++i) {
      p0[i] = cvtpk(__builtin_amdgcn_exp2f(s[2 * i]), __builtin_amdgcn_exp2f(s[2 * i + 1]));
      p1[i] = cvtpk(__builtin_amdgcn_exp2f(s[8 + 2 * i]), __builtin_amdgcn_exp2f(s[8 + 2 * i + 1]));
    }
    o = MFMA32(v0, __builtin_bit_cast(bf16x8, p0), o);
    o = MFMA32(v1, __builtin_bit_cast(bf16x8, p1), o);
  }
}

#define EX4(S, i) do { S[i] = __builtin_amdgcn_exp2f(S[i]); S[i + 1] = __builtin_amdgcn_exp2f(S[i + 1]); \
    S[i + 2] = __builtin_amdgcn_exp2f(S[i + 2]); S[i + 3] = __builtin_amdgcn_exp2f(S[i + 3]); } while (0)
#define PK8(P, S, i) do { P[0] = cvtpk(S[i], S[i + 1]); P[1] = cvtpk(S[i + 2], S[i + 3]); P[2] = cvtpk(S[i + 4], S[i + 5]); P[3] = cvtpk(S[i + 6], S[i + 7]); } while (0)
#define BF(P) __builtin_bit_cast(bf16x8, P)
constexpr int CPRE = 0;
#define WOPF(j) do { if ((j) < CPRE) wf[(j) / 7][(j) % 7] = WOFR(j); } while (0)
#define RDS(u, off) ldsr16(((u) < 4 ? B0 : B1) + ((u) & 3) * 8192 + (off))
#define CTILE(u, NX, PF) do { \
      \
    sB = MFMA32(k0, qB0, zero); SBAR(); if (NX) k0 = RDS((u) + 1, 0); EX4(sA, 0); WOPF(PF); SBAR(); \
    sB = MFMA32(k1, qB1, sB); SBAR(); if (NX) k1 = RDS((u) + 1, 1024); EX4(sA, 4); PK8(p0, sA, 0); SBAR(); \
    oB = MFMA32(v1, BF(p1), oB); SBAR(); v1 = RDS(u, 3072); EX4(sA, 8); WOPF((PF) + 1); SBAR(); \
    oA = MFMA32(v0, BF(p0), oA); SBAR(); EX4(sA, 12); PK8(p1, sA, 8); SBAR(); \
      \
    sA = MFMA32(k0, qA0, zero); SBAR(); EX4(sB, 0); WOPF((PF) + 2); SBAR(); \
    sA = MFMA32(k1, qA1, sA); SBAR(); EX4(sB, 4); PK8(p0, sB, 0); SBAR(); \
    oA = MFMA32(v1, BF(p1), oA); SBAR(); EX4(sB, 8); WOPF((PF) + 3); SBAR(); \
    oB = MFMA32(v0, BF(p0), oB); SBAR(); if (NX) v0 = RDS((u) + 1, 2048); EX4(sB, 12); PK8(p1, sB, 8); SBAR(); } while (0)
#define CSTAGE(LAST, PF) do { \
    CTILE(0, true, PF); CTILE(1, true, (PF) + 4); CTILE(2, true, (PF) + 8); \
    if (!(LAST)) RBAR(); \
    CTILE(3, !(LAST), (PF) + 12); } while (0)

DI void st16c(void* p, u32x4 v) {
  asm volatile("global_store_dwordx4 %0, %1, off sc1\n\ts_nop 1" ::"v"(p), "v"(v) : "memory");
}
DI void attn_phase(char* lds, const u16* __restrict__ x, const u16* __restrict__ Qf, const u16* __restrict__ Kf,
                   const u16* __restrict__ Vf, const u16* __restrict__ Wof, const u16* __restrict__ w1,
                   const u16* __restrict__ w2, u16* H, u16* HN, int nt1) {
  const int tid = threadIdx.x, lane = tid & 63, w = __builtin_amdgcn_readfirstlane(tid >> 6), hi = lane >> 5;
  const int tg = blockIdx.x;
  STAMP(1, 0);
  const int kh = w >> 2, ng = w & 3;
  const char* wob = (const char*)(Wof + (size_t)(7 * ng * 14 + 7 * kh) * 512);
  const unsigned wl = lane * 16;
  bf16x8 wf[7][7];
#define WOFR(i_) (*(const bf16x8*)(wob + (((i_) / 7) * 14 + (i_) % 7) * 1024 + wl))
#define LDWO(i0, i1) do { _Pragma("unroll") for (int i_ = i0; i_ < i1; ++i_) wf[i_ / 7][i_ % 7] = WOFR(i_); } while (0)
#define BOF(k) (*(const bf16x8*)(lds + K2_OT + ((4 * (k) + (lane >> 4)) * 8 + (lane & 7)) * 16))
#define HBST(acc) do { if ((lane & 15) < 8) { \
      _Pragma("unroll") for (int n = 0; n < 7; ++n) *(f32x4*)(lds + hb_row(kh * 8 + (lane & 7)) + (16 * (7 * ng + n) + 4 * (lane >> 4)) * 4) = acc[n]; \
    } } while (0)
  u32x4 xv;
  if (w < 4) {
    const f32x16 zero = {};
    const int qh = w & 1, kp = w >> 1;
    const lds_cptr rp0 = (lds_cptr)lds + K2_RING + lane * 16;
    const lds_cptr rp = rp0 + kp * 4096;
    lds_cptr B0 = rp, B1 = rp + 32768;
    const u16* qp = Qf + ((size_t)((tg * 4 + 2 * qh) * 2) * 64 + lane) * 8;
    bf16x8 qA0 = *(const bf16x8*)(qp), qA1 = *(const bf16x8*)(qp + 512), qB0 = *(const bf16x8*)(qp + 1024), qB1 = *(const bf16x8*)(qp + 1536);
    RBAR();
    bf16x8 k0 = ldsr16(rp0), k1 = ldsr16(rp0 + 1024), v0 = RDS(0, 2048), v1 = v0;
    (void)set_ref(qA1, 0.f, hi);
    (void)set_ref(qB1, 0.f, hi);
    f32x16 sA = MFMA32(k0, qA0, zero), sB = MFMA32(k0, qB0, zero);
    sA = MFMA32(k1, qA1, sA);
    sB = MFMA32(k1, qB1, sB);
    k0 = RDS(0, 0); k1 = RDS(0, 1024);
    (void)set_ref(qA1, tile_max(sA), hi);
    (void)set_ref(qB1, tile_max(sB), hi);
    sA = MFMA32(k0, qA0, zero);
    sA = MFMA32(k1, qA1, sA);
    f32x16 oA = zero, oB = zero;
    u32x4 p0, p1 = {0u, 0u, 0u, 0u};
    SBAR();
    STAMP(1, 8);
#pragma unroll 1
    for (int st = 0; st < 4; ++st) {
      CSTAGE(false, 64);
      B0 = B1; B1 = rp + ((st + 2) & 3) * 32768;
    }
    CSTAGE(false, 0);
    B0 = B1; B1 = rp + 2 * 32768;
    CSTAGE(false, 16);
    B0 = B1; B1 = rp + 3 * 32768;
    CSTAGE(false, 64);
    B0 = B1;
    CSTAGE(true, 64);
    oB = MFMA32(v1, BF(p1), oB);
    STAMP(1, 10);
    {
      const f32x16 snd = kp ? oA : oB;
#pragma unroll
      for (int r4 = 0; r4 < 4; ++r4)
        *(f32x4*)(lds + K2_X + ((w * 4 + r4) * 64 + lane) * 16) = (f32x4){snd[4 * r4], snd[4 * r4 + 1], snd[4 * r4 + 2], snd[4 * r4 + 3]};
    }
    f32x16 o0 = kp ? oB : oA;
    bf16x8 qk0 = kp ? qB0 : qA0, qk1 = kp ? qB1 : qA1;
    asm volatile("" : "+v"(o0), "+v"(qk0), "+v"(qk1));
    SBAR();
    LBAR();
#pragma unroll
    for (int r4 = 0; r4 < 4; ++r4) {
      const f32x4 t = *(const f32x4*)(lds + K2_X + (((w ^ 2) * 4 + r4) * 64 + lane) * 16);
      o0[4 * r4] += t[0]; o0[4 * r4 + 1] += t[1]; o0[4 * r4 + 2] += t[2]; o0[4 * r4 + 3] += t[3];
    }
    const int qb = 2 * qh + kp;
    {
      float chk = 0.f;
#pragma unroll
      for (int r = 0; r < 16; ++r) chk = __builtin_fmaf(o0[r], 0.f, chk);
      const bool bad = !(chk == 0.f) || !(o0[12] < 1.8e19f);
      if (__any(bad)) {
        attn_safe(Kf, Vf, qk0, qk1, o0, lane, hi);
      }
    }
    {
      auto sw = __builtin_amdgcn_permlane32_swap(__float_as_uint(o0[12]), __float_as_uint(o0[12]), false, false);
      const float il = 1.f / __uint_as_float(sw[1]);
      const int c = lane & 31;
#pragma unroll
      for (int r4 = 0; r4 < 4; ++r4) {
        const int d = 8 * r4 + 4 * hi, k = 28 * (4 * qb + (c >> 3)) + d;
        if (d < 28)
          *(u32x2*)(lds + K2_OT + ((k >> 3) * 8 + (c & 7)) * 16 + (k & 7) * 2) =
              (u32x2){cvtpk(o0[4 * r4] * il, o0[4 * r4 + 1] * il), cvtpk(o0[4 * r4 + 2] * il, o0[4 * r4 + 3] * il)};
      }
    }
    STAMP(1, 1);
    LBAR();
    STAMP(1, 2);
    LDWO(16, 39);
    xv = *(const u32x4*)(x + (size_t)(tg * 8 + w) * D + (lane < 56 ? lane : 55) * 8);
    SBAR();
    {
      bf16x8 bo[7];
#pragma unroll
      for (int k = 0; k < 7; ++k) bo[k] = BOF(k);
#pragma unroll
      for (int i = 0; i < 16; ++i) wf[i / 7][i % 7] = *(const bf16x8*)(lds + K2_RING + (i >> 3) * 32768 + w * 8192 + (i & 7) * 1024 + lane * 16);
      f32x4 acc[7];
#pragma unroll
      for (int n = 0; n < 7; ++n) acc[n] = (f32x4){0.f, 0.f, 0.f, 0.f};
#pragma unroll
      for (int i = 0; i < 39; ++i) acc[i / 7] = MFMA16(wf[i / 7][i % 7], bo[i % 7], acc[i / 7]);
      HBST(acc);
    }
  } else {
    const int hw = w - 4;
    __builtin_amdgcn_s_setprio(3);
    const unsigned ring = (unsigned)(uintptr_t)lds + K2_RING + hw * 8192;
    const u16* kp = Kf + (size_t)hw * 2048;
    const u16* vp = Vf + (size_t)hw * 2048;
    const unsigned l16 = lane * 16;
#define DMAS(s_) do { const unsigned sl_ = ring + ((s_) & 3) * 32768; \
    _Pragma("unroll") for (int u_ = 0; u_ < 2; ++u_) { \
      glds16s(kp + (s_) * 8192 + u_ * 1024, l16, sl_ + u_ * 4096); glds16s(kp + (s_) * 8192 + u_ * 1024 + 512, l16, sl_ + u_ * 4096 + 1024); \
      glds16s(vp + (s_) * 8192 + u_ * 1024, l16, sl_ + u_ * 4096 + 2048); glds16s(vp + (s_) * 8192 + u_ * 1024 + 512, l16, sl_ + u_ * 4096 + 3072); } } while (0)
    const unsigned dum = (unsigned)(uintptr_t)lds + K2_DUM + hw * 1024;
    const int xg = tg & 7, rk = tg >> 3;
    const int ts = xg < 4 ? 25 * xg : 100 + 24 * (xg - 4), te = ts + (xg < 4 ? 25 : 24);
    const int n0 = ts / 7, n1 = (te - 1) / 7;
    const int nb0 = n0 + 3 <= 27 ? n0 : 24;
    const u16* p1 = w1 + (size_t)(64 * rk + 16 * hw) * F + nb0 * 64;
    const u16* p1b = w1 + (size_t)(64 * rk + 16 * hw) * F + n1 * 64;
    const u16* p2 = w2 + (size_t)(56 * rk + 14 * hw) * S + 4 * xg * 64;
    const unsigned o1 = ((lane >> 5) * F + (lane & 31) * 8) * 2, o1b = ((lane >> 3) * F + (lane & 7) * 8) * 2, o2 = ((lane >> 5) * S + (lane & 31) * 8) * 2;
#define PFD(i0, i1) do { _Pragma("unroll") for (int i_ = i0; i_ < i1; ++i_) { \
      if (i_ < 8) glds16s(p1 + (size_t)i_ * 2 * F, o1, dum); \
      else if (i_ < 10) glds16s(p1b + (size_t)(i_ - 8) * 8 * F, o1b, dum); \
      else glds16s(p2 + (size_t)(i_ - 10) * 2 * S, o2, dum); } } while (0)
    DMAS(0); DMAS(1); DMAS(2);
    waitv<16>(); RBAR();
    LDWO(0, 12); SBAR(); PFD(0, 3);
    waitv<23>(); RBAR();
    DMAS(3); LDWO(12, 24); SBAR(); PFD(3, 6);
    waitv<38>(); RBAR();
    DMAS(4); LDWO(24, 36); SBAR(); PFD(6, 9);
    waitv<38>(); RBAR();
    DMAS(5); LDWO(36, 49); SBAR(); PFD(9, 12);
    waitv<39>(); RBAR();
    bf16x8 wx[10];
    DMAS(6);
#pragma unroll
    for (int i = 0; i < 10; ++i) wx[i] = *(const bf16x8*)(wob - 7 * 1024 + (((39 + i) / 7) * 14 + (39 + i) % 7) * 1024 + wl);
    SBAR(); PFD(12, 15);
    waitv<37>(); RBAR();
    DMAS(7); PFD(15, 17);
    waitv<23>(); RBAR();
#pragma unroll
    for (int i = 0; i < 8; ++i) glds16s(wob - 7 * 1024 + ((i / 7) * 14 + i % 7) * 1024, wl, ring + i * 1024);
    waitv<10>(); RBAR();
#pragma unroll
    for (int i = 8; i < 16; ++i) glds16s(wob - 7 * 1024 + ((i / 7) * 14 + i % 7) * 1024, wl, ring + 32768 + (i - 8) * 1024);
    xv = *(const u32x4*)(x + (size_t)(tg * 8 + w) * D + (lane < 56 ? lane : 55) * 8);
    SBAR();
    __builtin_amdgcn_s_setprio(0);
    RBAR();
    waitv<1>();
    STAMP(1, 1);
    LBAR();
    STAMP(1, 2);
    {
      f32x4 acc[7];
      {
        bf16x8 bo[7];
#pragma unroll
        for (int k = 0; k < 7; ++k) bo[k] = BOF(k);
        acc[5] = (f32x4){0.f, 0.f, 0.f, 0.f}; acc[6] = acc[5];
#pragma unroll
        for (int i = 0; i < 10; ++i) acc[(39 + i) / 7] = MFMA16(wx[i], bo[(39 + i) % 7], acc[(39 + i) / 7]);
      }
      SBAR();
      bf16x8 bo[7];
#pragma unroll
      for (int k = 0; k < 7; ++k) bo[k] = BOF(7 + k);
#pragma unroll
      for (int n = 0; n < 7; ++n) {
        if (n < 5) acc[n] = (f32x4){0.f, 0.f, 0.f, 0.f};
#pragma unroll
        for (int k = 0; k < 7; ++k) acc[n] = MFMA16(wf[n][k], bo[k], acc[n]);
      }
      HBST(acc);
    }
  }
  LBAR();
  STAMP(1, 3);
  if (nt1 >= 0) {
    const int r8 = lane >> 3, fe = (r8 >> 1) & 1;
    const u16* a0 = w1 + (size_t)(32 * w + r8) * F + 64 * nt1 + ((lane & 7) ^ (fe << 1)) * 8;
    const u16* a1 = w1 + (size_t)(32 * w + 8 + r8) * F + 64 * nt1 + ((lane & 7) ^ ((fe | 2) << 1)) * 8;
#pragma unroll
    for (int j = 0; j < 4; ++j) {
      if (j < 2 || w < 4) {
        const unsigned sl_ = (unsigned)(uintptr_t)lds + w * 16384 + j * 4096;
        const size_t o_ = (size_t)(4 + j) * 256 * F;
        glds16(a0 + o_, sl_); glds16(a1 + o_, sl_ + 1024); glds16(a0 + o_ + 16 * F, sl_ + 2048); glds16(a1 + o_ + 16 * F, sl_ + 3072);
      }
    }
  }
  {
    const int s = tg * 8 + w;
    const bool act = lane < 56;
    const int lc = act ? lane : 55;
    float v[8];
    const f32x4 a0 = *(const f32x4*)(lds + hb_row(w) + lc * 32), a1 = *(const f32x4*)(lds + hb_row(w) + lc * 32 + 16);
    const f32x4 c0 = *(const f32x4*)(lds + hb_row(8 + w) + lc * 32), c1 = *(const f32x4*)(lds + hb_row(8 + w) + lc * 32 + 16);
    u32x4 hv;
#pragma unroll
    for (int i = 0; i < 2; ++i) {
      hv[i] = cvtpk(bflo(xv[i]) + a0[2 * i] + c0[2 * i], bfhi(xv[i]) + a0[2 * i + 1] + c0[2 * i + 1]);
      hv[2 + i] = cvtpk(bflo(xv[2 + i]) + a1[2 * i] + c1[2 * i], bfhi(xv[2 + i]) + a1[2 * i + 1] + c1[2 * i + 1]);
    }
    if (act) st16c(H + (size_t)s * D + lane * 8, hv);
#pragma unroll
    for (int i = 0; i < 4; ++i) { v[2 * i] = act ? bflo(hv[i]) : 0.f; v[2 * i + 1] = act ? bfhi(hv[i]) : 0.f; }
#pragma unroll
    for (int pass = 0; pass < 2; ++pass) {
      float sm = 0.f;
#pragma unroll
      for (int i = 0; i < 8; ++i) sm += v[i];
      const float mean = wave_sum(sm) * (1.f / D);
      float q = 0.f;
#pragma unroll
      for (int i = 0; i < 8; ++i) { v[i] = act ? v[i] - mean : 0.f; q += v[i] * v[i]; }
      const float rs = __builtin_amdgcn_rsqf(wave_sum(q) * (1.f / D) + LN_EPS);
#pragma unroll
      for (int i = 0; i < 8; ++i) v[i] *= rs;
    }
    if (act) *(u32x4*)(lds + K2_HS + w * 912 + lane * 16) = (u32x4){cvtpk(v[0], v[1]), cvtpk(v[2], v[3]), cvtpk(v[4], v[5]), cvtpk(v[6], v[7])};
  }
  LBAR();
  if (tid < D / 2) {
    unsigned r[8];
#pragma unroll
    for (int i = 0; i < 8; ++i) r[i] = *(const unsigned*)(lds + K2_HS + i * 912 + tid * 4);
    u32x4 lo, hi2;
#pragma unroll
    for (int i = 0; i < 4; ++i) {
      lo[i] = (r[2 * i] & 0xffffu) | (r[2 * i + 1] << 16);
      hi2[i] = (r[2 * i] >> 16) | (r[2 * i + 1] & 0xffff0000u);
    }
    u32x4* dst = (u32x4*)(HN + ((size_t)tg * D + 2 * tid) * 8);
    st16c(dst, lo); st16c(dst + 1, hi2);
  }
  STAMP(1, 4);
}

constexpr int G_RING = 4, G_LDS = 8 * G_RING * 4096;
typedef __attribute__((address_space(1))) unsigned gu32;
typedef __attribute__((address_space(1))) unsigned long long gu64;
#define RLX_AGENT __ATOMIC_RELAXED, __HIP_MEMORY_SCOPE_AGENT
template <int OFF> DI void gload16c(bf16x8& d, const void* p) {
  asm volatile("global_load_dwordx4 %0, %1, off offset:%2 sc1" : "=v"(d) : "v"(p), "i"(OFF) : "memory");
}
template <int PH, int NSH>
DI void mlp_tile(char* lds, int t, const u16* act, const u16* __restrict__ W, const u16* __restrict__ bias, const u16* H, u16* out,
                 unsigned* cnt, unsigned want) {
  constexpr int KT = PH == 0 ? S : F, LDW = PH == 0 ? F : S, NK = KT / 256, NR = NK - G_RING, LA = 5;
  const int tid = threadIdx.x, lane = tid & 63, w = __builtin_amdgcn_readfirstlane(tid >> 6);
  const int nt = t / 7, mt = t - nt * 7;
  const unsigned lds0 = (unsigned)(uintptr_t)lds + w * (G_RING * 4096);
  const int g = lane >> 4, q4 = (lane & 15) >> 2, p = lane & 3, l15 = lane & 15;
  STAMP(2 + PH, 0);
  const int enb = w >> 1, emb = 2 * (w & 1);
  u32x2 eb;
  if (PH == 0) eb = *(const u32x2*)(bias + 64 * nt + 16 * enb + 4 * g);
  else eb[0] = bias[64 * nt + 16 * enb + l15];
  SBAR();
  const u16* asrc = act + ((size_t)(4 * w + g) * D + 64 * mt + l15) * 8;
  const int r8 = lane >> 3, fe = (r8 >> 1) & 1;
  const u16* wsrc0 = W + (size_t)(32 * w + r8) * LDW + 64 * nt + ((lane & 7) ^ (fe << 1)) * 8;
  const u16* wsrc1 = W + (size_t)(32 * w + 8 + r8) * LDW + 64 * nt + ((lane & 7) ^ ((fe | 2) << 1)) * 8;
  const int f = ((q4 >> 1) & 1) | ((g & 1) << 1);
  int woff[4];
#pragma unroll
  for (int nb = 0; nb < 4; ++nb) woff[nb] = w * (G_RING * 4096) + (8 * g + q4) * 128 + (((2 * nb + (p >> 1)) ^ (f << 1)) * 16) + 8 * (p & 1);
  bf16x8 af[NK][4];
  f32x4 acc[4][4] = {};
#define STAGE_W(i) do { const unsigned sl_ = lds0 + ((i) % G_RING) * 4096; const size_t o_ = (size_t)(i) * 256 * LDW; \
    glds16(wsrc0 + o_, sl_); glds16(wsrc1 + o_, sl_ + 1024); glds16(wsrc0 + o_ + 16 * LDW, sl_ + 2048); glds16(wsrc1 + o_ + 16 * LDW, sl_ + 3072); } while (0)
#define LOAD_A(i) do { const u16* ap_ = asrc + (size_t)(i) * 32 * D * 8; \
    gload16c<0>(af[i][0], ap_); gload16c<256>(af[i][1], ap_); gload16c<512>(af[i][2], ap_); gload16c<768>(af[i][3], ap_); } while (0)
#define RDW(WF, i) do { _Pragma("unroll") for (int nb_ = 0; nb_ < 4; ++nb_) { const lds_cptr sp_ = (lds_cptr)lds + woff[nb_] + ((i) % G_RING) * 4096; \
      WF[nb_] = cat8(trread(sp_), trread(sp_ + 512)); } } while (0)
#define MMA(WF, i) do { _Pragma("unroll") for (int nb_ = 0; nb_ < 4; ++nb_) _Pragma("unroll") for (int mb_ = 0; mb_ < 4; ++mb_) \
      acc[nb_][mb_] = PH == 0 ? MFMA16(WF[nb_], af[i][mb_], acc[nb_][mb_]) : MFMA16(af[i][mb_], WF[nb_], acc[nb_][mb_]); } while (0)
  bf16x8 wr[NR][4];
#pragma unroll
  for (int j = 0; j < NR; ++j)
    if (PH == 1 || (j >= 2 && w >= 4)) STAGE_W(G_RING + j);
  if (NR < G_RING) STAGE_W(G_RING - 1);
#pragma unroll
  for (int j = 0; j < NR; ++j) {
    waitv<12>();
    RDW(wr[j], G_RING + j);
    asm volatile("s_waitcnt lgkmcnt(0)" ::: "memory");
    STAGE_W(j);
  }
  STAMP(2 + PH, 1);
  if (w == 0) {
    gu32* cp = (gu32*)(cnt + (lane < NSH ? lane : 0) * 64);
    for (unsigned spins = 0; spins < (1u << 20); ++spins) {
      if (__all(__hip_atomic_load(cp, RLX_AGENT) >= want)) break;
      __builtin_amdgcn_s_sleep(1);
    }
  }
  asm volatile("s_barrier" ::: "memory");
  STAMP(2 + PH, 2);
  unsigned long long eh0 = 0, eh1 = 0;
  if (PH == 1) {
    const int s = 64 * nt + 16 * enb + l15;
    eh0 = __hip_atomic_load((gu64*)(H + (size_t)s * D + 64 * mt + 16 * emb + 4 * g), RLX_AGENT);
    eh1 = __hip_atomic_load((gu64*)(H + (size_t)s * D + 64 * mt + 16 * emb + 16 + 4 * g), RLX_AGENT);
  }
  SBAR();
#pragma unroll
  for (int j = 0; j < LA; ++j) LOAD_A((j + G_RING) % NK);
#pragma unroll
  for (int j = 0; j < NK; ++j) {
    const int i = (j + G_RING) % NK;
    const int rem = NK - 1 - j < LA - 1 ? NK - 1 - j : LA - 1;
    if (rem == 4) waitv4<16>(af[i][0], af[i][1], af[i][2], af[i][3]);
    else if (rem == 3) waitv4<12>(af[i][0], af[i][1], af[i][2], af[i][3]);
    else if (rem == 2) waitv4<8>(af[i][0], af[i][1], af[i][2], af[i][3]);
    else if (rem == 1) waitv4<4>(af[i][0], af[i][1], af[i][2], af[i][3]);
    else waitv4<0>(af[i][0], af[i][1], af[i][2], af[i][3]);
    if (j < NR) {
      MMA(wr[j], i);
    } else {
      bf16x8 wf[4];
      RDW(wf, i);
      MMA(wf, i);
    }
    SBAR();
    if (j + LA < NK) LOAD_A((j + LA + G_RING) % NK);
  }
#undef STAGE_W
#undef LOAD_A
#undef RDW
#undef MMA
  STAMP(2 + PH, 4);
  {
    f32x4* xb = (f32x4*)lds;
#pragma unroll
    for (int nb = 0; nb < 4; ++nb)
#pragma unroll
      for (int mb = 0; mb < 4; ++mb) xb[(w * 16 + nb * 4 + mb) * 64 + lane] = acc[nb][mb];
  }
  LBAR();
  {
    const f32x4* xb = (const f32x4*)lds;
#pragma unroll
    for (int e = 0; e < 2; ++e) {
      f32x4 a = xb[(2 * w + e) * 64 + lane];
#pragma unroll
      for (int v = 1; v < 8; ++v) a += xb[(v * 16 + 2 * w + e) * 64 + lane];
      const int mb = emb + e;
      if (PH == 0) {
        const int fi = 64 * nt + 16 * enb + 4 * g, d = 64 * mt + 16 * mb + l15;
        float z[4] = {a[0] + bflo(eb[0]), a[1] + bfhi(eb[0]), a[2] + bflo(eb[1]), a[3] + bfhi(eb[1])};
#pragma unroll
        for (int i = 0; i < 4; ++i) {
          const float zz = z[i], a2 = zz * (1.f + 0.044715f * zz * zz) * (-2.f * 0.7978845608028654f * 1.4426950408889634f);
          z[i] = zz * __builtin_amdgcn_rcpf(1.f + __builtin_amdgcn_exp2f(a2));
        }
        const unsigned long long v = (unsigned long long)cvtpk(z[0], z[1]) | ((unsigned long long)cvtpk(z[2], z[3]) << 32);
        __hip_atomic_store((gu64*)(out + ((size_t)(fi >> 3) * D + d) * 8 + (fi & 7)), v, RLX_AGENT);
      } else {
        const int d = 64 * mt + 16 * mb + 4 * g, s = 64 * nt + 16 * enb + l15;
        const float bb = bflo(eb[0]);
        const unsigned long long hv = e ? eh1 : eh0;
        const unsigned h0 = (unsigned)hv, h1 = (unsigned)(hv >> 32);
        *(u32x2*)(out + (size_t)s * D + d) =
            (u32x2){cvtpk(a[0] + bb + bflo(h0), a[1] + bb + bfhi(h0)), cvtpk(a[2] + bb + bflo(h1), a[3] + bb + bfhi(h1))};
      }
    }
  }
  STAMP(2 + PH, 5);
}
#define PUBLISH(c) do { asm volatile("s_waitcnt vmcnt(0) lgkmcnt(0)\n\ts_barrier" ::: "memory"); \
    if (threadIdx.x == 0) __hip_atomic_fetch_add((gu32*)(c), 1u, RLX_AGENT); } while (0)

extern "C" __global__ __launch_bounds__(512) void k_main(const u16* __restrict__ x, const u16* __restrict__ Qf, const u16* __restrict__ Kf,
                                              const u16* __restrict__ Vf, const u16* __restrict__ Wof, const u16* __restrict__ w1,
                                              const u16* __restrict__ b1, const u16* __restrict__ w2, const u16* __restrict__ b2,
                                              u16* H, u16* HN, u16* U, u16* out, unsigned* cnt) {
  __shared__ __attribute__((aligned(16))) char lds[K2_LDS];
  attn_phase(lds, x, Qf, Kf, Vf, Wof, w1, w2, H, HN, blockIdx.x < 196 ? xcd_remap(blockIdx.x, 196) / 7 : -1);
  PUBLISH(cnt + (8 + (blockIdx.x & 7)) * 64);
  if (blockIdx.x < 196) {
    const int t = xcd_remap(blockIdx.x, 196);
    mlp_tile<0, 8>(lds, t, HN, w1, b1, H, U, cnt + 8 * 64, 32u);
    PUBLISH(cnt + (t % 7) * 64);
  }
  if (blockIdx.x < 224) {
    const int t2 = xcd_remap(blockIdx.x, 224);
    mlp_tile<1, 1>(lds, t2, U, w2, b2, H, out, cnt + (t2 % 7) * 64, 28u);
  }
}

extern "C" void kernel_launch(void* const* d_in, const int* in_sizes, int n_in, void* d_out, int out_size, void* d_ws, size_t ws_size,
                              hipStream_t stream) {
  const u16* x = (const u16*)d_in[0];
  const u16* wq = (const u16*)d_in[1];
  const u16* wk = (const u16*)d_in[2];
  const u16* wv = (const u16*)d_in[3];
  const u16* wo = (const u16*)d_in[4];
  const u16* w1 = (const u16*)d_in[6];
  const u16* b1 = (const u16*)d_in[7];
  const u16* w2 = (const u16*)d_in[8];
  const u16* b2 = (const u16*)d_in[9];
  char* ws = (char*)d_ws;
  u16 *Qf = (u16*)(ws + WS_QF), *Kf = (u16*)(ws + WS_KF), *Vf = (u16*)(ws + WS_VF), *Wof = (u16*)(ws + WS_WOF);
  u16 *H = (u16*)(ws + WS_H), *HN = (u16*)(ws + WS_HN), *U = (u16*)(ws + WS_U);
  unsigned* cnt = (unsigned*)(ws + WS_CNT);
  k_qkv<<<256, 512, 0, stream>>>(x, wq, wk, wv, wo, Qf, Kf, Vf, Wof, cnt);
  k_main<<<256, 512, 0, stream>>>(x, Qf, Kf, Vf, Wof, w1, b1, w2, b2, H, HN, U, (u16*)d_out, cnt);
}
```

```cpp
#include <hip/hip_runtime.h>
#include <hip/hip_bf16.h>

typedef unsigned short u16;
using bf16x8 = __attribute__((ext_vector_type(8))) short;
using s16x4 = __attribute__((ext_vector_type(4))) short;
using f32x4 = __attribute__((ext_vector_type(4))) float;
using f32x16 = __attribute__((ext_vector_type(16))) float;
using u32x4 = __attribute__((ext_vector_type(4))) unsigned;
using u32x2 = __attribute__((ext_vector_type(2))) unsigned;
typedef short v4i16_t __attribute__((ext_vector_type(4)));
typedef __attribute__((address_space(3))) const char* lds_cptr;
typedef __bf16 bf16x2_t __attribute__((ext_vector_type(2)));
typedef float f32x2_t __attribute__((ext_vector_type(2)));

#define DI __device__ __forceinline__
#define MFMA32(a, b, c) __builtin_amdgcn_mfma_f32_32x32x16_bf16(a, b, c, 0, 0, 0)
#define MFMA16(a, b, c) __builtin_amdgcn_mfma_f32_16x16x32_bf16(a, b, c, 0, 0, 0)
#define SBAR() __builtin_amdgcn_sched_barrier(0)
#define LBAR() asm volatile("s_waitcnt lgkmcnt(0)\n\ts_barrier" ::: "memory")
#define WAITV_BAR(N) asm volatile("s_waitcnt vmcnt(" #N ") lgkmcnt(0)\n\ts_barrier" ::: "memory")

constexpr int S = 2048, D = 448, DH = 28, F = 1792;
constexpr float QSCALE = 0.18898223650461362f * 1.4426950408889634f;
constexpr float LN_EPS = 1e-5f;

constexpr size_t WS_QF = 0;
constexpr size_t WS_KF = WS_QF + 2097152;
constexpr size_t WS_VF = WS_KF + 131072;
constexpr size_t WS_WOF = WS_VF + 131072;
constexpr size_t WS_H = WS_WOF + 401408;
constexpr size_t WS_HN = WS_H + 1835008;
constexpr size_t WS_U = WS_HN + 1835008;
constexpr size_t WS_CNT = WS_U + 1605632;

#define STAMP(k, i)
#define CSTAMP(k, i)

DI unsigned cvtpk(float lo, float hi) {
  const f32x2_t v = {lo, hi};
  return __builtin_bit_cast(unsigned, __builtin_convertvector(v, bf16x2_t));
}
DI float bflo(unsigned u) { return __uint_as_float(u << 16); }
DI float bfhi(unsigned u) { return __uint_as_float(u & 0xffff0000u); }
template <int CTRL> DI float dppf(float v) {
  return __uint_as_float(__builtin_amdgcn_update_dpp(0u, __float_as_uint(v), CTRL, 0xf, 0xf, true));
}
DI float wave_sum(float v) {
  v += dppf<0xB1>(v);
  v += dppf<0x4E>(v);
  v += dppf<0x141>(v);
  v += dppf<0x140>(v);
  auto a = __builtin_amdgcn_permlane16_swap(__float_as_uint(v), __float_as_uint(v), false, false);
  v = __uint_as_float(a[0]) + __uint_as_float(a[1]);
  auto b = __builtin_amdgcn_permlane32_swap(__float_as_uint(v), __float_as_uint(v), false, false);
  return __uint_as_float(b[0]) + __uint_as_float(b[1]);
}
DI float half_max(float v) {
  auto b = __builtin_amdgcn_permlane32_swap(__float_as_uint(v), __float_as_uint(v), false, false);
  return __builtin_fmaxf(__uint_as_float(b[0]), __uint_as_float(b[1]));
}
DI void glds16(const void* g, unsigned lds_base) {
  unsigned sv;
  asm volatile("s_mov_b32 %0, m0\n\ts_mov_b32 m0, %2\n\ts_nop 0\n\tglobal_load_lds_dwordx4 %1, off\n\ts_mov_b32 m0, %0"
               : "=&s"(sv) : "v"(g), "s"(lds_base) : "memory");
}
DI void glds16s(const void* sbase, unsigned voff, unsigned lds_base) {
  unsigned sv;
  asm volatile("s_mov_b32 %0, m0\n\ts_mov_b32 m0, %3\n\ts_nop 0\n\tglobal_load_lds_dwordx4 %1, %2\n\ts_mov_b32 m0, %0"
               : "=&s"(sv) : "v"(voff), "s"(sbase), "s"(lds_base) : "memory");
}
DI void glds4(const void* g, unsigned lds_base) {
  unsigned sv;
  asm volatile("s_mov_b32 %0, m0\n\ts_mov_b32 m0, %2\n\ts_nop 0\n\tglobal_load_lds_dword %1, off\n\ts_mov_b32 m0, %0"
               : "=&s"(sv) : "v"(g), "s"(lds_base) : "memory");
}
template <int N> DI void waitv4(bf16x8& a, bf16x8& b, bf16x8& c, bf16x8& d) {
  asm volatile("s_waitcnt vmcnt(%4)" : "+v"(a), "+v"(b), "+v"(c), "+v"(d) : "i"(N) : "memory");
}
template <int OFF> DI void gload16(bf16x8& d, const void* p) {
  asm volatile("global_load_dwordx4 %0, %1, off offset:%2" : "=v"(d) : "v"(p), "i"(OFF) : "memory");
}
DI s16x4 trread(lds_cptr p) {
  return __builtin_bit_cast(s16x4, __builtin_amdgcn_ds_read_tr16_b64_v4i16((__attribute__((address_space(3))) v4i16_t*)p));
}
DI bf16x8 ldsr16(lds_cptr p) { return *(const __attribute__((address_space(3))) bf16x8*)p; }
DI bf16x8 cat8(s16x4 a, s16x4 b) { return __builtin_shufflevector(a, b, 0, 1, 2, 3, 4, 5, 6, 7); }
DI int xcd_remap(int bid, int nwg) {
  const int q = nwg >> 3, r = nwg & 7, x = bid & 7, o = bid >> 3;
  return (x < r ? x * (q + 1) : r * (q + 1) + (x - r) * q) + o;
}

constexpr int K1_WI = 448 * 128, K1_X = 2 * K1_WI, K1_XS = 528, K1_DUM = K1_X + 64 * K1_XS, K1_LDS = K1_DUM + 1024;
constexpr int KMAIN_KB = 28;
__global__ __launch_bounds__(512) void k_qkv(const u16* __restrict__ x, const u16* __restrict__ wq, const u16* __restrict__ wk,
                                             const u16* __restrict__ wv, const u16* __restrict__ wo, u16* __restrict__ Qf,
                                             u16* __restrict__ Kf, u16* __restrict__ Vf, u16* __restrict__ Wof, unsigned* __restrict__ cnt) {
  __shared__ __attribute__((aligned(16))) char lds[K1_LDS];
  const int tid = threadIdx.x, lane = tid & 63, w = __builtin_amdgcn_readfirstlane(tid >> 6);
  const int t = xcd_remap(blockIdx.x, 256), rt = t >> 2, ct = t & 3;
  const unsigned lds0 = (unsigned)(uintptr_t)lds;
  STAMP(0, 0);
  bf16x8 xr[4];
  const bool act = lane < 56;
  {
    const u16* xp_ = x + (size_t)(rt * 32 + w * 4) * D + (act ? lane : 55) * 8;
    gload16<0>(xr[0], xp_); gload16<896>(xr[1], xp_); gload16<1792>(xr[2], xp_); gload16<2688>(xr[3], xp_);
  }
#pragma unroll
  for (int im = 0; im < 2; ++im) {
    const int c64 = 2 * ct + im;
    if (c64 < 7) {
#pragma unroll
      for (int i = 0; i < 7; ++i) {
        const int piece = w * 7 + i, row = piece * 8 + (lane >> 3);
        const int f = ((row >> 1) & 1) | (((row >> 3) & 1) << 1);
        const int c = (lane & 7) ^ (f << 1);
        glds16(wq + (size_t)row * D + c64 * 64 + c * 8, lds0 + im * K1_WI + piece * 1024);
      }
    } else {
#pragma unroll
      for (int i = 0; i < 7; ++i) {
        const int pc = w * 7 + i < 50 ? w * 7 + i : 49, mat = pc >= 25, piece = pc - 25 * mat;
        const int e = piece * 512 + lane * 8;
        glds16((mat ? wv : wk) + (e < 448 * 28 ? e : 448 * 28 - 8), lds0 + im * K1_WI + mat * 25600 + piece * 1024);
      }
    }
  }
  SBAR();
  waitv4<14>(xr[0], xr[1], xr[2], xr[3]);
  {
    u32x4 xv[4];
#pragma unroll
    for (int r = 0; r < 4; ++r) xv[r] = __builtin_bit_cast(u32x4, xr[r]);
    float v[4][8], mean[4], rs[4];
#pragma unroll
    for (int r = 0; r < 4; ++r) {
      float s = 0.f;
#pragma unroll
      for (int i = 0; i < 4; ++i) {
        v[r][2 * i] = act ? bflo(xv[r][i]) : 0.f; v[r][2 * i + 1] = act ? bfhi(xv[r][i]) : 0.f;
        s += v[r][2 * i] + v[r][2 * i + 1];
      }
      mean[r] = s;
    }
#pragma unroll
    for (int r = 0; r < 4; ++r) mean[r] = wave_sum(mean[r]) * (1.f / D);
#pragma unroll
    for (int r = 0; r < 4; ++r) {
      float q = 0.f;
#pragma unroll
      for (int i = 0; i < 8; ++i) { v[r][i] = act ? v[r][i] - mean[r] : 0.f; q += v[r][i] * v[r][i]; }
      rs[r] = q;
    }
#pragma unroll
    for (int r = 0; r < 4; ++r) rs[r] = __builtin_amdgcn_rsqf(wave_sum(rs[r]) * (1.f / D) + LN_EPS);
    char* xdst = lds + K1_X + lane * K1_XS + w * 64;
#pragma unroll
    for (int r = 0; r < 4; ++r) {
      u32x4 o;
#pragma unroll
      for (int i = 0; i < 4; ++i) o[i] = cvtpk(v[r][2 * i] * rs[r], v[r][2 * i + 1] * rs[r]);
      *(u32x4*)(xdst + r * 16) = o;
    }
  }
  STAMP(0, 1);
  WAITV_BAR(0);
  STAMP(0, 2);
  if (w == 7 && (blockIdx.x >> 3) < KMAIN_KB) {
    unsigned long long pc;
    int off;
    asm volatile("s_getpc_b64 %0\n1:\n\ts_mov_b32 %1, k_main-1b" : "=s"(pc), "=s"(off));
    glds16s((const char*)(pc + (long long)off) + (blockIdx.x >> 3) * 1024, lane * 16, lds0 + K1_DUM);
  }
  u16 wt[8];
  const int frag = blockIdx.x + 256 * (w & 1);
  const bool dofrag = w < 2 && frag < 392;
  if (dofrag) {
    const int nb = frag / 14, ks = frag - nb * 14;
#pragma unroll
    for (int j = 0; j < 8; ++j) wt[j] = wo[(size_t)(32 * ks + 8 * (lane >> 4) + j) * D + 16 * nb + (lane & 15)];
  }
  SBAR();
  const int nb = w & 3, im = w >> 2, c64 = 2 * ct + im, g = lane >> 4, q4 = (lane & 15) >> 2, p = lane & 3, l15 = lane & 15;
  const int f = ((q4 >> 1) & 1) | ((g & 1) << 1);
  const bool kv = c64 == 7;
  const int rs_ = kv ? 56 : 128;
  const lds_cptr wp = (lds_cptr)lds + im * K1_WI + (8 * g + q4) * rs_ +
                      (kv ? (nb >> 1) * 25600 + (nb & 1) * 32 + 8 * p : (((2 * nb + (p >> 1)) ^ (f << 1)) * 16) + 8 * (p & 1));
  const lds_cptr xp = (lds_cptr)lds + K1_X + g * K1_XS + l15 * 16;
  f32x4 acc0 = {0.f, 0.f, 0.f, 0.f}, acc1 = {0.f, 0.f, 0.f, 0.f};
  {
    bf16x8 a[14], b0[14], b1[14];
#pragma unroll
    for (int ks = 0; ks < 14; ++ks) {
      a[ks] = cat8(trread(wp + ks * 32 * rs_), trread(wp + ks * 32 * rs_ + 4 * rs_));
      b0[ks] = ldsr16(xp + ks * 4 * K1_XS);
      b1[ks] = ldsr16(xp + ks * 4 * K1_XS + 256);
    }
    SBAR();
#pragma unroll
    for (int ks = 0; ks < 14; ++ks) {
      acc0 = MFMA16(a[ks], b0[ks], acc0);
      acc1 = MFMA16(a[ks], b1[ks], acc1);
    }
  }
  STAMP(0, 3);
#pragma unroll
  for (int tb = 0; tb < 2; ++tb) {
    const f32x4 a = tb ? acc1 : acc0;
    const int s = rt * 32 + 16 * tb + l15;
    const int nl = 16 * nb + 4 * g;
    if (c64 < 7) {
      const int n0 = 64 * c64 + nl, h = n0 / 28, d0 = n0 - 28 * h;
      const int tg = s >> 3, c = (h & 3) * 8 + (s & 7), b = h >> 2;
      const size_t off = ((((size_t)(tg * 4 + b) * 2 + (d0 >> 4)) * 64) + ((d0 >> 3) & 1) * 32 + c) * 8 + (d0 & 7);
      *(u32x2*)(Qf + off) = (u32x2){cvtpk(a[0] * QSCALE, a[1] * QSCALE), cvtpk(a[2] * QSCALE, a[3] * QSCALE)};
    } else {
      const int kt = s >> 5, kk = s & 31;
      const int d0 = 16 * (nb & 1) + 4 * g;
      if (nb < 2) {
        const size_t off = ((((size_t)kt * 2 + (d0 >> 4)) * 64) + ((d0 >> 3) & 1) * 32 + kk) * 8 + (d0 & 7);
        u32x2 v = {cvtpk(a[0], a[1]), cvtpk(a[2], a[3])};
        if (d0 == 28) v = (u32x2){0x3f803f80u, 0x00003f80u};
        *(u32x2*)(Kf + off) = v;
      } else {
        const int sp = kk >> 4, j = ((kk >> 3) & 1) * 4 + (kk & 3), hi = (kk >> 2) & 1;
        const size_t off = ((((size_t)kt * 2 + sp) * 64) + hi * 32 + d0) * 8 + j;
        unsigned v0 = cvtpk(a[0], a[1]), v1 = cvtpk(a[2], a[3]);
        if (d0 == 28) { v0 = 0x00003f80u; v1 = 0u; }
        Vf[off] = (u16)v0; Vf[off + 8] = (u16)(v0 >> 16); Vf[off + 16] = (u16)v1; Vf[off + 24] = (u16)(v1 >> 16);
      }
    }
  }
  if (dofrag) {
    u32x4 o;
#pragma unroll
    for (int i = 0; i < 4; ++i) o[i] = (unsigned)wt[2 * i] | ((unsigned)wt[2 * i + 1] << 16);
    *(u32x4*)(Wof + ((size_t)frag * 64 + lane) * 8) = o;
  }
  if (blockIdx.x == 0) { cnt[tid] = 0u; cnt[512 + tid] = 0u; }
  if (w == 7) asm volatile("s_waitcnt vmcnt(0)" ::: "memory");
  STAMP(0, 4);
}

constexpr int K2_RING = 0, K2_OT = 131072, K2_DUM = K2_OT + 7168, K2_X = K2_DUM + 4096, K2_LDS = K2_X + 16384;
constexpr int K2_HB = 65536, HBS = 452, K2_HS = K2_X;

DI float set_ref(bf16x8& q1, float m, int hi) {
  const float a = bflo(cvtpk(-m, 0.f)), r1 = -m - a, b = bflo(cvtpk(r1, 0.f)), c = bflo(cvtpk(r1 - b, 0.f));
  u32x4 v = __builtin_bit_cast(u32x4, q1);
  v[2] = hi ? cvtpk(a, b) : v[2];
  v[3] = hi ? cvtpk(c, 0.f) : v[3];
  q1 = __builtin_bit_cast(bf16x8, v);
  return -((a + b) + c);
}
DI float tile_max(const f32x16& s) {
  float a = __builtin_fmaxf(__builtin_fmaxf(s[0], s[1]), s[2]), b = __builtin_fmaxf(__builtin_fmaxf(s[3], s[4]), s[5]);
  a = __builtin_fmaxf(__builtin_fmaxf(a, s[6]), s[7]); b = __builtin_fmaxf(__builtin_fmaxf(b, s[8]), s[9]);
  a = __builtin_fmaxf(__builtin_fmaxf(a, s[10]), s[11]); b = __builtin_fmaxf(__builtin_fmaxf(b, s[12]), s[13]);
  a = __builtin_fmaxf(__builtin_fmaxf(a, s[14]), s[15]);
  return half_max(__builtin_fmaxf(a, b));
}
template <int N> DI void waitv() { asm volatile("s_waitcnt vmcnt(%0)" ::"i"(N) : "memory"); }
#define RBAR() asm volatile("s_barrier" ::: "memory")

DI void attn_safe(const u16* __restrict__ Kf, const u16* __restrict__ Vf, bf16x8 q0, bf16x8 q1, f32x16& o, int lane, int hi) {
  const f32x16 zero = {};
  float mref = 0.f;
  o = zero;
#pragma unroll 1
  for (int t = 0; t < 64; ++t) {
    const bf16x8 k0 = *(const bf16x8*)(Kf + t * 1024 + lane * 8), k1 = *(const bf16x8*)(Kf + t * 1024 + 512 + lane * 8);
    const bf16x8 v0 = *(const bf16x8*)(Vf + t * 1024 + lane * 8), v1 = *(const bf16x8*)(Vf + t * 1024 + 512 + lane * 8);
    (void)set_ref(q1, 0.f, hi);
    f32x16 s = MFMA32(k0, q0, zero);
    s = MFMA32(k1, q1, s);
    const float tm = tile_max(s);
    const float mn = set_ref(q1, t == 0 ? tm : __builtin_fmaxf(tm, mref), hi);
    const float al = t == 0 ? 0.f : __builtin_amdgcn_exp2f(mref - mn);
#pragma unroll
    for (int r = 0; r < 16; ++r) o[r] *= al;
    mref = mn;
    s = MFMA32(k0, q0, zero);
    s = MFMA32(k1, q1, s);
    u32x4 p0, p1;
#pragma unroll
    for (int i = 0; i < 4; ++i) {
      p0[i] = cvtpk(__builtin_amdgcn_exp2f(s[2 * i]), __builtin_amdgcn_exp2f(s[2 * i + 1]));
      p1[i] = cvtpk(__builtin_amdgcn_exp2f(s[8 + 2 * i]), __builtin_amdgcn_exp2f(s[8 + 2 * i + 1]));
    }
    o = MFMA32(v0, __builtin_bit_cast(bf16x8, p0), o);
    o = MFMA32(v1, __builtin_bit_cast(bf16x8, p1), o);
  }
}

#define EX4(S, i) do { S[i] = __builtin_amdgcn_exp2f(S[i]); S[i + 1] = __builtin_amdgcn_exp2f(S[i + 1]); \
    S[i + 2] = __builtin_amdgcn_exp2f(S[i + 2]); S[i + 3] = __builtin_amdgcn_exp2f(S[i + 3]); } while (0)
#define PK8(P, S, i) do { P[0] = cvtpk(S[i], S[i + 1]); P[1] = cvtpk(S[i + 2], S[i + 3]); P[2] = cvtpk(S[i + 4], S[i + 5]); P[3] = cvtpk(S[i + 6], S[i + 7]); } while (0)
#define BF(P) __builtin_bit_cast(bf16x8, P)
constexpr int CPRE = 0;
#define WOPF(j) do { if ((j) < CPRE) wf[(j) / 7][(j) % 7] = WOFR(j); } while (0)
#define RDS(u, off) ldsr16(((u) < 4 ? B0 : B1) + ((u) & 3) * 8192 + (off))
#define CTILE(u, NX, PF) do { \
      \
    sB = MFMA32(k0, qB0, zero); SBAR(); if (NX) k0 = RDS((u) + 1, 0); EX4(sA, 0); WOPF(PF); SBAR(); \
    sB = MFMA32(k1, qB1, sB); SBAR(); if (NX) k1 = RDS((u) + 1, 1024); EX4(sA, 4); PK8(p0, sA, 0); SBAR(); \
    oB = MFMA32(v1, BF(p1), oB); SBAR(); v1 = RDS(u, 3072); EX4(sA, 8); WOPF((PF) + 1); SBAR(); \
    oA = MFMA32(v0, BF(p0), oA); SBAR(); EX4(sA, 12); PK8(p1, sA, 8); SBAR(); \
      \
    sA = MFMA32(k0, qA0, zero); SBAR(); EX4(sB, 0); WOPF((PF) + 2); SBAR(); \
    sA = MFMA32(k1, qA1, sA); SBAR(); EX4(sB, 4); PK8(p0, sB, 0); SBAR(); \
    oA = MFMA32(v1, BF(p1), oA); SBAR(); EX4(sB, 8); WOPF((PF) + 3); SBAR(); \
    oB = MFMA32(v0, BF(p0), oB); SBAR(); if (NX) v0 = RDS((u) + 1, 2048); EX4(sB, 12); PK8(p1, sB, 8); SBAR(); } while (0)
#define CSTAGE(LAST, PF) do { \
    CTILE(0, true, PF); CTILE(1, true, (PF) + 4); CTILE(2, true, (PF) + 8); \
    if (!(LAST)) RBAR(); \
    CTILE(3, !(LAST), (PF) + 12); } while (0)

DI void st16c(void* p, u32x4 v) {
  asm volatile("global_store_dwordx4 %0, %1, off sc1\n\ts_nop 1" ::"v"(p), "v"(v) : "memory");
}
DI void attn_phase(char* lds, const u16* __restrict__ x, const u16* __restrict__ Qf, const u16* __restrict__ Kf,
                   const u16* __restrict__ Vf, const u16* __restrict__ Wof, const u16* __restrict__ w1,
                   const u16* __restrict__ w2, u16* H, u16* HN) {
  const int tid = threadIdx.x, lane = tid & 63, w = __builtin_amdgcn_readfirstlane(tid >> 6), hi = lane >> 5;
  const int tg = blockIdx.x;
  STAMP(1, 0);
  const int kh = w >> 2, ng = w & 3;
  const char* wob = (const char*)(Wof + (size_t)(7 * ng * 14 + 7 * kh) * 512);
  const unsigned wl = lane * 16;
  bf16x8 wf[7][7];
#define WOFR(i_) (*(const bf16x8*)(wob + (((i_) / 7) * 14 + (i_) % 7) * 1024 + wl))
#define LDWO(i0, i1) do { _Pragma("unroll") for (int i_ = i0; i_ < i1; ++i_) wf[i_ / 7][i_ % 7] = WOFR(i_); } while (0)
#define BOF(k) (*(const bf16x8*)(lds + K2_OT + ((4 * (k) + (lane >> 4)) * 8 + (lane & 7)) * 16))
#define HBST(acc) do { if ((lane & 15) < 8) { \
      _Pragma("unroll") for (int n = 0; n < 7; ++n) *(f32x4*)(lds + K2_HB + ((kh * 8 + (lane & 15)) * HBS + 16 * (7 * ng + n) + 4 * (lane >> 4)) * 4) = acc[n]; \
    } } while (0)
  u32x4 xv;
  if (w < 4) {
    const f32x16 zero = {};
    const int qh = w & 1, kp = w >> 1;
    const lds_cptr rp0 = (lds_cptr)lds + K2_RING + lane * 16;
    const lds_cptr rp = rp0 + kp * 4096;
    lds_cptr B0 = rp, B1 = rp + 32768;
    const u16* qp = Qf + ((size_t)((tg * 4 + 2 * qh) * 2) * 64 + lane) * 8;
    bf16x8 qA0 = *(const bf16x8*)(qp), qA1 = *(const bf16x8*)(qp + 512), qB0 = *(const bf16x8*)(qp + 1024), qB1 = *(const bf16x8*)(qp + 1536);
    RBAR();
    bf16x8 k0 = ldsr16(rp0), k1 = ldsr16(rp0 + 1024), v0 = RDS(0, 2048), v1 = v0;
    (void)set_ref(qA1, 0.f, hi);
    (void)set_ref(qB1, 0.f, hi);
    f32x16 sA = MFMA32(k0, qA0, zero), sB = MFMA32(k0, qB0, zero);
    sA = MFMA32(k1, qA1, sA);
    sB = MFMA32(k1, qB1, sB);
    k0 = RDS(0, 0); k1 = RDS(0, 1024);
    (void)set_ref(qA1, tile_max(sA), hi);
    (void)set_ref(qB1, tile_max(sB), hi);
    sA = MFMA32(k0, qA0, zero);
    sA = MFMA32(k1, qA1, sA);
    f32x16 oA = zero, oB = zero;
    u32x4 p0, p1 = {0u, 0u, 0u, 0u};
    SBAR();
    STAMP(1, 8);
#pragma unroll 1
    for (int st = 0; st < 4; ++st) {
      CSTAGE(false, 64);
      B0 = B1; B1 = rp + ((st + 2) & 3) * 32768;
    }
    CSTAGE(false, 0);
    B0 = B1; B1 = rp + 2 * 32768;
    CSTAGE(false, 16);
    B0 = B1; B1 = rp + 3 * 32768;
    CSTAGE(false, 64);
    B0 = B1;
    CSTAGE(true, 64);
    oB = MFMA32(v1, BF(p1), oB);
    STAMP(1, 10);
    {
      const f32x16 snd = kp ? oA : oB;
#pragma unroll
      for (int r4 = 0; r4 < 4; ++r4)
        *(f32x4*)(lds + K2_X + ((w * 4 + r4) * 64 + lane) * 16) = (f32x4){snd[4 * r4], snd[4 * r4 + 1], snd[4 * r4 + 2], snd[4 * r4 + 3]};
    }
    f32x16 o0 = kp ? oB : oA;
    bf16x8 qk0 = kp ? qB0 : qA0, qk1 = kp ? qB1 : qA1;
    asm volatile("" : "+v"(o0), "+v"(qk0), "+v"(qk1));
    SBAR();
    LBAR();
#pragma unroll
    for (int r4 = 0; r4 < 4; ++r4) {
      const f32x4 t = *(const f32x4*)(lds + K2_X + (((w ^ 2) * 4 + r4) * 64 + lane) * 16);
      o0[4 * r4] += t[0]; o0[4 * r4 + 1] += t[1]; o0[4 * r4 + 2] += t[2]; o0[4 * r4 + 3] += t[3];
    }
    const int qb = 2 * qh + kp;
    {
      float chk = 0.f;
#pragma unroll
      for (int r = 0; r < 16; ++r) chk = __builtin_fmaf(o0[r], 0.f, chk);
      const bool bad = !(chk == 0.f) || !(o0[12] < 1.8e19f);
      if (__any(bad)) {
        attn_safe(Kf, Vf, qk0, qk1, o0, lane, hi);
      }
    }
    {
      auto sw = __builtin_amdgcn_permlane32_swap(__float_as_uint(o0[12]), __float_as_uint(o0[12]), false, false);
      const float il = 1.f / __uint_as_float(sw[1]);
      const int c = lane & 31;
#pragma unroll
      for (int r4 = 0; r4 < 4; ++r4) {
        const int d = 8 * r4 + 4 * hi, k = 28 * (4 * qb + (c >> 3)) + d;
        if (d < 28)
          *(u32x2*)(lds + K2_OT + ((k >> 3) * 8 + (c & 7)) * 16 + (k & 7) * 2) =
              (u32x2){cvtpk(o0[4 * r4] * il, o0[4 * r4 + 1] * il), cvtpk(o0[4 * r4 + 2] * il, o0[4 * r4 + 3] * il)};
      }
    }
    STAMP(1, 1);
    LBAR();
    STAMP(1, 2);
    LDWO(16, 39);
    xv = *(const u32x4*)(x + (size_t)(tg * 8 + w) * D + (lane < 56 ? lane : 55) * 8);
    SBAR();
    {
      bf16x8 bo[7];
#pragma unroll
      for (int k = 0; k < 7; ++k) bo[k] = BOF(k);
#pragma unroll
      for (int i = 0; i < 16; ++i) wf[i / 7][i % 7] = *(const bf16x8*)(lds + K2_RING + (i >> 3) * 32768 + w * 8192 + (i & 7) * 1024 + lane * 16);
      f32x4 acc[7];
#pragma unroll
      for (int n = 0; n < 7; ++n) acc[n] = (f32x4){0.f, 0.f, 0.f, 0.f};
#pragma unroll
      for (int i = 0; i < 39; ++i) acc[i / 7] = MFMA16(wf[i / 7][i % 7], bo[i % 7], acc[i / 7]);
      HBST(acc);
    }
  } else {
    const int hw = w - 4;
    __builtin_amdgcn_s_setprio(3);
    const unsigned ring = (unsigned)(uintptr_t)lds + K2_RING + hw * 8192;
    const u16* kp = Kf + (size_t)hw * 2048;
    const u16* vp = Vf + (size_t)hw * 2048;
    const unsigned l16 = lane * 16;
#define DMAS(s_) do { const unsigned sl_ = ring + ((s_) & 3) * 32768; \
    _Pragma("unroll") for (int u_ = 0; u_ < 2; ++u_) { \
      glds16s(kp + (s_) * 8192 + u_ * 1024, l16, sl_ + u_ * 4096); glds16s(kp + (s_) * 8192 + u_ * 1024 + 512, l16, sl_ + u_ * 4096 + 1024); \
      glds16s(vp + (s_) * 8192 + u_ * 1024, l16, sl_ + u_ * 4096 + 2048); glds16s(vp + (s_) * 8192 + u_ * 1024 + 512, l16, sl_ + u_ * 4096 + 3072); } } while (0)
    const unsigned dum = (unsigned)(uintptr_t)lds + K2_DUM + hw * 1024;
    const int xg = tg & 7, rk = tg >> 3;
    const int ts = xg < 4 ? 25 * xg : 100 + 24 * (xg - 4), te = ts + (xg < 4 ? 25 : 24);
    const int n0 = ts / 7, n1 = (te - 1) / 7;
    const int nb0 = n0 + 3 <= 27 ? n0 : 24;
    const u16* p1 = w1 + (size_t)(64 * rk + 16 * hw) * F + nb0 * 64;
    const u16* p1b = w1 + (size_t)(64 * rk + 16 * hw) * F + n1 * 64;
    const u16* p2 = w2 + (size_t)(56 * rk + 14 * hw) * S + 4 * xg * 64;
    const unsigned o1 = ((lane >> 5) * F + (lane & 31) * 8) * 2, o1b = ((lane >> 3) * F + (lane & 7) * 8) * 2, o2 = ((lane >> 5) * S + (lane & 31) * 8) * 2;
#define PFD(i0, i1) do { _Pragma("unroll") for (int i_ = i0; i_ < i1; ++i_) { \
      if (i_ < 8) glds16s(p1 + (size_t)i_ * 2 * F, o1, dum); \
      else if (i_ < 10) glds16s(p1b + (size_t)(i_ - 8) * 8 * F, o1b, dum); \
      else glds16s(p2 + (size_t)(i_ - 10) * 2 * S, o2, dum); } } while (0)
    DMAS(0); DMAS(1); DMAS(2);
    waitv<16>(); RBAR();
    LDWO(0, 12); SBAR(); PFD(0, 3);
    waitv<23>(); RBAR();
    DMAS(3); LDWO(12, 24); SBAR(); PFD(3, 6);
    waitv<38>(); RBAR();
    DMAS(4); LDWO(24, 36); SBAR(); PFD(6, 9);
    waitv<38>(); RBAR();
    DMAS(5); LDWO(36, 49); SBAR(); PFD(9, 12);
    waitv<39>(); RBAR();
    bf16x8 wx[10];
    DMAS(6);
#pragma unroll
    for (int i = 0; i < 10; ++i) wx[i] = *(const bf16x8*)(wob - 7 * 1024 + (((39 + i) / 7) * 14 + (39 + i) % 7) * 1024 + wl);
    SBAR(); PFD(12, 15);
    waitv<37>(); RBAR();
    DMAS(7); PFD(15, 17);
    waitv<23>(); RBAR();
#pragma unroll
    for (int i = 0; i < 8; ++i) glds16s(wob - 7 * 1024 + ((i / 7) * 14 + i % 7) * 1024, wl, ring + i * 1024);
    waitv<10>(); RBAR();
#pragma unroll
    for (int i = 8; i < 16; ++i) glds16s(wob - 7 * 1024 + ((i / 7) * 14 + i % 7) * 1024, wl, ring + 32768 + (i - 8) * 1024);
    xv = *(const u32x4*)(x + (size_t)(tg * 8 + w) * D + (lane < 56 ? lane : 55) * 8);
    SBAR();
    __builtin_amdgcn_s_setprio(0);
    RBAR();
    waitv<1>();
    STAMP(1, 1);
    LBAR();
    STAMP(1, 2);
    {
      f32x4 acc[7];
      {
        bf16x8 bo[7];
#pragma unroll
        for (int k = 0; k < 7; ++k) bo[k] = BOF(k);
        acc[5] = (f32x4){0.f, 0.f, 0.f, 0.f}; acc[6] = acc[5];
#pragma unroll
        for (int i = 0; i < 10; ++i) acc[(39 + i) / 7] = MFMA16(wx[i], bo[(39 + i) % 7], acc[(39 + i) / 7]);
      }
      SBAR();
      bf16x8 bo[7];
#pragma unroll
      for (int k = 0; k < 7; ++k) bo[k] = BOF(7 + k);
#pragma unroll
      for (int n = 0; n < 7; ++n) {
        if (n < 5) acc[n] = (f32x4){0.f, 0.f, 0.f, 0.f};
#pragma unroll
        for (int k = 0; k < 7; ++k) acc[n] = MFMA16(wf[n][k], bo[k], acc[n]);
      }
      HBST(acc);
    }
  }
  LBAR();
  STAMP(1, 3);
  {
    const int s = tg * 8 + w;
    const bool act = lane < 56;
    const int lc = act ? lane : 55;
    float v[8];
    const f32x4 a0 = *(const f32x4*)(lds + K2_HB + (w * HBS + lc * 8) * 4), a1 = *(const f32x4*)(lds + K2_HB + (w * HBS + lc * 8 + 4) * 4);
    const f32x4 c0 = *(const f32x4*)(lds + K2_HB + ((8 + w) * HBS + lc * 8) * 4), c1 = *(const f32x4*)(lds + K2_HB + ((8 + w) * HBS + lc * 8 + 4) * 4);
    u32x4 hv;
#pragma unroll
    for (int i = 0; i < 2; ++i) {
      hv[i] = cvtpk(bflo(xv[i]) + a0[2 * i] + c0[2 * i], bfhi(xv[i]) + a0[2 * i + 1] + c0[2 * i + 1]);
      hv[2 + i] = cvtpk(bflo(xv[2 + i]) + a1[2 * i] + c1[2 * i], bfhi(xv[2 + i]) + a1[2 * i + 1] + c1[2 * i + 1]);
    }
    if (act) st16c(H + (size_t)s * D + lane * 8, hv);
#pragma unroll
    for (int i = 0; i < 4; ++i) { v[2 * i] = act ? bflo(hv[i]) : 0.f; v[2 * i + 1] = act ? bfhi(hv[i]) : 0.f; }
#pragma unroll
    for (int pass = 0; pass < 2; ++pass) {
      float sm = 0.f;
#pragma unroll
      for (int i = 0; i < 8; ++i) sm += v[i];
      const float mean = wave_sum(sm) * (1.f / D);
      float q = 0.f;
#pragma unroll
      for (int i = 0; i < 8; ++i) { v[i] = act ? v[i] - mean : 0.f; q += v[i] * v[i]; }
      const float rs = __builtin_amdgcn_rsqf(wave_sum(q) * (1.f / D) + LN_EPS);
#pragma unroll
      for (int i = 0; i < 8; ++i) v[i] *= rs;
    }
    if (act) *(u32x4*)(lds + K2_HS + w * 912 + lane * 16) = (u32x4){cvtpk(v[0], v[1]), cvtpk(v[2], v[3]), cvtpk(v[4], v[5]), cvtpk(v[6], v[7])};
  }
  LBAR();
  if (tid < D / 2) {
    unsigned r[8];
#pragma unroll
    for (int i = 0; i < 8; ++i) r[i] = *(const unsigned*)(lds + K2_HS + i * 912 + tid * 4);
    u32x4 lo, hi2;
#pragma unroll
    for (int i = 0; i < 4; ++i) {
      lo[i] = (r[2 * i] & 0xffffu) | (r[2 * i + 1] << 16);
      hi2[i] = (r[2 * i] >> 16) | (r[2 * i + 1] & 0xffff0000u);
    }
    u32x4* dst = (u32x4*)(HN + ((size_t)tg * D + 2 * tid) * 8);
    st16c(dst, lo); st16c(dst + 1, hi2);
  }
  STAMP(1, 4);
}

constexpr int G_RING = 4, G_LDS = 8 * G_RING * 4096;
typedef __attribute__((address_space(1))) unsigned gu32;
typedef __attribute__((address_space(1))) unsigned long long gu64;
#define RLX_AGENT __ATOMIC_RELAXED, __HIP_MEMORY_SCOPE_AGENT
template <int OFF> DI void gload16c(bf16x8& d, const void* p) {
  asm volatile("global_load_dwordx4 %0, %1, off offset:%2 sc1" : "=v"(d) : "v"(p), "i"(OFF) : "memory");
}
template <int PH, int NSH>
DI void mlp_tile(char* lds, int t, const u16* act, const u16* __restrict__ W, const u16* __restrict__ bias, const u16* H, u16* out,
                 unsigned* cnt, unsigned want) {
  constexpr int KT = PH == 0 ? S : F, LDW = PH == 0 ? F : S, NK = KT / 256, NR = NK - G_RING, LA = 5;
  const int tid = threadIdx.x, lane = tid & 63, w = __builtin_amdgcn_readfirstlane(tid >> 6);
  const int nt = t / 7, mt = t - nt * 7;
  const unsigned lds0 = (unsigned)(uintptr_t)lds + w * (G_RING * 4096);
  const int g = lane >> 4, q4 = (lane & 15) >> 2, p = lane & 3, l15 = lane & 15;
  STAMP(2 + PH, 0);
  const int enb = w >> 1, emb = 2 * (w & 1);
  u32x2 eb;
  if (PH == 0) eb = *(const u32x2*)(bias + 64 * nt + 16 * enb + 4 * g);
  else eb[0] = bias[64 * nt + 16 * enb + l15];
  SBAR();
  const u16* asrc = act + ((size_t)(4 * w + g) * D + 64 * mt + l15) * 8;
  const int r8 = lane >> 3, fe = (r8 >> 1) & 1;
  const u16* wsrc0 = W + (size_t)(32 * w + r8) * LDW + 64 * nt + ((lane & 7) ^ (fe << 1)) * 8;
  const u16* wsrc1 = W + (size_t)(32 * w + 8 + r8) * LDW + 64 * nt + ((lane & 7) ^ ((fe | 2) << 1)) * 8;
  const int f = ((q4 >> 1) & 1) | ((g & 1) << 1);
  int woff[4];
#pragma unroll
  for (int nb = 0; nb < 4; ++nb) woff[nb] = w * (G_RING * 4096) + (8 * g + q4) * 128 + (((2 * nb + (p >> 1)) ^ (f << 1)) * 16) + 8 * (p & 1);
  bf16x8 af[NK][4];
  f32x4 acc[4][4] = {};
#define STAGE_W(i) do { const unsigned sl_ = lds0 + ((i) % G_RING) * 4096; const size_t o_ = (size_t)(i) * 256 * LDW; \
    glds16(wsrc0 + o_, sl_); glds16(wsrc1 + o_, sl_ + 1024); glds16(wsrc0 + o_ + 16 * LDW, sl_ + 2048); glds16(wsrc1 + o_ + 16 * LDW, sl_ + 3072); } while (0)
#define LOAD_A(i) do { const u16* ap_ = asrc + (size_t)(i) * 32 * D * 8; \
    gload16c<0>(af[i][0], ap_); gload16c<256>(af[i][1], ap_); gload16c<512>(af[i][2], ap_); gload16c<768>(af[i][3], ap_); } while (0)
#define RDW(WF, i) do { _Pragma("unroll") for (int nb_ = 0; nb_ < 4; ++nb_) { const lds_cptr sp_ = (lds_cptr)lds + woff[nb_] + ((i) % G_RING) * 4096; \
      WF[nb_] = cat8(trread(sp_), trread(sp_ + 512)); } } while (0)
#define MMA(WF, i) do { _Pragma("unroll") for (int nb_ = 0; nb_ < 4; ++nb_) _Pragma("unroll") for (int mb_ = 0; mb_ < 4; ++mb_) \
      acc[nb_][mb_] = PH == 0 ? MFMA16(WF[nb_], af[i][mb_], acc[nb_][mb_]) : MFMA16(af[i][mb_], WF[nb_], acc[nb_][mb_]); } while (0)
  bf16x8 wr[NR][4];
#pragma unroll
  for (int j = 0; j < NR; ++j) STAGE_W(G_RING + j);
  if (NR < G_RING) STAGE_W(G_RING - 1);
#pragma unroll
  for (int j = 0; j < NR; ++j) {
    waitv<12>();
    RDW(wr[j], G_RING + j);
    asm volatile("s_waitcnt lgkmcnt(0)" ::: "memory");
    STAGE_W(j);
  }
  STAMP(2 + PH, 1);
  if (w == 0) {
    gu32* cp = (gu32*)(cnt + (lane < NSH ? lane : 0) * 64);
    for (unsigned spins = 0; spins < (1u << 20); ++spins) {
      if (__all(__hip_atomic_load(cp, RLX_AGENT) >= want)) break;
      __builtin_amdgcn_s_sleep(1);
    }
  }
  asm volatile("s_barrier" ::: "memory");
  STAMP(2 + PH, 2);
  unsigned long long eh0 = 0, eh1 = 0;
  if (PH == 1) {
    const int s = 64 * nt + 16 * enb + l15;
    eh0 = __hip_atomic_load((gu64*)(H + (size_t)s * D + 64 * mt + 16 * emb + 4 * g), RLX_AGENT);
    eh1 = __hip_atomic_load((gu64*)(H + (size_t)s * D + 64 * mt + 16 * emb + 16 + 4 * g), RLX_AGENT);
  }
  SBAR();
#pragma unroll
  for (int j = 0; j < LA; ++j) LOAD_A((j + G_RING) % NK);
#pragma unroll
  for (int j = 0; j < NK; ++j) {
    const int i = (j + G_RING) % NK;
    const int rem = NK - 1 - j < LA - 1 ? NK - 1 - j : LA - 1;
    if (rem == 4) waitv4<16>(af[i][0], af[i][1], af[i][2], af[i][3]);
    else if (rem == 3) waitv4<12>(af[i][0], af[i][1], af[i][2], af[i][3]);
    else if (rem == 2) waitv4<8>(af[i][0], af[i][1], af[i][2], af[i][3]);
    else if (rem == 1) waitv4<4>(af[i][0], af[i][1], af[i][2], af[i][3]);
    else waitv4<0>(af[i][0], af[i][1], af[i][2], af[i][3]);
    if (j < NR) {
      MMA(wr[j], i);
    } else {
      bf16x8 wf[4];
      RDW(wf, i);
      MMA(wf, i);
    }
    SBAR();
    if (j + LA < NK) LOAD_A((j + LA + G_RING) % NK);
  }
#undef STAGE_W
#undef LOAD_A
#undef RDW
#undef MMA
  STAMP(2 + PH, 4);
  {
    f32x4* xb = (f32x4*)lds;
#pragma unroll
    for (int nb = 0; nb < 4; ++nb)
#pragma unroll
      for (int mb = 0; mb < 4; ++mb) xb[(w * 16 + nb * 4 + mb) * 64 + lane] = acc[nb][mb];
  }
  LBAR();
  {
    const f32x4* xb = (const f32x4*)lds;
#pragma unroll
    for (int e = 0; e < 2; ++e) {
      f32x4 a = xb[(2 * w + e) * 64 + lane];
#pragma unroll
      for (int v = 1; v < 8; ++v) a += xb[(v * 16 + 2 * w + e) * 64 + lane];
      const int mb = emb + e;
      if (PH == 0) {
        const int fi = 64 * nt + 16 * enb + 4 * g, d = 64 * mt + 16 * mb + l15;
        float z[4] = {a[0] + bflo(eb[0]), a[1] + bfhi(eb[0]), a[2] + bflo(eb[1]), a[3] + bfhi(eb[1])};
#pragma unroll
        for (int i = 0; i < 4; ++i) {
          const float zz = z[i], a2 = zz * (1.f + 0.044715f * zz * zz) * (-2.f * 0.7978845608028654f * 1.4426950408889634f);
          z[i] = zz * __builtin_amdgcn_rcpf(1.f + __builtin_amdgcn_exp2f(a2));
        }
        const unsigned long long v = (unsigned long long)cvtpk(z[0], z[1]) | ((unsigned long long)cvtpk(z[2], z[3]) << 32);
        __hip_atomic_store((gu64*)(out + ((size_t)(fi >> 3) * D + d) * 8 + (fi & 7)), v, RLX_AGENT);
      } else {
        const int d = 64 * mt + 16 * mb + 4 * g, s = 64 * nt + 16 * enb + l15;
        const float bb = bflo(eb[0]);
        const unsigned long long hv = e ? eh1 : eh0;
        const unsigned h0 = (unsigned)hv, h1 = (unsigned)(hv >> 32);
        *(u32x2*)(out + (size_t)s * D + d) =
            (u32x2){cvtpk(a[0] + bb + bflo(h0), a[1] + bb + bfhi(h0)), cvtpk(a[2] + bb + bflo(h1), a[3] + bb + bfhi(h1))};
      }
    }
  }
  STAMP(2 + PH, 5);
}
#define PUBLISH(c) do { asm volatile("s_waitcnt vmcnt(0) lgkmcnt(0)\n\ts_barrier" ::: "memory"); \
    if (threadIdx.x == 0) __hip_atomic_fetch_add((gu32*)(c), 1u, RLX_AGENT); } while (0)

extern "C" __global__ __launch_bounds__(512) void k_main(const u16* __restrict__ x, const u16* __restrict__ Qf, const u16* __restrict__ Kf,
                                              const u16* __restrict__ Vf, const u16* __restrict__ Wof, const u16* __restrict__ w1,
                                              const u16* __restrict__ b1, const u16* __restrict__ w2, const u16* __restrict__ b2,
                                              u16* H, u16* HN, u16* U, u16* out, unsigned* cnt) {
  __shared__ __attribute__((aligned(16))) char lds[K2_LDS];
  attn_phase(lds, x, Qf, Kf, Vf, Wof, w1, w2, H, HN);
  PUBLISH(cnt + (8 + (blockIdx.x & 7)) * 64);
  if (blockIdx.x < 196) {
    const int t = xcd_remap(blockIdx.x, 196);
    mlp_tile<0, 8>(lds, t, HN, w1, b1, H, U, cnt + 8 * 64, 32u);
    PUBLISH(cnt + (t % 7) * 64);
  }
  if (blockIdx.x < 224) {
    const int t2 = xcd_remap(blockIdx.x, 224);
    mlp_tile<1, 1>(lds, t2, U, w2, b2, H, out, cnt + (t2 % 7) * 64, 28u);
  }
}

extern "C" void kernel_launch(void* const* d_in, const int* in_sizes, int n_in, void* d_out, int out_size, void* d_ws, size_t ws_size,
                              hipStream_t stream) {
  const u16* x = (const u16*)d_in[0];
  const u16* wq = (const u16*)d_in[1];
  const u16* wk = (const u16*)d_in[2];
  const u16* wv = (const u16*)d_in[3];
  const u16* wo = (const u16*)d_in[4];
  const u16* w1 = (const u16*)d_in[6];
  const u16* b1 = (const u16*)d_in[7];
  const u16* w2 = (const u16*)d_in[8];
  const u16* b2 = (const u16*)d_in[9];
  char* ws = (char*)d_ws;
  u16 *Qf = (u16*)(ws + WS_QF), *Kf = (u16*)(ws + WS_KF), *Vf = (u16*)(ws + WS_VF), *Wof = (u16*)(ws + WS_WOF);
  u16 *H = (u16*)(ws + WS_H), *HN = (u16*)(ws + WS_HN), *U = (u16*)(ws + WS_U);
  unsigned* cnt = (unsigned*)(ws + WS_CNT);
  k_qkv<<<256, 512, 0, stream>>>(x, wq, wk, wv, wo, Qf, Kf, Vf, Wof, cnt);
  k_main<<<256, 512, 0, stream>>>(x, Qf, Kf, Vf, Wof, w1, b1, w2, b2, H, HN, U, (u16*)d_out, cnt);
}
```

```cpp
#include <hip/hip_runtime.h>
#include <hip/hip_bf16.h>

typedef unsigned short u16;
using bf16x8 = __attribute__((ext_vector_type(8))) short;
using s16x4 = __attribute__((ext_vector_type(4))) short;
using f32x4 = __attribute__((ext_vector_type(4))) float;
using f32x16 = __attribute__((ext_vector_type(16))) float;
using u32x4 = __attribute__((ext_vector_type(4))) unsigned;
using u32x2 = __attribute__((ext_vector_type(2))) unsigned;
typedef short v4i16_t __attribute__((ext_vector_type(4)));
typedef __attribute__((address_space(3))) const char* lds_cptr;
typedef __bf16 bf16x2_t __attribute__((ext_vector_type(2)));
typedef float f32x2_t __attribute__((ext_vector_type(2)));

#define DI __device__ __forceinline__
#define MFMA32(a, b, c) __builtin_amdgcn_mfma_f32_32x32x16_bf16(a, b, c, 0, 0, 0)
#define MFMA16(a, b, c) __builtin_amdgcn_mfma_f32_16x16x32_bf16(a, b, c, 0, 0, 0)
#define SBAR() __builtin_amdgcn_sched_barrier(0)
#define LBAR() asm volatile("s_waitcnt lgkmcnt(0)\n\ts_barrier" ::: "memory")
#define WAITV_BAR(N) asm volatile("s_waitcnt vmcnt(" #N ") lgkmcnt(0)\n\ts_barrier" ::: "memory")

constexpr int S = 2048, D = 448, DH = 28, F = 1792;
constexpr float QSCALE = 0.18898223650461362f * 1.4426950408889634f;
constexpr float LN_EPS = 1e-5f;

constexpr size_t WS_QF = 0;
constexpr size_t WS_KF = WS_QF + 2097152;
constexpr size_t WS_VF = WS_KF + 131072;
constexpr size_t WS_WOF = WS_VF + 131072;
constexpr size_t WS_H = WS_WOF + 401408;
constexpr size_t WS_HN = WS_H + 1835008;
constexpr size_t WS_U = WS_HN + 1835008;
constexpr size_t WS_CNT = WS_U + 1605632;

#define STAMP(k, i)
#define CSTAMP(k, i)

DI unsigned cvtpk(float lo, float hi) {
  const f32x2_t v = {lo, hi};
  return __builtin_bit_cast(unsigned, __builtin_convertvector(v, bf16x2_t));
}
DI float bflo(unsigned u) { return __uint_as_float(u << 16); }
DI float bfhi(unsigned u) { return __uint_as_float(u & 0xffff0000u); }
template <int CTRL> DI float dppf(float v) {
  return __uint_as_float(__builtin_amdgcn_update_dpp(0u, __float_as_uint(v), CTRL, 0xf, 0xf, true));
}
DI float wave_sum(float v) {
  v += dppf<0xB1>(v);
  v += dppf<0x4E>(v);
  v += dppf<0x141>(v);
  v += dppf<0x140>(v);
  auto a = __builtin_amdgcn_permlane16_swap(__float_as_uint(v), __float_as_uint(v), false, false);
  v = __uint_as_float(a[0]) + __uint_as_float(a[1]);
  auto b = __builtin_amdgcn_permlane32_swap(__float_as_uint(v), __float_as_uint(v), false, false);
  return __uint_as_float(b[0]) + __uint_as_float(b[1]);
}
DI float half_max(float v) {
  auto b = __builtin_amdgcn_permlane32_swap(__float_as_uint(v), __float_as_uint(v), false, false);
  return __builtin_fmaxf(__uint_as_float(b[0]), __uint_as_float(b[1]));
}
DI void glds16(const void* g, unsigned lds_base) {
  unsigned sv;
  asm volatile("s_mov_b32 %0, m0\n\ts_mov_b32 m0, %2\n\ts_nop 0\n\tglobal_load_lds_dwordx4 %1, off\n\ts_mov_b32 m0, %0"
               : "=&s"(sv) : "v"(g), "s"(lds_base) : "memory");
}
DI void glds16s(const void* sbase, unsigned voff, unsigned lds_base) {
  unsigned sv;
  asm volatile("s_mov_b32 %0, m0\n\ts_mov_b32 m0, %3\n\ts_nop 0\n\tglobal_load_lds_dwordx4 %1, %2\n\ts_mov_b32 m0, %0"
               : "=&s"(sv) : "v"(voff), "s"(sbase), "s"(lds_base) : "memory");
}
DI void glds4(const void* g, unsigned lds_base) {
  unsigned sv;
  asm volatile("s_mov_b32 %0, m0\n\ts_mov_b32 m0, %2\n\ts_nop 0\n\tglobal_load_lds_dword %1, off\n\ts_mov_b32 m0, %0"
               : "=&s"(sv) : "v"(g), "s"(lds_base) : "memory");
}
template <int N> DI void waitv4(bf16x8& a, bf16x8& b, bf16x8& c, bf16x8& d) {
  asm volatile("s_waitcnt vmcnt(%4)" : "+v"(a), "+v"(b), "+v"(c), "+v"(d) : "i"(N) : "memory");
}
template <int OFF> DI void gload16(bf16x8& d, const void* p) {
  asm volatile("global_load_dwordx4 %0, %1, off offset:%2" : "=v"(d) : "v"(p), "i"(OFF) : "memory");
}
DI s16x4 trread(lds_cptr p) {
  return __builtin_bit_cast(s16x4, __builtin_amdgcn_ds_read_tr16_b64_v4i16((__attribute__((address_space(3))) v4i16_t*)p));
}
DI bf16x8 ldsr16(lds_cptr p) { return *(const __attribute__((address_space(3))) bf16x8*)p; }
DI bf16x8 cat8(s16x4 a, s16x4 b) { return __builtin_shufflevector(a, b, 0, 1, 2, 3, 4, 5, 6, 7); }
DI int xcd_remap(int bid, int nwg) {
  const int q = nwg >> 3, r = nwg & 7, x = bid & 7, o = bid >> 3;
  return (x < r ? x * (q + 1) : r * (q + 1) + (x - r) * q) + o;
}

constexpr int K1_WI = 448 * 128, K1_X = 2 * K1_WI, K1_XS = 528, K1_DUM = K1_X + 64 * K1_XS, K1_WO = K1_DUM + 1024, K1_LDS = K1_WO + 8192;
constexpr int KMAIN_KB = 28;
__global__ __launch_bounds__(512) void k_qkv(const u16* __restrict__ x, const u16* __restrict__ wq, const u16* __restrict__ wk,
                                             const u16* __restrict__ wv, const u16* __restrict__ wo, u16* __restrict__ Qf,
                                             u16* __restrict__ Kf, u16* __restrict__ Vf, u16* __restrict__ Wof, unsigned* __restrict__ cnt) {
  __shared__ __attribute__((aligned(16))) char lds[K1_LDS];
  const int tid = threadIdx.x, lane = tid & 63, w = __builtin_amdgcn_readfirstlane(tid >> 6);
  const int t = xcd_remap(blockIdx.x, 256), rt = t >> 2, ct = t & 3;
  const unsigned lds0 = (unsigned)(uintptr_t)lds;
  STAMP(0, 0);
  bf16x8 xr[4];
  const bool act = lane < 56;
  {
    const u16* xp_ = x + (size_t)(rt * 32 + w * 4) * D + (act ? lane : 55) * 8;
    gload16<0>(xr[0], xp_); gload16<896>(xr[1], xp_); gload16<1792>(xr[2], xp_); gload16<2688>(xr[3], xp_);
  }
#pragma unroll
  for (int im = 0; im < 2; ++im) {
    const int c64 = 2 * ct + im;
    if (c64 < 7) {
#pragma unroll
      for (int i = 0; i < 7; ++i) {
        const int piece = w * 7 + i, row = piece * 8 + (lane >> 3);
        const int f = ((row >> 1) & 1) | (((row >> 3) & 1) << 1);
        const int c = (lane & 7) ^ (f << 1);
        glds16(wq + (size_t)row * D + c64 * 64 + c * 8, lds0 + im * K1_WI + piece * 1024);
      }
    } else {
#pragma unroll
      for (int i = 0; i < 7; ++i) {
        const int pc = w * 7 + i < 50 ? w * 7 + i : 49, mat = pc >= 25, piece = pc - 25 * mat;
        const int e = piece * 512 + lane * 8;
        glds16((mat ? wv : wk) + (e < 448 * 28 ? e : 448 * 28 - 8), lds0 + im * K1_WI + mat * 25600 + piece * 1024);
      }
    }
  }
  const int frag0 = blockIdx.x + 256 * (w & 1);
  const bool dofrag = w < 2 && frag0 < 392;
  const int frag = dofrag ? frag0 : 0;
  {
    const int fnb = frag / 14, fks = frag - fnb * 14;
    glds16(wo + (size_t)(32 * fks + (lane >> 1)) * D + 16 * fnb + (lane & 1) * 8, lds0 + K1_WO + w * 1024);
  }
  SBAR();
  waitv4<15>(xr[0], xr[1], xr[2], xr[3]);
  {
    u32x4 xv[4];
#pragma unroll
    for (int r = 0; r < 4; ++r) xv[r] = __builtin_bit_cast(u32x4, xr[r]);
    float v[4][8], mean[4], rs[4];
#pragma unroll
    for (int r = 0; r < 4; ++r) {
      float s = 0.f;
#pragma unroll
      for (int i = 0; i < 4; ++i) {
        v[r][2 * i] = act ? bflo(xv[r][i]) : 0.f; v[r][2 * i + 1] = act ? bfhi(xv[r][i]) : 0.f;
        s += v[r][2 * i] + v[r][2 * i + 1];
      }
      mean[r] = s;
    }
#pragma unroll
    for (int r = 0; r < 4; ++r) mean[r] = wave_sum(mean[r]) * (1.f / D);
#pragma unroll
    for (int r = 0; r < 4; ++r) {
      float q = 0.f;
#pragma unroll
      for (int i = 0; i < 8; ++i) { v[r][i] = act ? v[r][i] - mean[r] : 0.f; q += v[r][i] * v[r][i]; }
      rs[r] = q;
    }
#pragma unroll
    for (int r = 0; r < 4; ++r) rs[r] = __builtin_amdgcn_rsqf(wave_sum(rs[r]) * (1.f / D) + LN_EPS);
    char* xdst = lds + K1_X + lane * K1_XS + w * 64;
#pragma unroll
    for (int r = 0; r < 4; ++r) {
      u32x4 o;
#pragma unroll
      for (int i = 0; i < 4; ++i) o[i] = cvtpk(v[r][2 * i] * rs[r], v[r][2 * i + 1] * rs[r]);
      *(u32x4*)(xdst + r * 16) = o;
    }
  }
  STAMP(0, 1);
  WAITV_BAR(0);
  STAMP(0, 2);
  if (w == 7 && (blockIdx.x >> 3) < KMAIN_KB) {
    unsigned long long pc;
    int off;
    asm volatile("s_getpc_b64 %0\n1:\n\ts_mov_b32 %1, k_main-1b" : "=s"(pc), "=s"(off));
    glds16s((const char*)(pc + (long long)off) + (blockIdx.x >> 3) * 1024, lane * 16, lds0 + K1_DUM);
  }
  if (dofrag) {
    const lds_cptr fp = (lds_cptr)lds + K1_WO + w * 1024 + (8 * (lane >> 4) + ((lane & 15) >> 2)) * 32 + 8 * (lane & 3);
    *(bf16x8*)(Wof + ((size_t)frag * 64 + lane) * 8) = cat8(trread(fp), trread(fp + 128));
  }
  const int nb = w & 3, im = w >> 2, c64 = 2 * ct + im, g = lane >> 4, q4 = (lane & 15) >> 2, p = lane & 3, l15 = lane & 15;
  const int f = ((q4 >> 1) & 1) | ((g & 1) << 1);
  const bool kv = c64 == 7;
  const int rs_ = kv ? 56 : 128;
  const lds_cptr wp = (lds_cptr)lds + im * K1_WI + (8 * g + q4) * rs_ +
                      (kv ? (nb >> 1) * 25600 + (nb & 1) * 32 + 8 * p : (((2 * nb + (p >> 1)) ^ (f << 1)) * 16) + 8 * (p & 1));
  const lds_cptr xp = (lds_cptr)lds + K1_X + g * K1_XS + l15 * 16;
  f32x4 acc0 = {0.f, 0.f, 0.f, 0.f}, acc1 = {0.f, 0.f, 0.f, 0.f};
  {
    bf16x8 a[14], b0[14], b1[14];
#pragma unroll
    for (int ks = 0; ks < 14; ++ks) {
      a[ks] = cat8(trread(wp + ks * 32 * rs_), trread(wp + ks * 32 * rs_ + 4 * rs_));
      b0[ks] = ldsr16(xp + ks * 4 * K1_XS);
      b1[ks] = ldsr16(xp + ks * 4 * K1_XS + 256);
    }
    SBAR();
#pragma unroll
    for (int ks = 0; ks < 14; ++ks) {
      acc0 = MFMA16(a[ks], b0[ks], acc0);
      acc1 = MFMA16(a[ks], b1[ks], acc1);
    }
  }
  STAMP(0, 3);
#pragma unroll
  for (int tb = 0; tb < 2; ++tb) {
    const f32x4 a = tb ? acc1 : acc0;
    const int s = rt * 32 + 16 * tb + l15;
    const int nl = 16 * nb + 4 * g;
    if (c64 < 7) {
      const int n0 = 64 * c64 + nl, h = n0 / 28, d0 = n0 - 28 * h;
      const int tg = s >> 3, c = (h & 3) * 8 + (s & 7), b = h >> 2;
      const size_t off = ((((size_t)(tg * 4 + b) * 2 + (d0 >> 4)) * 64) + ((d0 >> 3) & 1) * 32 + c) * 8 + (d0 & 7);
      *(u32x2*)(Qf + off) = (u32x2){cvtpk(a[0] * QSCALE, a[1] * QSCALE), cvtpk(a[2] * QSCALE, a[3] * QSCALE)};
    } else {
      const int kt = s >> 5, kk = s & 31;
      const int d0 = 16 * (nb & 1) + 4 * g;
      if (nb < 2) {
        const size_t off = ((((size_t)kt * 2 + (d0 >> 4)) * 64) + ((d0 >> 3) & 1) * 32 + kk) * 8 + (d0 & 7);
        u32x2 v = {cvtpk(a[0], a[1]), cvtpk(a[2], a[3])};
        if (d0 == 28) v = (u32x2){0x3f803f80u, 0x00003f80u};
        *(u32x2*)(Kf + off) = v;
      } else {
        const int sp = kk >> 4, j = ((kk >> 3) & 1) * 4 + (kk & 3), hi = (kk >> 2) & 1;
        const size_t off = ((((size_t)kt * 2 + sp) * 64) + hi * 32 + d0) * 8 + j;
        unsigned v0 = cvtpk(a[0], a[1]), v1 = cvtpk(a[2], a[3]);
        if (d0 == 28) { v0 = 0x00003f80u; v1 = 0u; }
        Vf[off] = (u16)v0; Vf[off + 8] = (u16)(v0 >> 16); Vf[off + 16] = (u16)v1; Vf[off + 24] = (u16)(v1 >> 16);
      }
    }
  }
  if (blockIdx.x == 0) { cnt[tid] = 0u; cnt[512 + tid] = 0u; }
  if (w == 7) asm volatile("s_waitcnt vmcnt(0)" ::: "memory");
  STAMP(0, 4);
}

constexpr int K2_RING = 0, K2_OT = 131072, K2_DUM = K2_OT + 7168, K2_X = K2_DUM + 4096, K2_LDS = K2_X + 16384;
constexpr int K2_HB = 65536, HBS = 452, K2_HS = K2_X;

DI float set_ref(bf16x8& q1, float m, int hi) {
  const float a = bflo(cvtpk(-m, 0.f)), r1 = -m - a, b = bflo(cvtpk(r1, 0.f)), c = bflo(cvtpk(r1 - b, 0.f));
  u32x4 v = __builtin_bit_cast(u32x4, q1);
  v[2] = hi ? cvtpk(a, b) : v[2];
  v[3] = hi ? cvtpk(c, 0.f) : v[3];
  q1 = __builtin_bit_cast(bf16x8, v);
  return -((a + b) + c);
}
DI float tile_max(const f32x16& s) {
  float a = __builtin_fmaxf(__builtin_fmaxf(s[0], s[1]), s[2]), b = __builtin_fmaxf(__builtin_fmaxf(s[3], s[4]), s[5]);
  a = __builtin_fmaxf(__builtin_fmaxf(a, s[6]), s[7]); b = __builtin_fmaxf(__builtin_fmaxf(b, s[8]), s[9]);
  a = __builtin_fmaxf(__builtin_fmaxf(a, s[10]), s[11]); b = __builtin_fmaxf(__builtin_fmaxf(b, s[12]), s[13]);
  a = __builtin_fmaxf(__builtin_fmaxf(a, s[14]), s[15]);
  return half_max(__builtin_fmaxf(a, b));
}
template <int N> DI void waitv() { asm volatile("s_waitcnt vmcnt(%0)" ::"i"(N) : "memory"); }
#define RBAR() asm volatile("s_barrier" ::: "memory")

DI void attn_safe(const u16* __restrict__ Kf, const u16* __restrict__ Vf, bf16x8 q0, bf16x8 q1, f32x16& o, int lane, int hi) {
  const f32x16 zero = {};
  float mref = 0.f;
  o = zero;
#pragma unroll 1
  for (int t = 0; t < 64; ++t) {
    const bf16x8 k0 = *(const bf16x8*)(Kf + t * 1024 + lane * 8), k1 = *(const bf16x8*)(Kf + t * 1024 + 512 + lane * 8);
    const bf16x8 v0 = *(const bf16x8*)(Vf + t * 1024 + lane * 8), v1 = *(const bf16x8*)(Vf + t * 1024 + 512 + lane * 8);
    (void)set_ref(q1, 0.f, hi);
    f32x16 s = MFMA32(k0, q0, zero);
    s = MFMA32(k1, q1, s);
    const float tm = tile_max(s);
    const float mn = set_ref(q1, t == 0 ? tm : __builtin_fmaxf(tm, mref), hi);
    const float al = t == 0 ? 0.f : __builtin_amdgcn_exp2f(mref - mn);
#pragma unroll
    for (int r = 0; r < 16; ++r) o[r] *= al;
    mref = mn;
    s = MFMA32(k0, q0, zero);
    s = MFMA32(k1, q1, s);
    u32x4 p0, p1;
#pragma unroll
    for (int i = 0; i < 4; ++i) {
      p0[i] = cvtpk(__builtin_amdgcn_exp2f(s[2 * i]), __builtin_amdgcn_exp2f(s[2 * i + 1]));
      p1[i] = cvtpk(__builtin_amdgcn_exp2f(s[8 + 2 * i]), __builtin_amdgcn_exp2f(s[8 + 2 * i + 1]));
    }
    o = MFMA32(v0, __builtin_bit_cast(bf16x8, p0), o);
    o = MFMA32(v1, __builtin_bit_cast(bf16x8, p1), o);
  }
}

#define EX4(S, i) do { S[i] = __builtin_amdgcn_exp2f(S[i]); S[i + 1] = __builtin_amdgcn_exp2f(S[i + 1]); \
    S[i + 2] = __builtin_amdgcn_exp2f(S[i + 2]); S[i + 3] = __builtin_amdgcn_exp2f(S[i + 3]); } while (0)
#define PK8(P, S, i) do { P[0] = cvtpk(S[i], S[i + 1]); P[1] = cvtpk(S[i + 2], S[i + 3]); P[2] = cvtpk(S[i + 4], S[i + 5]); P[3] = cvtpk(S[i + 6], S[i + 7]); } while (0)
#define BF(P) __builtin_bit_cast(bf16x8, P)
constexpr int CPRE = 0;
#define WOPF(j) do { if ((j) < CPRE) wf[(j) / 7][(j) % 7] = WOFR(j); } while (0)
#define RDS(u, off) ldsr16(((u) < 4 ? B0 : B1) + ((u) & 3) * 8192 + (off))
#define CTILE(u, NX, PF) do { \
      \
    sB = MFMA32(k0, qB0, zero); SBAR(); if (NX) k0 = RDS((u) + 1, 0); EX4(sA, 0); WOPF(PF); SBAR(); \
    sB = MFMA32(k1, qB1, sB); SBAR(); if (NX) k1 = RDS((u) + 1, 1024); EX4(sA, 4); PK8(p0, sA, 0); SBAR(); \
    oB = MFMA32(v1, BF(p1), oB); SBAR(); v1 = RDS(u, 3072); EX4(sA, 8); WOPF((PF) + 1); SBAR(); \
    oA = MFMA32(v0, BF(p0), oA); SBAR(); EX4(sA, 12); PK8(p1, sA, 8); SBAR(); \
      \
    sA = MFMA32(k0, qA0, zero); SBAR(); EX4(sB, 0); WOPF((PF) + 2); SBAR(); \
    sA = MFMA32(k1, qA1, sA); SBAR(); EX4(sB, 4); PK8(p0, sB, 0); SBAR(); \
    oA = MFMA32(v1, BF(p1), oA); SBAR(); EX4(sB, 8); WOPF((PF) + 3); SBAR(); \
    oB = MFMA32(v0, BF(p0), oB); SBAR(); if (NX) v0 = RDS((u) + 1, 2048); EX4(sB, 12); PK8(p1, sB, 8); SBAR(); } while (0)
#define CSTAGE(LAST, PF) do { \
    CTILE(0, true, PF); CTILE(1, true, (PF) + 4); CTILE(2, true, (PF) + 8); \
    if (!(LAST)) RBAR(); \
    CTILE(3, !(LAST), (PF) + 12); } while (0)

DI void st16c(void* p, u32x4 v) {
  asm volatile("global_store_dwordx4 %0, %1, off sc1\n\ts_nop 1" ::"v"(p), "v"(v) : "memory");
}
DI void attn_phase(char* lds, const u16* __restrict__ x, const u16* __restrict__ Qf, const u16* __restrict__ Kf,
                   const u16* __restrict__ Vf, const u16* __restrict__ Wof, const u16* __restrict__ w1,
                   const u16* __restrict__ w2, u16* H, u16* HN) {
  const int tid = threadIdx.x, lane = tid & 63, w = __builtin_amdgcn_readfirstlane(tid >> 6), hi = lane >> 5;
  const int tg = blockIdx.x;
  STAMP(1, 0);
  const int kh = w >> 2, ng = w & 3;
  const char* wob = (const char*)(Wof + (size_t)(7 * ng * 14 + 7 * kh) * 512);
  const unsigned wl = lane * 16;
  bf16x8 wf[7][7];
#define WOFR(i_) (*(const bf16x8*)(wob + (((i_) / 7) * 14 + (i_) % 7) * 1024 + wl))
#define LDWO(i0, i1) do { _Pragma("unroll") for (int i_ = i0; i_ < i1; ++i_) wf[i_ / 7][i_ % 7] = WOFR(i_); } while (0)
#define BOF(k) (*(const bf16x8*)(lds + K2_OT + ((4 * (k) + (lane >> 4)) * 8 + (lane & 7)) * 16))
#define HBST(acc) do { if ((lane & 15) < 8) { \
      _Pragma("unroll") for (int n = 0; n < 7; ++n) *(f32x4*)(lds + K2_HB + ((kh * 8 + (lane & 15)) * HBS + 16 * (7 * ng + n) + 4 * (lane >> 4)) * 4) = acc[n]; \
    } } while (0)
  u32x4 xv;
  if (w < 4) {
    const f32x16 zero = {};
    const int qh = w & 1, kp = w >> 1;
    const lds_cptr rp0 = (lds_cptr)lds + K2_RING + lane * 16;
    const lds_cptr rp = rp0 + kp * 4096;
    lds_cptr B0 = rp, B1 = rp + 32768;
    const u16* qp = Qf + ((size_t)((tg * 4 + 2 * qh) * 2) * 64 + lane) * 8;
    bf16x8 qA0 = *(const bf16x8*)(qp), qA1 = *(const bf16x8*)(qp + 512), qB0 = *(const bf16x8*)(qp + 1024), qB1 = *(const bf16x8*)(qp + 1536);
    RBAR();
    bf16x8 k0 = ldsr16(rp0), k1 = ldsr16(rp0 + 1024), v0 = RDS(0, 2048), v1 = v0;
    (void)set_ref(qA1, 0.f, hi);
    (void)set_ref(qB1, 0.f, hi);
    f32x16 sA = MFMA32(k0, qA0, zero), sB = MFMA32(k0, qB0, zero);
    sA = MFMA32(k1, qA1, sA);
    sB = MFMA32(k1, qB1, sB);
    k0 = RDS(0, 0); k1 = RDS(0, 1024);
    (void)set_ref(qA1, tile_max(sA), hi);
    (void)set_ref(qB1, tile_max(sB), hi);
    sA = MFMA32(k0, qA0, zero);
    sA = MFMA32(k1, qA1, sA);
    f32x16 oA = zero, oB = zero;
    u32x4 p0, p1 = {0u, 0u, 0u, 0u};
    SBAR();
    STAMP(1, 8);
#pragma unroll 1
    for (int st = 0; st < 4; ++st) {
      CSTAGE(false, 64);
      B0 = B1; B1 = rp + ((st + 2) & 3) * 32768;
    }
    CSTAGE(false, 0);
    B0 = B1; B1 = rp + 2 * 32768;
    CSTAGE(false, 16);
    B0 = B1; B1 = rp + 3 * 32768;
    CSTAGE(false, 64);
    B0 = B1;
    CSTAGE(true, 64);
    oB = MFMA32(v1, BF(p1), oB);
    STAMP(1, 10);
    {
      const f32x16 snd = kp ? oA : oB;
#pragma unroll
      for (int r4 = 0; r4 < 4; ++r4)
        *(f32x4*)(lds + K2_X + ((w * 4 + r4) * 64 + lane) * 16) = (f32x4){snd[4 * r4], snd[4 * r4 + 1], snd[4 * r4 + 2], snd[4 * r4 + 3]};
    }
    f32x16 o0 = kp ? oB : oA;
    bf16x8 qk0 = kp ? qB0 : qA0, qk1 = kp ? qB1 : qA1;
    asm volatile("" : "+v"(o0), "+v"(qk0), "+v"(qk1));
    SBAR();
    LBAR();
#pragma unroll
    for (int r4 = 0; r4 < 4; ++r4) {
      const f32x4 t = *(const f32x4*)(lds + K2_X + (((w ^ 2) * 4 + r4) * 64 + lane) * 16);
      o0[4 * r4] += t[0]; o0[4 * r4 + 1] += t[1]; o0[4 * r4 + 2] += t[2]; o0[4 * r4 + 3] += t[3];
    }
    const int qb = 2 * qh + kp;
    {
      float chk = 0.f;
#pragma unroll
      for (int r = 0; r < 16; ++r) chk = __builtin_fmaf(o0[r], 0.f, chk);
      const bool bad = !(chk == 0.f) || !(o0[12] < 1.8e19f);
      if (__any(bad)) {
        attn_safe(Kf, Vf, qk0, qk1, o0, lane, hi);
      }
    }
    {
      auto sw = __builtin_amdgcn_permlane32_swap(__float_as_uint(o0[12]), __float_as_uint(o0[12]), false, false);
      const float il = 1.f / __uint_as_float(sw[1]);
      const int c = lane & 31;
#pragma unroll
      for (int r4 = 0; r4 < 4; ++r4) {
        const int d = 8 * r4 + 4 * hi, k = 28 * (4 * qb + (c >> 3)) + d;
        if (d < 28)
          *(u32x2*)(lds + K2_OT + ((k >> 3) * 8 + (c & 7)) * 16 + (k & 7) * 2) =
              (u32x2){cvtpk(o0[4 * r4] * il, o0[4 * r4 + 1] * il), cvtpk(o0[4 * r4 + 2] * il, o0[4 * r4 + 3] * il)};
      }
    }
    STAMP(1, 1);
    LBAR();
    STAMP(1, 2);
    LDWO(16, 39);
    xv = *(const u32x4*)(x + (size_t)(tg * 8 + w) * D + (lane < 56 ? lane : 55) * 8);
    SBAR();
    {
      bf16x8 bo[7];
#pragma unroll
      for (int k = 0; k < 7; ++k) bo[k] = BOF(k);
#pragma unroll
      for (int i = 0; i < 16; ++i) wf[i / 7][i % 7] = *(const bf16x8*)(lds + K2_RING + (i >> 3) * 32768 + w * 8192 + (i & 7) * 1024 + lane * 16);
      f32x4 acc[7];
#pragma unroll
      for (int n = 0; n < 7; ++n) acc[n] = (f32x4){0.f, 0.f, 0.f, 0.f};
#pragma unroll
      for (int i = 0; i < 39; ++i) acc[i / 7] = MFMA16(wf[i / 7][i % 7], bo[i % 7], acc[i / 7]);
      HBST(acc);
    }
  } else {
    const int hw = w - 4;
    __builtin_amdgcn_s_setprio(3);
    const unsigned ring = (unsigned)(uintptr_t)lds + K2_RING + hw * 8192;
    const u16* kp = Kf + (size_t)hw * 2048;
    const u16* vp = Vf + (size_t)hw * 2048;
    const unsigned l16 = lane * 16;
#define DMAS(s_) do { const unsigned sl_ = ring + ((s_) & 3) * 32768; \
    _Pragma("unroll") for (int u_ = 0; u_ < 2; ++u_) { \
      glds16s(kp + (s_) * 8192 + u_ * 1024, l16, sl_ + u_ * 4096); glds16s(kp + (s_) * 8192 + u_ * 1024 + 512, l16, sl_ + u_ * 4096 + 1024); \
      glds16s(vp + (s_) * 8192 + u_ * 1024, l16, sl_ + u_ * 4096 + 2048); glds16s(vp + (s_) * 8192 + u_ * 1024 + 512, l16, sl_ + u_ * 4096 + 3072); } } while (0)
    const unsigned dum = (unsigned)(uintptr_t)lds + K2_DUM + hw * 1024;
    const int xg = tg & 7, rk = tg >> 3;
    const int ts = xg < 4 ? 25 * xg : 100 + 24 * (xg - 4), te = ts + (xg < 4 ? 25 : 24);
    const int n0 = ts / 7, n1 = (te - 1) / 7;
    const int nb0 = n0 + 3 <= 27 ? n0 : 24;
    const u16* p1 = w1 + (size_t)(64 * rk + 16 * hw) * F + nb0 * 64;
    const u16* p1b = w1 + (size_t)(64 * rk + 16 * hw) * F + n1 * 64;
    const u16* p2 = w2 + (size_t)(56 * rk + 14 * hw) * S + 4 * xg * 64;
    const unsigned o1 = ((lane >> 5) * F + (lane & 31) * 8) * 2, o1b = ((lane >> 3) * F + (lane & 7) * 8) * 2, o2 = ((lane >> 5) * S + (lane & 31) * 8) * 2;
#define PFD(i0, i1) do { _Pragma("unroll") for (int i_ = i0; i_ < i1; ++i_) { \
      if (i_ < 8) glds16s(p1 + (size_t)i_ * 2 * F, o1, dum); \
      else if (i_ < 10) glds16s(p1b + (size_t)(i_ - 8) * 8 * F, o1b, dum); \
      else glds16s(p2 + (size_t)(i_ - 10) * 2 * S, o2, dum); } } while (0)
    DMAS(0); DMAS(1); DMAS(2);
    waitv<16>(); RBAR();
    LDWO(0, 12); SBAR(); PFD(0, 3);
    waitv<23>(); RBAR();
    DMAS(3); LDWO(12, 24); SBAR(); PFD(3, 6);
    waitv<38>(); RBAR();
    DMAS(4); LDWO(24, 36); SBAR(); PFD(6, 9);
    waitv<38>(); RBAR();
    DMAS(5); LDWO(36, 49); SBAR(); PFD(9, 12);
    waitv<39>(); RBAR();
    bf16x8 wx[10];
    DMAS(6);
#pragma unroll
    for (int i = 0; i < 10; ++i) wx[i] = *(const bf16x8*)(wob - 7 * 1024 + (((39 + i) / 7) * 14 + (39 + i) % 7) * 1024 + wl);
    SBAR(); PFD(12, 15);
    waitv<37>(); RBAR();
    DMAS(7); PFD(15, 17);
    waitv<23>(); RBAR();
#pragma unroll
    for (int i = 0; i < 8; ++i) glds16s(wob - 7 * 1024 + ((i / 7) * 14 + i % 7) * 1024, wl, ring + i * 1024);
    waitv<10>(); RBAR();
#pragma unroll
    for (int i = 8; i < 16; ++i) glds16s(wob - 7 * 1024 + ((i / 7) * 14 + i % 7) * 1024, wl, ring + 32768 + (i - 8) * 1024);
    xv = *(const u32x4*)(x + (size_t)(tg * 8 + w) * D + (lane < 56 ? lane : 55) * 8);
    SBAR();
    __builtin_amdgcn_s_setprio(0);
    RBAR();
    waitv<1>();
    STAMP(1, 1);
    LBAR();
    STAMP(1, 2);
    {
      f32x4 acc[7];
      {
        bf16x8 bo[7];
#pragma unroll
        for (int k = 0; k < 7; ++k) bo[k] = BOF(k);
        acc[5] = (f32x4){0.f, 0.f, 0.f, 0.f}; acc[6] = acc[5];
#pragma unroll
        for (int i = 0; i < 10; ++i) acc[(39 + i) / 7] = MFMA16(wx[i], bo[(39 + i) % 7], acc[(39 + i) / 7]);
      }
      SBAR();
      bf16x8 bo[7];
#pragma unroll
      for (int k = 0; k < 7; ++k) bo[k] = BOF(7 + k);
#pragma unroll
      for (int n = 0; n < 7; ++n) {
        if (n < 5) acc[n] = (f32x4){0.f, 0.f, 0.f, 0.f};
#pragma unroll
        for (int k = 0; k < 7; ++k) acc[n] = MFMA16(wf[n][k], bo[k], acc[n]);
      }
      HBST(acc);
    }
  }
  LBAR();
  STAMP(1, 3);
  {
    const int s = tg * 8 + w;
    const bool act = lane < 56;
    const int lc = act ? lane : 55;
    float v[8];
    const f32x4 a0 = *(const f32x4*)(lds + K2_HB + (w * HBS + lc * 8) * 4), a1 = *(const f32x4*)(lds + K2_HB + (w * HBS + lc * 8 + 4) * 4);
    const f32x4 c0 = *(const f32x4*)(lds + K2_HB + ((8 + w) * HBS + lc * 8) * 4), c1 = *(const f32x4*)(lds + K2_HB + ((8 + w) * HBS + lc * 8 + 4) * 4);
    u32x4 hv;
#pragma unroll
    for (int i = 0; i < 2; ++i) {
      hv[i] = cvtpk(bflo(xv[i]) + a0[2 * i] + c0[2 * i], bfhi(xv[i]) + a0[2 * i + 1] + c0[2 * i + 1]);
      hv[2 + i] = cvtpk(bflo(xv[2 + i]) + a1[2 * i] + c1[2 * i], bfhi(xv[2 + i]) + a1[2 * i + 1] + c1[2 * i + 1]);
    }
    if (act) st16c(H + (size_t)s * D + lane * 8, hv);
#pragma unroll
    for (int i = 0; i < 4; ++i) { v[2 * i] = act ? bflo(hv[i]) : 0.f; v[2 * i + 1] = act ? bfhi(hv[i]) : 0.f; }
#pragma unroll
    for (int pass = 0; pass < 2; ++pass) {
      float sm = 0.f;
#pragma unroll
      for (int i = 0; i < 8; ++i) sm += v[i];
      const float mean = wave_sum(sm) * (1.f / D);
      float q = 0.f;
#pragma unroll
      for (int i = 0; i < 8; ++i) { v[i] = act ? v[i] - mean : 0.f; q += v[i] * v[i]; }
      const float rs = __builtin_amdgcn_rsqf(wave_sum(q) * (1.f / D) + LN_EPS);
#pragma unroll
      for (int i = 0; i < 8; ++i) v[i] *= rs;
    }
    if (act) *(u32x4*)(lds + K2_HS + w * 912 + lane * 16) = (u32x4){cvtpk(v[0], v[1]), cvtpk(v[2], v[3]), cvtpk(v[4], v[5]), cvtpk(v[6], v[7])};
  }
  LBAR();
  if (tid < D / 2) {
    unsigned r[8];
#pragma unroll
    for (int i = 0; i < 8; ++i) r[i] = *(const unsigned*)(lds + K2_HS + i * 912 + tid * 4);
    u32x4 lo, hi2;
#pragma unroll
    for (int i = 0; i < 4; ++i) {
      lo[i] = (r[2 * i] & 0xffffu) | (r[2 * i + 1] << 16);
      hi2[i] = (r[2 * i] >> 16) | (r[2 * i + 1] & 0xffff0000u);
    }
    u32x4* dst = (u32x4*)(HN + ((size_t)tg * D + 2 * tid) * 8);
    st16c(dst, lo); st16c(dst + 1, hi2);
  }
  STAMP(1, 4);
}

constexpr int G_RING = 4, G_LDS = 8 * G_RING * 4096;
typedef __attribute__((address_space(1))) unsigned gu32;
typedef __attribute__((address_space(1))) unsigned long long gu64;
#define RLX_AGENT __ATOMIC_RELAXED, __HIP_MEMORY_SCOPE_AGENT
template <int OFF> DI void gload16c(bf16x8& d, const void* p) {
  asm volatile("global_load_dwordx4 %0, %1, off offset:%2 sc1" : "=v"(d) : "v"(p), "i"(OFF) : "memory");
}
template <int PH, int NSH>
DI void mlp_tile(char* lds, int t, const u16* act, const u16* __restrict__ W, const u16* __restrict__ bias, const u16* H, u16* out,
                 unsigned* cnt, unsigned want) {
  constexpr int KT = PH == 0 ? S : F, LDW = PH == 0 ? F : S, NK = KT / 256, NR = NK - G_RING, LA = 5;
  const int tid = threadIdx.x, lane = tid & 63, w = __builtin_amdgcn_readfirstlane(tid >> 6);
  const int nt = t / 7, mt = t - nt * 7;
  const unsigned lds0 = (unsigned)(uintptr_t)lds + w * (G_RING * 4096);
  const int g = lane >> 4, q4 = (lane & 15) >> 2, p = lane & 3, l15 = lane & 15;
  STAMP(2 + PH, 0);
  const int enb = w >> 1, emb = 2 * (w & 1);
  u32x2 eb;
  if (PH == 0) eb = *(const u32x2*)(bias + 64 * nt + 16 * enb + 4 * g);
  else eb[0] = bias[64 * nt + 16 * enb + l15];
  SBAR();
  const u16* asrc = act + ((size_t)(4 * w + g) * D + 64 * mt + l15) * 8;
  const int r8 = lane >> 3, fe = (r8 >> 1) & 1;
  const u16* wsrc0 = W + (size_t)(32 * w + r8) * LDW + 64 * nt + ((lane & 7) ^ (fe << 1)) * 8;
  const u16* wsrc1 = W + (size_t)(32 * w + 8 + r8) * LDW + 64 * nt + ((lane & 7) ^ ((fe | 2) << 1)) * 8;
  const int f = ((q4 >> 1) & 1) | ((g & 1) << 1);
  int woff[4];
#pragma unroll
  for (int nb = 0; nb < 4; ++nb) woff[nb] = w * (G_RING * 4096) + (8 * g + q4) * 128 + (((2 * nb + (p >> 1)) ^ (f << 1)) * 16) + 8 * (p & 1);
  bf16x8 af[NK][4];
  f32x4 acc[4][4] = {};
#define STAGE_W(i) do { const unsigned sl_ = lds0 + ((i) % G_RING) * 4096; const size_t o_ = (size_t)(i) * 256 * LDW; \
    glds16(wsrc0 + o_, sl_); glds16(wsrc1 + o_, sl_ + 1024); glds16(wsrc0 + o_ + 16 * LDW, sl_ + 2048); glds16(wsrc1 + o_ + 16 * LDW, sl_ + 3072); } while (0)
#define LOAD_A(i) do { const u16* ap_ = asrc + (size_t)(i) * 32 * D * 8; \
    gload16c<0>(af[i][0], ap_); gload16c<256>(af[i][1], ap_); gload16c<512>(af[i][2], ap_); gload16c<768>(af[i][3], ap_); } while (0)
#define RDW(WF, i) do { _Pragma("unroll") for (int nb_ = 0; nb_ < 4; ++nb_) { const lds_cptr sp_ = (lds_cptr)lds + woff[nb_] + ((i) % G_RING) * 4096; \
      WF[nb_] = cat8(trread(sp_), trread(sp_ + 512)); } } while (0)
#define MMA(WF, i) do { _Pragma("unroll") for (int nb_ = 0; nb_ < 4; ++nb_) _Pragma("unroll") for (int mb_ = 0; mb_ < 4; ++mb_) \
      acc[nb_][mb_] = PH == 0 ? MFMA16(WF[nb_], af[i][mb_], acc[nb_][mb_]) : MFMA16(af[i][mb_], WF[nb_], acc[nb_][mb_]); } while (0)
  bf16x8 wr[NR][4];
#pragma unroll
  for (int j = 0; j < NR; ++j) STAGE_W(G_RING + j);
  if (NR < G_RING) STAGE_W(G_RING - 1);
#pragma unroll
  for (int j = 0; j < NR; ++j) {
    waitv<12>();
    RDW(wr[j], G_RING + j);
    asm volatile("s_waitcnt lgkmcnt(0)" ::: "memory");
    STAGE_W(j);
  }
  STAMP(2 + PH, 1);
  if (w == 0) {
    gu32* cp = (gu32*)(cnt + (lane < NSH ? lane : 0) * 64);
    for (unsigned spins = 0; spins < (1u << 20); ++spins) {
      if (__all(__hip_atomic_load(cp, RLX_AGENT) >= want)) break;
      __builtin_amdgcn_s_sleep(1);
    }
  }
  asm volatile("s_barrier" ::: "memory");
  STAMP(2 + PH, 2);
  unsigned long long eh0 = 0, eh1 = 0;
  if (PH == 1) {
    const int s = 64 * nt + 16 * enb + l15;
    eh0 = __hip_atomic_load((gu64*)(H + (size_t)s * D + 64 * mt + 16 * emb + 4 * g), RLX_AGENT);
    eh1 = __hip_atomic_load((gu64*)(H + (size_t)s * D + 64 * mt + 16 * emb + 16 + 4 * g), RLX_AGENT);
  }
  SBAR();
#pragma unroll
  for (int j = 0; j < LA; ++j) LOAD_A((j + G_RING) % NK);
#pragma unroll
  for (int j = 0; j < NK; ++j) {
    const int i = (j + G_RING) % NK;
    const int rem = NK - 1 - j < LA - 1 ? NK - 1 - j : LA - 1;
    if (rem == 4) waitv4<16>(af[i][0], af[i][1], af[i][2], af[i][3]);
    else if (rem == 3) waitv4<12>(af[i][0], af[i][1], af[i][2], af[i][3]);
    else if (rem == 2) waitv4<8>(af[i][0], af[i][1], af[i][2], af[i][3]);
    else if (rem == 1) waitv4<4>(af[i][0], af[i][1], af[i][2], af[i][3]);
    else waitv4<0>(af[i][0], af[i][1], af[i][2], af[i][3]);
    if (j < NR) {
      MMA(wr[j], i);
    } else {
      bf16x8 wf[4];
      RDW(wf, i);
      MMA(wf, i);
    }
    SBAR();
    if (j + LA < NK) LOAD_A((j + LA + G_RING) % NK);
  }
#undef STAGE_W
#undef LOAD_A
#undef RDW
#undef MMA
  STAMP(2 + PH, 4);
  {
    f32x4* xb = (f32x4*)lds;
#pragma unroll
    for (int nb = 0; nb < 4; ++nb)
#pragma unroll
      for (int mb = 0; mb < 4; ++mb) xb[(w * 16 + nb * 4 + mb) * 64 + lane] = acc[nb][mb];
  }
  LBAR();
  {
    const f32x4* xb = (const f32x4*)lds;
#pragma unroll
    for (int e = 0; e < 2; ++e) {
      f32x4 a = xb[(2 * w + e) * 64 + lane];
#pragma unroll
      for (int v = 1; v < 8; ++v) a += xb[(v * 16 + 2 * w + e) * 64 + lane];
      const int mb = emb + e;
      if (PH == 0) {
        const int fi = 64 * nt + 16 * enb + 4 * g, d = 64 * mt + 16 * mb + l15;
        float z[4] = {a[0] + bflo(eb[0]), a[1] + bfhi(eb[0]), a[2] + bflo(eb[1]), a[3] + bfhi(eb[1])};
#pragma unroll
        for (int i = 0; i < 4; ++i) {
          const float zz = z[i], a2 = zz * (1.f + 0.044715f * zz * zz) * (-2.f * 0.7978845608028654f * 1.4426950408889634f);
          z[i] = zz * __builtin_amdgcn_rcpf(1.f + __builtin_amdgcn_exp2f(a2));
        }
        const unsigned long long v = (unsigned long long)cvtpk(z[0], z[1]) | ((unsigned long long)cvtpk(z[2], z[3]) << 32);
        __hip_atomic_store((gu64*)(out + ((size_t)(fi >> 3) * D + d) * 8 + (fi & 7)), v, RLX_AGENT);
      } else {
        const int d = 64 * mt + 16 * mb + 4 * g, s = 64 * nt + 16 * enb + l15;
        const float bb = bflo(eb[0]);
        const unsigned long long hv = e ? eh1 : eh0;
        const unsigned h0 = (unsigned)hv, h1 = (unsigned)(hv >> 32);
        *(u32x2*)(out + (size_t)s * D + d) =
            (u32x2){cvtpk(a[0] + bb + bflo(h0), a[1] + bb + bfhi(h0)), cvtpk(a[2] + bb + bflo(h1), a[3] + bb + bfhi(h1))};
      }
    }
  }
  STAMP(2 + PH, 5);
}
#define PUBLISH(c) do { asm volatile("s_waitcnt vmcnt(0) lgkmcnt(0)\n\ts_barrier" ::: "memory"); \
    if (threadIdx.x == 0) __hip_atomic_fetch_add((gu32*)(c), 1u, RLX_AGENT); } while (0)

extern "C" __global__ __launch_bounds__(512) void k_main(const u16* __restrict__ x, const u16* __restrict__ Qf, const u16* __restrict__ Kf,
                                              const u16* __restrict__ Vf, const u16* __restrict__ Wof, const u16* __restrict__ w1,
                                              const u16* __restrict__ b1, const u16* __restrict__ w2, const u16* __restrict__ b2,
                                              u16* H, u16* HN, u16* U, u16* out, unsigned* cnt) {
  __shared__ __attribute__((aligned(16))) char lds[K2_LDS];
  attn_phase(lds, x, Qf, Kf, Vf, Wof, w1, w2, H, HN);
  PUBLISH(cnt + (8 + (blockIdx.x & 7)) * 64);
  if (blockIdx.x < 196) {
    const int t = xcd_remap(blockIdx.x, 196);
    mlp_tile<0, 8>(lds, t, HN, w1, b1, H, U, cnt + 8 * 64, 32u);
    PUBLISH(cnt + (t % 7) * 64);
  }
  if (blockIdx.x < 224) {
    const int t2 = xcd_remap(blockIdx.x, 224);
    mlp_tile<1, 1>(lds, t2, U, w2, b2, H, out, cnt + (t2 % 7) * 64, 28u);
  }
}

extern "C" void kernel_launch(void* const* d_in, const int* in_sizes, int n_in, void* d_out, int out_size, void* d_ws, size_t ws_size,
                              hipStream_t stream) {
  const u16* x = (const u16*)d_in[0];
  const u16* wq = (const u16*)d_in[1];
  const u16* wk = (const u16*)d_in[2];
  const u16* wv = (const u16*)d_in[3];
  const u16* wo = (const u16*)d_in[4];
  const u16* w1 = (const u16*)d_in[6];
  const u16* b1 = (const u16*)d_in[7];
  const u16* w2 = (const u16*)d_in[8];
  const u16* b2 = (const u16*)d_in[9];
  char* ws = (char*)d_ws;
  u16 *Qf = (u16*)(ws + WS_QF), *Kf = (u16*)(ws + WS_KF), *Vf = (u16*)(ws + WS_VF), *Wof = (u16*)(ws + WS_WOF);
  u16 *H = (u16*)(ws + WS_H), *HN = (u16*)(ws + WS_HN), *U = (u16*)(ws + WS_U);
  unsigned* cnt = (unsigned*)(ws + WS_CNT);
  k_qkv<<<256, 512, 0, stream>>>(x, wq, wk, wv, wo, Qf, Kf, Vf, Wof, cnt);
  k_main<<<256, 512, 0, stream>>>(x, Qf, Kf, Vf, Wof, w1, b1, w2, b2, H, HN, U, (u16*)d_out, cnt);
}
```

```cpp
#include <hip/hip_runtime.h>
#include <hip/hip_bf16.h>

typedef unsigned short u16;
using bf16x8 = __attribute__((ext_vector_type(8))) short;
using s16x4 = __attribute__((ext_vector_type(4))) short;
using f32x4 = __attribute__((ext_vector_type(4))) float;
using f32x16 = __attribute__((ext_vector_type(16))) float;
using u32x4 = __attribute__((ext_vector_type(4))) unsigned;
using u32x2 = __attribute__((ext_vector_type(2))) unsigned;
typedef short v4i16_t __attribute__((ext_vector_type(4)));
typedef __attribute__((address_space(3))) const char* lds_cptr;
typedef __bf16 bf16x2_t __attribute__((ext_vector_type(2)));
typedef float f32x2_t __attribute__((ext_vector_type(2)));

#define DI __device__ __forceinline__
#define MFMA32(a, b, c) __builtin_amdgcn_mfma_f32_32x32x16_bf16(a, b, c, 0, 0, 0)
#define MFMA16(a, b, c) __builtin_amdgcn_mfma_f32_16x16x32_bf16(a, b, c, 0, 0, 0)
#define SBAR() __builtin_amdgcn_sched_barrier(0)
#define LBAR() asm volatile("s_waitcnt lgkmcnt(0)\n\ts_barrier" ::: "memory")
#define WAITV_BAR(N) asm volatile("s_waitcnt vmcnt(" #N ") lgkmcnt(0)\n\ts_barrier" ::: "memory")

constexpr int S = 2048, D = 448, DH = 28, F = 1792;
constexpr float QSCALE = 0.18898223650461362f * 1.4426950408889634f;
constexpr float LN_EPS = 1e-5f;

constexpr size_t WS_QF = 0;
constexpr size_t WS_KF = WS_QF + 2097152;
constexpr size_t WS_VF = WS_KF + 131072;
constexpr size_t WS_WOF = WS_VF + 131072;
constexpr size_t WS_H = WS_WOF + 401408;
constexpr size_t WS_HN = WS_H + 1835008;
constexpr size_t WS_U = WS_HN + 1835008;
constexpr size_t WS_CNT = WS_U + 1605632;


DI unsigned cvtpk(float lo, float hi) {
  const f32x2_t v = {lo, hi};
  return __builtin_bit_cast(unsigned, __builtin_convertvector(v, bf16x2_t));
}
DI float bflo(unsigned u) { return __uint_as_float(u << 16); }
DI float bfhi(unsigned u) { return __uint_as_float(u & 0xffff0000u); }
template <int CTRL> DI float dppf(float v) {
  return __uint_as_float(__builtin_amdgcn_update_dpp(0u, __float_as_uint(v), CTRL, 0xf, 0xf, true));
}
DI float wave_sum(float v) {
  v += dppf<0xB1>(v);
  v += dppf<0x4E>(v);
  v += dppf<0x141>(v);
  v += dppf<0x140>(v);
  auto a = __builtin_amdgcn_permlane16_swap(__float_as_uint(v), __float_as_uint(v), false, false);
  v = __uint_as_float(a[0]) + __uint_as_float(a[1]);
  auto b = __builtin_amdgcn_permlane32_swap(__float_as_uint(v), __float_as_uint(v), false, false);
  return __uint_as_float(b[0]) + __uint_as_float(b[1]);
}
DI float half_max(float v) {
  auto b = __builtin_amdgcn_permlane32_swap(__float_as_uint(v), __float_as_uint(v), false, false);
  return __builtin_fmaxf(__uint_as_float(b[0]), __uint_as_float(b[1]));
}
DI void glds16(const void* g, unsigned lds_base) {
  unsigned sv;
  asm volatile("s_mov_b32 %0, m0\n\ts_mov_b32 m0, %2\n\ts_nop 0\n\tglobal_load_lds_dwordx4 %1, off\n\ts_mov_b32 m0, %0"
               : "=&s"(sv) : "v"(g), "s"(lds_base) : "memory");
}
DI void glds16s(const void* sbase, unsigned voff, unsigned lds_base) {
  unsigned sv;
  asm volatile("s_mov_b32 %0, m0\n\ts_mov_b32 m0, %3\n\ts_nop 0\n\tglobal_load_lds_dwordx4 %1, %2\n\ts_mov_b32 m0, %0"
               : "=&s"(sv) : "v"(voff), "s"(sbase), "s"(lds_base) : "memory");
}
template <int N> DI void waitv4(bf16x8& a, bf16x8& b, bf16x8& c, bf16x8& d) {
  asm volatile("s_waitcnt vmcnt(%4)" : "+v"(a), "+v"(b), "+v"(c), "+v"(d) : "i"(N) : "memory");
}
template <int OFF> DI void gload16(bf16x8& d, const void* p) {
  asm volatile("global_load_dwordx4 %0, %1, off offset:%2" : "=v"(d) : "v"(p), "i"(OFF) : "memory");
}
DI s16x4 trread(lds_cptr p) {
  return __builtin_bit_cast(s16x4, __builtin_amdgcn_ds_read_tr16_b64_v4i16((__attribute__((address_space(3))) v4i16_t*)p));
}
DI bf16x8 ldsr16(lds_cptr p) { return *(const __attribute__((address_space(3))) bf16x8*)p; }
DI bf16x8 cat8(s16x4 a, s16x4 b) { return __builtin_shufflevector(a, b, 0, 1, 2, 3, 4, 5, 6, 7); }
DI int xcd_remap(int bid, int nwg) {
  const int q = nwg >> 3, r = nwg & 7, x = bid & 7, o = bid >> 3;
  return (x < r ? x * (q + 1) : r * (q + 1) + (x - r) * q) + o;
}

constexpr int K1_WI = 448 * 128, K1_X = 2 * K1_WI, K1_XS = 528, K1_DUM = K1_X + 64 * K1_XS, K1_WO = K1_DUM + 1024, K1_LDS = K1_WO + 8192;
__global__ __launch_bounds__(512) void k_qkv(const u16* __restrict__ x, const u16* __restrict__ wq, const u16* __restrict__ wk,
                                             const u16* __restrict__ wv, const u16* __restrict__ wo, u16* __restrict__ Qf,
                                             u16* __restrict__ Kf, u16* __restrict__ Vf, u16* __restrict__ Wof, unsigned* __restrict__ cnt) {
  __shared__ __attribute__((aligned(16))) char lds[K1_LDS];
  const int tid = threadIdx.x, lane = tid & 63, w = __builtin_amdgcn_readfirstlane(tid >> 6);
  const int t = xcd_remap(blockIdx.x, 256), rt = t >> 2, ct = t & 3;
  const unsigned lds0 = (unsigned)(uintptr_t)lds;
  bf16x8 xr[4];
  const bool act = lane < 56;
  {
    const u16* xp_ = x + (size_t)(rt * 32 + w * 4) * D + (act ? lane : 55) * 8;
    gload16<0>(xr[0], xp_); gload16<896>(xr[1], xp_); gload16<1792>(xr[2], xp_); gload16<2688>(xr[3], xp_);
  }
#pragma unroll
  for (int im = 0; im < 2; ++im) {
    const int c64 = 2 * ct + im;
    if (c64 < 7) {
#pragma unroll
      for (int i = 0; i < 7; ++i) {
        const int piece = w * 7 + i, row = piece * 8 + (lane >> 3);
        const int f = ((row >> 1) & 1) | (((row >> 3) & 1) << 1);
        const int c = (lane & 7) ^ (f << 1);
        glds16(wq + (size_t)row * D + c64 * 64 + c * 8, lds0 + im * K1_WI + piece * 1024);
      }
    } else {
#pragma unroll
      for (int i = 0; i < 7; ++i) {
        const int pc = w * 7 + i < 50 ? w * 7 + i : 49, mat = pc >= 25, piece = pc - 25 * mat;
        const int e = piece * 512 + lane * 8;
        glds16((mat ? wv : wk) + (e < 448 * 28 ? e : 448 * 28 - 8), lds0 + im * K1_WI + mat * 25600 + piece * 1024);
      }
    }
  }
  const int frag0 = blockIdx.x + 256 * (w & 1);
  const bool dofrag = w < 2 && frag0 < 392;
  const int frag = dofrag ? frag0 : 0;
  {
    const int fnb = frag / 14, fks = frag - fnb * 14;
    glds16(wo + (size_t)(32 * fks + (lane >> 1)) * D + 16 * fnb + (lane & 1) * 8, lds0 + K1_WO + w * 1024);
  }
  SBAR();
  waitv4<15>(xr[0], xr[1], xr[2], xr[3]);
  {
    u32x4 xv[4];
#pragma unroll
    for (int r = 0; r < 4; ++r) xv[r] = __builtin_bit_cast(u32x4, xr[r]);
    float v[4][8], mean[4], rs[4];
#pragma unroll
    for (int r = 0; r < 4; ++r) {
      float s = 0.f;
#pragma unroll
      for (int i = 0; i < 4; ++i) {
        v[r][2 * i] = act ? bflo(xv[r][i]) : 0.f; v[r][2 * i + 1] = act ? bfhi(xv[r][i]) : 0.f;
        s += v[r][2 * i] + v[r][2 * i + 1];
      }
      mean[r] = s;
    }
#pragma unroll
    for (int r = 0; r < 4; ++r) mean[r] = wave_sum(mean[r]) * (1.f / D);
#pragma unroll
    for (int r = 0; r < 4; ++r) {
      float q = 0.f;
#pragma unroll
      for (int i = 0; i < 8; ++i) { v[r][i] = act ? v[r][i] - mean[r] : 0.f; q += v[r][i] * v[r][i]; }
      rs[r] = q;
    }
#pragma unroll
    for (int r = 0; r < 4; ++r) rs[r] = __builtin_amdgcn_rsqf(wave_sum(rs[r]) * (1.f / D) + LN_EPS);
    char* xdst = lds + K1_X + lane * K1_XS + w * 64;
#pragma unroll
    for (int r = 0; r < 4; ++r) {
      u32x4 o;
#pragma unroll
      for (int i = 0; i < 4; ++i) o[i] = cvtpk(v[r][2 * i] * rs[r], v[r][2 * i + 1] * rs[r]);
      *(u32x4*)(xdst + r * 16) = o;
    }
  }
  WAITV_BAR(0);
  if (w == 7) {
    unsigned long long pc;
    int off, len;
    asm volatile("s_getpc_b64 %0\n1:\n\ts_mov_b32 %1, k_main-1b\n\ts_mov_b32 %2, k_main_tail-k_main" : "=s"(pc), "=s"(off), "=s"(len));
    if ((int)((blockIdx.x >> 3) + 1) * 1024 <= len)
      glds16s((const char*)(pc + (long long)off) + (blockIdx.x >> 3) * 1024, lane * 16, lds0 + K1_DUM);
  }
  if (dofrag) {
    const lds_cptr fp = (lds_cptr)lds + K1_WO + w * 1024 + (8 * (lane >> 4) + ((lane & 15) >> 2)) * 32 + 8 * (lane & 3);
    *(bf16x8*)(Wof + ((size_t)frag * 64 + lane) * 8) = cat8(trread(fp), trread(fp + 128));
  }
  const int nb = w & 3, im = w >> 2, c64 = 2 * ct + im, g = lane >> 4, q4 = (lane & 15) >> 2, p = lane & 3, l15 = lane & 15;
  const int f = ((q4 >> 1) & 1) | ((g & 1) << 1);
  const bool kv = c64 == 7;
  const int rs_ = kv ? 56 : 128;
  const lds_cptr wp = (lds_cptr)lds + im * K1_WI + (8 * g + q4) * rs_ +
                      (kv ? (nb >> 1) * 25600 + (nb & 1) * 32 + 8 * p : (((2 * nb + (p >> 1)) ^ (f << 1)) * 16) + 8 * (p & 1));
  const lds_cptr xp = (lds_cptr)lds + K1_X + g * K1_XS + l15 * 16;
  f32x4 acc0 = {0.f, 0.f, 0.f, 0.f}, acc1 = {0.f, 0.f, 0.f, 0.f};
  {
    bf16x8 a[14], b0[14], b1[14];
#pragma unroll
    for (int ks = 0; ks < 14; ++ks) {
      a[ks] = cat8(trread(wp + ks * 32 * rs_), trread(wp + ks * 32 * rs_ + 4 * rs_));
      b0[ks] = ldsr16(xp + ks * 4 * K1_XS);
      b1[ks] = ldsr16(xp + ks * 4 * K1_XS + 256);
    }
    SBAR();
#pragma unroll
    for (int ks = 0; ks < 14; ++ks) {
      acc0 = MFMA16(a[ks], b0[ks], acc0);
      acc1 = MFMA16(a[ks], b1[ks], acc1);
    }
  }
#pragma unroll
  for (int tb = 0; tb < 2; ++tb) {
    const f32x4 a = tb ? acc1 : acc0;
    const int s = rt * 32 + 16 * tb + l15;
    const int nl = 16 * nb + 4 * g;
    if (c64 < 7) {
      const int n0 = 64 * c64 + nl, h = n0 / 28, d0 = n0 - 28 * h;
      const int tg = s >> 3, c = (h & 3) * 8 + (s & 7), b = h >> 2;
      const size_t off = ((((size_t)(tg * 4 + b) * 2 + (d0 >> 4)) * 64) + ((d0 >> 3) & 1) * 32 + c) * 8 + (d0 & 7);
      *(u32x2*)(Qf + off) = (u32x2){cvtpk(a[0] * QSCALE, a[1] * QSCALE), cvtpk(a[2] * QSCALE, a[3] * QSCALE)};
    } else {
      const int kt = s >> 5, kk = s & 31;
      const int d0 = 16 * (nb & 1) + 4 * g;
      if (nb < 2) {
        const size_t off = ((((size_t)kt * 2 + (d0 >> 4)) * 64) + ((d0 >> 3) & 1) * 32 + kk) * 8 + (d0 & 7);
        u32x2 v = {cvtpk(a[0], a[1]), cvtpk(a[2], a[3])};
        if (d0 == 28) v = (u32x2){0x3f803f80u, 0x00003f80u};
        *(u32x2*)(Kf + off) = v;
      } else {
        const int sp = kk >> 4, j = ((kk >> 3) & 1) * 4 + (kk & 3), hi = (kk >> 2) & 1;
        const size_t off = ((((size_t)kt * 2 + sp) * 64) + hi * 32 + d0) * 8 + j;
        unsigned v0 = cvtpk(a[0], a[1]), v1 = cvtpk(a[2], a[3]);
        if (d0 == 28) { v0 = 0x00003f80u; v1 = 0u; }
        Vf[off] = (u16)v0; Vf[off + 8] = (u16)(v0 >> 16); Vf[off + 16] = (u16)v1; Vf[off + 24] = (u16)(v1 >> 16);
      }
    }
  }
  if (blockIdx.x == 0) { cnt[tid] = 0u; cnt[512 + tid] = 0u; }
  if (w == 7) asm volatile("s_waitcnt vmcnt(0)" ::: "memory");
}

constexpr int K2_RING = 0, K2_OT = 131072, K2_DUM = K2_OT + 7168, K2_X = K2_DUM + 4096, K2_LDS = K2_X + 16384;
constexpr int K2_HB = 65536, HBS = 452, K2_HS = K2_X;

DI float set_ref(bf16x8& q1, float m, int hi) {
  const float a = bflo(cvtpk(-m, 0.f)), r1 = -m - a, b = bflo(cvtpk(r1, 0.f)), c = bflo(cvtpk(r1 - b, 0.f));
  u32x4 v = __builtin_bit_cast(u32x4, q1);
  v[2] = hi ? cvtpk(a, b) : v[2];
  v[3] = hi ? cvtpk(c, 0.f) : v[3];
  q1 = __builtin_bit_cast(bf16x8, v);
  return -((a + b) + c);
}
DI float tile_max(const f32x16& s) {
  float a = __builtin_fmaxf(__builtin_fmaxf(s[0], s[1]), s[2]), b = __builtin_fmaxf(__builtin_fmaxf(s[3], s[4]), s[5]);
  a = __builtin_fmaxf(__builtin_fmaxf(a, s[6]), s[7]); b = __builtin_fmaxf(__builtin_fmaxf(b, s[8]), s[9]);
  a = __builtin_fmaxf(__builtin_fmaxf(a, s[10]), s[11]); b = __builtin_fmaxf(__builtin_fmaxf(b, s[12]), s[13]);
  a = __builtin_fmaxf(__builtin_fmaxf(a, s[14]), s[15]);
  return half_max(__builtin_fmaxf(a, b));
}
template <int N> DI void waitv() { asm volatile("s_waitcnt vmcnt(%0)" ::"i"(N) : "memory"); }
#define RBAR() asm volatile("s_barrier" ::: "memory")

DI void attn_safe(const u16* __restrict__ Kf, const u16* __restrict__ Vf, bf16x8 q0, bf16x8 q1, f32x16& o, int lane, int hi) {
  const f32x16 zero = {};
  float mref = 0.f;
  o = zero;
#pragma unroll 1
  for (int t = 0; t < 64; ++t) {
    const bf16x8 k0 = *(const bf16x8*)(Kf + t * 1024 + lane * 8), k1 = *(const bf16x8*)(Kf + t * 1024 + 512 + lane * 8);
    const bf16x8 v0 = *(const bf16x8*)(Vf + t * 1024 + lane * 8), v1 = *(const bf16x8*)(Vf + t * 1024 + 512 + lane * 8);
    (void)set_ref(q1, 0.f, hi);
    f32x16 s = MFMA32(k0, q0, zero);
    s = MFMA32(k1, q1, s);
    const float tm = tile_max(s);
    const float mn = set_ref(q1, t == 0 ? tm : __builtin_fmaxf(tm, mref), hi);
    const float al = t == 0 ? 0.f : __builtin_amdgcn_exp2f(mref - mn);
#pragma unroll
    for (int r = 0; r < 16; ++r) o[r] *= al;
    mref = mn;
    s = MFMA32(k0, q0, zero);
    s = MFMA32(k1, q1, s);
    u32x4 p0, p1;
#pragma unroll
    for (int i = 0; i < 4; ++i) {
      p0[i] = cvtpk(__builtin_amdgcn_exp2f(s[2 * i]), __builtin_amdgcn_exp2f(s[2 * i + 1]));
      p1[i] = cvtpk(__builtin_amdgcn_exp2f(s[8 + 2 * i]), __builtin_amdgcn_exp2f(s[8 + 2 * i + 1]));
    }
    o = MFMA32(v0, __builtin_bit_cast(bf16x8, p0), o);
    o = MFMA32(v1, __builtin_bit_cast(bf16x8, p1), o);
  }
}

#define EX4(S, i) do { S[i] = __builtin_amdgcn_exp2f(S[i]); S[i + 1] = __builtin_amdgcn_exp2f(S[i + 1]); \
    S[i + 2] = __builtin_amdgcn_exp2f(S[i + 2]); S[i + 3] = __builtin_amdgcn_exp2f(S[i + 3]); } while (0)
#define PK8(P, S, i) do { P[0] = cvtpk(S[i], S[i + 1]); P[1] = cvtpk(S[i + 2], S[i + 3]); P[2] = cvtpk(S[i + 4], S[i + 5]); P[3] = cvtpk(S[i + 6], S[i + 7]); } while (0)
#define BF(P) __builtin_bit_cast(bf16x8, P)
#define RDS(u, off) ldsr16(((u) < 4 ? B0 : B1) + ((u) & 3) * 8192 + (off))
#define CTILE(u, NX) do { \
      \
    sB = MFMA32(k0, qB0, zero); SBAR(); if (NX) k0 = RDS((u) + 1, 0); EX4(sA, 0); SBAR(); \
    sB = MFMA32(k1, qB1, sB); SBAR(); if (NX) k1 = RDS((u) + 1, 1024); EX4(sA, 4); PK8(p0, sA, 0); SBAR(); \
    oB = MFMA32(v1, BF(p1), oB); SBAR(); v1 = RDS(u, 3072); EX4(sA, 8); SBAR(); \
    oA = MFMA32(v0, BF(p0), oA); SBAR(); EX4(sA, 12); PK8(p1, sA, 8); SBAR(); \
      \
    sA = MFMA32(k0, qA0, zero); SBAR(); EX4(sB, 0); SBAR(); \
    sA = MFMA32(k1, qA1, sA); SBAR(); EX4(sB, 4); PK8(p0, sB, 0); SBAR(); \
    oA = MFMA32(v1, BF(p1), oA); SBAR(); EX4(sB, 8); SBAR(); \
    oB = MFMA32(v0, BF(p0), oB); SBAR(); if (NX) v0 = RDS((u) + 1, 2048); EX4(sB, 12); PK8(p1, sB, 8); SBAR(); } while (0)
#define CSTAGE(LAST) do { \
    CTILE(0, true); CTILE(1, true); CTILE(2, true); \
    if (!(LAST)) RBAR(); \
    CTILE(3, !(LAST)); } while (0)

DI void st16c(void* p, u32x4 v) {
  asm volatile("global_store_dwordx4 %0, %1, off sc1\n\ts_nop 1" ::"v"(p), "v"(v) : "memory");
}
DI void attn_phase(char* lds, const u16* __restrict__ x, const u16* __restrict__ Qf, const u16* __restrict__ Kf,
                   const u16* __restrict__ Vf, const u16* __restrict__ Wof, const u16* __restrict__ w1,
                   const u16* __restrict__ w2, u16* H, u16* HN) {
  const int tid = threadIdx.x, lane = tid & 63, w = __builtin_amdgcn_readfirstlane(tid >> 6), hi = lane >> 5;
  const int tg = blockIdx.x;
  const int kh = w >> 2, ng = w & 3;
  const char* wob = (const char*)(Wof + (size_t)(7 * ng * 14 + 7 * kh) * 512);
  const unsigned wl = lane * 16;
  bf16x8 wf[7][7];
#define WOFR(i_) (*(const bf16x8*)(wob + (((i_) / 7) * 14 + (i_) % 7) * 1024 + wl))
#define LDWO(i0, i1) do { _Pragma("unroll") for (int i_ = i0; i_ < i1; ++i_) wf[i_ / 7][i_ % 7] = WOFR(i_); } while (0)
#define BOF(k) (*(const bf16x8*)(lds + K2_OT + ((4 * (k) + (lane >> 4)) * 8 + (lane & 7)) * 16))
#define HBST(acc) do { if ((lane & 15) < 8) { \
      _Pragma("unroll") for (int n = 0; n < 7; ++n) *(f32x4*)(lds + K2_HB + ((kh * 8 + (lane & 15)) * HBS + 16 * (7 * ng + n) + 4 * (lane >> 4)) * 4) = acc[n]; \
    } } while (0)
  u32x4 xv;
  if (w < 4) {
    const f32x16 zero = {};
    const int qh = w & 1, kp = w >> 1;
    const lds_cptr rp0 = (lds_cptr)lds + K2_RING + lane * 16;
    const lds_cptr rp = rp0 + kp * 4096;
    lds_cptr B0 = rp, B1 = rp + 32768;
    const u16* qp = Qf + ((size_t)((tg * 4 + 2 * qh) * 2) * 64 + lane) * 8;
    bf16x8 qA0 = *(const bf16x8*)(qp), qA1 = *(const bf16x8*)(qp + 512), qB0 = *(const bf16x8*)(qp + 1024), qB1 = *(const bf16x8*)(qp + 1536);
    RBAR();
    bf16x8 k0 = ldsr16(rp0), k1 = ldsr16(rp0 + 1024), v0 = RDS(0, 2048), v1 = v0;
    (void)set_ref(qA1, 0.f, hi);
    (void)set_ref(qB1, 0.f, hi);
    f32x16 sA = MFMA32(k0, qA0, zero), sB = MFMA32(k0, qB0, zero);
    sA = MFMA32(k1, qA1, sA);
    sB = MFMA32(k1, qB1, sB);
    k0 = RDS(0, 0); k1 = RDS(0, 1024);
    (void)set_ref(qA1, tile_max(sA), hi);
    (void)set_ref(qB1, tile_max(sB), hi);
    sA = MFMA32(k0, qA0, zero);
    sA = MFMA32(k1, qA1, sA);
    f32x16 oA = zero, oB = zero;
    u32x4 p0, p1 = {0u, 0u, 0u, 0u};
    SBAR();
#pragma unroll 1
    for (int st = 0; st < 7; ++st) {
      CSTAGE(false);
      B0 = B1; B1 = rp + ((st + 2) & 3) * 32768;
    }
    CSTAGE(true);
    oB = MFMA32(v1, BF(p1), oB);
    {
      const f32x16 snd = kp ? oA : oB;
#pragma unroll
      for (int r4 = 0; r4 < 4; ++r4)
        *(f32x4*)(lds + K2_X + ((w * 4 + r4) * 64 + lane) * 16) = (f32x4){snd[4 * r4], snd[4 * r4 + 1], snd[4 * r4 + 2], snd[4 * r4 + 3]};
    }
    f32x16 o0 = kp ? oB : oA;
    bf16x8 qk0 = kp ? qB0 : qA0, qk1 = kp ? qB1 : qA1;
    asm volatile("" : "+v"(o0), "+v"(qk0), "+v"(qk1));
    SBAR();
    LBAR();
#pragma unroll
    for (int r4 = 0; r4 < 4; ++r4) {
      const f32x4 t = *(const f32x4*)(lds + K2_X + (((w ^ 2) * 4 + r4) * 64 + lane) * 16);
      o0[4 * r4] += t[0]; o0[4 * r4 + 1] += t[1]; o0[4 * r4 + 2] += t[2]; o0[4 * r4 + 3] += t[3];
    }
    const int qb = 2 * qh + kp;
    {
      float chk = 0.f;
#pragma unroll
      for (int r = 0; r < 16; ++r) chk = __builtin_fmaf(o0[r], 0.f, chk);
      const bool bad = !(chk == 0.f) || !(o0[12] < 1.8e19f);
      if (__any(bad)) {
        attn_safe(Kf, Vf, qk0, qk1, o0, lane, hi);
      }
    }
    {
      auto sw = __builtin_amdgcn_permlane32_swap(__float_as_uint(o0[12]), __float_as_uint(o0[12]), false, false);
      const float il = 1.f / __uint_as_float(sw[1]);
      const int c = lane & 31;
#pragma unroll
      for (int r4 = 0; r4 < 4; ++r4) {
        const int d = 8 * r4 + 4 * hi, k = 28 * (4 * qb + (c >> 3)) + d;
        if (d < 28)
          *(u32x2*)(lds + K2_OT + ((k >> 3) * 8 + (c & 7)) * 16 + (k & 7) * 2) =
              (u32x2){cvtpk(o0[4 * r4] * il, o0[4 * r4 + 1] * il), cvtpk(o0[4 * r4 + 2] * il, o0[4 * r4 + 3] * il)};
      }
    }
    LBAR();
    LDWO(16, 39);
    xv = *(const u32x4*)(x + (size_t)(tg * 8 + w) * D + (lane < 56 ? lane : 55) * 8);
    SBAR();
    {
      bf16x8 bo[7];
#pragma unroll
      for (int k = 0; k < 7; ++k) bo[k] = BOF(k);
#pragma unroll
      for (int i = 0; i < 16; ++i) wf[i / 7][i % 7] = *(const bf16x8*)(lds + K2_RING + (i >> 3) * 32768 + w * 8192 + (i & 7) * 1024 + lane * 16);
      f32x4 acc[7];
#pragma unroll
      for (int n = 0; n < 7; ++n) acc[n] = (f32x4){0.f, 0.f, 0.f, 0.f};
#pragma unroll
      for (int i = 0; i < 39; ++i) acc[i / 7] = MFMA16(wf[i / 7][i % 7], bo[i % 7], acc[i / 7]);
      HBST(acc);
    }
  } else {
    const int hw = w - 4;
    __builtin_amdgcn_s_setprio(3);
    const unsigned ring = (unsigned)(uintptr_t)lds + K2_RING + hw * 8192;
    const u16* kp = Kf + (size_t)hw * 2048;
    const u16* vp = Vf + (size_t)hw * 2048;
    const unsigned l16 = lane * 16;
#define DMAS(s_) do { const unsigned sl_ = ring + ((s_) & 3) * 32768; \
    _Pragma("unroll") for (int u_ = 0; u_ < 2; ++u_) { \
      glds16s(kp + (s_) * 8192 + u_ * 1024, l16, sl_ + u_ * 4096); glds16s(kp + (s_) * 8192 + u_ * 1024 + 512, l16, sl_ + u_ * 4096 + 1024); \
      glds16s(vp + (s_) * 8192 + u_ * 1024, l16, sl_ + u_ * 4096 + 2048); glds16s(vp + (s_) * 8192 + u_ * 1024 + 512, l16, sl_ + u_ * 4096 + 3072); } } while (0)
    const unsigned dum = (unsigned)(uintptr_t)lds + K2_DUM + hw * 1024;
    const int xg = tg & 7, rk = tg >> 3;
    const int ts = xg < 4 ? 25 * xg : 100 + 24 * (xg - 4), te = ts + (xg < 4 ? 25 : 24);
    const int n0 = ts / 7, n1 = (te - 1) / 7;
    const int nb0 = n0 + 3 <= 27 ? n0 : 24;
    const u16* p1 = w1 + (size_t)(64 * rk + 16 * hw) * F + nb0 * 64;
    const u16* p1b = w1 + (size_t)(64 * rk + 16 * hw) * F + n1 * 64;
    const u16* p2 = w2 + (size_t)(56 * rk + 14 * hw) * S + 4 * xg * 64;
    const unsigned o1 = ((lane >> 5) * F + (lane & 31) * 8) * 2, o1b = ((lane >> 3) * F + (lane & 7) * 8) * 2, o2 = ((lane >> 5) * S + (lane & 31) * 8) * 2;
#define PFD(i0, i1) do { _Pragma("unroll") for (int i_ = i0; i_ < i1; ++i_) { \
      if (i_ < 8) glds16s(p1 + (size_t)i_ * 2 * F, o1, dum); \
      else if (i_ < 10) glds16s(p1b + (size_t)(i_ - 8) * 8 * F, o1b, dum); \
      else glds16s(p2 + (size_t)(i_ - 10) * 2 * S, o2, dum); } } while (0)
    DMAS(0); DMAS(1); DMAS(2);
    waitv<16>(); RBAR();
    LDWO(0, 12); SBAR(); PFD(0, 3);
    waitv<23>(); RBAR();
    DMAS(3); LDWO(12, 24); SBAR(); PFD(3, 6);
    waitv<38>(); RBAR();
    DMAS(4); LDWO(24, 36); SBAR(); PFD(6, 9);
    waitv<38>(); RBAR();
    DMAS(5); LDWO(36, 49); SBAR(); PFD(9, 12);
    waitv<39>(); RBAR();
    bf16x8 wx[10];
    DMAS(6);
#pragma unroll
    for (int i = 0; i < 10; ++i) wx[i] = *(const bf16x8*)(wob - 7 * 1024 + (((39 + i) / 7) * 14 + (39 + i) % 7) * 1024 + wl);
    SBAR(); PFD(12, 15);
    waitv<37>(); RBAR();
    DMAS(7); PFD(15, 17);
    waitv<23>(); RBAR();
#pragma unroll
    for (int i = 0; i < 8; ++i) glds16s(wob - 7 * 1024 + ((i / 7) * 14 + i % 7) * 1024, wl, ring + i * 1024);
    waitv<10>(); RBAR();
#pragma unroll
    for (int i = 8; i < 16; ++i) glds16s(wob - 7 * 1024 + ((i / 7) * 14 + i % 7) * 1024, wl, ring + 32768 + (i - 8) * 1024);
    xv = *(const u32x4*)(x + (size_t)(tg * 8 + w) * D + (lane < 56 ? lane : 55) * 8);
    SBAR();
    __builtin_amdgcn_s_setprio(0);
    RBAR();
    waitv<1>();
    LBAR();
    {
      f32x4 acc[7];
      {
        bf16x8 bo[7];
#pragma unroll
        for (int k = 0; k < 7; ++k) bo[k] = BOF(k);
        acc[5] = (f32x4){0.f, 0.f, 0.f, 0.f}; acc[6] = acc[5];
#pragma unroll
        for (int i = 0; i < 10; ++i) acc[(39 + i) / 7] = MFMA16(wx[i], bo[(39 + i) % 7], acc[(39 + i) / 7]);
      }
      SBAR();
      bf16x8 bo[7];
#pragma unroll
      for (int k = 0; k < 7; ++k) bo[k] = BOF(7 + k);
#pragma unroll
      for (int n = 0; n < 7; ++n) {
        if (n < 5) acc[n] = (f32x4){0.f, 0.f, 0.f, 0.f};
#pragma unroll
        for (int k = 0; k < 7; ++k) acc[n] = MFMA16(wf[n][k], bo[k], acc[n]);
      }
      HBST(acc);
    }
  }
  LBAR();
  {
    const int s = tg * 8 + w;
    const bool act = lane < 56;
    const int lc = act ? lane : 55;
    float v[8];
    const f32x4 a0 = *(const f32x4*)(lds + K2_HB + (w * HBS + lc * 8) * 4), a1 = *(const f32x4*)(lds + K2_HB + (w * HBS + lc * 8 + 4) * 4);
    const f32x4 c0 = *(const f32x4*)(lds + K2_HB + ((8 + w) * HBS + lc * 8) * 4), c1 = *(const f32x4*)(lds + K2_HB + ((8 + w) * HBS + lc * 8 + 4) * 4);
    u32x4 hv;
#pragma unroll
    for (int i = 0; i < 2; ++i) {
      hv[i] = cvtpk(bflo(xv[i]) + a0[2 * i] + c0[2 * i], bfhi(xv[i]) + a0[2 * i + 1] + c0[2 * i + 1]);
      hv[2 + i] = cvtpk(bflo(xv[2 + i]) + a1[2 * i] + c1[2 * i], bfhi(xv[2 + i]) + a1[2 * i + 1] + c1[2 * i + 1]);
    }
    if (act) st16c(H + (size_t)s * D + lane * 8, hv);
#pragma unroll
    for (int i = 0; i < 4; ++i) { v[2 * i] = act ? bflo(hv[i]) : 0.f; v[2 * i + 1] = act ? bfhi(hv[i]) : 0.f; }
#pragma unroll
    for (int pass = 0; pass < 2; ++pass) {
      float sm = 0.f;
#pragma unroll
      for (int i = 0; i < 8; ++i) sm += v[i];
      const float mean = wave_sum(sm) * (1.f / D);
      float q = 0.f;
#pragma unroll
      for (int i = 0; i < 8; ++i) { v[i] = act ? v[i] - mean : 0.f; q += v[i] * v[i]; }
      const float rs = __builtin_amdgcn_rsqf(wave_sum(q) * (1.f / D) + LN_EPS);
#pragma unroll
      for (int i = 0; i < 8; ++i) v[i] *= rs;
    }
    if (act) *(u32x4*)(lds + K2_HS + w * 912 + lane * 16) = (u32x4){cvtpk(v[0], v[1]), cvtpk(v[2], v[3]), cvtpk(v[4], v[5]), cvtpk(v[6], v[7])};
  }
  LBAR();
  if (tid < D / 2) {
    unsigned r[8];
#pragma unroll
    for (int i = 0; i < 8; ++i) r[i] = *(const unsigned*)(lds + K2_HS + i * 912 + tid * 4);
    u32x4 lo, hi2;
#pragma unroll
    for (int i = 0; i < 4; ++i) {
      lo[i] = (r[2 * i] & 0xffffu) | (r[2 * i + 1] << 16);
      hi2[i] = (r[2 * i] >> 16) | (r[2 * i + 1] & 0xffff0000u);
    }
    u32x4* dst = (u32x4*)(HN + ((size_t)tg * D + 2 * tid) * 8);
    st16c(dst, lo); st16c(dst + 1, hi2);
  }
}

constexpr int G_RING = 4, G_LDS = 8 * G_RING * 4096;
typedef __attribute__((address_space(1))) unsigned gu32;
typedef __attribute__((address_space(1))) unsigned long long gu64;
#define RLX_AGENT __ATOMIC_RELAXED, __HIP_MEMORY_SCOPE_AGENT
template <int OFF> DI void gload16c(bf16x8& d, const void* p) {
  asm volatile("global_load_dwordx4 %0, %1, off offset:%2 sc1" : "=v"(d) : "v"(p), "i"(OFF) : "memory");
}
template <int PH, int NSH>
DI void mlp_tile(char* lds, int t, const u16* act, const u16* __restrict__ W, const u16* __restrict__ bias, const u16* H, u16* out,
                 unsigned* cnt, unsigned want) {
  constexpr int KT = PH == 0 ? S : F, LDW = PH == 0 ? F : S, NK = KT / 256, NR = NK - G_RING, LA = 5;
  const int tid = threadIdx.x, lane = tid & 63, w = __builtin_amdgcn_readfirstlane(tid >> 6);
  const int nt = t / 7, mt = t - nt * 7;
  const unsigned lds0 = (unsigned)(uintptr_t)lds + w * (G_RING * 4096);
  const int g = lane >> 4, q4 = (lane & 15) >> 2, p = lane & 3, l15 = lane & 15;
  const int enb = w >> 1, emb = 2 * (w & 1);
  u32x2 eb;
  if (PH == 0) eb = *(const u32x2*)(bias + 64 * nt + 16 * enb + 4 * g);
  else eb[0] = bias[64 * nt + 16 * enb + l15];
  SBAR();
  const u16* asrc = act + ((size_t)(4 * w + g) * D + 64 * mt + l15) * 8;
  const int r8 = lane >> 3, fe = (r8 >> 1) & 1;
  const u16* wsrc0 = W + (size_t)(32 * w + r8) * LDW + 64 * nt + ((lane & 7) ^ (fe << 1)) * 8;
  const u16* wsrc1 = W + (size_t)(32 * w + 8 + r8) * LDW + 64 * nt + ((lane & 7) ^ ((fe | 2) << 1)) * 8;
  const int f = ((q4 >> 1) & 1) | ((g & 1) << 1);
  int woff[4];
#pragma unroll
  for (int nb = 0; nb < 4; ++nb) woff[nb] = w * (G_RING * 4096) + (8 * g + q4) * 128 + (((2 * nb + (p >> 1)) ^ (f << 1)) * 16) + 8 * (p & 1);
  bf16x8 af[NK][4];
  f32x4 acc[4][4] = {};
#define STAGE_W(i) do { const unsigned sl_ = lds0 + ((i) % G_RING) * 4096; const size_t o_ = (size_t)(i) * 256 * LDW; \
    glds16(wsrc0 + o_, sl_); glds16(wsrc1 + o_, sl_ + 1024); glds16(wsrc0 + o_ + 16 * LDW, sl_ + 2048); glds16(wsrc1 + o_ + 16 * LDW, sl_ + 3072); } while (0)
#define LOAD_A(i) do { const u16* ap_ = asrc + (size_t)(i) * 32 * D * 8; \
    gload16c<0>(af[i][0], ap_); gload16c<256>(af[i][1], ap_); gload16c<512>(af[i][2], ap_); gload16c<768>(af[i][3], ap_); } while (0)
#define RDW(WF, i) do { _Pragma("unroll") for (int nb_ = 0; nb_ < 4; ++nb_) { const lds_cptr sp_ = (lds_cptr)lds + woff[nb_] + ((i) % G_RING) * 4096; \
      WF[nb_] = cat8(trread(sp_), trread(sp_ + 512)); } } while (0)
#define MMA(WF, i) do { _Pragma("unroll") for (int nb_ = 0; nb_ < 4; ++nb_) _Pragma("unroll") for (int mb_ = 0; mb_ < 4; ++mb_) \
      acc[nb_][mb_] = PH == 0 ? MFMA16(WF[nb_], af[i][mb_], acc[nb_][mb_]) : MFMA16(af[i][mb_], WF[nb_], acc[nb_][mb_]); } while (0)
  bf16x8 wr[NR][4];
#pragma unroll
  for (int j = 0; j < NR; ++j) STAGE_W(G_RING + j);
  if (NR < G_RING) STAGE_W(G_RING - 1);
#pragma unroll
  for (int j = 0; j < NR; ++j) {
    waitv<12>();
    RDW(wr[j], G_RING + j);
    asm volatile("s_waitcnt lgkmcnt(0)" ::: "memory");
    STAGE_W(j);
  }
  if (w == 0) {
    gu32* cp = (gu32*)(cnt + (lane < NSH ? lane : 0) * 64);
    for (unsigned spins = 0; spins < (1u << 20); ++spins) {
      if (__all(__hip_atomic_load(cp, RLX_AGENT) >= want)) break;
      __builtin_amdgcn_s_sleep(1);
    }
  }
  asm volatile("s_barrier" ::: "memory");
  unsigned long long eh0 = 0, eh1 = 0;
  if (PH == 1) {
    const int s = 64 * nt + 16 * enb + l15;
    eh0 = __hip_atomic_load((gu64*)(H + (size_t)s * D + 64 * mt + 16 * emb + 4 * g), RLX_AGENT);
    eh1 = __hip_atomic_load((gu64*)(H + (size_t)s * D + 64 * mt + 16 * emb + 16 + 4 * g), RLX_AGENT);
  }
  SBAR();
#pragma unroll
  for (int j = 0; j < LA; ++j) LOAD_A((j + G_RING) % NK);
#pragma unroll
  for (int j = 0; j < NK; ++j) {
    const int i = (j + G_RING) % NK;
    const int rem = NK - 1 - j < LA - 1 ? NK - 1 - j : LA - 1;
    if (rem == 4) waitv4<16>(af[i][0], af[i][1], af[i][2], af[i][3]);
    else if (rem == 3) waitv4<12>(af[i][0], af[i][1], af[i][2], af[i][3]);
    else if (rem == 2) waitv4<8>(af[i][0], af[i][1], af[i][2], af[i][3]);
    else if (rem == 1) waitv4<4>(af[i][0], af[i][1], af[i][2], af[i][3]);
    else waitv4<0>(af[i][0], af[i][1], af[i][2], af[i][3]);
    if (j < NR) {
      MMA(wr[j], i);
    } else {
      bf16x8 wf[4];
      RDW(wf, i);
      MMA(wf, i);
    }
    SBAR();
    if (j + LA < NK) LOAD_A((j + LA + G_RING) % NK);
  }
#undef STAGE_W
#undef LOAD_A
#undef RDW
#undef MMA
  {
    f32x4* xb = (f32x4*)lds;
#pragma unroll
    for (int nb = 0; nb < 4; ++nb)
#pragma unroll
      for (int mb = 0; mb < 4; ++mb) xb[(w * 16 + nb * 4 + mb) * 64 + lane] = acc[nb][mb];
  }
  LBAR();
  {
    const f32x4* xb = (const f32x4*)lds;
#pragma unroll
    for (int e = 0; e < 2; ++e) {
      f32x4 a = xb[(2 * w + e) * 64 + lane];
#pragma unroll
      for (int v = 1; v < 8; ++v) a += xb[(v * 16 + 2 * w + e) * 64 + lane];
      const int mb = emb + e;
      if (PH == 0) {
        const int fi = 64 * nt + 16 * enb + 4 * g, d = 64 * mt + 16 * mb + l15;
        float z[4] = {a[0] + bflo(eb[0]), a[1] + bfhi(eb[0]), a[2] + bflo(eb[1]), a[3] + bfhi(eb[1])};
#pragma unroll
        for (int i = 0; i < 4; ++i) {
          const float zz = z[i], a2 = zz * (1.f + 0.044715f * zz * zz) * (-2.f * 0.7978845608028654f * 1.4426950408889634f);
          z[i] = zz * __builtin_amdgcn_rcpf(1.f + __builtin_amdgcn_exp2f(a2));
        }
        const unsigned long long v = (unsigned long long)cvtpk(z[0], z[1]) | ((unsigned long long)cvtpk(z[2], z[3]) << 32);
        __hip_atomic_store((gu64*)(out + ((size_t)(fi >> 3) * D + d) * 8 + (fi & 7)), v, RLX_AGENT);
      } else {
        const int d = 64 * mt + 16 * mb + 4 * g, s = 64 * nt + 16 * enb + l15;
        const float bb = bflo(eb[0]);
        const unsigned long long hv = e ? eh1 : eh0;
        const unsigned h0 = (unsigned)hv, h1 = (unsigned)(hv >> 32);
        *(u32x2*)(out + (size_t)s * D + d) =
            (u32x2){cvtpk(a[0] + bb + bflo(h0), a[1] + bb + bfhi(h0)), cvtpk(a[2] + bb + bflo(h1), a[3] + bb + bfhi(h1))};
      }
    }
  }
}
#define PUBLISH(c) do { asm volatile("s_waitcnt vmcnt(0) lgkmcnt(0)\n\ts_barrier" ::: "memory"); \
    if (threadIdx.x == 0) __hip_atomic_fetch_add((gu32*)(c), 1u, RLX_AGENT); } while (0)

extern "C" __global__ __launch_bounds__(512, 2) void k_main(const u16* __restrict__ x, const u16* __restrict__ Qf, const u16* __restrict__ Kf,
                                              const u16* __restrict__ Vf, const u16* __restrict__ Wof, const u16* __restrict__ w1,
                                              const u16* __restrict__ b1, const u16* __restrict__ w2, const u16* __restrict__ b2,
                                              u16* H, u16* HN, u16* U, u16* out, unsigned* cnt) {
  __shared__ __attribute__((aligned(16))) char lds[K2_LDS];
  attn_phase(lds, x, Qf, Kf, Vf, Wof, w1, w2, H, HN);
  PUBLISH(cnt + (8 + (blockIdx.x & 7)) * 64);
  if (blockIdx.x < 196) {
    const int t = xcd_remap(blockIdx.x, 196);
    mlp_tile<0, 8>(lds, t, HN, w1, b1, H, U, cnt + 8 * 64, 32u);
    PUBLISH(cnt + (t % 7) * 64);
  }
  if (blockIdx.x < 224) {
    const int t2 = xcd_remap(blockIdx.x, 224);
    mlp_tile<1, 1>(lds, t2, U, w2, b2, H, out, cnt + (t2 % 7) * 64, 28u);
    asm volatile("k_main_tail:" ::: "memory");
  }
}

extern "C" void kernel_launch(void* const* d_in, const int* in_sizes, int n_in, void* d_out, int out_size, void* d_ws, size_t ws_size,
                              hipStream_t stream) {
  const u16* x = (const u16*)d_in[0];
  const u16* wq = (const u16*)d_in[1];
  const u16* wk = (const u16*)d_in[2];
  const u16* wv = (const u16*)d_in[3];
  const u16* wo = (const u16*)d_in[4];
  const u16* w1 = (const u16*)d_in[6];
  const u16* b1 = (const u16*)d_in[7];
  const u16* w2 = (const u16*)d_in[8];
  const u16* b2 = (const u16*)d_in[9];
  char* ws = (char*)d_ws;
  u16 *Qf = (u16*)(ws + WS_QF), *Kf = (u16*)(ws + WS_KF), *Vf = (u16*)(ws + WS_VF), *Wof = (u16*)(ws + WS_WOF);
  u16 *H = (u16*)(ws + WS_H), *HN = (u16*)(ws + WS_HN), *U = (u16*)(ws + WS_U);
  unsigned* cnt = (unsigned*)(ws + WS_CNT);
  k_qkv<<<256, 512, 0, stream>>>(x, wq, wk, wv, wo, Qf, Kf, Vf, Wof, cnt);
  k_main<<<256, 512, 0, stream>>>(x, Qf, Kf, Vf, Wof, w1, b1, w2, b2, H, HN, U, (u16*)d_out, cnt);
}
```
